```python
import torch

def _softmax(x, axis=-1):
    return torch.softmax(x, dim=axis)
def _gelu(x):
    return torch.nn.functional.gelu(x, approximate="tanh")
def _silu(x):
    return torch.nn.functional.silu(x)
def _sigmoid(x):
    return torch.sigmoid(x)
def _layernorm(x, axis=-1, eps=1e-5):
    m = x.mean(dim=axis, keepdim=True)
    v = x.var(dim=axis, keepdim=True, unbiased=False)
    return (x - m) / torch.sqrt(v + eps)
def _scatter_add(tgt, idx, src, axis=0):
    return tgt.clone().index_add_(axis, idx, src)
def _segment_sum(src, seg, n):
    out = torch.zeros((n,) + src.shape[1:], dtype=src.dtype)
    return out.index_add_(0, seg, src)
def _pad(x, pw):
    flat = tuple(v for pair in reversed(pw) for v in pair)
    return torch.nn.functional.pad(x, flat)

kernel_name = 'recurrent_scan__ssm_conv_h3__glu_gated_swiglu__prompt'

def setup_inputs():

    _w = (lambda a: (lambda t: t.to(torch.bfloat16) if t.is_floating_point() else t)(torch.as_tensor(a)))
    def _A_setup():
        B, S, D = 1, 2048, 448
        ks = 4
        return {
            "q": _w((lambda *s: torch.randn(*s, dtype=torch.bfloat16) * 0.05)(B, S, D)),
            "k": _w((lambda *s: torch.randn(*s, dtype=torch.bfloat16) * 0.8)(B, S, D)),
            "v": _w((lambda *s: torch.randn(*s, dtype=torch.bfloat16) * 0.8)(B, S, D)),
            "shift_k": _w((lambda *s: torch.randn(*s, dtype=torch.bfloat16) * 0.8)(D, ks) * 0.5),
            "ssm_k": _w((lambda *s: torch.randn(*s, dtype=torch.bfloat16) * 0.8)(D, S) * (1.0 / S ** 0.5)),
            "ssm_D": _w((lambda *s: torch.randn(*s, dtype=torch.bfloat16) * 0.8)(D)),
        }
    _A_in = _A_setup()

    def _B_setup():
        B, S, D, F = 1, 2048, 448, 1792
        return {
            "x": _w((lambda *s: torch.randn(*s, dtype=torch.bfloat16) * 0.05)(B, S, D)),
            "w1": _w((lambda *s: torch.randn(*s, dtype=torch.bfloat16) * 0.2)(D, F) * (2.0 / D) ** 0.5),
            "w3": _w((lambda *s: torch.randn(*s, dtype=torch.bfloat16) * 0.2)(D, F) * (2.0 / D) ** 0.5),
            "w2": _w((lambda *s: torch.randn(*s, dtype=torch.bfloat16) * 0.2)(F, D) * (2.0 / F) ** 0.5),
        }
    _B_in = _B_setup()

    return {
        "A_q": _A_in["q"],
        "A_k": _A_in["k"],
        "A_v": _A_in["v"],
        "A_shift_k": _A_in["shift_k"],
        "A_ssm_k": _A_in["ssm_k"],
        "A_ssm_D": _A_in["ssm_D"],
        "B_x": _B_in["x"],
        "B_w1": _B_in["w1"],
        "B_w3": _B_in["w3"],
        "B_w2": _B_in["w2"],
    }

def reference(A_q, A_k, A_v, A_shift_k, A_ssm_k, A_ssm_D, B_x, B_w1, B_w3, B_w2):
    def _A_ref(q, k, v, shift_k, ssm_k, ssm_D):
        B, S, D = q.shape
        ks = shift_k.shape[-1]
        k_pad = _pad(k, ((0, 0), (ks - 1, 0), (0, 0)))
        k_shift = torch.zeros_like(k)
        for t in range(ks):
            k_shift = k_shift + k_pad[:, t:t + S, :] * shift_k[:, ks - 1 - t][None, None, :]
        kv = k_shift * v
        L = 2 * S
        kv_pad = _pad(kv, ((0, 0), (0, S), (0, 0)))
        kf_pad = _pad(ssm_k, ((0, 0), (0, S)))
        kv_f = (lambda x, axes=(-2,-1): torch.fft.rfft2(x.float(), dim=axes))(kv_pad[:, None, :, :], axes=(1, 2))
        kf_f = (lambda x, axes=(-2,-1): torch.fft.rfft2(x.float(), dim=axes))(kf_pad[:, None, :], axes=(1, 2))
        kf_f = (lambda x, *d: x.permute(*d))(kf_f, 1, 2, 0)
        y_f = kv_f * kf_f[None, :, :, :]
        y = (lambda x, s, axes=(-2,-1): torch.fft.irfft2(x, s=s, dim=axes).to(torch.bfloat16))(y_f, (1, L), axes=(1, 2))[:, 0, :S, :]
        y = y + kv * ssm_D[None, None, :]
        return q * y

    def _B_ref(x, w1, w3, w2):
        gate = torch.einsum("bsd,df->bsf", x, w1)
        up = torch.einsum("bsd,df->bsf", x, w3)
        h = _silu(gate) * up
        out = torch.einsum("bsf,fd->bsd", h, w2)
        return out

    x = A_q
    xn = _layernorm(x, axis=-1)
    s = _A_ref(xn, A_k, A_v, A_shift_k, A_ssm_k, A_ssm_D)
    g = _silu(_B_ref(xn, B_w1, B_w3, B_w2))
    return x + s * g
```

```cpp
#include <hip/hip_runtime.h>
#include <hip/hip_bf16.h>
#include <stdint.h>

typedef unsigned short u16;
constexpr int S = 2048, D = 448, F = 1792;

__device__ __forceinline__ float bf2f(u16 x) { return __uint_as_float(((unsigned)x) << 16); }
__device__ __forceinline__ u16 f2bf(float f) { __hip_bfloat16 b = __float2bfloat16(f); return *(u16*)&b; }
__device__ __forceinline__ float rbf(float f) { return bf2f(f2bf(f)); }

__global__ void k_ln(const u16* x, u16* xn) {
    int row = blockIdx.x, lane = threadIdx.x;
    float v[7]; float s = 0.f;
    for (int i = 0; i < 7; ++i) { v[i] = bf2f(x[row * D + lane + 64 * i]); s += v[i]; }
    for (int m = 32; m >= 1; m >>= 1) s += __shfl_xor(s, m);
    float mean = s / D;
    float q = 0.f;
    for (int i = 0; i < 7; ++i) { float d = v[i] - mean; q += d * d; }
    for (int m = 32; m >= 1; m >>= 1) q += __shfl_xor(q, m);
    float var = rbf(q / D);
    float mb = rbf(mean);
    float den = rbf(sqrtf(rbf(var + 1e-5f)));
    for (int i = 0; i < 7; ++i) xn[row * D + lane + 64 * i] = f2bf(rbf(v[i] - mb) / den);
}

__global__ void k_kv(const u16* k, const u16* v, const u16* sk, u16* kv) {
    int idx = blockIdx.x * blockDim.x + threadIdx.x;
    if (idx >= S * D) return;
    int s = idx / D, d = idx % D;
    float acc = 0.f;
    for (int t = 0; t < 4; ++t) {
        int r = s + t - 3;
        float kk = r >= 0 ? bf2f(k[r * D + d]) : 0.f;
        acc = rbf(acc + rbf(kk * bf2f(sk[d * 4 + 3 - t])));
    }
    kv[idx] = f2bf(acc * bf2f(v[idx]));
}

__global__ void k_conv(const u16* kv, const u16* h, const u16* Dv, u16* y) {
    int idx = blockIdx.x * blockDim.x + threadIdx.x;
    if (idx >= S * D) return;
    int s = idx / D, d = idx % D;
    float acc = 0.f;
    for (int j = 0; j <= s; ++j) acc += bf2f(h[d * S + (s - j)]) * bf2f(kv[j * D + d]);
    float yy = rbf(acc);
    y[idx] = f2bf(yy + rbf(bf2f(kv[idx]) * bf2f(Dv[d])));
}

__global__ void k_g1(const u16* xn, const u16* w1, const u16* w3, u16* h) {
    int idx = blockIdx.x * blockDim.x + threadIdx.x;
    if (idx >= S * F) return;
    int s = idx / F, f = idx % F;
    float g = 0.f, u = 0.f;
    for (int k = 0; k < D; ++k) { float a = bf2f(xn[s * D + k]); g += a * bf2f(w1[k * F + f]); u += a * bf2f(w3[k * F + f]); }
    g = rbf(g); u = rbf(u);
    float sg = rbf(g / (1.f + __expf(-g)));
    h[idx] = f2bf(sg * u);
}

__global__ void k_g2(const u16* h, const u16* w2, const u16* x, const u16* xn, const u16* y, u16* out) {
    int idx = blockIdx.x * blockDim.x + threadIdx.x;
    if (idx >= S * D) return;
    int s = idx / D, d = idx % D;
    float o = 0.f;
    for (int f = 0; f < F; ++f) o += bf2f(h[s * F + f]) * bf2f(w2[f * D + d]);
    o = rbf(o);
    float g = rbf(o / (1.f + __expf(-o)));
    float sv = rbf(bf2f(xn[idx]) * bf2f(y[idx]));
    out[idx] = f2bf(bf2f(x[idx]) + rbf(sv * g));
}

extern "C" void kernel_launch(void* const* d_in, const int* in_sizes, int n_in,
                              void* d_out, int out_size, void* d_ws, size_t ws_size,
                              hipStream_t stream) {
    const u16* q = (const u16*)d_in[0];
    const u16* k = (const u16*)d_in[1];
    const u16* v = (const u16*)d_in[2];
    const u16* sk = (const u16*)d_in[3];
    const u16* hk = (const u16*)d_in[4];
    const u16* Dv = (const u16*)d_in[5];
    const u16* w1 = (const u16*)d_in[7];
    const u16* w3 = (const u16*)d_in[8];
    const u16* w2 = (const u16*)d_in[9];
    u16* ws = (u16*)d_ws;
    u16* xn = ws; u16* kv = xn + S * D; u16* y = kv + S * D; u16* h = y + S * D;
    k_ln<<<S, 64, 0, stream>>>(q, xn);
    k_kv<<<(S * D + 255) / 256, 256, 0, stream>>>(k, v, sk, kv);
    k_conv<<<(S * D + 255) / 256, 256, 0, stream>>>(kv, hk, Dv, y);
    k_g1<<<(S * F + 255) / 256, 256, 0, stream>>>(xn, w1, w3, h);
    k_g2<<<(S * D + 255) / 256, 256, 0, stream>>>(h, w2, q, xn, y, (u16*)d_out);
}
```

```cpp
#include <hip/hip_runtime.h>
#include <stdint.h>

typedef unsigned short u16;
typedef unsigned long long u64;
typedef short s16x4 __attribute__((ext_vector_type(4)));
typedef short s16x8 __attribute__((ext_vector_type(8)));
typedef __bf16 bf16x2 __attribute__((ext_vector_type(2)));
typedef __bf16 bf16x8 __attribute__((ext_vector_type(8)));
typedef float f32x2 __attribute__((ext_vector_type(2)));
typedef float f32x4 __attribute__((ext_vector_type(4)));
typedef float f32x16 __attribute__((ext_vector_type(16)));
typedef unsigned u32x2 __attribute__((ext_vector_type(2)));
typedef unsigned u32x4 __attribute__((ext_vector_type(4)));
#define LAS __attribute__((address_space(3)))
#define GAS __attribute__((address_space(1)))
#define RLX_AGENT __ATOMIC_RELAXED, __HIP_MEMORY_SCOPE_AGENT

constexpr int S = 2048, D = 448, F = 1792;

constexpr unsigned WS_EPOCH = 0;
constexpr unsigned WS_FKV = 256;
constexpr unsigned WS_FH = 1024;
constexpr unsigned WS_FY = 4096;
constexpr unsigned WS_STATS = 8192;
constexpr unsigned WS_U = 262144;
constexpr unsigned WS_Y = WS_U + D * S * 2;
constexpr unsigned WS_H = WS_Y + D * S * 2;
constexpr unsigned WS_END = WS_H + 64 * 112 * 1024;
constexpr unsigned WS_DBG = WS_END;

constexpr int L_CTL = 0;
constexpr int L_EXCOL = 256;
constexpr int L_EXROW = 1280;
constexpr int L_RING = 16384;
constexpr int L_SLOT = 49152;
constexpr int L_CONV = 12288;
constexpr int C_CH = 26112, C_HS = 4160, C_U = 16640, C_US = 4640;
constexpr int L_W2 = L_RING + L_SLOT;
constexpr int L_TOTAL = 163840;

constexpr u64 TAG_SALT = 0x9E3779B97F4A7C15ull;
constexpr unsigned SPIN_CAP = 1u << 18;

struct Params {
    const u16* x; const u16* k; const u16* v; const u16* sk; const u16* hk; const u16* dv;
    const u16* w1; const u16* w3; const u16* w2; u16* out; char* ws;
};

__device__ __forceinline__ float bflo(unsigned p) { return __uint_as_float(p << 16); }
__device__ __forceinline__ float bfhi(unsigned p) { return __uint_as_float(p & 0xffff0000u); }
__device__ __forceinline__ unsigned pk(float a, float b) {
    f32x2 f = {a, b};
    return __builtin_bit_cast(unsigned, __builtin_convertvector(f, bf16x2));
}
__device__ __forceinline__ float rbf(float a) { return bflo(pk(a, a)); }
__device__ __forceinline__ float dot2(unsigned a, unsigned b, float c) {
    return __builtin_amdgcn_fdot2_f32_bf16(__builtin_bit_cast(bf16x2, a), __builtin_bit_cast(bf16x2, b), c, false);
}
__device__ __forceinline__ void glds16(const void* base, unsigned voff, unsigned ldst) {
    asm volatile("s_mov_b32 m0, %2\n\ts_nop 0\n\tglobal_load_lds_dwordx4 %0, %1" :: "v"(voff), "s"(base), "s"(ldst));
}
__device__ __forceinline__ u32x4 lds128(LAS char* p) { return *(LAS u32x4*)p; }
__device__ __forceinline__ u32x2 ldstr(LAS char* p) {
    return __builtin_bit_cast(u32x2, __builtin_amdgcn_ds_read_tr16_b64_v4i16((LAS s16x4*)p));
}
__device__ __forceinline__ u32x4 ldstr2(LAS char* p, int off) {
    u32x2 a = ldstr(p), b = ldstr(p + off);
    return (u32x4){a.x, a.y, b.x, b.y};
}
__device__ __forceinline__ f32x16 mfma32(u32x4 a, u32x4 b, f32x16 c) {
    return __builtin_amdgcn_mfma_f32_32x32x16_bf16(__builtin_bit_cast(bf16x8, a), __builtin_bit_cast(bf16x8, b), c, 0, 0, 0);
}
__device__ __forceinline__ f32x4 mfma16(u32x4 a, u32x4 b, f32x4 c) {
    return __builtin_amdgcn_mfma_f32_16x16x32_bf16(__builtin_bit_cast(bf16x8, a), __builtin_bit_cast(bf16x8, b), c, 0, 0, 0);
}
__device__ __forceinline__ float silu_f(float g) {
    return g * __builtin_amdgcn_rcpf(1.f + __builtin_amdgcn_exp2f(g * -1.44269504088896341f));
}
#define WAIT_VM(n) asm volatile("s_waitcnt vmcnt(" #n ")" ::: "memory")
#define WAIT_LGKM0() asm volatile("s_waitcnt lgkmcnt(0)" ::: "memory")
#define WAIT_BAR(w) asm volatile("s_waitcnt " w "\n\ts_barrier" ::: "memory")
#define SCHED() __builtin_amdgcn_sched_barrier(0)
#define CAS __attribute__((address_space(4)))
#define NSTAMP 18

__device__ __forceinline__ void wait_flags(char* ws, unsigned off, int n, u64 tag, int lane) {
    GAS u64* p = (GAS u64*)(ws + off) + (lane < n ? lane : 0);
#pragma unroll 1
    for (unsigned spins = 0; spins < SPIN_CAP; ++spins) {
        u64 val = __hip_atomic_load(p, RLX_AGENT);
        if (__all(val == tag)) break;
        __builtin_amdgcn_s_sleep(2);
    }
    __builtin_amdgcn_fence(__ATOMIC_ACQUIRE, "wavefront");
}

__device__ __forceinline__ u64 flag_load(char* ws, unsigned off, int n, int lane) {
    u64 val;
    asm volatile("global_load_dwordx2 %0, %1, %2 sc1" : "=v"(val) : "v"(off + (lane < n ? lane : 0) * 8u), "s"(ws) : "memory");
    return val;
}
__device__ __forceinline__ void arrive_publish(LAS char* ctr, char* ws, unsigned off, u64 tag, int lane) {
    if (lane == 0) {
        const unsigned old = __hip_atomic_fetch_add((LAS unsigned*)ctr, 1u, __ATOMIC_RELAXED, __HIP_MEMORY_SCOPE_WORKGROUP);
        if (old == 7u) __hip_atomic_store((GAS u64*)(ws + off), tag, RLX_AGENT);
    }
}

#define STAMP(i)
#define STAMP_DECL()
#define STAMP_OUT(n)

__global__ __launch_bounds__(512, 2) void fused_kernel(Params p) {
    extern __shared__ __attribute__((aligned(16))) char smem_raw[];
    LAS char* sm = (LAS char*)smem_raw;
    const int tid = threadIdx.x, lane = tid & 63;
    const int wave = __builtin_amdgcn_readfirstlane(tid >> 6);
    const int xcd = blockIdx.x & 7, idx = blockIdx.x >> 3;
    const __amdgpu_buffer_rsrc_t wsr = __builtin_amdgcn_make_buffer_rsrc(p.ws, 0, WS_END, 0x00020000);
    asm volatile("" :: "s"(p.x), "s"(p.k), "s"(p.v), "s"(p.sk), "s"(p.hk), "s"(p.dv), "s"(p.w1), "s"(p.w3), "s"(p.w2), "s"(p.out), "s"(p.ws));
    STAMP_DECL();
    STAMP(0);

    if (idx < 4) {
        const int pb = xcd * 4 + idx;
        const u64 epoch = __hip_atomic_load((GAS u64*)(p.ws + WS_EPOCH), RLX_AGENT);
        if (wave < 7) {
            const int cg = wave * 8 + (lane & 7), rg = lane >> 3;
            const int s0 = pb * 64 + rg * 8;
            u32x4 kr[11], vr[8], sw[4];
#pragma unroll
            for (int i = 0; i < 11; ++i) {
                int r = s0 - 3 + i;
                kr[i] = *(const u32x4*)(p.k + (size_t)(r < 0 ? 0 : r) * D + cg * 8);
            }
#pragma unroll
            for (int i = 0; i < 8; ++i) vr[i] = *(const u32x4*)(p.v + (size_t)(s0 + i) * D + cg * 8);
#pragma unroll
            for (int i = 0; i < 4; ++i) sw[i] = *(const u32x4*)(p.sk + cg * 32 + i * 8);
            if (s0 == 0) {
#pragma unroll
                for (int i = 0; i < 3; ++i) kr[i] = (u32x4){0u, 0u, 0u, 0u};
            }
#pragma unroll
            for (int c = 0; c < 8; ++c) {
                const unsigned wa = sw[c >> 1][(c & 1) * 2], wb = sw[c >> 1][(c & 1) * 2 + 1];
                const float c0 = bflo(wa), c1 = bfhi(wa), c2 = bflo(wb), c3 = bfhi(wb);
                float kk[11], o[8];
#pragma unroll
                for (int i = 0; i < 11; ++i) kk[i] = (c & 1) ? bfhi(kr[i][c >> 1]) : bflo(kr[i][c >> 1]);
#pragma unroll
                for (int i = 0; i < 8; ++i) {
                    float a = kk[i] * c3;
                    a = fmaf(kk[i + 1], c2, a);
                    a = fmaf(kk[i + 2], c1, a);
                    a = fmaf(kk[i + 3], c0, a);
                    const float vv = (c & 1) ? bfhi(vr[i][c >> 1]) : bflo(vr[i][c >> 1]);
                    o[i] = a * vv;
                }
                u32x4 st = {pk(o[7], o[6]), pk(o[5], o[4]), pk(o[3], o[2]), pk(o[1], o[0])};
                __builtin_amdgcn_raw_buffer_store_b128(st, wsr, WS_U + (unsigned)(cg * 8 + c) * 4096u + (unsigned)(2040 - s0) * 2u, 0, 16);
            }
        }
        WAIT_BAR("vmcnt(0)");
        const u64 tag = epoch ^ TAG_SALT;
        if (tid == 0) __hip_atomic_store((GAS u64*)(p.ws + WS_FKV) + pb, tag, RLX_AGENT);
        STAMP(1);
        STAMP_OUT(2);
        if (pb == 0 && wave == 0) {
            wait_flags(p.ws, WS_FKV, 32, tag, lane);
#pragma unroll 1
            for (int m = 0; m < 16; ++m) wait_flags(p.ws, WS_FH + m * 128, 14, tag, lane);
            if (lane == 0) __hip_atomic_store((GAS u64*)(p.ws + WS_EPOCH), epoch + 1, RLX_AGENT);
        }
        return;
    }

    const int gi = idx - 4, ml = gi & 3, nl = gi >> 2;
    const int mt = 4 * (xcd >> 1) + ml, nt = 7 * (xcd & 1) + nl;
    const int rot1 = (2 * ml + nl) % 7;
    const int m64 = 4 * xcd + ml, n64 = nl;
    const int rot2 = (2 * nl + 4 * ml) % 14;
    const int gpair = xcd * 28 + gi;
    const int wm = wave >> 2, wn = wave & 3;
    const int r32 = lane & 31, hh = lane >> 5, blk = (lane >> 4) & 1, q4 = (lane & 15) >> 2, p4 = lane & 3;

    const unsigned lb = (unsigned)(size_t)sm;
    const unsigned xo0 = (unsigned)((lane >> 3) * D + (((lane & 7) ^ (lane >> 4)) * 8)) * 2u;
    const unsigned xo1 = (unsigned)((8 + (lane >> 3)) * D + ((((lane & 7) ^ (lane >> 4)) ^ 4) * 8)) * 2u;
    const unsigned wo = (unsigned)((lane >> 4) * F + (((lane & 15) ^ ((lane >> 4) << 2)) * 8)) * 2u;
    const u16* xbase = p.x + (size_t)(mt * 128 + wave * 16) * D;
    const u16* wbase = ((wave & 1) ? p.w3 : p.w1) + (size_t)((wave >> 1) * 16) * F + nt * 128;
    auto piece_g1 = [&](int si, int i) {
        int s = si + rot1; s = s >= 7 ? s - 7 : s;
        const unsigned base = lb + L_RING + (si % 3) * L_SLOT;
        if (i < 2) glds16(xbase + s * 64, i ? xo1 : xo0, base + wave * 2048 + i * 1024);
        else glds16(wbase + (size_t)(s * 64 + (i - 2) * 4) * F, wo, base + 16384 + (wave & 1) * 16384 + (wave >> 1) * 4096 + (i - 2) * 1024);
    };
#pragma unroll
    for (int i = 0; i < 6; ++i) piece_g1(0, i);
#pragma unroll
    for (int i = 0; i < 6; ++i) piece_g1(1, i);
    u64 epoch_v;
    asm volatile("global_load_dwordx2 %0, %1, %2 sc1" : "=v"(epoch_v) : "v"(WS_EPOCH), "s"(p.ws) : "memory");
    SCHED();

    const int cw = wave & 3, chn = wave >> 2, dch = 2 * gpair + chn;
    const u32x4 h0 = *(const CAS u32x4*)(p.hk + (size_t)dch * S);
    const unsigned dpair = *(const CAS unsigned*)(p.dv + 2 * gpair);
    if (tid < 320) *(LAS unsigned*)(sm + L_CTL + tid * 4) = 0u;

    const unsigned w2o = (unsigned)((lane >> 3) * D + (((lane & 7) ^ (((lane >> 4) & 1) << 2)) * 8)) * 2u;
    const u16* w2base = p.w2 + (size_t)(wave * 224) * D + n64 * 64;
    auto piece_w2 = [&](int i, int half, int dst) {
        int t = i + rot2; t = t >= 14 ? t - 14 : t;
        glds16(w2base + (size_t)(t * 16 + half * 8) * D, w2o, lb + dst + half * 1024);
    };
    const int w2i = L_W2 + wave * 6144;
    LAS char* w2l = sm + w2i;

    int xa[4], wa[4];
#pragma unroll
    for (int pp = 0; pp < 4; ++pp) {
        const int st = (wn + 2 * wm + pp) & 3;
        xa[pp] = L_RING + (64 * wm + r32) * 128 + (((2 * st + hh) ^ ((r32 >> 1) & 7)) * 16);
        wa[pp] = L_RING + 16384 + (16 * st + 8 * hh + q4) * 256 + (((4 * wn + 2 * blk + (p4 >> 1)) ^ (q4 << 2)) * 16) + 8 * (p4 & 1);
    }
    f32x16 acc[2][2];
#pragma unroll
    for (int a = 0; a < 2; ++a)
#pragma unroll
        for (int b = 0; b < 2; ++b)
#pragma unroll
            for (int r = 0; r < 16; ++r) acc[a][b][r] = 0.f;
    float rsum[2] = {0.f, 0.f}, rsq[2] = {0.f, 0.f}, csum[2] = {0.f, 0.f};
    u32x4 fx[2][2], fw[2][2];
    auto load_frags = [&](int g) {
        const int so = ((g >> 2) % 3) * L_SLOT, pp = g & 3, b = g & 1;
        fw[b][0] = ldstr2(sm + wa[pp] + so, 1024);
        fx[b][0] = lds128(sm + xa[pp] + so);
        fw[b][1] = ldstr2(sm + wa[pp] + so + 16384, 1024);
        fx[b][1] = lds128(sm + xa[pp] + so + 4096);
    };
    auto piece = [&](int si, int i) {
        if (si + 2 < 7) piece_g1(si + 2, i);
        else piece_w2((si - 5) * 3 + (i >> 1), i & 1, w2i + (si - 5) * L_SLOT + (i >> 1) * 2048);
    };
    WAIT_BAR("vmcnt(7) lgkmcnt(0)");
    STAMP(1);
    load_frags(0);
    piece(0, 0);
    piece(0, 1);
#pragma unroll
    for (int g = 0; g < 28; ++g) {
        SCHED();
        const int b = g & 1, pp = g & 3, si = g >> 2;
        if (g == 11) STAMP(2);
        if (pp == 3 && g < 27) WAIT_BAR("vmcnt(6) lgkmcnt(0)");
        if (g == 11) STAMP(3);
        if (g < 27) load_frags(g + 1);
        SCHED();
        acc[0][0] = mfma32(fw[b][0], fx[b][0], acc[0][0]);
        acc[0][1] = mfma32(fw[b][1], fx[b][0], acc[0][1]);
        if (pp == 0) {
#pragma unroll
            for (int i = 0; i < 4; ++i) {
                rsum[0] = dot2(fx[b][0][i], 0x3f803f80u, rsum[0]);
                rsq[0] = dot2(fx[b][0][i], fx[b][0][i], rsq[0]);
            }
        }
        if (pp < 2) {
#pragma unroll
            for (int i = 0; i < 4; ++i) csum[0] = dot2(fw[b][0][i], 0x3f803f80u, csum[0]);
        }
        SCHED();
        if (pp == 3) { if (g < 27) piece(si + 1, 0); }
        else piece(si, pp == 0 ? 2 : pp == 1 ? 4 : 5);
        SCHED();
        acc[1][0] = mfma32(fw[b][0], fx[b][1], acc[1][0]);
        acc[1][1] = mfma32(fw[b][1], fx[b][1], acc[1][1]);
        if (pp == 0) {
#pragma unroll
            for (int i = 0; i < 4; ++i) {
                rsum[1] = dot2(fx[b][1][i], 0x3f803f80u, rsum[1]);
                rsq[1] = dot2(fx[b][1][i], fx[b][1][i], rsq[1]);
            }
        }
        if (pp < 2) {
#pragma unroll
            for (int i = 0; i < 4; ++i) csum[1] = dot2(fw[b][1][i], 0x3f803f80u, csum[1]);
        }
        SCHED();
        if (pp == 3) { if (g < 27) piece(si + 1, 1); }
        else if (pp == 0) piece(si, 3);
    }
    SCHED();
    STAMP(4);
    asm volatile("" : "+v"(epoch_v));
    const u64 tag = (((u64)(unsigned)__builtin_amdgcn_readfirstlane((unsigned)(epoch_v >> 32)) << 32) |
                     (u64)(unsigned)__builtin_amdgcn_readfirstlane((unsigned)epoch_v)) ^ TAG_SALT;
    u64 fkv = flag_load(p.ws, WS_FKV, 32, lane);
#pragma unroll
    for (int sb = 0; sb < 2; ++sb) {
        const float a = rsum[sb] + __shfl_xor(rsum[sb], 32), b = rsq[sb] + __shfl_xor(rsq[sb], 32);
        *(LAS float*)(sm + L_EXROW + (64 * wm + 32 * sb + r32) * 32 + hh * 16 + wn * 4) = hh ? b : a;
    }
    {
        const float a = csum[0] + __shfl_xor(csum[0], 32), b = csum[1] + __shfl_xor(csum[1], 32);
        __hip_atomic_fetch_add((LAS float*)(sm + L_EXCOL + hh * 512 + (32 * wn + r32) * 4), hh ? b : a, __ATOMIC_RELAXED, __HIP_MEMORY_SCOPE_WORKGROUP);
    }
    const int w2a0 = (4 * hh + q4) * 128 + (((2 * blk + (p4 >> 1)) ^ (((q4 >> 1) & 1) << 2)) * 16) + 8 * (p4 & 1);
    const int w2a1 = w2a0 ^ 64;
    u32x4 w2r[6][2];
    WAIT_VM(1);
#pragma unroll
    for (int i = 0; i < 6; ++i) {
        LAS char* sl = w2l + (i / 3) * L_SLOT + (i % 3) * 2048;
        w2r[i][0] = ldstr2(sl + w2a0, 1024);
        w2r[i][1] = ldstr2(sl + w2a1, 1024);
    }
    WAIT_BAR("lgkmcnt(0)");
#pragma unroll
    for (int i = 0; i < 6; ++i) asm volatile("" : "+v"(w2r[i][0]), "+v"(w2r[i][1]));
    STAMP(5);

    WAIT_VM(0);
    asm volatile("" : "+v"(fkv));
    if (!__all(fkv == tag)) wait_flags(p.ws, WS_FKV, 32, tag, lane);
    STAMP(6);
    LAS char* cb = sm + L_CONV + chn * C_CH;
    const int x8 = 64 * cw + lane;
    u32x4 u1; unsigned unx;
    {
        const unsigned uo = WS_U + (unsigned)dch * 4096u + (unsigned)x8 * 16u;
        asm volatile("global_load_dwordx4 %0, %1, %2 sc1" : "=v"(u1) : "v"(uo), "s"(p.ws) : "memory");
        asm volatile("global_load_dword %0, %1, %2 sc1" : "=v"(unx) : "v"(x8 == 255 ? uo : uo + 16u), "s"(p.ws) : "memory");
        const u16* hrow = p.hk + (size_t)dch * S + 2 * cw + 2;
#pragma unroll
        for (int i = 0; i < 4; ++i) {
            const unsigned vo = (i == 3 && lane == 63) ? 62u * 16u : lane * 16u;
            glds16(hrow + 512 * i, vo, lb + L_CONV + chn * C_CH + cw * C_HS + (4 + 64 * i) * 16);
        }
    }
    float rinv[2], mrv[2];
    u32x2 stat[2];
#pragma unroll
    for (int sb = 0; sb < 2; ++sb) {
        const f32x4 a = *(LAS f32x4*)(sm + L_EXROW + (64 * wm + 32 * sb + r32) * 32);
        const f32x4 b = *(LAS f32x4*)(sm + L_EXROW + (64 * wm + 32 * sb + r32) * 32 + 16);
        const float mean = ((a[0] + a[1]) + (a[2] + a[3])) * (1.f / D);
        const float ex2 = ((b[0] + b[1]) + (b[2] + b[3])) * (1.f / D);
        const float var = fmaxf(ex2 - mean * mean, 0.f);
        const float mb = rbf(mean);
        const float den = rbf(__builtin_amdgcn_sqrtf(rbf(rbf(var) + 1e-5f)));
        rinv[sb] = __builtin_amdgcn_rcpf(den);
        mrv[sb] = -mb * rinv[sb];
        stat[sb] = (u32x2){__float_as_uint(mb), __float_as_uint(rinv[sb])};
    }
    f32x4 cs[2][4];
#pragma unroll
    for (int m = 0; m < 2; ++m)
#pragma unroll
        for (int a = 0; a < 4; ++a) cs[m][a] = *(LAS f32x4*)(sm + L_EXCOL + m * 512 + (32 * wn + 8 * a + 4 * hh) * 4);
    {
        u32x4 val = {0u, 0u, 0u, 0u};
        if (lane == 3) {
            val[0] = cw >= 3 ? h0[0] : 0u;
            val[1] = cw >= 2 ? (cw == 3 ? h0[1] : h0[0]) : 0u;
            val[2] = cw >= 1 ? (cw == 3 ? h0[2] : cw == 2 ? h0[1] : h0[0]) : 0u;
            val[3] = cw == 3 ? h0[3] : cw == 2 ? h0[2] : cw == 1 ? h0[1] : h0[0];
        }
        const int zo = lane < 4 ? cw * C_HS + lane * 16 : C_U + (cw & 1) * C_US + (252 + lane) * 16;
        if (lane < 32) *(LAS u32x4*)(cb + zo) = val;
    }
    STAMP(7);
    __builtin_amdgcn_raw_buffer_store_b64(hh ? stat[1] : stat[0], wsr,
        wn == 0 ? WS_STATS + (unsigned)(nt * S + mt * 128 + 64 * wm + lane) * 8u : 0xfffffff0u, 0, 16);
#pragma unroll
    for (int sb = 0; sb < 2; ++sb) {
#pragma unroll
        for (int t2 = 0; t2 < 2; ++t2) {
            float hv[8];
#pragma unroll
            for (int j = 0; j < 8; ++j) {
                const int r = 8 * t2 + j;
                const float g = fmaf(mrv[sb], cs[0][r >> 2][r & 3], acc[sb][0][r] * rinv[sb]);
                const float u = fmaf(mrv[sb], cs[1][r >> 2][r & 3], acc[sb][1][r] * rinv[sb]);
                hv[j] = silu_f(g) * u;
                if (j % 3 == 1) {
                    const int i = (sb * 2 + t2) * 3 + j / 3;
                    SCHED();
                    piece_w2(6 + (i >> 1), i & 1, w2i + ((i >> 1) / 3) * L_SLOT + ((i >> 1) % 3) * 2048);
                    SCHED();
                }
            }
            u32x4 st = {pk(hv[0], hv[1]), pk(hv[2], hv[3]), pk(hv[4], hv[5]), pk(hv[6], hv[7])};
            const unsigned fi = (unsigned)((4 * mt + 2 * wm + sb) * 112 + 8 * nt + 2 * wn + t2);
            __builtin_amdgcn_raw_buffer_store_b128(st, wsr, WS_H + fi * 1024u + lane * 16u, 0, 16);
        }
    }
    STAMP(8);
    WAIT_VM(17);
    asm volatile("" : "+v"(u1), "+v"(unx));
    {
        const unsigned nx = x8 == 255 ? 0u : unx;
        const u32x4 u0 = {__builtin_amdgcn_alignbit(u1[1], u1[0], 16), __builtin_amdgcn_alignbit(u1[2], u1[1], 16),
                          __builtin_amdgcn_alignbit(u1[3], u1[2], 16), __builtin_amdgcn_alignbit(nx, u1[3], 16)};
        *(LAS u32x4*)(cb + C_U + x8 * 16) = u0;
        *(LAS u32x4*)(cb + C_U + C_US + x8 * 16) = u1;
    }
    WAIT_BAR("lgkmcnt(0)");
    STAMP(9);
    const int n16 = lane & 15, kg = lane >> 4;
    f32x4 ca[8];
#pragma unroll
    for (int j = 0; j < 8; ++j) ca[j] = (f32x4){0.f, 0.f, 0.f, 0.f};
    {
        LAS char* ap = cb + (n16 & 3) * C_HS + ((n16 >> 2) + kg + 8 * cw) * 16;
        LAS char* bp = cb + C_U + (n16 & 1) * C_US + (252 - 4 * (n16 >> 1) + 8 * cw + kg) * 16 - 7 * 512;
#pragma unroll
        for (int rr = 0; rr < 2; ++rr) {
            u32x4 fa[8], fb[8];
#pragma unroll
            for (int i = 0; i < 8; ++i) {
                fa[i] = lds128(ap + rr * 64 + i * 512);
                fb[i] = lds128(bp + rr * 64 + (7 - i) * 512);
            }
#pragma unroll
            for (int j = 0; j < 8; ++j)
#pragma unroll
                for (int pp = 0; pp <= j; ++pp) ca[j] = mfma16(fa[pp], fb[j - pp], ca[j]);
            if (rr == 0) {
                SCHED();
                WAIT_VM(0);
                arrive_publish(sm + L_CTL, p.ws, WS_FH + mt * 128 + nt * 8, tag, lane);
                STAMP(10);
            }
        }
    }
    STAMP(11);
    const int sl0 = 32 * (n16 >> 1) + 8 * kg + 4 * (n16 & 1);
    u32x2 kvr[2];
#pragma unroll
    for (int jj = 0; jj < 2; ++jj) kvr[jj] = *(LAS u32x2*)(cb + C_U + C_US + (2044 - 256 * (2 * cw + jj) - sl0) * 2);
    WAIT_BAR("lgkmcnt(0)");
    asm volatile("" : "+v"(kvr[0]), "+v"(kvr[1]));
    f32x4 own[2] = {ca[0], ca[1]};
#pragma unroll
    for (int v = 0; v < 4; ++v) {
        if (cw != v) {
            LAS char* wp = cb + v * 6144 + ((cw - v - 1) & 3) * 2048 + lane * 16;
            *(LAS f32x4*)wp = ca[2 * v];
            *(LAS f32x4*)(wp + 1024) = ca[2 * v + 1];
        } else { own[0] = ca[2 * v]; own[1] = ca[2 * v + 1]; }
    }
    const int hn0 = (14 * wave) >> 3, hn1 = (14 * wave + 13) >> 3;
    const unsigned fho = WS_FH + (m64 >> 1) * 128 + hn0 * 8;
    u64 fh = flag_load(p.ws, fho, hn1 - hn0 + 1, lane);
    WAIT_BAR("lgkmcnt(0)");
    STAMP(12);
    const int latei = L_CONV + chn * C_CH + cw * 6144;
    LAS char* late = sm + latei;
    {
        const float dval = chn ? bfhi(dpair) : bflo(dpair);
        const bool odd = n16 & 1;
        f32x4 y4[2];
#pragma unroll
        for (int jj = 0; jj < 2; ++jj) {
            LAS char* rp = late + jj * 1024 + lane * 16;
            y4[jj] = (own[jj] + *(LAS f32x4*)rp) + (*(LAS f32x4*)(rp + 2048) + *(LAS f32x4*)(rp + 4096));
        }
        WAIT_LGKM0();
        asm volatile("" : "+v"(y4[0]), "+v"(y4[1]));
#pragma unroll
        for (int i = 0; i < 4; ++i) piece_w2(12 + (i >> 1), i & 1, latei + (i >> 1) * 2048);
#pragma unroll
        for (int jj = 0; jj < 2; ++jj) {
            const float r0 = __shfl_xor(odd ? y4[jj][0] : y4[jj][2], 1), r1 = __shfl_xor(odd ? y4[jj][1] : y4[jj][3], 1);
            const float y0 = odd ? r0 : y4[jj][0], y1 = odd ? y4[jj][2] : r0, y2 = odd ? r1 : y4[jj][1], y3 = odd ? y4[jj][3] : r1;
            const float o0 = rbf(y0) + rbf(bfhi(kvr[jj][1]) * dval), o1 = rbf(y1) + rbf(bflo(kvr[jj][1]) * dval);
            const float o2 = rbf(y2) + rbf(bfhi(kvr[jj][0]) * dval), o3 = rbf(y3) + rbf(bflo(kvr[jj][0]) * dval);
            u32x2 st = {pk(o0, o1), pk(o2, o3)};
            __builtin_amdgcn_raw_buffer_store_b64(st, wsr, WS_Y + (unsigned)dch * 4096u + (unsigned)(256 * (2 * cw + jj) + sl0) * 2u, 0, 16);
        }
    }
    STAMP(13);
    WAIT_VM(6);
    asm volatile("" : "+v"(fh));
    if (!__all(fh == tag)) wait_flags(p.ws, fho, hn1 - hn0 + 1, tag, lane);
    STAMP(14);
    const int fdb = wave >> 2, fsb = (wave >> 1) & 1;
    const int srow = 64 * m64 + 32 * fsb + r32;
    const int dd = 64 * n64 + 32 * fdb + 16 * (wave & 1) + 8 * hh;
    const unsigned fyo = WS_FY + (unsigned)(32 * n64 + 16 * fdb + 8 * (wave & 1)) * 8u;
    f32x16 ac2[2][2];
#pragma unroll
    for (int a = 0; a < 2; ++a)
#pragma unroll
        for (int b = 0; b < 2; ++b)
#pragma unroll
            for (int r = 0; r < 16; ++r) ac2[a][b][r] = 0.f;
    u64 fy;
    u32x4 xv; u32x2 stv;
    {
        const unsigned hb = WS_H + (unsigned)(2 * m64 * 112 + 14 * wave) * 1024u + lane * 16u;
        constexpr int HD = 8;
        u32x4 hf[HD][2];
        auto load_h = [&](int i) {
            int t = i + rot2; t = t >= 14 ? t - 14 : t;
            hf[i % HD][0] = __builtin_amdgcn_raw_buffer_load_b128(wsr, hb, t * 1024, 16);
            hf[i % HD][1] = __builtin_amdgcn_raw_buffer_load_b128(wsr, hb + 112u * 1024u, t * 1024, 16);
        };
#pragma unroll
        for (int i = 0; i < HD; ++i) load_h(i);
        u32x4 a0 = w2r[0][0], a1 = w2r[0][1];
#pragma unroll
        for (int i = 0; i < 14; ++i) {
            SCHED();
            u32x4 b0, b1;
            if (i + 1 < 6) { b0 = w2r[i + 1][0]; b1 = w2r[i + 1][1]; }
            else if (i + 1 < 14) {
                LAS char* sl = i + 1 < 12 ? w2l + ((i - 5) / 3) * L_SLOT + ((i - 5) % 3) * 2048 : late + (i - 11) * 2048;
                b0 = ldstr2(sl + w2a0, 1024);
                b1 = ldstr2(sl + w2a1, 1024);
            }
            SCHED();
            ac2[0][0] = mfma32(a0, hf[i % HD][0], ac2[0][0]);
            ac2[1][0] = mfma32(a1, hf[i % HD][0], ac2[1][0]);
            ac2[0][1] = mfma32(a0, hf[i % HD][1], ac2[0][1]);
            ac2[1][1] = mfma32(a1, hf[i % HD][1], ac2[1][1]);
            SCHED();
            if (i + HD < 14) load_h(i + HD);
            if (i == 0) {
                WAIT_VM(16);
                arrive_publish(sm + L_CTL + 4, p.ws, WS_FY + gpair * 8, tag, lane);
            }
            if (i == 6) {
                xv = *(const u32x4*)(p.x + (size_t)srow * D + dd);
                stv = __builtin_amdgcn_raw_buffer_load_b64(wsr, WS_STATS + (unsigned)(hn0 * S + srow) * 8u, 0, 16);
            }
            if (i == 10) fy = flag_load(p.ws, fyo, 8, lane);
            a0 = b0; a1 = b1;
        }
        SCHED();
    }
    STAMP(15);
    WAIT_VM(0);
    asm volatile("" : "+v"(fy));
    if (!__all(fy == tag)) wait_flags(p.ws, fyo, 8, tag, lane);
    unsigned yv[8];
#pragma unroll
    for (int i = 0; i < 8; ++i)
        yv[i] = __builtin_amdgcn_raw_buffer_load_b32(wsr, WS_Y + (unsigned)(dd + i) * 4096u + (unsigned)(srow & ~1) * 2u, 0, 16);
    WAIT_LGKM0();
#pragma unroll
    for (int v = 0; v < 8; ++v) {
        LAS char* sl = (v < 6 ? w2l + (v / 3) * L_SLOT + (v % 3) * 2048 : late + (v - 6) * 2048) + lane * 16;
        const f32x16& t = ac2[v >> 2][(v >> 1) & 1];
        const int r0 = 8 * (v & 1);
        *(LAS f32x4*)sl = (f32x4){t[r0], t[r0 + 1], t[r0 + 2], t[r0 + 3]};
        *(LAS f32x4*)(sl + 1024) = (f32x4){t[r0 + 4], t[r0 + 5], t[r0 + 6], t[r0 + 7]};
    }
    WAIT_BAR("lgkmcnt(0)");
    STAMP(16);
    f32x4 o0 = {0.f, 0.f, 0.f, 0.f}, o1 = {0.f, 0.f, 0.f, 0.f};
    {
        const int rbase = wave < 6 ? L_W2 + (wave / 3) * L_SLOT + (wave % 3) * 2048 : L_CONV + (wave - 6) * 2048;
        const int rhi = wave < 6 ? 4 * 6144 : C_CH;
        LAS char* rp = sm + rbase + hh * 1024 + r32 * 16;
#pragma unroll
        for (int w = 0; w < 8; ++w) {
            o0 += *(LAS f32x4*)(rp + (w & 3) * 6144 + (w >> 2) * rhi);
            o1 += *(LAS f32x4*)(rp + (w & 3) * 6144 + (w >> 2) * rhi + 512);
        }
    }
    {
        const float mb = __uint_as_float(stv[0]), ri = __uint_as_float(stv[1]);
        float res[8];
#pragma unroll
        for (int i = 0; i < 8; ++i) {
            const float o = rbf(i < 4 ? o0[i & 3] : o1[i & 3]);
            const float g = rbf(silu_f(o));
            const float xx = (i & 1) ? bfhi(xv[i >> 1]) : bflo(xv[i >> 1]);
            const float xn = rbf(rbf(xx - mb) * ri);
            const float yy = (srow & 1) ? bfhi(yv[i]) : bflo(yv[i]);
            res[i] = xx + rbf(rbf(xn * yy) * g);
        }
        u32x4 st = {pk(res[0], res[1]), pk(res[2], res[3]), pk(res[4], res[5]), pk(res[6], res[7])};
        *(u32x4*)(p.out + (size_t)srow * D + dd) = st;
    }
    STAMP(17);
    STAMP_OUT(18);
}

extern "C" void kernel_launch(void* const* d_in, const int* in_sizes, int n_in,
                              void* d_out, int out_size, void* d_ws, size_t ws_size,
                              hipStream_t stream) {
    Params p;
    p.x = (const u16*)d_in[0]; p.k = (const u16*)d_in[1]; p.v = (const u16*)d_in[2];
    p.sk = (const u16*)d_in[3]; p.hk = (const u16*)d_in[4]; p.dv = (const u16*)d_in[5];
    p.w1 = (const u16*)d_in[7]; p.w3 = (const u16*)d_in[8]; p.w2 = (const u16*)d_in[9];
    p.out = (u16*)d_out; p.ws = (char*)d_ws;
    (void)hipFuncSetAttribute((const void*)fused_kernel, hipFuncAttributeMaxDynamicSharedMemorySize, L_TOTAL);
    fused_kernel<<<256, 512, L_TOTAL, stream>>>(p);
}
```

```cpp
#include <hip/hip_runtime.h>
#include <stdint.h>
#include <utility>

typedef unsigned short u16;
typedef unsigned long long u64;
typedef short s16x4 __attribute__((ext_vector_type(4)));
typedef short s16x8 __attribute__((ext_vector_type(8)));
typedef __bf16 bf16x2 __attribute__((ext_vector_type(2)));
typedef __bf16 bf16x8 __attribute__((ext_vector_type(8)));
typedef float f32x2 __attribute__((ext_vector_type(2)));
typedef float f32x4 __attribute__((ext_vector_type(4)));
typedef float f32x16 __attribute__((ext_vector_type(16)));
typedef unsigned u32x2 __attribute__((ext_vector_type(2)));
typedef unsigned u32x4 __attribute__((ext_vector_type(4)));
typedef unsigned u32x16 __attribute__((ext_vector_type(16)));
#define LAS __attribute__((address_space(3)))
#define GAS __attribute__((address_space(1)))
#define RLX_AGENT __ATOMIC_RELAXED, __HIP_MEMORY_SCOPE_AGENT

constexpr int S = 2048, D = 448, F = 1792;

constexpr unsigned WS_EPOCH = 0;
constexpr unsigned WS_FH = 1024;
constexpr unsigned WS_STATS = 8192;
constexpr unsigned WS_CSUM = 24576;
constexpr unsigned WS_U = 65536;
constexpr unsigned WS_Y = WS_U + D * S * 4;
constexpr unsigned WS_H = WS_Y + D * S * 4;
constexpr unsigned WS_END = WS_H + 64 * 112 * 1024;
constexpr unsigned WS_DBG = WS_END;

constexpr int L_KV = 0;
constexpr int L_CTL = 0;
constexpr int L_CSUM = 256;
constexpr int L_STAT = 1280;
constexpr int L_RAW = 2048;
constexpr int L_UNX = 6144;
constexpr int L_SLOT = 49152;
constexpr int L_CONV = 12288;
constexpr int C_CH = 26112, C_HS = 4160, C_U = 16640, C_US = 4640;
constexpr int L_W2 = 16384 + L_SLOT;
constexpr int L_TOTAL = 163840;
__host__ __device__ constexpr int ring(int stage) { return 16384 + (stage % 3) * L_SLOT; }
constexpr unsigned CODE_PREFETCH = 24576;
#define POLL_SLEEP 4
#define HDEPTH 8
#define ROT2 (8 * ml)
#define TOUCH 0
#define MPRIO 1
#define ROT1 ((2 * ml + nl) % 7)

constexpr u64 TAG_SALT = 0x9E3779B97F4A7C15ull;
constexpr unsigned SPIN_CAP = 1u << 18;

struct Params {
    const u16* x; const u16* k; const u16* v; const u16* sk; const u16* hk; const u16* dv;
    const u16* w1; const u16* w3; const u16* w2; u16* out; char* ws;
};

__device__ __forceinline__ float bflo(unsigned p) { return __uint_as_float(p << 16); }
__device__ __forceinline__ float bfhi(unsigned p) { return __uint_as_float(p & 0xffff0000u); }
__device__ __forceinline__ float bflo1(unsigned p) { float r; asm("v_lshlrev_b32 %0, 16, %1" : "=v"(r) : "v"(p)); return r; }
__device__ __forceinline__ float bfhi1(unsigned p) { float r; asm("v_and_b32 %0, 0xffff0000, %1" : "=v"(r) : "v"(p)); return r; }
__device__ __forceinline__ float mul1(float a, float b) { float r; asm("v_mul_f32 %0, %1, %2" : "=v"(r) : "v"(a), "v"(b)); return r; }
__device__ __forceinline__ float add1(float a, float b) { float r; asm("v_add_f32 %0, %1, %2" : "=v"(r) : "v"(a), "v"(b)); return r; }
__device__ __forceinline__ unsigned vmov1(unsigned s) { unsigned r; asm volatile("v_mov_b32 %0, %1" : "=v"(r) : "s"(s)); return r; }
__device__ __forceinline__ unsigned pk(float a, float b) {
    f32x2 f = {a, b};
    return __builtin_bit_cast(unsigned, __builtin_convertvector(f, bf16x2));
}
__device__ __forceinline__ float rbf(float a) { return bflo(pk(a, a)); }
__device__ __forceinline__ float dot2(unsigned a, unsigned b, float c) {
    return __builtin_amdgcn_fdot2_f32_bf16(__builtin_bit_cast(bf16x2, a), __builtin_bit_cast(bf16x2, b), c, false);
}
__device__ __forceinline__ void glds16(const void* base, unsigned voff, unsigned ldst) {
    u64 t;
    asm volatile("s_mov_b32 m0, %3\n\ts_mov_b64 %0, %2\n\tglobal_load_lds_dwordx4 %1, %0" : "=&s"(t) : "v"(voff), "s"(base), "s"(ldst));
}
__device__ __forceinline__ void glds16x4(const void* base, u32x4 vo, unsigned ldst) {
    u64 t;
    asm volatile("s_mov_b32 m0, %6\n\ts_mov_b64 %0, %5\n\tglobal_load_lds_dwordx4 %1, %0\n\tglobal_load_lds_dwordx4 %2, %0 offset:1024\n\t"
                 "global_load_lds_dwordx4 %3, %0 offset:2048\n\tglobal_load_lds_dwordx4 %4, %0 offset:3072"
                 : "=&s"(t) : "v"(vo[0]), "v"(vo[1]), "v"(vo[2]), "v"(vo[3]), "s"(base), "s"(ldst));
}
__device__ __forceinline__ void glds16x2c(const void* base, unsigned voff, unsigned ldst) {
    u64 t;
    asm volatile("s_mov_b32 m0, %3\n\ts_mov_b64 %0, %2\n\tglobal_load_lds_dwordx4 %1, %0 sc1\n\tglobal_load_lds_dwordx4 %1, %0 offset:1024 sc1"
                 : "=&s"(t) : "v"(voff), "s"(base), "s"(ldst));
}
__device__ __forceinline__ void glds16c(const void* base, unsigned voff, unsigned ldst) {
    u64 t;
    asm volatile("s_mov_b32 m0, %3\n\ts_mov_b64 %0, %2\n\tglobal_load_lds_dwordx4 %1, %0 sc1" : "=&s"(t) : "v"(voff), "s"(base), "s"(ldst));
}
__device__ __forceinline__ void glds4c(const void* base, unsigned voff, unsigned ldst) {
    u64 t;
    asm volatile("s_mov_b32 m0, %3\n\ts_mov_b64 %0, %2\n\tglobal_load_lds_dword %1, %0 sc1" : "=&s"(t) : "v"(voff), "s"(base), "s"(ldst));
}
__device__ __forceinline__ const u16* sptr(const u16* base, int off) {
    const u16* r = base + __builtin_amdgcn_readfirstlane(off);
    asm("" : "+s"(r));
    return r;
}
__device__ __forceinline__ u64 vsel(bool c, const void* a, u64 b) {
    u64 va = (u64)a;
    asm("" : "+v"(va), "+v"(b));
    return c ? va : b;
}
__device__ __forceinline__ LAS char* lp(int a) { return (LAS char*)(size_t)(unsigned)a; }
__device__ __forceinline__ u32x4 lds128(LAS char* p) { return *(LAS u32x4*)p; }
__device__ __forceinline__ u32x2 ldstr(LAS char* p) {
    return __builtin_bit_cast(u32x2, __builtin_amdgcn_ds_read_tr16_b64_v4i16((LAS s16x4*)p));
}
__device__ __forceinline__ u32x4 ldstr2(LAS char* p, int off) {
    u32x2 a = ldstr(p), b = ldstr(p + off);
    return (u32x4){a.x, a.y, b.x, b.y};
}
__device__ __forceinline__ f32x16 mfma32(u32x4 a, u32x4 b, f32x16 c) {
    return __builtin_amdgcn_mfma_f32_32x32x16_bf16(__builtin_bit_cast(bf16x8, a), __builtin_bit_cast(bf16x8, b), c, 0, 0, 0);
}
__device__ __forceinline__ f32x4 mfma16(u32x4 a, u32x4 b, f32x4 c) {
    return __builtin_amdgcn_mfma_f32_16x16x32_bf16(__builtin_bit_cast(bf16x8, a), __builtin_bit_cast(bf16x8, b), c, 0, 0, 0);
}
__device__ __forceinline__ float silu_f(float g) {
    return g * __builtin_amdgcn_rcpf(1.f + __builtin_amdgcn_exp2f(g * -1.44269504088896341f));
}
template <class Fn, int... I>
__device__ __forceinline__ void static_for(Fn&& fn, std::integer_sequence<int, I...>) { (fn(std::integral_constant<int, I>{}), ...); }
#define WAIT_VM(n) asm volatile("s_waitcnt vmcnt(" #n ")" ::: "memory")
#define WAIT_LGKM0() asm volatile("s_waitcnt lgkmcnt(0)" ::: "memory")
#define WAIT_BAR(w) asm volatile("s_waitcnt " w "\n\ts_barrier" ::: "memory")
#define SCHED() __builtin_amdgcn_sched_barrier(0)
#define CAS __attribute__((address_space(4)))
#define NSTAMP 18

__device__ __forceinline__ void wait_flags(char* ws, unsigned off, int n, u64 tag, int lane) {
    GAS u64* p = (GAS u64*)(ws + off) + (lane < n ? lane : 0);
#pragma unroll 1
    for (unsigned spins = 0; spins < SPIN_CAP; ++spins) {
        u64 val = __hip_atomic_load(p, RLX_AGENT);
        if (__all(val == tag)) break;
        __builtin_amdgcn_s_sleep(POLL_SLEEP);
    }
    __builtin_amdgcn_fence(__ATOMIC_ACQUIRE, "wavefront");
}

__device__ __forceinline__ void wait_lds(LAS char* word) {
#pragma unroll 1
    for (unsigned spins = 0; spins < (SPIN_CAP << 4); ++spins) {
        if (__builtin_amdgcn_readfirstlane(__hip_atomic_load((LAS unsigned*)word, __ATOMIC_RELAXED, __HIP_MEMORY_SCOPE_WORKGROUP))) break;
        __builtin_amdgcn_s_sleep(1);
    }
    asm volatile("" ::: "memory");
}
__device__ __forceinline__ void arrive_publish(LAS char* ctr, char* ws, unsigned off, u64 tag, int lane) {
    if (lane == 0) {
        const unsigned old = __hip_atomic_fetch_add((LAS unsigned*)ctr, 1u, __ATOMIC_RELAXED, __HIP_MEMORY_SCOPE_WORKGROUP);
        if (old == 3u) __hip_atomic_store((GAS u64*)(ws + off), tag, RLX_AGENT);
    }
}

#define STAMP(i)
#define STAMPG(i)
#define STAMP_DECL()
#define STAMP_OUT(n)

__global__ __launch_bounds__(512, 2) void fused_kernel(Params p) {
    extern __shared__ __attribute__((aligned(16))) char smem_raw[];
    u64 pc;
    asm volatile("s_getpc_b64 %0" : "=s"(pc));
    LAS char* sm = (LAS char*)smem_raw;
    const int tid = threadIdx.x, lane = tid & 63;
    const int wave = __builtin_amdgcn_readfirstlane(tid >> 6);
    const int xcd = blockIdx.x & 7, idx = blockIdx.x >> 3;
    const __amdgpu_buffer_rsrc_t wsr = __builtin_amdgcn_make_buffer_rsrc(p.ws, 0, WS_END, 0x00020000);
    asm volatile("" :: "s"(p.x), "s"(p.k), "s"(p.v), "s"(p.sk), "s"(p.hk), "s"(p.dv), "s"(p.w1), "s"(p.w3), "s"(p.w2), "s"(p.out), "s"(p.ws));
    STAMP_DECL();
    STAMP(0);

    if (idx < 4) {
        unsigned pf = 0;
        if (idx == 0 && wave == 7) {
            const GAS char* code = (const GAS char*)(pc & ~(u64)255);
#pragma unroll
            for (unsigned i = 0; i < CODE_PREFETCH / 8192; ++i) pf ^= *(const GAS unsigned*)(code + i * 8192 + lane * 128);
        }
        const int ca = (xcd >> 1) * 4 + idx, cmat = ca >= 7, cnt = 7 * (xcd & 1) + (ca >= 7 ? ca - 7 : ca);
        const bool job = ca < 14;
        u32x4 xr[7], wr[14];
        if (job) {
            const u16* wp = (cmat ? p.w3 : p.w1) + (size_t)(wave * 56 + (lane >> 4)) * F + cnt * 128 + (lane & 15) * 8;
#pragma unroll
            for (int i = 0; i < 14; ++i) wr[i] = *(const u32x4*)(wp + (size_t)(4 * i) * F);
        }
        const int row = (8 * (xcd >> 1) + 4 * (xcd & 1) + idx) * 64 + wave * 8 + (lane >> 3);
#pragma unroll
        for (int i = 0; i < 7; ++i) xr[i] = *(const u32x4*)(p.x + (size_t)row * D + i * 64 + (lane & 7) * 8);
        const unsigned pf2 = *(const unsigned*)((const char*)p.hk + (size_t)((xcd * 4 + idx) * 448 + wave * 56 + (lane < 56 ? lane : 0)) * 128);
        u64 epoch;
        asm volatile("s_load_dwordx2 %0, %1, 0x0\n\ts_waitcnt lgkmcnt(0)" : "=s"(epoch) : "s"(p.ws) : "memory");
        const u64 tag = epoch ^ TAG_SALT;
        const unsigned tag32 = (unsigned)tag;
        if (job) {
            float c[8];
#pragma unroll
            for (int j = 0; j < 8; ++j) c[j] = 0.f;
#pragma unroll
            for (int i = 0; i < 14; ++i)
#pragma unroll
                for (int j = 0; j < 4; ++j) { c[2 * j] += bflo(wr[i][j]); c[2 * j + 1] += bfhi(wr[i][j]); }
            LAS char* cp = sm + ((wave * 4 + (lane >> 4)) * 128 + (lane & 15) * 8) * 4;
            *(LAS f32x4*)cp = (f32x4){c[0], c[1], c[2], c[3]};
            *(LAS f32x4*)(cp + 16) = (f32x4){c[4], c[5], c[6], c[7]};
        }
        {
            float sum = 0.f;
#pragma unroll
            for (int i = 0; i < 7; ++i)
#pragma unroll
                for (int j = 0; j < 4; ++j) sum = dot2(xr[i][j], 0x3f803f80u, sum);
            sum += __shfl_xor(sum, 1); sum += __shfl_xor(sum, 2); sum += __shfl_xor(sum, 4);
            const float mean = sum * (1.f / D);
            float sq = 0.f;
#pragma unroll
            for (int i = 0; i < 7; ++i)
#pragma unroll
                for (int j = 0; j < 4; ++j) {
                    const float a = bflo(xr[i][j]) - mean, b = bfhi(xr[i][j]) - mean;
                    sq = fmaf(a, a, sq); sq = fmaf(b, b, sq);
                }
            sq += __shfl_xor(sq, 1); sq += __shfl_xor(sq, 2); sq += __shfl_xor(sq, 4);
            const u32x2 st = {tag32, pk(mean, __builtin_amdgcn_sqrtf(rbf(rbf(sq * (1.f / D)) + 1e-5f)))};
            __builtin_amdgcn_raw_buffer_store_b64(st, wsr, (lane & 7) == 0 ? WS_STATS + (unsigned)row * 8u : 0xfffffff0u, 0, 16);
        }
        if (job) {
            WAIT_BAR("lgkmcnt(0)");
            float t = 0.f;
#pragma unroll
            for (int i = 0; i < 8; ++i) t += *(LAS float*)(sm + (((tid & 3) * 8 + i) * 128 + (tid >> 2)) * 4);
            t += __shfl_xor(t, 1); t += __shfl_xor(t, 2);
            __builtin_amdgcn_raw_buffer_store_b64((u32x2){tag32, __float_as_uint(t)}, wsr,
                (tid & 3) == 0 ? WS_CSUM + (unsigned)((cnt * 2 + cmat) * 128 + (tid >> 2)) * 8u : 0xfffffff0u, 0, 16);
        }
        STAMP(1);
        asm volatile("" :: "v"(pf), "v"(pf2));
        STAMP_OUT(2);
        if (blockIdx.x == 0 && wave == 0) {
#pragma unroll 1
            for (int m = 0; m < 4; ++m) {
                GAS u64* gp = (GAS u64*)(p.ws + WS_STATS) + (m * 64 + lane) * 8;
#pragma unroll 1
                for (unsigned spins = 0; spins < SPIN_CAP; ++spins) {
                    if (__all((unsigned)__hip_atomic_load(gp, RLX_AGENT) == tag32)) break;
                    __builtin_amdgcn_s_sleep(16);
                }
            }
            __builtin_amdgcn_s_sleep(100);
#pragma unroll 1
            for (int m = 0; m < 16; ++m) wait_flags(p.ws, WS_FH + m * 128, 14, tag, lane);
            if (lane == 0) __hip_atomic_store((GAS u64*)(p.ws + WS_EPOCH), epoch + 1, RLX_AGENT);
        }
        return;
    }

    const int gi = idx - 4, ml = gi & 3, nl = gi >> 2;
    const int mt = 4 * (xcd >> 1) + ml, nt = 7 * (xcd & 1) + nl;
    const int rot1 = ROT1;
    const int m64 = 4 * xcd + ml, n64 = nl;
    const int gpair = xcd * 28 + gi;
    const int r32 = lane & 31, hh = lane >> 5, blk = (lane >> 4) & 1, q4 = (lane & 15) >> 2, p4 = lane & 3;
    const unsigned lb = (unsigned)(size_t)sm;
    const int wq = wave & 3;
    u32x4 wvo;
    wvo[0] = (unsigned)((lane >> 4) * F + (((lane & 15) ^ ((lane >> 4) << 2)) * 8)) * 2u;
#pragma unroll
    for (int j = 1; j < 4; ++j) wvo[j] = wvo[0] + j * (8 * F - 1024);
    const u16* w1b = sptr(p.w1, wq * 16 * F + nt * 128);
    const u16* w3b = sptr(p.w3, wq * 16 * F + nt * 128);
    auto kst = [&](int si) { const int s = si + rot1; return s >= 7 ? s - 7 : s; };
    auto group_w = [&](int si, int m) { glds16x4(sptr(m ? w3b : w1b, kst(si) * 64 * F), wvo, lb + ring(si) + 16384 + m * 16384 + wq * 4096); };
    if (wave < 4) { group_w(0, 0); group_w(0, 1); }
    SCHED();
    const int rot2 = ROT2;
    const int w2i = L_W2 + wq * 12288;
    const int fdb = wave >> 2, fsb = (wave >> 1) & 1;
    auto fresh_lane = [&]() { int l = lane; asm volatile("" : "+v"(l)); return l; };
    unsigned hb;
    int srow, dd;
    u32x2 yv[8], stv;
    u32x4 xv;
    auto g2_setup = [&]() {
        const int l = fresh_lane();
        hb = WS_H + (unsigned)(2 * m64 * 112 + 28 * wq) * 1024u + l * 16u;
    };
    auto load_x = [&]() {
        const int l = fresh_lane();
        srow = 64 * m64 + 32 * fsb + (l & 31);
        dd = 64 * n64 + 32 * fdb + 16 * (wave & 1) + 8 * (l >> 5);
        xv = __builtin_amdgcn_raw_buffer_load_b128(__builtin_amdgcn_make_buffer_rsrc((void*)p.x, 0, S * D * 2, 0x00020000), (unsigned)(srow * D + dd) * 2u, 0, 0);
    };
    auto load_y = [&]() {
        const unsigned yo = WS_Y + (unsigned)dd * 8192u + (unsigned)(srow >> 1) * 8u;
#pragma unroll
        for (int i = 0; i < 8; ++i) yv[i] = __builtin_amdgcn_raw_buffer_load_b64(wsr, yo, i * 8192, 16);
        stv = __builtin_amdgcn_raw_buffer_load_b64(wsr, WS_STATS + (unsigned)srow * 8u, 0, 16);
    };
    auto pair_w2 = [&](u32x4 vo, int i, int dst) {
        int t = i + rot2; t = t >= 28 ? t - 28 : t;
        glds16x4(sptr(p.w2, (wq * 448 + t * 16) * D + n64 * 64), vo, lb + dst);
    };
    auto w2_off = [&]() {
        const int l = fresh_lane();
        const unsigned o = (unsigned)((l >> 3) * D + (((l & 7) ^ (((l >> 4) & 1) << 2)) * 8)) * 2u;
        return (u32x4){o, o + (16 * D - 1024), o + 2 * (16 * D - 1024), o + 3 * (16 * D - 1024)};
    };
    unsigned tag32;

    if (wave < 4) {
        u64 epoch;
        asm volatile("s_load_dwordx2 %0, %1, 0x0" : "=s"(epoch) : "s"(p.ws) : "memory");
        __builtin_amdgcn_s_setprio(MPRIO);
        const int wm = wave >> 1, wn = wave & 1;
        int xa[4], wa0, wa1;
#pragma unroll
        for (int i = 0; i < 4; ++i) xa[i] = lb + ring(0) + (((64 * wm + r32) * 128 + ((hh ^ ((r32 >> 1) & 7)) * 16)) ^ (i * 32));
        wa0 = 16384 + (8 * hh + q4) * 256 + (((8 * wn + 2 * blk + (p4 >> 1)) ^ (q4 << 2)) * 16) + 8 * (p4 & 1);
        wa1 = (wa0 ^ 64) + lb + ring(0); wa0 += lb + ring(0);
        f32x16 acc[2][2][2];
#pragma unroll
        for (int i = 0; i < 8; ++i)
#pragma unroll
            for (int r = 0; r < 16; ++r) acc[i >> 2][(i >> 1) & 1][i & 1][r] = 0.f;
        u32x4 fx[2][2], fw[2][2][2];
        auto load_frag = [&](int g, int j) {
            const int pp = g & 3, b = g & 1;
            if (j == 0) {
                if (g >= 4) {
                    const int d = ring(g >> 2) - ring((g >> 2) - 1);
                    if (pp == 0) { wa0 += d; wa1 += d; asm volatile("" : "+v"(wa0), "+v"(wa1)); }
                    xa[pp] += d; asm volatile("" : "+v"(xa[pp]));
                }
                fw[b][0][0] = ldstr2(lp(wa0) + pp * 4096, 1024);
            }
            if (j == 1) { fx[b][0] = lds128(lp(xa[pp])); fx[b][1] = lds128(lp(xa[pp]) + 4096); }
            if (j == 2) fw[b][0][1] = ldstr2(lp(wa0) + pp * 4096 + 16384, 1024);
            if (j == 3) fw[b][1][0] = ldstr2(lp(wa1) + pp * 4096, 1024);
            if (j == 4) fw[b][1][1] = ldstr2(lp(wa1) + pp * 4096 + 16384, 1024);
        };
        asm volatile("s_waitcnt vmcnt(0) lgkmcnt(0)\n\ts_barrier" : "+s"(epoch) :: "memory");
        const u64 tag = epoch ^ TAG_SALT;
        tag32 = (unsigned)tag;
        STAMP(1);
#pragma unroll
        for (int j = 0; j < 5; ++j) load_frag(0, j);
        f32x4 cs[2][2][4];
        unsigned tch[5];
        const unsigned tco = (unsigned)(gi * 4 + wq + (lane < 23 ? lane : 22) * 112) * 128u;
        static_for([&](auto gc) {
            constexpr int g = decltype(gc)::value, b = g & 1, pp = g & 3;
            SCHED();
            if (g == 11) STAMP(2);
            if (pp == 3) { STAMPG(2 + 2 * (g >> 2)); WAIT_BAR("lgkmcnt(0)"); STAMPG(3 + 2 * (g >> 2)); }
            if (g == 11) STAMP(3);
#pragma unroll
            for (int i = 0; i < 8; ++i) {
                const int ft = i >> 2, m = (i >> 1) & 1, sb = i & 1;
                acc[sb][ft][m] = mfma32(fw[b][ft][m], fx[b][sb], acc[sb][ft][m]);
                SCHED();
                if constexpr (TOUCH && pp == 1 && g >= 4 && g < 24) {
                    if (i == 6) {
                        constexpr int k = (g >> 2) - 1;
                        const unsigned o = k < 4 ? tco + k * (23u * 112u * 128u) : (lane < 20 ? tco + 4 * (23u * 112u * 128u) : tco);
                        u64 t;
                        asm volatile("s_mov_b64 %1, %3\n\tglobal_load_dword %0, %2, %1" : "=v"(tch[k]), "=&s"(t) : "v"(o), "s"(p.w2));
                    }
                }
                if (g < 27) { if (i < 5) load_frag(g + 1, i); }
                else cs[i >> 2][0][i & 3] = *(LAS f32x4*)(sm + L_CSUM + (64 * wn + 32 * (i >> 2) + 8 * (i & 3) + 4 * hh) * 4);
                SCHED();
            }
        }, std::make_integer_sequence<int, 28>{});
        STAMP(4);
        unsigned sa[2];
        sa[0] = *(LAS unsigned*)(sm + L_STAT + (64 * wm + r32) * 4);
        sa[1] = *(LAS unsigned*)(sm + L_STAT + (64 * wm + 32 + r32) * 4);
#pragma unroll
        for (int i = 0; i < 8; ++i) cs[i >> 2][1][i & 3] = *(LAS f32x4*)(sm + L_CSUM + 512 + (64 * wn + 32 * (i >> 2) + 8 * (i & 3) + 4 * hh) * 4);
#pragma unroll
        for (int sb = 0; sb < 2; ++sb) {
            const float ri = __builtin_amdgcn_rcpf(bfhi(sa[sb])) * -1.44269504088896341f, nm = -bflo(sa[sb]), d2 = bfhi(sa[sb]) * bfhi(sa[sb]);
            const f32x2 ri2 = {ri, ri}, nm2 = {nm, nm}, dd2 = {d2, d2};
#pragma unroll
            for (int ft = 0; ft < 2; ++ft) {
#pragma unroll
                for (int t2 = 0; t2 < 2; ++t2) {
                    f32x2 gu[4], ee[4];
#pragma unroll
                    for (int j = 0; j < 4; ++j) {
                        const int r = 8 * t2 + 2 * j;
                        const f32x2 g = __builtin_elementwise_fma(nm2, (f32x2){cs[ft][0][r >> 2][r & 3], cs[ft][0][r >> 2][(r & 3) + 1]}, (f32x2){acc[sb][ft][0][r], acc[sb][ft][0][r + 1]});
                        const f32x2 u = __builtin_elementwise_fma(nm2, (f32x2){cs[ft][1][r >> 2][r & 3], cs[ft][1][r >> 2][(r & 3) + 1]}, (f32x2){acc[sb][ft][1][r], acc[sb][ft][1][r + 1]});
                        const f32x2 t = g * ri2;
                        ee[j] = (f32x2){__builtin_amdgcn_exp2f(t[0]), __builtin_amdgcn_exp2f(t[1])};
                        gu[j] = g * u;
                    }
                    u32x4 st;
#pragma unroll
                    for (int j = 0; j < 4; ++j) {
                        const f32x2 dn = __builtin_elementwise_fma(ee[j], dd2, dd2);
                        const f32x2 hv = gu[j] * (f32x2){__builtin_amdgcn_rcpf(dn[0]), __builtin_amdgcn_rcpf(dn[1])};
                        st[j] = pk(hv[0], hv[1]);
                    }
                    const unsigned fi = (unsigned)((4 * mt + 2 * wm + sb) * 112 + 8 * nt + 4 * wn + 2 * ft + t2);
                    __builtin_amdgcn_raw_buffer_store_b128(st, wsr, WS_H + fi * 1024u + lane * 16u, 0, 16);
                }
            }
        }
        SCHED();
        __hip_atomic_store((LAS unsigned*)(sm + L_CTL + 32 + wq * 4), 1u, __ATOMIC_RELAXED, __HIP_MEMORY_SCOPE_WORKGROUP);
        STAMP(5);
        const int lw = fresh_lane();
        const int w2a0 = lb + w2i + (4 * (lw >> 5) + ((lw & 15) >> 2)) * 128 + (((2 * ((lw >> 4) & 1) + ((lw & 3) >> 1)) ^ (((lw >> 3) & 1) << 2)) * 16) + 8 * (lw & 1);
        const int w2a1 = w2a0 ^ 64;
        const u32x4 w2o = w2_off();
        WAIT_VM(0);
        if (TOUCH) asm volatile("" :: "v"(tch[0]), "v"(tch[1]), "v"(tch[2]), "v"(tch[3]), "v"(tch[4]));
        arrive_publish(sm + L_CTL, p.ws, WS_FH + mt * 128 + nt * 8, tag, lane);
        STAMP(6);
        u32x4 w2r[12][2];
        wait_lds(sm + L_CTL + 48 + wq * 4);
#pragma unroll
        for (int h = 0; h < 2; ++h) {
#pragma unroll
            for (int i = 0; i < 6; ++i) {
                w2r[6 * h + i][0] = ldstr2(lp(w2a0) + h * L_SLOT + i * 2048, 1024);
                w2r[6 * h + i][1] = ldstr2(lp(w2a1) + h * L_SLOT + i * 2048, 1024);
            }
            WAIT_LGKM0();
#pragma unroll
            for (int i = 0; i < 6; ++i) asm volatile("" : "+v"(w2r[6 * h + i][0]), "+v"(w2r[6 * h + i][1]));
#pragma unroll
            for (int i = 0; i < 3; ++i) pair_w2(w2o, 12 + 6 * h + 2 * i, w2i + h * L_SLOT + i * 4096);
        }
        STAMP(7);
        if (wave == 0) {
            wait_flags(p.ws, WS_FH + (m64 >> 1) * 128, 14, tag, lane);
            __hip_atomic_store((LAS unsigned*)(sm + L_CTL + 4), 1u, __ATOMIC_RELAXED, __HIP_MEMORY_SCOPE_WORKGROUP);
        } else { WAIT_VM(0); wait_lds(sm + L_CTL + 4); }
        STAMP(11);
        g2_setup();
        u32x4 hf[28][2];
        auto load_h = [&](int i, int j) {
            int t = i + rot2; t = t >= 28 ? t - 28 : t;
            hf[i][j] = __builtin_amdgcn_raw_buffer_load_b128(wsr, hb + j * (112u * 1024u), t * 1024, 16);
        };
        constexpr int HD = HDEPTH;
#pragma unroll
        for (int i = 0; i < HD; ++i) { load_h(i, 0); load_h(i, 1); }
        STAMP(14);
        f32x16 ac2[2][2];
#pragma unroll
        for (int i = 0; i < 4; ++i)
#pragma unroll
            for (int r = 0; r < 16; ++r) ac2[i >> 1][i & 1][r] = 0.f;
        u32x4 a0 = w2r[0][0], a1 = w2r[0][1];
        static_for([&](auto ic) {
            constexpr int i = decltype(ic)::value, n = i + 1;
            SCHED();
            if (i == 23) WAIT_VM(0);
            u32x4 b0, b1;
            if constexpr (n < 12) { b0 = w2r[n][0]; b1 = w2r[n][1]; }
            else if constexpr (n < 28) {
                constexpr int so = n < 24 ? ((n - 12) / 6) * L_SLOT + ((n - 12) % 6) * 2048 : (n - 24) * 2048;
                b0 = ldstr2(lp(w2a0) + so, 1024);
                b1 = ldstr2(lp(w2a1) + so, 1024);
            }
            SCHED();
            ac2[0][0] = mfma32(a0, hf[i][0], ac2[0][0]);
            SCHED();
            if constexpr (i + HD < 28) load_h(i + HD, 0);
            SCHED();
            ac2[1][0] = mfma32(a1, hf[i][0], ac2[1][0]);
            ac2[0][1] = mfma32(a0, hf[i][1], ac2[0][1]);
            SCHED();
            if constexpr (i + HD < 28) load_h(i + HD, 1);
            SCHED();
            ac2[1][1] = mfma32(a1, hf[i][1], ac2[1][1]);
            SCHED();
            if constexpr (i == 14 || i == 16) pair_w2(w2o, 24 + (i - 14), w2i + (i - 14) * 2048);
            if (i == 20) load_x();
            if (i == 23) load_y();
            a0 = b0; a1 = b1;
        }, std::make_integer_sequence<int, 28>{});
        SCHED();
        STAMP(15);
        WAIT_LGKM0();
#pragma unroll
        for (int v = 0; v < 8; ++v) {
            LAS char* sl = sm + w2i + (v < 6 ? v * 2048 : L_SLOT + (v - 6) * 2048) + lw * 16;
            const f32x16& t = ac2[v >> 2][(v >> 1) & 1];
            const int r0 = 8 * (v & 1);
            *(LAS f32x4*)sl = (f32x4){t[r0], t[r0 + 1], t[r0 + 2], t[r0 + 3]};
            *(LAS f32x4*)(sl + 1024) = (f32x4){t[r0 + 4], t[r0 + 5], t[r0 + 6], t[r0 + 7]};
        }
    } else {
        u64 epoch;
        asm volatile("s_load_dwordx2 %0, %1, 0x0" : "=s"(epoch) : "s"(p.ws) : "memory");
        const int kr0 = (gpair & 31) * 64, kc0 = (gpair >> 5) * 64;
        const int chn = wq >> 1, par = wq & 1, dch = 2 * gpair + chn;
        const int priv = 16384 + 16384 + wq * 4096 + 3072;
        u32x4 xvo;
#pragma unroll
        for (int j = 0; j < 4; ++j) xvo[j] = (unsigned)((8 * j + (lane >> 3)) * D + ((((lane & 7) ^ (lane >> 4)) ^ (4 * (j & 1))) * 8)) * 2u - 1024u * j;
        const u16* xb = sptr(p.x, (mt * 128 + wq * 32) * D);
        auto group_x = [&](int si) { glds16x4(sptr(xb, kst(si) * 64), xvo, lb + ring(si) + wq * 4096); };
        group_x(0);
        {
            const bool isk = lane >= 8 && lane < 14;
            u64 mb_ = vsel(lane == 15, p.dv, vsel(lane == 14, p.hk, vsel(isk, p.k, (u64)p.sk)));
            unsigned mo = (unsigned)((kc0 + wq * 16) * 8 + (lane & 7) * 16);
            mo = isk ? (unsigned)((kr0 ? kr0 - 3 + ((lane - 8) >> 1) : 0) * D + kc0 + wq * 16 + (lane & 1) * 8) * 2u : mo;
            mo = lane == 14 ? (unsigned)dch * 4096u : mo;
            mo = lane == 15 ? (unsigned)(dch & ~7) * 2u : mo;
            const u64 ma = mb_ + mo;
            asm volatile("s_mov_b32 m0, %1\n\ts_nop 0\n\tglobal_load_lds_dwordx4 %0, off" :: "v"(ma), "s"(lb + ring(2) + priv));
        }
        group_x(1); group_w(1, 0); group_w(1, 1);
        const int cbi = L_CONV + chn * C_CH;
        LAS char* cb = sm + cbi;
        const int c8 = lane & 7;
        int ka0[2], kam[2], zo;
        unsigned kvo[2], uso[2];
#pragma unroll
        for (int rd = 0; rd < 2; ++rd) {
            const int ck = 2 * wq + rd, rr = lane >> 3;
            const int bq = 2 * (lane >> 4) + (p4 >> 1), bm = bq ? bq - 1 : 0;
            ka0[rd] = lb + L_KV + (8 * bq + q4) * 128 + ((ck ^ (((q4 >> 1) & 1) | ((bq & 3) << 1))) * 16) + 8 * (p4 & 1);
            kam[rd] = lb + L_KV + (8 * bm + 4 + q4) * 128 + ((ck ^ (((q4 >> 1) & 1) | ((bm & 3) << 1))) * 16) + 8 * (p4 & 1);
            kvo[rd] = (unsigned)(rr * D + (((lane & 7) ^ (((rr >> 1) & 1) | ((ck & 3) << 1))) * 8)) * 2u;
            uso[rd] = WS_U + (unsigned)(kc0 + ck * 8 + rr) * 8192u + (unsigned)(1984 - kr0 + c8 * 8) * 2u;
            asm volatile("" : "+v"(ka0[rd]), "+v"(kam[rd]), "+v"(kvo[rd]), "+v"(uso[rd]));
        }
        int tsrc = ((7 - c8) * 8 + (lane >> 3)) * 4;
        asm volatile("" : "+v"(tsrc));
        const int cc = 2 * par + ((lane >> 2) & 1);
        zo = cbi + C_U + par * C_US + (248 + (lane < 36 ? lane : 35)) * 16;
        zo = lane < 8 ? cbi + cc * C_HS + (lane & 3) * 16 : zo;
        u32x4 w2o = w2_off();
        asm volatile("" : "+v"(zo), "+v"(w2o));
        asm volatile("s_waitcnt vmcnt(12) lgkmcnt(0)\n\ts_barrier" : "+s"(epoch) :: "memory");
        tag32 = (unsigned)(epoch ^ TAG_SALT);
        STAMP(1);
        u32x2 skv[2]; unsigned hal[2][3];
        LAS char* mp = sm + ring(2) + priv;
#pragma unroll
        for (int rd = 0; rd < 2; ++rd) {
            skv[rd] = *(LAS u32x2*)(mp + (rd * 8 + c8) * 8);
            asm volatile("" ::: "memory");
#pragma unroll
            for (int i = 0; i < 3; ++i) hal[rd][i] = *(LAS u16*)(mp + 128 + i * 32 + rd * 16 + c8 * 2);
        }
        u32x4 h0 = lds128(mp + 224);
        unsigned dbits = *(LAS u16*)(mp + 240 + (dch & 7) * 2);
        group_x(2); group_w(2, 0);
        WAIT_LGKM0();
        asm volatile("" : "+v"(skv[0]), "+v"(skv[1]), "+v"(hal[0][0]), "+v"(hal[0][1]), "+v"(hal[0][2]), "+v"(hal[1][0]), "+v"(hal[1][1]), "+v"(hal[1][2]), "+v"(h0), "+v"(dbits));
        group_w(2, 1);
        STAMPG(2); WAIT_BAR("vmcnt(12)"); STAMPG(3);
        group_x(3); group_w(3, 0); group_w(3, 1);
#pragma unroll
        for (int j = 0; j < 2; ++j) {
            glds16(sptr(p.k, (kr0 + (2 * wq + j) * 8) * D + kc0), kvo[j], lb + L_KV + (2 * wq + j) * 1024);
            glds16(sptr(p.v, (kr0 + (2 * wq + j) * 8) * D + kc0), kvo[j], lb + L_KV + 8192 + (2 * wq + j) * 1024);
        }
        STAMPG(4); WAIT_BAR("vmcnt(16)"); STAMPG(5);
        group_x(4); group_w(4, 0); group_w(4, 1);
        u32x4 bval;
        {
            const bool s1 = !(cc & 1), s2 = !(cc & 2);
            const unsigned a0 = s1 ? 0u : h0[0], a1 = s1 ? h0[0] : h0[1], a2 = s1 ? h0[1] : h0[2], a3 = s1 ? h0[2] : h0[3];
            const unsigned lm = (lane == 3 || lane == 7) ? 0xffffffffu : 0u;
            bval = (u32x4){(s2 ? 0u : a0) & lm, (s2 ? 0u : a1) & lm, (s2 ? a0 : a2) & lm, (s2 ? a1 : a3) & lm};
            asm volatile("" : "+v"(bval));
            const unsigned hm = kr0 ? 0xffffu : 0u;
#pragma unroll
            for (int i = 0; i < 6; ++i) { hal[i / 3][i % 3] = (hal[i / 3][i % 3] & hm) << 16; asm volatile("" : "+v"(hal[i / 3][i % 3])); }
        }
        STAMP(2);
        STAMPG(6); WAIT_BAR("vmcnt(12)"); STAMPG(7);
        STAMP(3);
        u32x2 kt[2][3], vt[2][2];
#pragma unroll
        for (int rd = 0; rd < 2; ++rd) {
            kt[rd][0] = ldstr(lp(kam[rd])); kt[rd][1] = ldstr(lp(ka0[rd])); kt[rd][2] = ldstr(lp(ka0[rd]) + 512);
            vt[rd][0] = ldstr(lp(ka0[rd]) + 8192); vt[rd][1] = ldstr(lp(ka0[rd]) + 8192 + 512);
        }
        unsigned po[2][2];
        auto prep = [&](int rd, int half) {
            SCHED();
            const float c0 = bflo1(skv[rd][0]), c1 = bfhi1(skv[rd][0]), c2 = bflo1(skv[rd][1]), c3 = bfhi1(skv[rd][1]);
            const u32x2 ka = kt[rd][half], kb = kt[rd][half + 1], vv = vt[rd][half];
            float kk[7] = {bfhi1(ka[0]), bflo1(ka[1]), bfhi1(ka[1]), bflo1(kb[0]), bfhi1(kb[0]), bflo1(kb[1]), bfhi1(kb[1])};
            if (half == 0) {
#pragma unroll
                for (int i = 0; i < 3; ++i) kk[i] = lane < 8 ? __uint_as_float(hal[rd][i]) : kk[i];
            }
            const float v4[4] = {bflo1(vv[0]), bfhi1(vv[0]), bflo1(vv[1]), bfhi1(vv[1])};
            float o[4];
#pragma unroll
            for (int i = 0; i < 4; ++i) {
                o[i] = fmaf(kk[i + 3], c0, fmaf(kk[i + 2], c1, fmaf(kk[i + 1], c2, kk[i] * c3))) * v4[i];
                asm volatile("" : "+v"(o[i]));
            }
            po[half][0] = __builtin_amdgcn_ds_bpermute(tsrc, pk(o[3], o[2]));
            po[half][1] = __builtin_amdgcn_ds_bpermute(tsrc, pk(o[1], o[0]));
            SCHED();
        };
        auto prep_store = [&](int rd) {
            __builtin_amdgcn_raw_buffer_store_b128((u32x4){vmov1(tag32), po[1][0], vmov1(tag32), po[1][1]}, wsr, uso[rd], 0, 16);
            __builtin_amdgcn_raw_buffer_store_b128((u32x4){vmov1(tag32), po[0][0], vmov1(tag32), po[0][1]}, wsr, uso[rd], 4096, 16);
        };
        group_x(5); prep(0, 0); group_w(5, 0); prep(0, 1); group_w(5, 1); prep_store(0);
        STAMPG(8); WAIT_BAR("vmcnt(12) lgkmcnt(0)"); STAMPG(9);
        group_x(6); prep(1, 0); group_w(6, 0); prep(1, 1); group_w(6, 1); prep_store(1);
        STAMPG(10); WAIT_BAR("vmcnt(12)"); STAMPG(11);
        const unsigned fo = lane * 16u;
        const unsigned ao = wq < 2 ? WS_CSUM + (unsigned)(nt * 2 + wq) * 1024u : WS_STATS + mt * 1024u;
        glds16c(p.ws + ao, fo, lb + L_RAW + wq * 1024);
#pragma unroll
        for (int i = 0; i < 3; ++i) pair_w2(w2o, 2 * i, w2i + i * 4096);
        if (lane < 16) *(LAS unsigned*)(sm + L_CTL + lane * 4) = 0u;
        STAMPG(12); WAIT_BAR("vmcnt(12) lgkmcnt(0)"); STAMPG(13);
        u32x4 av = lds128(sm + L_RAW + wq * 1024 + lane * 16);
#pragma unroll 1
        for (unsigned spins = 0; spins < SPIN_CAP; ++spins) {
            if (__all(((av[0] ^ tag32) | (av[2] ^ tag32)) == 0u)) break;
            if (spins) __builtin_amdgcn_s_sleep(4);
            asm volatile("" ::: "memory");
            av = __builtin_amdgcn_raw_buffer_load_b128(wsr, ao + lane * 16u, 0, 16);
            asm volatile("" : "+v"(av));
        }
        *(LAS u32x2*)(sm + L_CSUM + (wq < 2 ? wq : 2) * 512 + lane * 8) = (u32x2){av[1], av[3]};
#pragma unroll
        for (int i = 0; i < 3; ++i) pair_w2(w2o, 6 + 2 * i, w2i + L_SLOT + i * 4096);
        STAMP(4);
        STAMPG(14); WAIT_BAR("lgkmcnt(0)"); STAMPG(15);
        STAMP(5);
        __builtin_amdgcn_s_setprio(2);
        const unsigned ubase = WS_U + (unsigned)dch * 8192u;
        glds16x2c(p.ws + ubase + par * 2048, fo, lb + cbi + C_U + par * 2048);
        glds16x2c(p.ws + ubase + 4096 + par * 2048, fo, lb + cbi + C_U + C_US + par * 2048);
        glds4c(p.ws + ubase + (1 - par) * 2048, (lane & 1) * 4u, lb + L_UNX + wq * 256);
        {
            const u32x4 fvo = {fo, fo, fo, lane == 63 ? 62u * 16u : fo};
#pragma unroll
            for (int c = 0; c < 2; ++c) glds16x4(sptr(p.hk, dch * S + 4 * par + 2 * c + 2), fvo, lb + cbi + (2 * par + c) * C_HS + 64);
        }
        *(LAS u32x4*)(sm + zo) = bval;
        WAIT_VM(8);
        __hip_atomic_store((LAS unsigned*)(sm + L_CTL + 48 + wq * 4), 1u, __ATOMIC_RELAXED, __HIP_MEMORY_SCOPE_WORKGROUP);
        {
            u32x4 ua[2], ub[2]; u32x2 un[2];
#pragma unroll
            for (int rd = 0; rd < 2; ++rd) {
                const int x8 = 128 * par + 64 * rd + lane;
                ua[rd] = lds128(cb + C_U + x8 * 16); ub[rd] = lds128(cb + C_U + C_US + x8 * 16);
                un[rd] = *(LAS u32x2*)((rd == 1 && lane == 63) ? sm + L_UNX + wq * 256 : cb + C_U + x8 * 16 + 16);
            }
#pragma unroll 1
            for (unsigned spins = 0; spins < SPIN_CAP; ++spins) {
                unsigned df = 0;
#pragma unroll
                for (int rd = 0; rd < 2; ++rd) df |= (ua[rd][0] ^ tag32) | (ua[rd][2] ^ tag32) | (ub[rd][0] ^ tag32) | (ub[rd][2] ^ tag32) | (un[rd][0] ^ tag32);
                if (__all(df == 0u)) break;
                __builtin_amdgcn_s_sleep(4);
                asm volatile("" ::: "memory");
#pragma unroll
                for (int rd = 0; rd < 2; ++rd) {
                    const int x8 = 128 * par + 64 * rd + lane;
                    const unsigned uo = ubase + (unsigned)x8 * 16u;
                    ua[rd] = __builtin_amdgcn_raw_buffer_load_b128(wsr, uo, 0, 16);
                    ub[rd] = __builtin_amdgcn_raw_buffer_load_b128(wsr, uo + 4096u, 0, 16);
                    un[rd] = __builtin_amdgcn_raw_buffer_load_b64(wsr, x8 == 255 ? uo : uo + 16u, 0, 16);
                }
                asm volatile("" : "+v"(ua[0]), "+v"(ub[0]), "+v"(un[0]), "+v"(ua[1]), "+v"(ub[1]), "+v"(un[1]));
            }
#pragma unroll
            for (int rd = 0; rd < 2; ++rd) {
                const int x8 = 128 * par + 64 * rd + lane;
                const unsigned nx = x8 == 255 ? 0u : un[rd][1];
                *(LAS u32x4*)(cb + C_U + x8 * 16) = (u32x4){__builtin_amdgcn_alignbit(ua[rd][3], ua[rd][1], 16), __builtin_amdgcn_alignbit(ub[rd][1], ua[rd][3], 16),
                                                            __builtin_amdgcn_alignbit(ub[rd][3], ub[rd][1], 16), __builtin_amdgcn_alignbit(nx, ub[rd][3], 16)};
                *(LAS u32x4*)(cb + C_U + C_US + x8 * 16) = (u32x4){ua[rd][1], ua[rd][3], ub[rd][1], ub[rd][3]};
            }
        }
        asm volatile("s_waitcnt vmcnt(0) lgkmcnt(0)" ::: "memory");
        __hip_atomic_store((LAS unsigned*)(sm + L_CTL + 16 + wq * 4), 1u, __ATOMIC_RELAXED, __HIP_MEMORY_SCOPE_WORKGROUP);
        wait_lds(sm + L_CTL + 16 + (wq ^ 1) * 4);
        STAMP(7);
        wait_lds(sm + L_CTL + 32 + wq * 4);
        STAMP(6);
        const int n16 = lane & 15, kg = lane >> 4;
        f32x4 ca[4];
#pragma unroll
        for (int j = 0; j < 4; ++j) ca[j] = (f32x4){0.f, 0.f, 0.f, 0.f};
        LAS char* ap = cb + (n16 & 3) * C_HS + ((n16 >> 2) + kg) * 16;
        LAS char* bp = cb + C_U + (n16 & 1) * C_US + (252 - 4 * (n16 >> 1) + kg) * 16 - 7 * 512;
        auto conv_res = [&](auto pc_, int r0, int r1) {
            constexpr int P = decltype(pc_)::value;
            constexpr int T[4] = {P, 3 - P, 4 + P, 7 - P};
#pragma unroll
            for (int r = r0; r < r1; ++r) {
                u32x4 fa[8], fb[8];
#pragma unroll
                for (int i = 0; i <= T[3]; ++i) {
                    fa[i] = lds128(ap + r * 64 + i * 512);
                    fb[i] = lds128(bp + r * 64 + (7 - i) * 512);
                }
#pragma unroll
                for (int j = 0; j < 4; ++j)
#pragma unroll
                    for (int pp = 0; pp <= T[j]; ++pp) ca[j] = mfma16(fa[pp], fb[T[j] - pp], ca[j]);
            }
        };
        if (par) conv_res(std::integral_constant<int, 1>{}, 0, 8); else conv_res(std::integral_constant<int, 0>{}, 0, 8);
        SCHED();
        STAMP(9);
        {
            const float dval = bflo(dbits);
            const int ly = fresh_lane();
            const bool odd = ly & 1;
            const int sl0 = 32 * ((ly & 15) >> 1) + 8 * (ly >> 4) + 4 * (ly & 1);
#pragma unroll
            for (int j = 0; j < 4; ++j) {
                const int tj = (j == 0 ? 0 : j == 1 ? 3 : j == 2 ? 4 : 7) ^ par;
                const u32x2 kvr = *(LAS u32x2*)(cb + C_U + C_US + (2044 - 256 * tj - sl0) * 2);
                const float r0 = __shfl_xor(odd ? ca[j][0] : ca[j][2], 1), r1 = __shfl_xor(odd ? ca[j][1] : ca[j][3], 1);
                const unsigned ya = pk(odd ? r0 : ca[j][0], odd ? ca[j][2] : r0), yb = pk(odd ? r1 : ca[j][1], odd ? ca[j][3] : r1);
                const unsigned ka = pk(mul1(bfhi1(kvr[1]), dval), mul1(bflo1(kvr[1]), dval)), kb = pk(mul1(bfhi1(kvr[0]), dval), mul1(bflo1(kvr[0]), dval));
                __builtin_amdgcn_raw_buffer_store_b128((u32x4){vmov1(tag32), pk(add1(bflo1(ya), bflo1(ka)), add1(bfhi1(ya), bfhi1(ka))),
                                                               vmov1(tag32), pk(add1(bflo1(yb), bflo1(kb)), add1(bfhi1(yb), bfhi1(kb)))},
                    wsr, WS_Y + (unsigned)dch * 8192u + (unsigned)(256 * tj + sl0) * 4u, 0, 16);
            }
        }
        STAMP(10);
        __builtin_amdgcn_s_setprio(0);
        load_x();
        wait_lds(sm + L_CTL + 4);
        __builtin_amdgcn_s_sleep(40);
        load_y();
        STAMP(15);
    }
    WAIT_BAR("lgkmcnt(0)");
    STAMP(16);
    f32x4 o0 = {0.f, 0.f, 0.f, 0.f}, o1 = {0.f, 0.f, 0.f, 0.f};
    {
        const int lz = fresh_lane();
        LAS char* rm = sm + L_W2 + wave * 2048 + (wave >= 6 ? L_SLOT - 12288 : 0) + (lz >> 5) * 1024 + (lz & 31) * 16;
#pragma unroll
        for (int w = 0; w < 4; ++w) {
            o0 += *(LAS f32x4*)(rm + w * 12288);
            o1 += *(LAS f32x4*)(rm + w * 12288 + 512);
        }
    }
#pragma unroll 1
    for (unsigned spins = 0; spins < SPIN_CAP; ++spins) {
        unsigned df = stv[0] ^ tag32;
#pragma unroll
        for (int i = 0; i < 8; ++i) df |= yv[i][0] ^ tag32;
        if (__all(df == 0u)) break;
        __builtin_amdgcn_s_sleep(4);
        asm volatile("" ::: "memory");
        load_y();
    }
    {
        const float mb = bflo(stv[1]), ri = __builtin_amdgcn_rcpf(bfhi(stv[1]));
        const unsigned ysh = (srow & 1) ? 0u : 16u;
        u32x4 st;
#pragma unroll
        for (int i = 0; i < 4; ++i) {
            const unsigned op = i < 2 ? pk(o0[2 * i], o0[2 * i + 1]) : pk(o1[2 * i - 4], o1[2 * i - 3]);
            const unsigned gp = pk(silu_f(bflo(op)), silu_f(bfhi(op)));
            const float xa0 = bflo(xv[i]), xa1 = bfhi(xv[i]);
            const unsigned tp = pk(xa0 - mb, xa1 - mb);
            const unsigned np = pk(bflo(tp) * ri, bfhi(tp) * ri);
            const unsigned sp = pk(bflo(np) * bfhi(yv[2 * i][1] << ysh), bfhi(np) * bfhi(yv[2 * i + 1][1] << ysh));
            const unsigned mp = pk(bflo(sp) * bflo(gp), bfhi(sp) * bfhi(gp));
            st[i] = pk(xa0 + bflo(mp), xa1 + bfhi(mp));
        }
        *(u32x4*)(p.out + (size_t)srow * D + dd) = st;
    }
    STAMP(17);
    STAMP_OUT(18);
}

extern "C" void kernel_launch(void* const* d_in, const int* in_sizes, int n_in,
                              void* d_out, int out_size, void* d_ws, size_t ws_size,
                              hipStream_t stream) {
    Params p;
    p.x = (const u16*)d_in[0]; p.k = (const u16*)d_in[1]; p.v = (const u16*)d_in[2];
    p.sk = (const u16*)d_in[3]; p.hk = (const u16*)d_in[4]; p.dv = (const u16*)d_in[5];
    p.w1 = (const u16*)d_in[7]; p.w3 = (const u16*)d_in[8]; p.w2 = (const u16*)d_in[9];
    p.out = (u16*)d_out; p.ws = (char*)d_ws;
    (void)hipFuncSetAttribute((const void*)fused_kernel, hipFuncAttributeMaxDynamicSharedMemorySize, L_TOTAL);
    fused_kernel<<<256, 512, L_TOTAL, stream>>>(p);
}
```

```cpp
#include <hip/hip_runtime.h>
#include <stdint.h>
#include <utility>

typedef unsigned short u16;
typedef unsigned long long u64;
typedef short s16x4 __attribute__((ext_vector_type(4)));
typedef short s16x8 __attribute__((ext_vector_type(8)));
typedef __bf16 bf16x2 __attribute__((ext_vector_type(2)));
typedef __bf16 bf16x8 __attribute__((ext_vector_type(8)));
typedef float f32x2 __attribute__((ext_vector_type(2)));
typedef float f32x4 __attribute__((ext_vector_type(4)));
typedef float f32x16 __attribute__((ext_vector_type(16)));
typedef unsigned u32x2 __attribute__((ext_vector_type(2)));
typedef unsigned u32x4 __attribute__((ext_vector_type(4)));
typedef unsigned u32x16 __attribute__((ext_vector_type(16)));
#define LAS __attribute__((address_space(3)))
#define GAS __attribute__((address_space(1)))
#define RLX_AGENT __ATOMIC_RELAXED, __HIP_MEMORY_SCOPE_AGENT

constexpr int S = 2048, D = 448, F = 1792;

constexpr unsigned WS_EPOCH = 0;
constexpr unsigned WS_FH = 1024;
constexpr unsigned WS_STATS = 8192;
constexpr unsigned WS_CSUM = 24576;
constexpr unsigned WS_U = 65536;
constexpr unsigned WS_Y = WS_U + D * S * 4;
constexpr unsigned WS_H = WS_Y + D * S * 4;
constexpr unsigned WS_END = WS_H + 64 * 112 * 1024;
constexpr unsigned WS_DBG = WS_END;

constexpr int L_KV = 0;
constexpr int L_CTL = 0;
constexpr int L_CSUM = 256;
constexpr int L_STAT = 1280;
constexpr int L_RAW = 2048;
constexpr int L_UNX = 6144;
constexpr int L_SLOT = 49152;
constexpr int L_CONV = 12288;
constexpr int C_CH = 26112, C_HS = 4160, C_U = 16640, C_US = 4640;
constexpr int L_W2 = 16384 + L_SLOT;
constexpr int L_TOTAL = 163840;
__host__ __device__ constexpr int ring(int stage) { return 16384 + (stage % 3) * L_SLOT; }
constexpr unsigned CODE_PREFETCH = 24576;
#define POLL_SLEEP 4
#define HDEPTH 8
#define ROT2 (8 * ml)
#define TOUCH 0
#define MPRIO 1
#define TLBW 1
#define ROT1 0

constexpr u64 TAG_SALT = 0x9E3779B97F4A7C15ull;
constexpr unsigned SPIN_CAP = 1u << 18;

struct Params {
    const u16* x; const u16* k; const u16* v; const u16* sk; const u16* hk; const u16* dv;
    const u16* w1; const u16* w3; const u16* w2; u16* out; char* ws;
};

__device__ __forceinline__ float bflo(unsigned p) { return __uint_as_float(p << 16); }
__device__ __forceinline__ float bfhi(unsigned p) { return __uint_as_float(p & 0xffff0000u); }
__device__ __forceinline__ float bflo1(unsigned p) { float r; asm("v_lshlrev_b32 %0, 16, %1" : "=v"(r) : "v"(p)); return r; }
__device__ __forceinline__ float bfhi1(unsigned p) { float r; asm("v_and_b32 %0, 0xffff0000, %1" : "=v"(r) : "v"(p)); return r; }
__device__ __forceinline__ float mul1(float a, float b) { float r; asm("v_mul_f32 %0, %1, %2" : "=v"(r) : "v"(a), "v"(b)); return r; }
__device__ __forceinline__ float add1(float a, float b) { float r; asm("v_add_f32 %0, %1, %2" : "=v"(r) : "v"(a), "v"(b)); return r; }
__device__ __forceinline__ unsigned vmov1(unsigned s) { unsigned r; asm volatile("v_mov_b32 %0, %1" : "=v"(r) : "s"(s)); return r; }
__device__ __forceinline__ unsigned pk(float a, float b) {
    f32x2 f = {a, b};
    return __builtin_bit_cast(unsigned, __builtin_convertvector(f, bf16x2));
}
__device__ __forceinline__ float rbf(float a) { return bflo(pk(a, a)); }
__device__ __forceinline__ float dot2(unsigned a, unsigned b, float c) {
    return __builtin_amdgcn_fdot2_f32_bf16(__builtin_bit_cast(bf16x2, a), __builtin_bit_cast(bf16x2, b), c, false);
}
__device__ __forceinline__ void glds16(const void* base, unsigned voff, unsigned ldst) {
    u64 t;
    asm volatile("s_mov_b32 m0, %3\n\ts_mov_b64 %0, %2\n\tglobal_load_lds_dwordx4 %1, %0" : "=&s"(t) : "v"(voff), "s"(base), "s"(ldst));
}
__device__ __forceinline__ void glds16x4(const void* base, u32x4 vo, unsigned ldst) {
    u64 t;
    asm volatile("s_mov_b32 m0, %6\n\ts_mov_b64 %0, %5\n\tglobal_load_lds_dwordx4 %1, %0\n\tglobal_load_lds_dwordx4 %2, %0 offset:1024\n\t"
                 "global_load_lds_dwordx4 %3, %0 offset:2048\n\tglobal_load_lds_dwordx4 %4, %0 offset:3072"
                 : "=&s"(t) : "v"(vo[0]), "v"(vo[1]), "v"(vo[2]), "v"(vo[3]), "s"(base), "s"(ldst));
}
__device__ __forceinline__ void glds16x2c(const void* base, unsigned voff, unsigned ldst) {
    u64 t;
    asm volatile("s_mov_b32 m0, %3\n\ts_mov_b64 %0, %2\n\tglobal_load_lds_dwordx4 %1, %0 sc1\n\tglobal_load_lds_dwordx4 %1, %0 offset:1024 sc1"
                 : "=&s"(t) : "v"(voff), "s"(base), "s"(ldst));
}
__device__ __forceinline__ void glds16c(const void* base, unsigned voff, unsigned ldst) {
    u64 t;
    asm volatile("s_mov_b32 m0, %3\n\ts_mov_b64 %0, %2\n\tglobal_load_lds_dwordx4 %1, %0 sc1" : "=&s"(t) : "v"(voff), "s"(base), "s"(ldst));
}
__device__ __forceinline__ void glds4c(const void* base, unsigned voff, unsigned ldst) {
    u64 t;
    asm volatile("s_mov_b32 m0, %3\n\ts_mov_b64 %0, %2\n\tglobal_load_lds_dword %1, %0 sc1" : "=&s"(t) : "v"(voff), "s"(base), "s"(ldst));
}
__device__ __forceinline__ const u16* sptr(const u16* base, int off) {
    const u16* r = base + __builtin_amdgcn_readfirstlane(off);
    asm("" : "+s"(r));
    return r;
}
__device__ __forceinline__ u64 vsel(bool c, const void* a, u64 b) {
    u64 va = (u64)a;
    asm("" : "+v"(va), "+v"(b));
    return c ? va : b;
}
__device__ __forceinline__ LAS char* lp(int a) { return (LAS char*)(size_t)(unsigned)a; }
__device__ __forceinline__ u32x4 lds128(LAS char* p) { return *(LAS u32x4*)p; }
__device__ __forceinline__ u32x2 ldstr(LAS char* p) {
    return __builtin_bit_cast(u32x2, __builtin_amdgcn_ds_read_tr16_b64_v4i16((LAS s16x4*)p));
}
__device__ __forceinline__ u32x4 ldstr2(LAS char* p, int off) {
    u32x2 a = ldstr(p), b = ldstr(p + off);
    return (u32x4){a.x, a.y, b.x, b.y};
}
__device__ __forceinline__ f32x16 mfma32(u32x4 a, u32x4 b, f32x16 c) {
    return __builtin_amdgcn_mfma_f32_32x32x16_bf16(__builtin_bit_cast(bf16x8, a), __builtin_bit_cast(bf16x8, b), c, 0, 0, 0);
}
__device__ __forceinline__ f32x4 mfma16(u32x4 a, u32x4 b, f32x4 c) {
    return __builtin_amdgcn_mfma_f32_16x16x32_bf16(__builtin_bit_cast(bf16x8, a), __builtin_bit_cast(bf16x8, b), c, 0, 0, 0);
}
__device__ __forceinline__ float silu_f(float g) {
    return g * __builtin_amdgcn_rcpf(1.f + __builtin_amdgcn_exp2f(g * -1.44269504088896341f));
}
template <class Fn, int... I>
__device__ __forceinline__ void static_for(Fn&& fn, std::integer_sequence<int, I...>) { (fn(std::integral_constant<int, I>{}), ...); }
#define WAIT_VM(n) asm volatile("s_waitcnt vmcnt(" #n ")" ::: "memory")
#define WAIT_LGKM0() asm volatile("s_waitcnt lgkmcnt(0)" ::: "memory")
#define WAIT_BAR(w) asm volatile("s_waitcnt " w "\n\ts_barrier" ::: "memory")
#define SCHED() __builtin_amdgcn_sched_barrier(0)
#define CAS __attribute__((address_space(4)))
#define NSTAMP 18

__device__ __forceinline__ void wait_flags(char* ws, unsigned off, int n, u64 tag, int lane) {
    GAS u64* p = (GAS u64*)(ws + off) + (lane < n ? lane : 0);
#pragma unroll 1
    for (unsigned spins = 0; spins < SPIN_CAP; ++spins) {
        u64 val = __hip_atomic_load(p, RLX_AGENT);
        if (__all(val == tag)) break;
        __builtin_amdgcn_s_sleep(POLL_SLEEP);
    }
    __builtin_amdgcn_fence(__ATOMIC_ACQUIRE, "wavefront");
}

__device__ __forceinline__ void wait_lds(LAS char* word) {
#pragma unroll 1
    for (unsigned spins = 0; spins < (SPIN_CAP << 4); ++spins) {
        if (__builtin_amdgcn_readfirstlane(__hip_atomic_load((LAS unsigned*)word, __ATOMIC_RELAXED, __HIP_MEMORY_SCOPE_WORKGROUP))) break;
        __builtin_amdgcn_s_sleep(1);
    }
    asm volatile("" ::: "memory");
}
__device__ __forceinline__ void arrive_publish(LAS char* ctr, char* ws, unsigned off, u64 tag, int lane) {
    if (lane == 0) {
        const unsigned old = __hip_atomic_fetch_add((LAS unsigned*)ctr, 1u, __ATOMIC_RELAXED, __HIP_MEMORY_SCOPE_WORKGROUP);
        if (old == 3u) __hip_atomic_store((GAS u64*)(ws + off), tag, RLX_AGENT);
    }
}

#define STAMP(i)
#define STAMPG(i)
#define STAMP_DECL()
#define STAMP_OUT(n)

__global__ __launch_bounds__(512, 2) void fused_kernel(Params p) {
    extern __shared__ __attribute__((aligned(16))) char smem_raw[];
    u64 pc;
    asm volatile("s_getpc_b64 %0" : "=s"(pc));
    LAS char* sm = (LAS char*)smem_raw;
    const int tid = threadIdx.x, lane = tid & 63;
    const int wave = __builtin_amdgcn_readfirstlane(tid >> 6);
    const int xcd = blockIdx.x & 7, idx = blockIdx.x >> 3;
    const __amdgpu_buffer_rsrc_t wsr = __builtin_amdgcn_make_buffer_rsrc(p.ws, 0, WS_END, 0x00020000);
    asm volatile("" :: "s"(p.x), "s"(p.k), "s"(p.v), "s"(p.sk), "s"(p.hk), "s"(p.dv), "s"(p.w1), "s"(p.w3), "s"(p.w2), "s"(p.out), "s"(p.ws));
    STAMP_DECL();
    STAMP(0);

    if (idx < 4) {
        unsigned pf = 0;
        if (idx == 0 && wave == 7) {
            const GAS char* code = (const GAS char*)(pc & ~(u64)255);
#pragma unroll
            for (unsigned i = 0; i < CODE_PREFETCH / 8192; ++i) pf ^= *(const GAS unsigned*)(code + i * 8192 + lane * 128);
        }
        if (TLBW && idx == 1 && wave >= 5) {
            if (wave == 7) {
#pragma unroll
                for (unsigned i = 0; i < 4; ++i) { const unsigned o = (i * 64 + lane) << 16; pf ^= *(const unsigned*)(p.ws + (o < WS_END ? o : 0u)); }
            } else {
                const unsigned o = (unsigned)(lane & 31) << 16;
                const char* b0 = wave == 5 ? (const char*)p.out : (const char*)p.k;
                const char* b1 = wave == 5 ? (const char*)p.w2 : (const char*)p.v;
                pf ^= *(const unsigned*)((lane < 32 ? b0 : b1) + (o < 1605632u ? o : 0u));
                pf ^= *(const unsigned*)((lane < 32 ? b0 : b1) + 1605632u - 4u);
            }
        }
        const int ca = (xcd >> 1) * 4 + idx, cmat = ca >= 7, cnt = 7 * (xcd & 1) + (ca >= 7 ? ca - 7 : ca);
        const bool job = ca < 14;
        u32x4 xr[7], wr[14];
        if (job) {
            const u16* wp = (cmat ? p.w3 : p.w1) + (size_t)(wave * 56 + (lane >> 4)) * F + cnt * 128 + (lane & 15) * 8;
#pragma unroll
            for (int i = 0; i < 14; ++i) wr[i] = *(const u32x4*)(wp + (size_t)(4 * i) * F);
        }
        const int row = (8 * (xcd >> 1) + 4 * (xcd & 1) + idx) * 64 + wave * 8 + (lane >> 3);
#pragma unroll
        for (int i = 0; i < 7; ++i) xr[i] = *(const u32x4*)(p.x + (size_t)row * D + i * 64 + (lane & 7) * 8);
        const unsigned pf2 = *(const unsigned*)((const char*)p.hk + (size_t)((xcd * 4 + idx) * 448 + wave * 56 + (lane < 56 ? lane : 0)) * 128);
        u64 epoch;
        asm volatile("s_load_dwordx2 %0, %1, 0x0\n\ts_waitcnt lgkmcnt(0)" : "=s"(epoch) : "s"(p.ws) : "memory");
        const u64 tag = epoch ^ TAG_SALT;
        const unsigned tag32 = (unsigned)tag;
        if (job) {
            float c[8];
#pragma unroll
            for (int j = 0; j < 8; ++j) c[j] = 0.f;
#pragma unroll
            for (int i = 0; i < 14; ++i)
#pragma unroll
                for (int j = 0; j < 4; ++j) { c[2 * j] += bflo(wr[i][j]); c[2 * j + 1] += bfhi(wr[i][j]); }
            LAS char* cp = sm + ((wave * 4 + (lane >> 4)) * 128 + (lane & 15) * 8) * 4;
            *(LAS f32x4*)cp = (f32x4){c[0], c[1], c[2], c[3]};
            *(LAS f32x4*)(cp + 16) = (f32x4){c[4], c[5], c[6], c[7]};
        }
        {
            float sum = 0.f;
#pragma unroll
            for (int i = 0; i < 7; ++i)
#pragma unroll
                for (int j = 0; j < 4; ++j) sum = dot2(xr[i][j], 0x3f803f80u, sum);
            sum += __shfl_xor(sum, 1); sum += __shfl_xor(sum, 2); sum += __shfl_xor(sum, 4);
            const float mean = sum * (1.f / D);
            float sq = 0.f;
#pragma unroll
            for (int i = 0; i < 7; ++i)
#pragma unroll
                for (int j = 0; j < 4; ++j) {
                    const float a = bflo(xr[i][j]) - mean, b = bfhi(xr[i][j]) - mean;
                    sq = fmaf(a, a, sq); sq = fmaf(b, b, sq);
                }
            sq += __shfl_xor(sq, 1); sq += __shfl_xor(sq, 2); sq += __shfl_xor(sq, 4);
            const u32x2 st = {tag32, pk(mean, __builtin_amdgcn_sqrtf(rbf(rbf(sq * (1.f / D)) + 1e-5f)))};
            __builtin_amdgcn_raw_buffer_store_b64(st, wsr, (lane & 7) == 0 ? WS_STATS + (unsigned)row * 8u : 0xfffffff0u, 0, 16);
        }
        if (job) {
            WAIT_BAR("lgkmcnt(0)");
            float t = 0.f;
#pragma unroll
            for (int i = 0; i < 8; ++i) t += *(LAS float*)(sm + (((tid & 3) * 8 + i) * 128 + (tid >> 2)) * 4);
            t += __shfl_xor(t, 1); t += __shfl_xor(t, 2);
            __builtin_amdgcn_raw_buffer_store_b64((u32x2){tag32, __float_as_uint(t)}, wsr,
                (tid & 3) == 0 ? WS_CSUM + (unsigned)((cnt * 2 + cmat) * 128 + (tid >> 2)) * 8u : 0xfffffff0u, 0, 16);
        }
        STAMP(1);
        asm volatile("" :: "v"(pf), "v"(pf2));
        STAMP_OUT(2);
        if (blockIdx.x == 0 && wave == 0) {
#pragma unroll 1
            for (int m = 0; m < 4; ++m) {
                GAS u64* gp = (GAS u64*)(p.ws + WS_STATS) + (m * 64 + lane) * 8;
#pragma unroll 1
                for (unsigned spins = 0; spins < SPIN_CAP; ++spins) {
                    if (__all((unsigned)__hip_atomic_load(gp, RLX_AGENT) == tag32)) break;
                    __builtin_amdgcn_s_sleep(16);
                }
            }
            __builtin_amdgcn_s_sleep(100);
#pragma unroll 1
            for (int m = 0; m < 16; ++m) wait_flags(p.ws, WS_FH + m * 128, 14, tag, lane);
            if (lane == 0) __hip_atomic_store((GAS u64*)(p.ws + WS_EPOCH), epoch + 1, RLX_AGENT);
        }
        return;
    }

    const int gi = idx - 4, ml = gi & 3, nl = gi >> 2;
    const int mt = 4 * (xcd >> 1) + ml, nt = 7 * (xcd & 1) + nl;
    const int rot1 = ROT1;
    const int m64 = 4 * xcd + ml, n64 = nl;
    const int gpair = xcd * 28 + gi;
    const int r32 = lane & 31, hh = lane >> 5, blk = (lane >> 4) & 1, q4 = (lane & 15) >> 2, p4 = lane & 3;
    const unsigned lb = (unsigned)(size_t)sm;
    const int wq = wave & 3;
    u32x4 wvo;
    wvo[0] = (unsigned)((lane >> 4) * F + (((lane & 15) ^ ((lane >> 4) << 2)) * 8)) * 2u;
#pragma unroll
    for (int j = 1; j < 4; ++j) wvo[j] = wvo[0] + j * (8 * F - 1024);
    const u16* w1b = sptr(p.w1, wq * 16 * F + nt * 128);
    const u16* w3b = sptr(p.w3, wq * 16 * F + nt * 128);
    auto kst = [&](int si) { const int s = si + rot1; return s >= 7 ? s - 7 : s; };
    auto group_w = [&](int si, int m) { glds16x4(sptr(m ? w3b : w1b, kst(si) * 64 * F), wvo, lb + ring(si) + 16384 + m * 16384 + wq * 4096); };
    if (wave < 4) { group_w(0, 0); group_w(0, 1); }
    SCHED();
    const int rot2 = ROT2;
    const int w2i = L_W2 + wq * 12288;
    const int fdb = wave >> 2, fsb = (wave >> 1) & 1;
    auto fresh_lane = [&]() { int l = lane; asm volatile("" : "+v"(l)); return l; };
    unsigned hb;
    int srow, dd;
    u32x2 yv[8], stv;
    u32x4 xv;
    auto g2_setup = [&]() {
        const int l = fresh_lane();
        hb = WS_H + (unsigned)(2 * m64 * 112 + 28 * wq) * 1024u + l * 16u;
    };
    auto load_x = [&]() {
        const int l = fresh_lane();
        srow = 64 * m64 + 32 * fsb + (l & 31);
        dd = 64 * n64 + 32 * fdb + 16 * (wave & 1) + 8 * (l >> 5);
        xv = __builtin_amdgcn_raw_buffer_load_b128(__builtin_amdgcn_make_buffer_rsrc((void*)p.x, 0, S * D * 2, 0x00020000), (unsigned)(srow * D + dd) * 2u, 0, 0);
    };
    auto load_y = [&]() {
        const unsigned yo = WS_Y + (unsigned)dd * 8192u + (unsigned)(srow >> 1) * 8u;
#pragma unroll
        for (int i = 0; i < 8; ++i) yv[i] = __builtin_amdgcn_raw_buffer_load_b64(wsr, yo, i * 8192, 16);
        stv = __builtin_amdgcn_raw_buffer_load_b64(wsr, WS_STATS + (unsigned)srow * 8u, 0, 16);
    };
    auto pair_w2 = [&](u32x4 vo, int i, int dst) {
        int t = i + rot2; t = t >= 28 ? t - 28 : t;
        glds16x4(sptr(p.w2, (wq * 448 + t * 16) * D + n64 * 64), vo, lb + dst);
    };
    auto w2_off = [&]() {
        const int l = fresh_lane();
        const unsigned o = (unsigned)((l >> 3) * D + (((l & 7) ^ (((l >> 4) & 1) << 2)) * 8)) * 2u;
        return (u32x4){o, o + (16 * D - 1024), o + 2 * (16 * D - 1024), o + 3 * (16 * D - 1024)};
    };
    unsigned tag32;

    if (wave < 4) {
        u64 epoch;
        asm volatile("s_load_dwordx2 %0, %1, 0x0" : "=s"(epoch) : "s"(p.ws) : "memory");
        __builtin_amdgcn_s_setprio(MPRIO);
        const int wm = wave >> 1, wn = wave & 1;
        int xa[4], wa0, wa1;
#pragma unroll
        for (int i = 0; i < 4; ++i) xa[i] = lb + ring(0) + (((64 * wm + r32) * 128 + ((hh ^ ((r32 >> 1) & 7)) * 16)) ^ (i * 32));
        wa0 = 16384 + (8 * hh + q4) * 256 + (((8 * wn + 2 * blk + (p4 >> 1)) ^ (q4 << 2)) * 16) + 8 * (p4 & 1);
        wa1 = (wa0 ^ 64) + lb + ring(0); wa0 += lb + ring(0);
        f32x16 acc[2][2][2];
#pragma unroll
        for (int i = 0; i < 8; ++i)
#pragma unroll
            for (int r = 0; r < 16; ++r) acc[i >> 2][(i >> 1) & 1][i & 1][r] = 0.f;
        u32x4 fx[2][2], fw[2][2][2];
        auto load_frag = [&](int g, int j) {
            const int pp = g & 3, b = g & 1;
            if (j == 0) {
                if (g >= 4) {
                    const int d = ring(g >> 2) - ring((g >> 2) - 1);
                    if (pp == 0) { wa0 += d; wa1 += d; asm volatile("" : "+v"(wa0), "+v"(wa1)); }
                    xa[pp] += d; asm volatile("" : "+v"(xa[pp]));
                }
                fw[b][0][0] = ldstr2(lp(wa0) + pp * 4096, 1024);
            }
            if (j == 1) { fx[b][0] = lds128(lp(xa[pp])); fx[b][1] = lds128(lp(xa[pp]) + 4096); }
            if (j == 2) fw[b][0][1] = ldstr2(lp(wa0) + pp * 4096 + 16384, 1024);
            if (j == 3) fw[b][1][0] = ldstr2(lp(wa1) + pp * 4096, 1024);
            if (j == 4) fw[b][1][1] = ldstr2(lp(wa1) + pp * 4096 + 16384, 1024);
        };
        asm volatile("s_waitcnt vmcnt(0) lgkmcnt(0)\n\ts_barrier" : "+s"(epoch) :: "memory");
        const u64 tag = epoch ^ TAG_SALT;
        tag32 = (unsigned)tag;
        STAMP(1);
#pragma unroll
        for (int j = 0; j < 5; ++j) load_frag(0, j);
        f32x4 cs[2][2][4];
        unsigned tch[5];
        const unsigned tco = (unsigned)(gi * 4 + wq + (lane < 23 ? lane : 22) * 112) * 128u;
        static_for([&](auto gc) {
            constexpr int g = decltype(gc)::value, b = g & 1, pp = g & 3;
            SCHED();
            if (g == 11) STAMP(2);
            if (pp == 3) { STAMPG(2 + 2 * (g >> 2)); WAIT_BAR("lgkmcnt(0)"); STAMPG(3 + 2 * (g >> 2)); }
            if (g == 11) STAMP(3);
#pragma unroll
            for (int i = 0; i < 8; ++i) {
                const int ft = i >> 2, m = (i >> 1) & 1, sb = i & 1;
                acc[sb][ft][m] = mfma32(fw[b][ft][m], fx[b][sb], acc[sb][ft][m]);
                SCHED();
                if constexpr (TOUCH && pp == 1 && g >= 4 && g < 24) {
                    if (i == 6) {
                        constexpr int k = (g >> 2) - 1;
                        const unsigned o = k < 4 ? tco + k * (23u * 112u * 128u) : (lane < 20 ? tco + 4 * (23u * 112u * 128u) : tco);
                        u64 t;
                        asm volatile("s_mov_b64 %1, %3\n\tglobal_load_dword %0, %2, %1" : "=v"(tch[k]), "=&s"(t) : "v"(o), "s"(p.w2));
                    }
                }
                if (g < 27) { if (i < 5) load_frag(g + 1, i); }
                else cs[i >> 2][0][i & 3] = *(LAS f32x4*)(sm + L_CSUM + (64 * wn + 32 * (i >> 2) + 8 * (i & 3) + 4 * hh) * 4);
                SCHED();
            }
        }, std::make_integer_sequence<int, 28>{});
        STAMP(4);
        unsigned sa[2];
        sa[0] = *(LAS unsigned*)(sm + L_STAT + (64 * wm + r32) * 4);
        sa[1] = *(LAS unsigned*)(sm + L_STAT + (64 * wm + 32 + r32) * 4);
#pragma unroll
        for (int i = 0; i < 8; ++i) cs[i >> 2][1][i & 3] = *(LAS f32x4*)(sm + L_CSUM + 512 + (64 * wn + 32 * (i >> 2) + 8 * (i & 3) + 4 * hh) * 4);
#pragma unroll
        for (int sb = 0; sb < 2; ++sb) {
            const float ri = __builtin_amdgcn_rcpf(bfhi(sa[sb])) * -1.44269504088896341f, nm = -bflo(sa[sb]), d2 = bfhi(sa[sb]) * bfhi(sa[sb]);
            const f32x2 ri2 = {ri, ri}, nm2 = {nm, nm}, dd2 = {d2, d2};
#pragma unroll
            for (int ft = 0; ft < 2; ++ft) {
#pragma unroll
                for (int t2 = 0; t2 < 2; ++t2) {
                    f32x2 gu[4], ee[4];
#pragma unroll
                    for (int j = 0; j < 4; ++j) {
                        const int r = 8 * t2 + 2 * j;
                        const f32x2 g = __builtin_elementwise_fma(nm2, (f32x2){cs[ft][0][r >> 2][r & 3], cs[ft][0][r >> 2][(r & 3) + 1]}, (f32x2){acc[sb][ft][0][r], acc[sb][ft][0][r + 1]});
                        const f32x2 u = __builtin_elementwise_fma(nm2, (f32x2){cs[ft][1][r >> 2][r & 3], cs[ft][1][r >> 2][(r & 3) + 1]}, (f32x2){acc[sb][ft][1][r], acc[sb][ft][1][r + 1]});
                        const f32x2 t = g * ri2;
                        ee[j] = (f32x2){__builtin_amdgcn_exp2f(t[0]), __builtin_amdgcn_exp2f(t[1])};
                        gu[j] = g * u;
                    }
                    u32x4 st;
#pragma unroll
                    for (int j = 0; j < 4; ++j) {
                        const f32x2 dn = __builtin_elementwise_fma(ee[j], dd2, dd2);
                        const f32x2 hv = gu[j] * (f32x2){__builtin_amdgcn_rcpf(dn[0]), __builtin_amdgcn_rcpf(dn[1])};
                        st[j] = pk(hv[0], hv[1]);
                    }
                    const unsigned fi = (unsigned)((4 * mt + 2 * wm + sb) * 112 + 8 * nt + 4 * wn + 2 * ft + t2);
                    __builtin_amdgcn_raw_buffer_store_b128(st, wsr, WS_H + fi * 1024u + lane * 16u, 0, 16);
                }
            }
        }
        SCHED();
        __hip_atomic_store((LAS unsigned*)(sm + L_CTL + 32 + wq * 4), 1u, __ATOMIC_RELAXED, __HIP_MEMORY_SCOPE_WORKGROUP);
        STAMP(5);
        const int lw = fresh_lane();
        const int w2a0 = lb + w2i + (4 * (lw >> 5) + ((lw & 15) >> 2)) * 128 + (((2 * ((lw >> 4) & 1) + ((lw & 3) >> 1)) ^ (((lw >> 3) & 1) << 2)) * 16) + 8 * (lw & 1);
        const int w2a1 = w2a0 ^ 64;
        const u32x4 w2o = w2_off();
        WAIT_VM(0);
        if (TOUCH) asm volatile("" :: "v"(tch[0]), "v"(tch[1]), "v"(tch[2]), "v"(tch[3]), "v"(tch[4]));
        arrive_publish(sm + L_CTL, p.ws, WS_FH + mt * 128 + nt * 8, tag, lane);
        STAMP(6);
        u32x4 w2r[12][2];
        wait_lds(sm + L_CTL + 48 + wq * 4);
#pragma unroll
        for (int h = 0; h < 2; ++h) {
#pragma unroll
            for (int i = 0; i < 6; ++i) {
                w2r[6 * h + i][0] = ldstr2(lp(w2a0) + h * L_SLOT + i * 2048, 1024);
                w2r[6 * h + i][1] = ldstr2(lp(w2a1) + h * L_SLOT + i * 2048, 1024);
            }
            WAIT_LGKM0();
#pragma unroll
            for (int i = 0; i < 6; ++i) asm volatile("" : "+v"(w2r[6 * h + i][0]), "+v"(w2r[6 * h + i][1]));
#pragma unroll
            for (int i = 0; i < 3; ++i) pair_w2(w2o, 12 + 6 * h + 2 * i, w2i + h * L_SLOT + i * 4096);
        }
        STAMP(7);
        if (wave == 0) {
            wait_flags(p.ws, WS_FH + (m64 >> 1) * 128, 14, tag, lane);
            __hip_atomic_store((LAS unsigned*)(sm + L_CTL + 4), 1u, __ATOMIC_RELAXED, __HIP_MEMORY_SCOPE_WORKGROUP);
        } else { WAIT_VM(0); wait_lds(sm + L_CTL + 4); }
        STAMP(11);
        g2_setup();
        u32x4 hf[28][2];
        auto load_h = [&](int i, int j) {
            int t = i + rot2; t = t >= 28 ? t - 28 : t;
            hf[i][j] = __builtin_amdgcn_raw_buffer_load_b128(wsr, hb + j * (112u * 1024u), t * 1024, 16);
        };
        constexpr int HD = HDEPTH;
#pragma unroll
        for (int i = 0; i < HD; ++i) { load_h(i, 0); load_h(i, 1); }
        STAMP(14);
        f32x16 ac2[2][2];
#pragma unroll
        for (int i = 0; i < 4; ++i)
#pragma unroll
            for (int r = 0; r < 16; ++r) ac2[i >> 1][i & 1][r] = 0.f;
        u32x4 a0 = w2r[0][0], a1 = w2r[0][1];
        static_for([&](auto ic) {
            constexpr int i = decltype(ic)::value, n = i + 1;
            SCHED();
            if (i == 23) WAIT_VM(0);
            u32x4 b0, b1;
            if constexpr (n < 12) { b0 = w2r[n][0]; b1 = w2r[n][1]; }
            else if constexpr (n < 28) {
                constexpr int so = n < 24 ? ((n - 12) / 6) * L_SLOT + ((n - 12) % 6) * 2048 : (n - 24) * 2048;
                b0 = ldstr2(lp(w2a0) + so, 1024);
                b1 = ldstr2(lp(w2a1) + so, 1024);
            }
            SCHED();
            ac2[0][0] = mfma32(a0, hf[i][0], ac2[0][0]);
            SCHED();
            if constexpr (i + HD < 28) load_h(i + HD, 0);
            SCHED();
            ac2[1][0] = mfma32(a1, hf[i][0], ac2[1][0]);
            ac2[0][1] = mfma32(a0, hf[i][1], ac2[0][1]);
            SCHED();
            if constexpr (i + HD < 28) load_h(i + HD, 1);
            SCHED();
            ac2[1][1] = mfma32(a1, hf[i][1], ac2[1][1]);
            SCHED();
            if constexpr (i == 14 || i == 16) pair_w2(w2o, 24 + (i - 14), w2i + (i - 14) * 2048);
            if (i == 20) load_x();
            if (i == 23) load_y();
            a0 = b0; a1 = b1;
        }, std::make_integer_sequence<int, 28>{});
        SCHED();
        STAMP(15);
        WAIT_LGKM0();
#pragma unroll
        for (int v = 0; v < 8; ++v) {
            LAS char* sl = sm + w2i + (v < 6 ? v * 2048 : L_SLOT + (v - 6) * 2048) + lw * 16;
            const f32x16& t = ac2[v >> 2][(v >> 1) & 1];
            const int r0 = 8 * (v & 1);
            *(LAS f32x4*)sl = (f32x4){t[r0], t[r0 + 1], t[r0 + 2], t[r0 + 3]};
            *(LAS f32x4*)(sl + 1024) = (f32x4){t[r0 + 4], t[r0 + 5], t[r0 + 6], t[r0 + 7]};
        }
    } else {
        u64 epoch;
        asm volatile("s_load_dwordx2 %0, %1, 0x0" : "=s"(epoch) : "s"(p.ws) : "memory");
        const int kr0 = (gpair & 31) * 64, kc0 = (gpair >> 5) * 64;
        const int chn = wq >> 1, par = wq & 1, dch = 2 * gpair + chn;
        const int priv = 16384 + 16384 + wq * 4096 + 3072;
        u32x4 xvo;
#pragma unroll
        for (int j = 0; j < 4; ++j) xvo[j] = (unsigned)((8 * j + (lane >> 3)) * D + ((((lane & 7) ^ (lane >> 4)) ^ (4 * (j & 1))) * 8)) * 2u - 1024u * j;
        const u16* xb = sptr(p.x, (mt * 128 + wq * 32) * D);
        auto group_x = [&](int si) { glds16x4(sptr(xb, kst(si) * 64), xvo, lb + ring(si) + wq * 4096); };
        group_x(0);
        {
            const bool isk = lane >= 8 && lane < 14;
            u64 mb_ = vsel(lane == 15, p.dv, vsel(lane == 14, p.hk, vsel(isk, p.k, (u64)p.sk)));
            unsigned mo = (unsigned)((kc0 + wq * 16) * 8 + (lane & 7) * 16);
            mo = isk ? (unsigned)((kr0 ? kr0 - 3 + ((lane - 8) >> 1) : 0) * D + kc0 + wq * 16 + (lane & 1) * 8) * 2u : mo;
            mo = lane == 14 ? (unsigned)dch * 4096u : mo;
            mo = lane == 15 ? (unsigned)(dch & ~7) * 2u : mo;
            const u64 ma = mb_ + mo;
            asm volatile("s_mov_b32 m0, %1\n\ts_nop 0\n\tglobal_load_lds_dwordx4 %0, off" :: "v"(ma), "s"(lb + ring(2) + priv));
        }
        group_x(1); group_w(1, 0); group_w(1, 1);
        const int cbi = L_CONV + chn * C_CH;
        LAS char* cb = sm + cbi;
        const int c8 = lane & 7;
        int ka0[2], kam[2], zo;
        unsigned kvo[2], uso[2];
#pragma unroll
        for (int rd = 0; rd < 2; ++rd) {
            const int ck = 2 * wq + rd, rr = lane >> 3;
            const int bq = 2 * (lane >> 4) + (p4 >> 1), bm = bq ? bq - 1 : 0;
            ka0[rd] = lb + L_KV + (8 * bq + q4) * 128 + ((ck ^ (((q4 >> 1) & 1) | ((bq & 3) << 1))) * 16) + 8 * (p4 & 1);
            kam[rd] = lb + L_KV + (8 * bm + 4 + q4) * 128 + ((ck ^ (((q4 >> 1) & 1) | ((bm & 3) << 1))) * 16) + 8 * (p4 & 1);
            kvo[rd] = (unsigned)(rr * D + (((lane & 7) ^ (((rr >> 1) & 1) | ((ck & 3) << 1))) * 8)) * 2u;
            uso[rd] = WS_U + (unsigned)(kc0 + ck * 8 + rr) * 8192u + (unsigned)(1984 - kr0 + c8 * 8) * 2u;
            asm volatile("" : "+v"(ka0[rd]), "+v"(kam[rd]), "+v"(kvo[rd]), "+v"(uso[rd]));
        }
        int tsrc = ((7 - c8) * 8 + (lane >> 3)) * 4;
        asm volatile("" : "+v"(tsrc));
        const int cc = 2 * par + ((lane >> 2) & 1);
        zo = cbi + C_U + par * C_US + (248 + (lane < 36 ? lane : 35)) * 16;
        zo = lane < 8 ? cbi + cc * C_HS + (lane & 3) * 16 : zo;
        u32x4 w2o = w2_off();
        asm volatile("" : "+v"(zo), "+v"(w2o));
        asm volatile("s_waitcnt vmcnt(12) lgkmcnt(0)\n\ts_barrier" : "+s"(epoch) :: "memory");
        tag32 = (unsigned)(epoch ^ TAG_SALT);
        STAMP(1);
        u32x2 skv[2]; unsigned hal[2][3];
        LAS char* mp = sm + ring(2) + priv;
#pragma unroll
        for (int rd = 0; rd < 2; ++rd) {
            skv[rd] = *(LAS u32x2*)(mp + (rd * 8 + c8) * 8);
            asm volatile("" ::: "memory");
#pragma unroll
            for (int i = 0; i < 3; ++i) hal[rd][i] = *(LAS u16*)(mp + 128 + i * 32 + rd * 16 + c8 * 2);
        }
        u32x4 h0 = lds128(mp + 224);
        unsigned dbits = *(LAS u16*)(mp + 240 + (dch & 7) * 2);
        group_x(2); group_w(2, 0);
        WAIT_LGKM0();
        asm volatile("" : "+v"(skv[0]), "+v"(skv[1]), "+v"(hal[0][0]), "+v"(hal[0][1]), "+v"(hal[0][2]), "+v"(hal[1][0]), "+v"(hal[1][1]), "+v"(hal[1][2]), "+v"(h0), "+v"(dbits));
        group_w(2, 1);
        STAMPG(2); WAIT_BAR("vmcnt(12)"); STAMPG(3);
        group_x(3); group_w(3, 0); group_w(3, 1);
#pragma unroll
        for (int j = 0; j < 2; ++j) {
            glds16(sptr(p.k, (kr0 + (2 * wq + j) * 8) * D + kc0), kvo[j], lb + L_KV + (2 * wq + j) * 1024);
            glds16(sptr(p.v, (kr0 + (2 * wq + j) * 8) * D + kc0), kvo[j], lb + L_KV + 8192 + (2 * wq + j) * 1024);
        }
        STAMPG(4); WAIT_BAR("vmcnt(16)"); STAMPG(5);
        group_x(4); group_w(4, 0); group_w(4, 1);
        u32x4 bval;
        {
            const bool s1 = !(cc & 1), s2 = !(cc & 2);
            const unsigned a0 = s1 ? 0u : h0[0], a1 = s1 ? h0[0] : h0[1], a2 = s1 ? h0[1] : h0[2], a3 = s1 ? h0[2] : h0[3];
            const unsigned lm = (lane == 3 || lane == 7) ? 0xffffffffu : 0u;
            bval = (u32x4){(s2 ? 0u : a0) & lm, (s2 ? 0u : a1) & lm, (s2 ? a0 : a2) & lm, (s2 ? a1 : a3) & lm};
            asm volatile("" : "+v"(bval));
            const unsigned hm = kr0 ? 0xffffu : 0u;
#pragma unroll
            for (int i = 0; i < 6; ++i) { hal[i / 3][i % 3] = (hal[i / 3][i % 3] & hm) << 16; asm volatile("" : "+v"(hal[i / 3][i % 3])); }
        }
        STAMP(2);
        STAMPG(6); WAIT_BAR("vmcnt(12)"); STAMPG(7);
        STAMP(3);
        u32x2 kt[2][3], vt[2][2];
#pragma unroll
        for (int rd = 0; rd < 2; ++rd) {
            kt[rd][0] = ldstr(lp(kam[rd])); kt[rd][1] = ldstr(lp(ka0[rd])); kt[rd][2] = ldstr(lp(ka0[rd]) + 512);
            vt[rd][0] = ldstr(lp(ka0[rd]) + 8192); vt[rd][1] = ldstr(lp(ka0[rd]) + 8192 + 512);
        }
        unsigned po[2][2];
        auto prep = [&](int rd, int half) {
            SCHED();
            const float c0 = bflo1(skv[rd][0]), c1 = bfhi1(skv[rd][0]), c2 = bflo1(skv[rd][1]), c3 = bfhi1(skv[rd][1]);
            const u32x2 ka = kt[rd][half], kb = kt[rd][half + 1], vv = vt[rd][half];
            float kk[7] = {bfhi1(ka[0]), bflo1(ka[1]), bfhi1(ka[1]), bflo1(kb[0]), bfhi1(kb[0]), bflo1(kb[1]), bfhi1(kb[1])};
            if (half == 0) {
#pragma unroll
                for (int i = 0; i < 3; ++i) kk[i] = lane < 8 ? __uint_as_float(hal[rd][i]) : kk[i];
            }
            const float v4[4] = {bflo1(vv[0]), bfhi1(vv[0]), bflo1(vv[1]), bfhi1(vv[1])};
            float o[4];
#pragma unroll
            for (int i = 0; i < 4; ++i) {
                o[i] = fmaf(kk[i + 3], c0, fmaf(kk[i + 2], c1, fmaf(kk[i + 1], c2, kk[i] * c3))) * v4[i];
                asm volatile("" : "+v"(o[i]));
            }
            po[half][0] = __builtin_amdgcn_ds_bpermute(tsrc, pk(o[3], o[2]));
            po[half][1] = __builtin_amdgcn_ds_bpermute(tsrc, pk(o[1], o[0]));
            SCHED();
        };
        auto prep_store = [&](int rd) {
            __builtin_amdgcn_raw_buffer_store_b128((u32x4){vmov1(tag32), po[1][0], vmov1(tag32), po[1][1]}, wsr, uso[rd], 0, 16);
            __builtin_amdgcn_raw_buffer_store_b128((u32x4){vmov1(tag32), po[0][0], vmov1(tag32), po[0][1]}, wsr, uso[rd], 4096, 16);
        };
        group_x(5); prep(0, 0); group_w(5, 0); prep(0, 1); group_w(5, 1); prep_store(0);
        STAMPG(8); WAIT_BAR("vmcnt(12) lgkmcnt(0)"); STAMPG(9);
        group_x(6); prep(1, 0); group_w(6, 0); prep(1, 1); group_w(6, 1); prep_store(1);
        STAMPG(10); WAIT_BAR("vmcnt(12)"); STAMPG(11);
        const unsigned fo = lane * 16u;
        const unsigned ao = wq < 2 ? WS_CSUM + (unsigned)(nt * 2 + wq) * 1024u : WS_STATS + mt * 1024u;
        glds16c(p.ws + ao, fo, lb + L_RAW + wq * 1024);
#pragma unroll
        for (int i = 0; i < 3; ++i) pair_w2(w2o, 2 * i, w2i + i * 4096);
        if (lane < 16) *(LAS unsigned*)(sm + L_CTL + lane * 4) = 0u;
        STAMPG(12); WAIT_BAR("vmcnt(12) lgkmcnt(0)"); STAMPG(13);
        u32x4 av = lds128(sm + L_RAW + wq * 1024 + lane * 16);
#pragma unroll 1
        for (unsigned spins = 0; spins < SPIN_CAP; ++spins) {
            if (__all(((av[0] ^ tag32) | (av[2] ^ tag32)) == 0u)) break;
            if (spins) __builtin_amdgcn_s_sleep(4);
            asm volatile("" ::: "memory");
            av = __builtin_amdgcn_raw_buffer_load_b128(wsr, ao + lane * 16u, 0, 16);
            asm volatile("" : "+v"(av));
        }
        *(LAS u32x2*)(sm + L_CSUM + (wq < 2 ? wq : 2) * 512 + lane * 8) = (u32x2){av[1], av[3]};
#pragma unroll
        for (int i = 0; i < 3; ++i) pair_w2(w2o, 6 + 2 * i, w2i + L_SLOT + i * 4096);
        STAMP(4);
        STAMPG(14); WAIT_BAR("lgkmcnt(0)"); STAMPG(15);
        STAMP(5);
        __builtin_amdgcn_s_setprio(2);
        const unsigned ubase = WS_U + (unsigned)dch * 8192u;
        glds16x2c(p.ws + ubase + par * 2048, fo, lb + cbi + C_U + par * 2048);
        glds16x2c(p.ws + ubase + 4096 + par * 2048, fo, lb + cbi + C_U + C_US + par * 2048);
        glds4c(p.ws + ubase + (1 - par) * 2048, (lane & 1) * 4u, lb + L_UNX + wq * 256);
        {
            const u32x4 fvo = {fo, fo, fo, lane == 63 ? 62u * 16u : fo};
#pragma unroll
            for (int c = 0; c < 2; ++c) glds16x4(sptr(p.hk, dch * S + 4 * par + 2 * c + 2), fvo, lb + cbi + (2 * par + c) * C_HS + 64);
        }
        *(LAS u32x4*)(sm + zo) = bval;
        WAIT_VM(8);
        __hip_atomic_store((LAS unsigned*)(sm + L_CTL + 48 + wq * 4), 1u, __ATOMIC_RELAXED, __HIP_MEMORY_SCOPE_WORKGROUP);
        {
            u32x4 ua[2], ub[2]; u32x2 un[2];
#pragma unroll
            for (int rd = 0; rd < 2; ++rd) {
                const int x8 = 128 * par + 64 * rd + lane;
                ua[rd] = lds128(cb + C_U + x8 * 16); ub[rd] = lds128(cb + C_U + C_US + x8 * 16);
                un[rd] = *(LAS u32x2*)((rd == 1 && lane == 63) ? sm + L_UNX + wq * 256 : cb + C_U + x8 * 16 + 16);
            }
#pragma unroll 1
            for (unsigned spins = 0; spins < SPIN_CAP; ++spins) {
                unsigned df = 0;
#pragma unroll
                for (int rd = 0; rd < 2; ++rd) df |= (ua[rd][0] ^ tag32) | (ua[rd][2] ^ tag32) | (ub[rd][0] ^ tag32) | (ub[rd][2] ^ tag32) | (un[rd][0] ^ tag32);
                if (__all(df == 0u)) break;
                __builtin_amdgcn_s_sleep(4);
                asm volatile("" ::: "memory");
#pragma unroll
                for (int rd = 0; rd < 2; ++rd) {
                    const int x8 = 128 * par + 64 * rd + lane;
                    const unsigned uo = ubase + (unsigned)x8 * 16u;
                    ua[rd] = __builtin_amdgcn_raw_buffer_load_b128(wsr, uo, 0, 16);
                    ub[rd] = __builtin_amdgcn_raw_buffer_load_b128(wsr, uo + 4096u, 0, 16);
                    un[rd] = __builtin_amdgcn_raw_buffer_load_b64(wsr, x8 == 255 ? uo : uo + 16u, 0, 16);
                }
                asm volatile("" : "+v"(ua[0]), "+v"(ub[0]), "+v"(un[0]), "+v"(ua[1]), "+v"(ub[1]), "+v"(un[1]));
            }
#pragma unroll
            for (int rd = 0; rd < 2; ++rd) {
                const int x8 = 128 * par + 64 * rd + lane;
                const unsigned nx = x8 == 255 ? 0u : un[rd][1];
                *(LAS u32x4*)(cb + C_U + x8 * 16) = (u32x4){__builtin_amdgcn_alignbit(ua[rd][3], ua[rd][1], 16), __builtin_amdgcn_alignbit(ub[rd][1], ua[rd][3], 16),
                                                            __builtin_amdgcn_alignbit(ub[rd][3], ub[rd][1], 16), __builtin_amdgcn_alignbit(nx, ub[rd][3], 16)};
                *(LAS u32x4*)(cb + C_U + C_US + x8 * 16) = (u32x4){ua[rd][1], ua[rd][3], ub[rd][1], ub[rd][3]};
            }
        }
        asm volatile("s_waitcnt vmcnt(0) lgkmcnt(0)" ::: "memory");
        __hip_atomic_store((LAS unsigned*)(sm + L_CTL + 16 + wq * 4), 1u, __ATOMIC_RELAXED, __HIP_MEMORY_SCOPE_WORKGROUP);
        wait_lds(sm + L_CTL + 16 + (wq ^ 1) * 4);
        STAMP(7);
        wait_lds(sm + L_CTL + 32 + wq * 4);
        STAMP(6);
        const int n16 = lane & 15, kg = lane >> 4;
        f32x4 ca[4];
#pragma unroll
        for (int j = 0; j < 4; ++j) ca[j] = (f32x4){0.f, 0.f, 0.f, 0.f};
        LAS char* ap = cb + (n16 & 3) * C_HS + ((n16 >> 2) + kg) * 16;
        LAS char* bp = cb + C_U + (n16 & 1) * C_US + (252 - 4 * (n16 >> 1) + kg) * 16 - 7 * 512;
        auto conv_res = [&](auto pc_, int r0, int r1) {
            constexpr int P = decltype(pc_)::value;
            constexpr int T[4] = {P, 3 - P, 4 + P, 7 - P};
#pragma unroll
            for (int r = r0; r < r1; ++r) {
                u32x4 fa[8], fb[8];
#pragma unroll
                for (int i = 0; i <= T[3]; ++i) {
                    fa[i] = lds128(ap + r * 64 + i * 512);
                    fb[i] = lds128(bp + r * 64 + (7 - i) * 512);
                }
#pragma unroll
                for (int j = 0; j < 4; ++j)
#pragma unroll
                    for (int pp = 0; pp <= T[j]; ++pp) ca[j] = mfma16(fa[pp], fb[T[j] - pp], ca[j]);
            }
        };
        if (par) conv_res(std::integral_constant<int, 1>{}, 0, 8); else conv_res(std::integral_constant<int, 0>{}, 0, 8);
        SCHED();
        STAMP(9);
        {
            const float dval = bflo(dbits);
            const int ly = fresh_lane();
            const bool odd = ly & 1;
            const int sl0 = 32 * ((ly & 15) >> 1) + 8 * (ly >> 4) + 4 * (ly & 1);
#pragma unroll
            for (int j = 0; j < 4; ++j) {
                const int tj = (j == 0 ? 0 : j == 1 ? 3 : j == 2 ? 4 : 7) ^ par;
                const u32x2 kvr = *(LAS u32x2*)(cb + C_U + C_US + (2044 - 256 * tj - sl0) * 2);
                const float r0 = __shfl_xor(odd ? ca[j][0] : ca[j][2], 1), r1 = __shfl_xor(odd ? ca[j][1] : ca[j][3], 1);
                const unsigned ya = pk(odd ? r0 : ca[j][0], odd ? ca[j][2] : r0), yb = pk(odd ? r1 : ca[j][1], odd ? ca[j][3] : r1);
                const unsigned ka = pk(mul1(bfhi1(kvr[1]), dval), mul1(bflo1(kvr[1]), dval)), kb = pk(mul1(bfhi1(kvr[0]), dval), mul1(bflo1(kvr[0]), dval));
                __builtin_amdgcn_raw_buffer_store_b128((u32x4){vmov1(tag32), pk(add1(bflo1(ya), bflo1(ka)), add1(bfhi1(ya), bfhi1(ka))),
                                                               vmov1(tag32), pk(add1(bflo1(yb), bflo1(kb)), add1(bfhi1(yb), bfhi1(kb)))},
                    wsr, WS_Y + (unsigned)dch * 8192u + (unsigned)(256 * tj + sl0) * 4u, 0, 16);
            }
        }
        STAMP(10);
        __builtin_amdgcn_s_setprio(0);
        load_x();
        wait_lds(sm + L_CTL + 4);
        __builtin_amdgcn_s_sleep(40);
        load_y();
        STAMP(15);
    }
    WAIT_BAR("lgkmcnt(0)");
    STAMP(16);
    f32x4 o0 = {0.f, 0.f, 0.f, 0.f}, o1 = {0.f, 0.f, 0.f, 0.f};
    {
        const int lz = fresh_lane();
        LAS char* rm = sm + L_W2 + wave * 2048 + (wave >= 6 ? L_SLOT - 12288 : 0) + (lz >> 5) * 1024 + (lz & 31) * 16;
#pragma unroll
        for (int w = 0; w < 4; ++w) {
            o0 += *(LAS f32x4*)(rm + w * 12288);
            o1 += *(LAS f32x4*)(rm + w * 12288 + 512);
        }
    }
#pragma unroll 1
    for (unsigned spins = 0; spins < SPIN_CAP; ++spins) {
        unsigned df = stv[0] ^ tag32;
#pragma unroll
        for (int i = 0; i < 8; ++i) df |= yv[i][0] ^ tag32;
        if (__all(df == 0u)) break;
        __builtin_amdgcn_s_sleep(4);
        asm volatile("" ::: "memory");
        load_y();
    }
    {
        const float mb = bflo(stv[1]), ri = __builtin_amdgcn_rcpf(bfhi(stv[1]));
        const unsigned ysh = (srow & 1) ? 0u : 16u;
        u32x4 st;
#pragma unroll
        for (int i = 0; i < 4; ++i) {
            const unsigned op = i < 2 ? pk(o0[2 * i], o0[2 * i + 1]) : pk(o1[2 * i - 4], o1[2 * i - 3]);
            const unsigned gp = pk(silu_f(bflo(op)), silu_f(bfhi(op)));
            const float xa0 = bflo(xv[i]), xa1 = bfhi(xv[i]);
            const unsigned tp = pk(xa0 - mb, xa1 - mb);
            const unsigned np = pk(bflo(tp) * ri, bfhi(tp) * ri);
            const unsigned sp = pk(bflo(np) * bfhi(yv[2 * i][1] << ysh), bfhi(np) * bfhi(yv[2 * i + 1][1] << ysh));
            const unsigned mp = pk(bflo(sp) * bflo(gp), bfhi(sp) * bfhi(gp));
            st[i] = pk(xa0 + bflo(mp), xa1 + bfhi(mp));
        }
        *(u32x4*)(p.out + (size_t)srow * D + dd) = st;
    }
    STAMP(17);
    STAMP_OUT(18);
}

extern "C" void kernel_launch(void* const* d_in, const int* in_sizes, int n_in,
                              void* d_out, int out_size, void* d_ws, size_t ws_size,
                              hipStream_t stream) {
    Params p;
    p.x = (const u16*)d_in[0]; p.k = (const u16*)d_in[1]; p.v = (const u16*)d_in[2];
    p.sk = (const u16*)d_in[3]; p.hk = (const u16*)d_in[4]; p.dv = (const u16*)d_in[5];
    p.w1 = (const u16*)d_in[7]; p.w3 = (const u16*)d_in[8]; p.w2 = (const u16*)d_in[9];
    p.out = (u16*)d_out; p.ws = (char*)d_ws;
    (void)hipFuncSetAttribute((const void*)fused_kernel, hipFuncAttributeMaxDynamicSharedMemorySize, L_TOTAL);
    fused_kernel<<<256, 512, L_TOTAL, stream>>>(p);
}
```

```cpp
#include <hip/hip_runtime.h>
#include <stdint.h>
#include <utility>

typedef unsigned short u16;
typedef unsigned long long u64;
typedef short s16x4 __attribute__((ext_vector_type(4)));
typedef short s16x8 __attribute__((ext_vector_type(8)));
typedef __bf16 bf16x2 __attribute__((ext_vector_type(2)));
typedef __bf16 bf16x8 __attribute__((ext_vector_type(8)));
typedef float f32x2 __attribute__((ext_vector_type(2)));
typedef float f32x4 __attribute__((ext_vector_type(4)));
typedef float f32x16 __attribute__((ext_vector_type(16)));
typedef unsigned u32x2 __attribute__((ext_vector_type(2)));
typedef unsigned u32x4 __attribute__((ext_vector_type(4)));
typedef unsigned u32x16 __attribute__((ext_vector_type(16)));
#define LAS __attribute__((address_space(3)))
#define GAS __attribute__((address_space(1)))
#define RLX_AGENT __ATOMIC_RELAXED, __HIP_MEMORY_SCOPE_AGENT

constexpr int S = 2048, D = 448, F = 1792;

constexpr unsigned WS_EPOCH = 0;
constexpr unsigned WS_FH = 1024;
constexpr unsigned WS_STATS = 8192;
constexpr unsigned WS_CSUM = 24576;
constexpr unsigned WS_U = 65536;
constexpr unsigned WS_Y = WS_U + D * S * 4;
constexpr unsigned WS_H = WS_Y + D * S * 4;
constexpr unsigned WS_END = WS_H + 64 * 112 * 1024;
constexpr unsigned WS_DBG = WS_END;

constexpr int L_KV = 0;
constexpr int L_CTL = 0;
constexpr int L_CSUM = 256;
constexpr int L_STAT = 1280;
constexpr int L_KVO = 8192;
constexpr int L_UNX = 6144;
constexpr int L_SLOT = 49152;
constexpr int L_CONV = 12288;
constexpr int C_CH = 26112, C_HS = 4160, C_U = 16640, C_US = 4640;
constexpr int L_W2 = 16384 + L_SLOT;
constexpr int L_TOTAL = 163840;
__host__ __device__ constexpr int ring(int stage) { return 16384 + (stage % 3) * L_SLOT; }
constexpr unsigned CODE_PREFETCH = 24576;
#define POLL_SLEEP 4
#define HDEPTH 6
#define ROT2 (8 * ml)
#define MPRIO 1
#define HPF 0
#define YSLEEP 16
#define TOUCH 0
#define TOUCH0 4
#define HGROW 4
#define ROT1 0

constexpr u64 TAG_SALT = 0x9E3779B97F4A7C15ull;
constexpr unsigned SPIN_CAP = 1u << 18;

struct Params {
    const u16* x; const u16* k; const u16* v; const u16* sk; const u16* hk; const u16* dv;
    const u16* w1; const u16* w3; const u16* w2; u16* out; char* ws;
};

__device__ __forceinline__ float bflo(unsigned p) { return __uint_as_float(p << 16); }
__device__ __forceinline__ float bfhi(unsigned p) { return __uint_as_float(p & 0xffff0000u); }
__device__ __forceinline__ float bflo1(unsigned p) { float r; asm("v_lshlrev_b32 %0, 16, %1" : "=v"(r) : "v"(p)); return r; }
__device__ __forceinline__ float bfhi1(unsigned p) { float r; asm("v_and_b32 %0, 0xffff0000, %1" : "=v"(r) : "v"(p)); return r; }
__device__ __forceinline__ float mul1(float a, float b) { float r; asm("v_mul_f32 %0, %1, %2" : "=v"(r) : "v"(a), "v"(b)); return r; }
__device__ __forceinline__ void fmac1(float& d, float a, float b) { asm("v_fmac_f32 %0, %1, %2" : "+v"(d) : "v"(a), "v"(b)); }
__device__ __forceinline__ float dpp_xor1(float v) {
    return __int_as_float(__builtin_amdgcn_update_dpp(0, __float_as_int(v), 0xB1, 0xf, 0xf, true));
}
__device__ __forceinline__ float add1(float a, float b) { float r; asm("v_add_f32 %0, %1, %2" : "=v"(r) : "v"(a), "v"(b)); return r; }
__device__ __forceinline__ unsigned vmov1(unsigned s) { unsigned r; asm volatile("v_mov_b32 %0, %1" : "=v"(r) : "s"(s)); return r; }
__device__ __forceinline__ unsigned pk(float a, float b) {
    f32x2 f = {a, b};
    return __builtin_bit_cast(unsigned, __builtin_convertvector(f, bf16x2));
}
__device__ __forceinline__ float rbf(float a) { return bflo(pk(a, a)); }
__device__ __forceinline__ float dot2(unsigned a, unsigned b, float c) {
    return __builtin_amdgcn_fdot2_f32_bf16(__builtin_bit_cast(bf16x2, a), __builtin_bit_cast(bf16x2, b), c, false);
}
__device__ __forceinline__ void glds16(const void* base, unsigned voff, unsigned ldst) {
    u64 t;
    asm volatile("s_mov_b32 m0, %3\n\ts_mov_b64 %0, %2\n\tglobal_load_lds_dwordx4 %1, %0" : "=&s"(t) : "v"(voff), "s"(base), "s"(ldst));
}
__device__ __forceinline__ void glds16x4(const void* base, u32x4 vo, unsigned ldst) {
    u64 t;
    asm volatile("s_mov_b32 m0, %6\n\ts_mov_b64 %0, %5\n\tglobal_load_lds_dwordx4 %1, %0\n\tglobal_load_lds_dwordx4 %2, %0 offset:1024\n\t"
                 "global_load_lds_dwordx4 %3, %0 offset:2048\n\tglobal_load_lds_dwordx4 %4, %0 offset:3072"
                 : "=&s"(t) : "v"(vo[0]), "v"(vo[1]), "v"(vo[2]), "v"(vo[3]), "s"(base), "s"(ldst));
}
__device__ __forceinline__ void glds16x4c(const void* base, u32x4 vo, unsigned ldst) {
    u64 t;
    asm volatile("s_mov_b32 m0, %6\n\ts_mov_b64 %0, %5\n\tglobal_load_lds_dwordx4 %1, %0 sc1\n\tglobal_load_lds_dwordx4 %2, %0 offset:1024 sc1\n\t"
                 "global_load_lds_dwordx4 %3, %0 offset:2048 sc1\n\tglobal_load_lds_dwordx4 %4, %0 offset:3072 sc1"
                 : "=&s"(t) : "v"(vo[0]), "v"(vo[1]), "v"(vo[2]), "v"(vo[3]), "s"(base), "s"(ldst) : "memory");
}
__device__ __forceinline__ void glds16x2c(const void* base, unsigned voff, unsigned ldst) {
    u64 t;
    asm volatile("s_mov_b32 m0, %3\n\ts_mov_b64 %0, %2\n\tglobal_load_lds_dwordx4 %1, %0 sc1\n\tglobal_load_lds_dwordx4 %1, %0 offset:1024 sc1"
                 : "=&s"(t) : "v"(voff), "s"(base), "s"(ldst));
}
__device__ __forceinline__ void glds16c(const void* base, unsigned voff, unsigned ldst) {
    u64 t;
    asm volatile("s_mov_b32 m0, %3\n\ts_mov_b64 %0, %2\n\tglobal_load_lds_dwordx4 %1, %0 sc1" : "=&s"(t) : "v"(voff), "s"(base), "s"(ldst));
}
__device__ __forceinline__ void glds4c(const void* base, unsigned voff, unsigned ldst) {
    u64 t;
    asm volatile("s_mov_b32 m0, %3\n\ts_mov_b64 %0, %2\n\tglobal_load_lds_dword %1, %0 sc1" : "=&s"(t) : "v"(voff), "s"(base), "s"(ldst));
}
__device__ __forceinline__ const u16* sptr(const u16* base, int off) {
    const u16* r = base + __builtin_amdgcn_readfirstlane(off);
    asm("" : "+s"(r));
    return r;
}
__device__ __forceinline__ u64 vsel(bool c, const void* a, u64 b) {
    u64 va = (u64)a;
    asm("" : "+v"(va), "+v"(b));
    return c ? va : b;
}
__device__ __forceinline__ LAS char* lp(int a) { return (LAS char*)(size_t)(unsigned)a; }
__device__ __forceinline__ u32x4 lds128(LAS char* p) { return *(LAS u32x4*)p; }
__device__ __forceinline__ u32x2 ldstr(LAS char* p) {
    return __builtin_bit_cast(u32x2, __builtin_amdgcn_ds_read_tr16_b64_v4i16((LAS s16x4*)p));
}
__device__ __forceinline__ u32x4 ldstr2(LAS char* p, int off) {
    u32x2 a = ldstr(p), b = ldstr(p + off);
    return (u32x4){a.x, a.y, b.x, b.y};
}
__device__ __forceinline__ f32x16 mfma32(u32x4 a, u32x4 b, f32x16 c) {
    return __builtin_amdgcn_mfma_f32_32x32x16_bf16(__builtin_bit_cast(bf16x8, a), __builtin_bit_cast(bf16x8, b), c, 0, 0, 0);
}
__device__ __forceinline__ f32x4 mfma16(u32x4 a, u32x4 b, f32x4 c) {
    return __builtin_amdgcn_mfma_f32_16x16x32_bf16(__builtin_bit_cast(bf16x8, a), __builtin_bit_cast(bf16x8, b), c, 0, 0, 0);
}
__device__ __forceinline__ float silu_f(float g) {
    return g * __builtin_amdgcn_rcpf(1.f + __builtin_amdgcn_exp2f(g * -1.44269504088896341f));
}
template <class Fn, int... I>
__device__ __forceinline__ void static_for(Fn&& fn, std::integer_sequence<int, I...>) { (fn(std::integral_constant<int, I>{}), ...); }
#define WAIT_VM(n) asm volatile("s_waitcnt vmcnt(" #n ")" ::: "memory")
#define WAIT_LGKM0() asm volatile("s_waitcnt lgkmcnt(0)" ::: "memory")
#define WAIT_BAR(w) asm volatile("s_waitcnt " w "\n\ts_barrier" ::: "memory")
#define SCHED() __builtin_amdgcn_sched_barrier(0)
#define CAS __attribute__((address_space(4)))
#define NSTAMP 18

__device__ __forceinline__ void wait_flags(char* ws, unsigned off, int n, u64 tag, int lane) {
    GAS u64* p = (GAS u64*)(ws + off) + (lane < n ? lane : 0);
#pragma unroll 1
    for (unsigned spins = 0; spins < SPIN_CAP; ++spins) {
        u64 val = __hip_atomic_load(p, RLX_AGENT);
        if (__all(val == tag)) break;
        __builtin_amdgcn_s_sleep(POLL_SLEEP);
    }
    __builtin_amdgcn_fence(__ATOMIC_ACQUIRE, "wavefront");
}

__device__ __forceinline__ void wait_lds(LAS char* word) {
#pragma unroll 1
    for (unsigned spins = 0; spins < (SPIN_CAP << 4); ++spins) {
        if (__builtin_amdgcn_readfirstlane(__hip_atomic_load((LAS unsigned*)word, __ATOMIC_RELAXED, __HIP_MEMORY_SCOPE_WORKGROUP))) break;
        __builtin_amdgcn_s_sleep(1);
    }
    asm volatile("" ::: "memory");
}
__device__ __forceinline__ void wait_lds_eq(LAS char* word, unsigned want) {
#pragma unroll 1
    for (unsigned spins = 0; spins < (1u << 22); ++spins) {
        if ((unsigned)__builtin_amdgcn_readfirstlane(__hip_atomic_load((LAS unsigned*)word, __ATOMIC_RELAXED, __HIP_MEMORY_SCOPE_WORKGROUP)) == want) break;
        __builtin_amdgcn_s_sleep(1);
    }
    asm volatile("" ::: "memory");
}
__device__ __forceinline__ void arrive_publish(LAS char* ctr, char* ws, unsigned off, u64 tag, int lane) {
    if (lane == 0) {
        const unsigned old = __hip_atomic_fetch_add((LAS unsigned*)ctr, 1u, __ATOMIC_RELAXED, __HIP_MEMORY_SCOPE_WORKGROUP);
        if (old == 3u) __hip_atomic_store((GAS u64*)(ws + off), tag, RLX_AGENT);
    }
}

#define STAMP(i)
#define STAMPG(i)
#define STAMPA(i)
#define STAMP_DECL()
#define STAMP_OUT(n)

__global__ __launch_bounds__(512, 2) void fused_kernel(Params p) {
    extern __shared__ __attribute__((aligned(16))) char smem_raw[];
    u64 pc;
    asm volatile("s_getpc_b64 %0" : "=s"(pc));
    LAS char* sm = (LAS char*)smem_raw;
    const int tid = threadIdx.x, lane = tid & 63;
    const int wave = __builtin_amdgcn_readfirstlane(tid >> 6);
    const int xcd = blockIdx.x & 7, idx = blockIdx.x >> 3;
    const __amdgpu_buffer_rsrc_t wsr = __builtin_amdgcn_make_buffer_rsrc(p.ws, 0, WS_END, 0x00020000);
    asm volatile("" :: "s"(p.x), "s"(p.k), "s"(p.v), "s"(p.sk), "s"(p.hk), "s"(p.dv), "s"(p.w1), "s"(p.w3), "s"(p.w2), "s"(p.out), "s"(p.ws));
    STAMP_DECL();
    STAMP(0);

    if (idx < 4) {
        unsigned pf = 0;
        if (idx == 0 && wave == 7) {
            const GAS char* code = (const GAS char*)(pc & ~(u64)255);
#pragma unroll
            for (unsigned i = 0; i < CODE_PREFETCH / 8192; ++i) pf ^= *(const GAS unsigned*)(code + i * 8192 + lane * 128);
        }
        const int ca = (xcd >> 1) * 4 + idx, cmat = ca >= 7, cnt = 7 * (xcd & 1) + (ca >= 7 ? ca - 7 : ca);
        const bool job = ca < 14;
        u32x4 xr[7], wr[14];
        const int row = (8 * (xcd >> 1) + 4 * (xcd & 1) + idx) * 64 + wave * 8 + (lane >> 3);
#pragma unroll
        for (int i = 0; i < 7; ++i) xr[i] = *(const u32x4*)(p.x + (size_t)row * D + i * 64 + (lane & 7) * 8);
        if (job) {
            asm volatile("s_barrier" ::: "memory");
            const u16* wp = (cmat ? p.w3 : p.w1) + (size_t)(wave * 4 + (lane >> 4)) * F + cnt * 128 + (lane & 15) * 8;
#pragma unroll
            for (int i = 0; i < 14; ++i) wr[i] = *(const u32x4*)(wp + (size_t)(32 * i) * F);
        }
        const unsigned pf2 = *(const unsigned*)((const char*)p.hk + (size_t)((xcd * 4 + idx) * 448 + wave * 56 + (lane < 56 ? lane : 0)) * 128);
        u64 epoch;
        asm volatile("s_load_dwordx2 %0, %1, 0x0\n\ts_waitcnt lgkmcnt(0)" : "=s"(epoch) : "s"(p.ws) : "memory");
        const u64 tag = epoch ^ TAG_SALT;
        const unsigned tag32 = (unsigned)tag;
        {
            float sum = 0.f;
#pragma unroll
            for (int i = 0; i < 7; ++i)
#pragma unroll
                for (int j = 0; j < 4; ++j) sum = dot2(xr[i][j], 0x3f803f80u, sum);
            sum += __shfl_xor(sum, 1); sum += __shfl_xor(sum, 2); sum += __shfl_xor(sum, 4);
            const float mean = sum * (1.f / D);
            float sq = 0.f;
#pragma unroll
            for (int i = 0; i < 7; ++i)
#pragma unroll
                for (int j = 0; j < 4; ++j) {
                    const float a = bflo(xr[i][j]) - mean, b = bfhi(xr[i][j]) - mean;
                    sq = fmaf(a, a, sq); sq = fmaf(b, b, sq);
                }
            sq += __shfl_xor(sq, 1); sq += __shfl_xor(sq, 2); sq += __shfl_xor(sq, 4);
            const u32x2 st = {tag32, pk(mean, __builtin_amdgcn_sqrtf(rbf(rbf(sq * (1.f / D)) + 1e-5f)))};
            __builtin_amdgcn_raw_buffer_store_b64(st, wsr, (lane & 7) == 0 ? WS_STATS + (unsigned)row * 8u : 0xfffffff0u, 0, 16);
        }
        STAMPA(2);
        if (job) {
            float c[8];
#pragma unroll
            for (int j = 0; j < 8; ++j) c[j] = 0.f;
#pragma unroll
            for (int i = 0; i < 14; ++i)
#pragma unroll
                for (int j = 0; j < 4; ++j) { c[2 * j] += bflo(wr[i][j]); c[2 * j + 1] += bfhi(wr[i][j]); }
            LAS char* cp = sm + ((wave * 4 + (lane >> 4)) * 136 + (lane & 15) * 8) * 4;
            *(LAS f32x4*)cp = (f32x4){c[0], c[1], c[2], c[3]};
            *(LAS f32x4*)(cp + 16) = (f32x4){c[4], c[5], c[6], c[7]};
        }
        STAMPA(3);
        if (job) {
            WAIT_BAR("lgkmcnt(0)");
            float t = 0.f;
#pragma unroll
            for (int i = 0; i < 8; ++i) t += *(LAS float*)(sm + ((i * 4 + (tid & 3)) * 136 + (tid >> 2)) * 4);
            t += __shfl_xor(t, 1); t += __shfl_xor(t, 2);
            __builtin_amdgcn_raw_buffer_store_b64((u32x2){tag32, __float_as_uint(t)}, wsr,
                (tid & 3) == 0 ? WS_CSUM + (unsigned)((cnt * 2 + cmat) * 128 + (tid >> 2)) * 8u : 0xfffffff0u, 0, 16);
        }
        STAMP(1);
        asm volatile("" :: "v"(pf), "v"(pf2));
        STAMP_OUT(4);
        if (blockIdx.x == 0 && wave == 0) {
#pragma unroll 1
            for (int m = 0; m < 4; ++m) {
                GAS u64* gp = (GAS u64*)(p.ws + WS_STATS) + (m * 64 + lane) * 8;
#pragma unroll 1
                for (unsigned spins = 0; spins < SPIN_CAP; ++spins) {
                    if (__all((unsigned)__hip_atomic_load(gp, RLX_AGENT) == tag32)) break;
                    __builtin_amdgcn_s_sleep(16);
                }
            }
            __builtin_amdgcn_s_sleep(100);
#pragma unroll 1
            for (int m = 0; m < 16; ++m) wait_flags(p.ws, WS_FH + m * 128, 14, tag, lane);
            if (lane == 0) __hip_atomic_store((GAS u64*)(p.ws + WS_EPOCH), epoch + 1, RLX_AGENT);
        }
        return;
    }

    const int gi = idx - 4, ml = gi & 3, nl = gi >> 2;
    const int mt = 4 * (xcd >> 1) + ml, nt = 7 * (xcd & 1) + nl;
    const int rot1 = ROT1;
    const int m64 = 4 * xcd + ml, n64 = nl;
    const int gpair = xcd * 28 + gi;
    const int r32 = lane & 31, hh = lane >> 5, blk = (lane >> 4) & 1, q4 = (lane & 15) >> 2, p4 = lane & 3;
    const unsigned lb = (unsigned)(size_t)sm;
    const int wq = wave & 3;
    u32x4 wvo;
    wvo[0] = (unsigned)((lane >> 4) * F + (((lane & 15) ^ ((lane >> 4) << 2)) * 8)) * 2u;
#pragma unroll
    for (int j = 1; j < 4; ++j) wvo[j] = wvo[0] + j * (8 * F - 1024);
    const u16* w1b = sptr(p.w1, wq * 16 * F + nt * 128);
    const u16* w3b = sptr(p.w3, wq * 16 * F + nt * 128);
    auto kst = [&](int si) { if (si == 0) return 0; const int s = si + rot1; return s >= 7 ? s - 6 : s; };
    auto group_w = [&](int si, int m) { glds16x4(sptr(m ? w3b : w1b, kst(si) * 64 * F), wvo, lb + ring(si) + 16384 + m * 16384 + wq * 4096); };
    const int kr0 = (gpair & 31) * 64, kc0 = (gpair >> 5) * 64;
    const int chn = wq >> 1, par = wq & 1, dch = 2 * gpair + chn;
    if (wave < 4) group_w(0, 0);
    {
        const bool isk = lane >= 8 && lane < 14;
        u64 mb_ = vsel(lane == 15, p.dv, vsel(lane == 14, p.hk, vsel(isk, p.k, (u64)p.sk)));
        unsigned mo = (unsigned)((kc0 + wq * 16) * 8 + (lane & 7) * 16);
        mo = isk ? (unsigned)((kr0 ? kr0 - 3 + ((lane - 8) >> 1) : 0) * D + kc0 + wq * 16 + (lane & 1) * 8) * 2u : mo;
        mo = lane == 14 ? (unsigned)dch * 4096u : mo;
        mo = lane == 15 ? (unsigned)(dch & ~7) * 2u : mo;
        const u64 ma = mb_ + mo;
        asm volatile("s_mov_b32 m0, %1\n\ts_nop 0\n\tglobal_load_lds_dwordx4 %0, off" :: "v"(ma), "s"(lb + wave * 1024));
    }
    if (wave < 4) group_w(0, 1);
    SCHED();
    const int rot2 = ROT2;
    const int w2i = L_W2 + wq * 12288;
    auto fresh_lane = [&]() { int l = lane; asm volatile("" : "+v"(l)); return l; };
    const int latei = L_CONV + wq * 8192;
    auto pair_w2 = [&](u32x4 vo, int i, int dst) {
        int t = i + rot2; t = t >= 28 ? t - 28 : t;
        glds16x4(sptr(p.w2, (wq * 448 + t * 16) * D + n64 * 64), vo, lb + dst);
    };
    auto w2_off = [&]() {
        const int l = fresh_lane();
        const unsigned o = (unsigned)((l >> 3) * D + (((l & 7) ^ (((l >> 4) & 1) << 2)) * 8)) * 2u;
        return (u32x4){o, o + (16 * D - 1024), o + 2 * (16 * D - 1024), o + 3 * (16 * D - 1024)};
    };

    if (wave < 4) {
        u64 epoch;
        asm volatile("s_load_dwordx2 %0, %1, 0x0" : "=s"(epoch) : "s"(p.ws) : "memory");
        __builtin_amdgcn_s_setprio(MPRIO);
        const int wm = wave >> 1, wn = wave & 1;
        int xa[4], wa0, wa1;
#pragma unroll
        for (int i = 0; i < 4; ++i) xa[i] = lb + ring(0) + (((64 * wm + r32) * 128 + ((hh ^ ((r32 >> 1) & 7)) * 16)) ^ (i * 32));
        wa0 = 16384 + (8 * hh + q4) * 256 + (((8 * wn + 2 * blk + (p4 >> 1)) ^ (q4 << 2)) * 16) + 8 * (p4 & 1);
        wa1 = (wa0 ^ 64) + lb + ring(0); wa0 += lb + ring(0);
        f32x16 acc[2][2][2];
#pragma unroll
        for (int i = 0; i < 8; ++i)
#pragma unroll
            for (int r = 0; r < 16; ++r) acc[i >> 2][(i >> 1) & 1][i & 1][r] = 0.f;
        u32x4 fx[2][2], fw[2][2][2];
        auto load_frag = [&](int g, int j) {
            const int pp = g & 3, b = g & 1;
            if (j == 0) {
                if (g >= 4) {
                    const int d = ring(g >> 2) - ring((g >> 2) - 1);
                    if (pp == 0) { wa0 += d; wa1 += d; asm volatile("" : "+v"(wa0), "+v"(wa1)); }
                    xa[pp] += d; asm volatile("" : "+v"(xa[pp]));
                }
                fw[b][0][0] = ldstr2(lp(wa0) + pp * 4096, 1024);
            }
            if (j == 1) { fx[b][0] = lds128(lp(xa[pp])); fx[b][1] = lds128(lp(xa[pp]) + 4096); }
            if (j == 2) fw[b][0][1] = ldstr2(lp(wa0) + pp * 4096 + 16384, 1024);
            if (j == 3) fw[b][1][0] = ldstr2(lp(wa1) + pp * 4096, 1024);
            if (j == 4) fw[b][1][1] = ldstr2(lp(wa1) + pp * 4096 + 16384, 1024);
        };
        int ka0[2], kam[2], kwo;
#pragma unroll
        for (int rd = 0; rd < 2; ++rd) {
            const int ck = 2 * wq + rd;
            const int bq = 2 * (lane >> 4) + (p4 >> 1), bm = bq ? bq - 1 : 0;
            ka0[rd] = lb + L_KV + (8 * bq + q4) * 128 + ((ck ^ (((q4 >> 1) & 1) | ((bq & 3) << 1))) * 16) + 8 * (p4 & 1);
            kam[rd] = lb + L_KV + (8 * bm + 4 + q4) * 128 + ((ck ^ (((q4 >> 1) & 1) | ((bm & 3) << 1))) * 16) + 8 * (p4 & 1);
        }
        kwo = lb + L_KVO + wq * 2048 + (lane & 7) * 128 + (((7 - (lane >> 3)) ^ (lane & 7)) * 16);
        asm volatile("" : "+v"(ka0[0]), "+v"(ka0[1]), "+v"(kam[0]), "+v"(kam[1]), "+v"(kwo));
        WAIT_VM(4);
        u32x2 skv[2], hal[2];
#pragma unroll
        for (int rd = 0; rd < 2; ++rd) {
            LAS char* mp = sm + wave * 1024;
            skv[rd] = *(LAS u32x2*)(mp + (rd * 8 + (lane & 7)) * 8);
            const unsigned hm = kr0 ? 0xffffffffu : 0u;
            const unsigned a = *(LAS u16*)(mp + 128 + rd * 16 + (lane & 7) * 2), b = *(LAS u16*)(mp + 160 + rd * 16 + (lane & 7) * 2),
                           c = *(LAS u16*)(mp + 192 + rd * 16 + (lane & 7) * 2);
            hal[rd] = (u32x2){(a << 16) & hm, (b | (c << 16)) & hm};
            asm volatile("" : "+v"(skv[rd]), "+v"(hal[rd]));
        }
        asm volatile("s_waitcnt vmcnt(0) lgkmcnt(0)\n\ts_barrier" : "+s"(epoch) :: "memory");
        const u64 tag = epoch ^ TAG_SALT;
        STAMP(1);
        u32x2 kt[2][3], vt[2][2];
        float cf[4], kk[11], ko[8];
        u32x4 kpk;
        auto fill = [&](auto kc) {
            constexpr int k = decltype(kc)::value;
            if constexpr (k < 5) {
#pragma unroll
                for (int rd = 0; rd < 2; ++rd) {
                    if constexpr (k == 0) kt[rd][0] = ldstr(lp(kam[rd]));
                    else if constexpr (k < 3) kt[rd][k] = ldstr(lp(ka0[rd]) + (k - 1) * 512);
                    else vt[rd][k - 3] = ldstr(lp(ka0[rd]) + 8192 + (k - 3) * 512);
                }
            } else if constexpr (k >= 6 && k < 6 + 2 * 16) {
                constexpr int rd = (k - 6) / 16, s = (k - 6) % 16;
                if constexpr (s == 0) { cf[0] = bflo1(skv[rd][0]); cf[1] = bfhi1(skv[rd][0]); cf[2] = bflo1(skv[rd][1]); cf[3] = bfhi1(skv[rd][1]); }
                else if constexpr (s == 1) {
                    const unsigned a = lane < 8 ? hal[rd][0] : kt[rd][0][0], b = lane < 8 ? hal[rd][1] : kt[rd][0][1];
                    kk[0] = bfhi1(a); kk[1] = bflo1(b); kk[2] = bfhi1(b);
                } else if constexpr (s < 4) {
                    const u32x2 t = kt[rd][s - 1];
                    kk[4 * s - 5] = bflo1(t[0]); kk[4 * s - 4] = bfhi1(t[0]); kk[4 * s - 3] = bflo1(t[1]); kk[4 * s - 2] = bfhi1(t[1]);
                } else if constexpr (s < 12) {
                    constexpr int tp = (s - 4) >> 1, o0 = ((s - 4) & 1) * 4;
#pragma unroll
                    for (int i = o0; i < o0 + 4; ++i) {
                        if (tp == 0) ko[i] = mul1(kk[i], cf[3]);
                        else fmac1(ko[i], kk[i + tp], cf[3 - tp]);
                    }
                } else {
                    constexpr int j = s - 12;
                    const unsigned v = vt[rd][j >> 1][j & 1];
                    kpk[3 - j] = pk(mul1(ko[2 * j + 1], bfhi1(v)), mul1(ko[2 * j], bflo1(v)));
                    if constexpr (j == 3) *(LAS u32x4*)(lp(kwo) + rd * 1024) = kpk;
                }
            }
        };
#pragma unroll
        for (int j = 0; j < 5; ++j) load_frag(0, j);
        f32x4 cs[2][2][4];
        unsigned tch[4];
        unsigned tco = (unsigned)(gi * 4 + wq + (lane < 28 ? lane : 27) * 448) * 128u;
        asm volatile("" : "+v"(tco));
        static_for([&](auto gc) {
            constexpr int g = decltype(gc)::value, b = g & 1, pp = g & 3;
            SCHED();
            if (g == 11) STAMP(2);
            if (pp == 3 && g < 27) { STAMPG(2 + 2 * (g >> 2)); WAIT_BAR("lgkmcnt(0)"); STAMPG(3 + 2 * (g >> 2)); }
            if (g == 27) {
                WAIT_LGKM0();
                if (lane == 0) __hip_atomic_fetch_add((LAS unsigned*)(sm + L_CTL + 12), 1u, __ATOMIC_RELAXED, __HIP_MEMORY_SCOPE_WORKGROUP);
            }
            if (g == 11) STAMP(3);
            static_for([&](auto ic) {
                constexpr int i = decltype(ic)::value, ft = i >> 2, m = (i >> 1) & 1, sb = i & 1;
                acc[sb][ft][m] = mfma32(fw[b][ft][m], fx[b][sb], acc[sb][ft][m]);
                SCHED();
                if (g < 27 && i < 5) load_frag(g + 1, i);
                if constexpr (TOUCH && i == 4 && pp == 2 && g >= TOUCH0 && g < TOUCH0 + 16) {
                    u64 t;
                    asm volatile("s_mov_b64 %1, %3\n\tglobal_load_dword %0, %2, %1" : "=v"(tch[(g - TOUCH0) >> 2]), "=&s"(t)
                                 : "v"(tco), "s"((const char*)p.w2 + ((g - TOUCH0) >> 2) * (112 * 128)));
                }
                if constexpr (i >= 5 && g >= 7) fill(std::integral_constant<int, (g - 7) * 3 + i - 5>{});
                SCHED();
            }, std::make_integer_sequence<int, 8>{});
        }, std::make_integer_sequence<int, 28>{});
        STAMP(4);
        wait_lds_eq(sm + L_CTL + 64, 3u);
        unsigned sa[2];
#pragma unroll
        for (int i = 0; i < 8; ++i) cs[i >> 2][0][i & 3] = *(LAS f32x4*)(sm + L_CSUM + (64 * wn + 32 * (i >> 2) + 8 * (i & 3) + 4 * hh) * 4);
        sa[0] = *(LAS unsigned*)(sm + L_STAT + (64 * wm + r32) * 4);
        sa[1] = *(LAS unsigned*)(sm + L_STAT + (64 * wm + 32 + r32) * 4);
#pragma unroll
        for (int i = 0; i < 8; ++i) cs[i >> 2][1][i & 3] = *(LAS f32x4*)(sm + L_CSUM + 512 + (64 * wn + 32 * (i >> 2) + 8 * (i & 3) + 4 * hh) * 4);
        const int lw = fresh_lane();
        const int w2a0 = lb + w2i + (4 * (lw >> 5) + ((lw & 15) >> 2)) * 128 + (((2 * ((lw >> 4) & 1) + ((lw & 3) >> 1)) ^ (((lw >> 3) & 1) << 2)) * 16) + 8 * (lw & 1);
        const int w2a1 = w2a0 ^ 64;
        u32x4 w2r[12][2];
        auto park = [&](int h) {
#pragma unroll
            for (int i = 0; i < 6; ++i) {
                w2r[6 * h + i][0] = ldstr2(lp(w2a0) + h * L_SLOT + i * 2048, 1024);
                w2r[6 * h + i][1] = ldstr2(lp(w2a1) + h * L_SLOT + i * 2048, 1024);
            }
            asm volatile("" ::: "memory");
        };
#pragma unroll
        for (int sb = 0; sb < 2; ++sb) {
            const float ri = __builtin_amdgcn_rcpf(bfhi(sa[sb])) * -1.44269504088896341f, nm = -bflo(sa[sb]), d2 = bfhi(sa[sb]) * bfhi(sa[sb]);
            const f32x2 ri2 = {ri, ri}, nm2 = {nm, nm}, dd2 = {d2, d2};
#pragma unroll
            for (int ft = 0; ft < 2; ++ft) {
#pragma unroll
                for (int t2 = 0; t2 < 2; ++t2) {
                    f32x2 gu[4], ee[4];
#pragma unroll
                    for (int j = 0; j < 4; ++j) {
                        const int r = 8 * t2 + 2 * j;
                        const f32x2 g = __builtin_elementwise_fma(nm2, (f32x2){cs[ft][0][r >> 2][r & 3], cs[ft][0][r >> 2][(r & 3) + 1]}, (f32x2){acc[sb][ft][0][r], acc[sb][ft][0][r + 1]});
                        const f32x2 u = __builtin_elementwise_fma(nm2, (f32x2){cs[ft][1][r >> 2][r & 3], cs[ft][1][r >> 2][(r & 3) + 1]}, (f32x2){acc[sb][ft][1][r], acc[sb][ft][1][r + 1]});
                        const f32x2 t = g * ri2;
                        ee[j] = (f32x2){__builtin_amdgcn_exp2f(t[0]), __builtin_amdgcn_exp2f(t[1])};
                        gu[j] = g * u;
                    }
                    u32x4 st;
#pragma unroll
                    for (int j = 0; j < 4; ++j) {
                        const f32x2 dn = __builtin_elementwise_fma(ee[j], dd2, dd2);
                        const f32x2 hv = gu[j] * (f32x2){__builtin_amdgcn_rcpf(dn[0]), __builtin_amdgcn_rcpf(dn[1])};
                        st[j] = pk(hv[0], hv[1]);
                    }
                    const unsigned fi = (unsigned)((4 * mt + 2 * wm + sb) * 112 + 8 * nt + 4 * wn + 2 * ft + t2);
                    __builtin_amdgcn_raw_buffer_store_b128(st, wsr, WS_H + fi * 1024u + lane * 16u, 0, 16);
                }
            }
        }
        SCHED();
        STAMP(5);
        const u32x4 w2o = w2_off();
        wait_lds(sm + L_CTL + 112 + wq * 4);
        park(0);
        park(1);
        __hip_atomic_store((LAS unsigned*)(sm + L_CTL + 32 + wq * 4), 1u, __ATOMIC_RELAXED, __HIP_MEMORY_SCOPE_WORKGROUP);
        WAIT_VM(0);
        if (TOUCH) asm volatile("" :: "v"(tch[0]), "v"(tch[1]), "v"(tch[2]), "v"(tch[3]));
        arrive_publish(sm + L_CTL, p.ws, WS_FH + mt * 128 + nt * 8, tag, lane);
        STAMP(6);
        WAIT_LGKM0();
#pragma unroll
        for (int i = 0; i < 12; ++i) asm volatile("" : "+v"(w2r[i][0]), "+v"(w2r[i][1]));
#pragma unroll
        for (int i = 0; i < 3; ++i) pair_w2(w2o, 12 + 2 * i, w2i + i * 4096);
#pragma unroll
        for (int i = 0; i < 3; ++i) pair_w2(w2o, 18 + 2 * i, w2i + L_SLOT + i * 4096);
        STAMP(7);
        const unsigned need = ((2u << ((28 * wq + 27) >> 3)) - 1u) & ~((1u << ((28 * wq) >> 3)) - 1u);
        unsigned hpf[2] = {0u, 0u};
        if (wave == 0) {
            GAS u64* fp = (GAS u64*)(p.ws + WS_FH + (m64 >> 1) * 128) + (lw < 14 ? lw : 0);
#pragma unroll 1
            for (unsigned spins = 0; spins < SPIN_CAP; ++spins) {
                unsigned m = (unsigned)__ballot(__hip_atomic_load(fp, RLX_AGENT) == tag) & 0x3fffu;
                if (spins == SPIN_CAP - 1) m = 0x3fffu;
                __hip_atomic_store((LAS unsigned*)(sm + L_CTL + 4), m, __ATOMIC_RELAXED, __HIP_MEMORY_SCOPE_WORKGROUP);
                if (m == 0x3fffu) break;
                __builtin_amdgcn_s_sleep(POLL_SLEEP);
            }
            __builtin_amdgcn_fence(__ATOMIC_ACQUIRE, "wavefront");
        } else {
            WAIT_VM(0);
            int pp[2]; unsigned po[2];
#pragma unroll
            for (int k = 0; k < 2; ++k) {
                const int e = (wq - 1) * 90 + k * 45 + (lw < 45 ? lw : 44), pr = e / 19;
                int ln = nl * 19 + (e - pr * 19); ln = ln < 128 ? ln : 127;
                pp[k] = pr < 14 ? pr : 13;
                po[k] = WS_H + (unsigned)((2 * m64 + (ln >> 6)) * 112 + 8 * pp[k]) * 1024u + (unsigned)(ln & 63) * 128u;
            }
            unsigned done = 0;
#pragma unroll 1
            for (unsigned spins = 0; spins < (1u << 22); ++spins) {
                const unsigned m = __builtin_amdgcn_readfirstlane(__hip_atomic_load((LAS unsigned*)(sm + L_CTL + 4), __ATOMIC_RELAXED, __HIP_MEMORY_SCOPE_WORKGROUP));
                const unsigned fresh = m & ~done;
                if (HPF && fresh) {
#pragma unroll
                    for (int k = 0; k < 2; ++k) {
                        const unsigned o = ((fresh >> pp[k]) & 1u) ? po[k] : 0xfffffff0u;
                        asm volatile("buffer_load_dword %0, %1, %2, 0 offen sc1" : "+v"(hpf[k]) : "v"(o), "s"(wsr) : "memory");
                    }
                }
                done = m;
                if ((m & need) == need) break;
                __builtin_amdgcn_s_sleep(1);
            }
            asm volatile("" ::: "memory");
        }
        STAMP(11);
        const unsigned hb = WS_H + (unsigned)(2 * m64 * 112 + 28 * wq) * 1024u + lw * 16u;
        u32x4 hf[28][2];
        auto load_h = [&](int i, int j) {
            int t = i + rot2; t = t >= 28 ? t - 28 : t;
            hf[i][j] = __builtin_amdgcn_raw_buffer_load_b128(wsr, hb + j * (112u * 1024u), t * 1024, 16);
        };
        constexpr int HD = HDEPTH, HG = HGROW;
#pragma unroll
        for (int i = 0; i < HD; ++i) { load_h(i, 0); load_h(i, 1); }
        STAMP(14);
        f32x16 ac2[2][2];
#pragma unroll
        for (int i = 0; i < 4; ++i)
#pragma unroll
            for (int r = 0; r < 16; ++r) ac2[i >> 1][i & 1][r] = 0.f;
        u32x4 a0 = w2r[0][0], a1 = w2r[0][1];
        static_for([&](auto ic) {
            constexpr int i = decltype(ic)::value, n = i + 1;
            SCHED();
            if (i == 23) wait_lds(sm + L_CTL + 80 + wq * 4);
            u32x4 b0, b1;
            if constexpr (n < 12) { b0 = w2r[n][0]; b1 = w2r[n][1]; }
            else if constexpr (n < 28) {
                constexpr int so = n < 24 ? ((n - 12) / 6) * L_SLOT + ((n - 12) % 6) * 2048 : (n - 24) * 2048;
                const int lo = n < 24 ? 0 : latei - w2i;
                b0 = ldstr2(lp(w2a0 + lo) + so, 1024);
                b1 = ldstr2(lp(w2a1 + lo) + so, 1024);
            }
            SCHED();
            ac2[0][0] = mfma32(a0, hf[i][0], ac2[0][0]);
            SCHED();
            constexpr int l0 = i < HG ? HD + 2 * i : HD + HG + i;
            if constexpr (l0 < 28) load_h(l0, 0);
            SCHED();
            ac2[1][0] = mfma32(a1, hf[i][0], ac2[1][0]);
            SCHED();
            if constexpr (l0 < 28) load_h(l0, 1);
            SCHED();
            ac2[0][1] = mfma32(a0, hf[i][1], ac2[0][1]);
            SCHED();
            if constexpr (i < HG) load_h(l0 + 1, 0);
            SCHED();
            ac2[1][1] = mfma32(a1, hf[i][1], ac2[1][1]);
            SCHED();
            if constexpr (i < HG) load_h(l0 + 1, 1);
            a0 = b0; a1 = b1;
        }, std::make_integer_sequence<int, 28>{});
        SCHED();
        asm volatile("" :: "v"(hpf[0]), "v"(hpf[1]));
        STAMP(15);
        WAIT_LGKM0();
#pragma unroll
        for (int v = 0; v < 8; ++v) {
            LAS char* sl = sm + w2i + (v < 6 ? v * 2048 : L_SLOT + (v - 6) * 2048) + lw * 16;
            const f32x16& t = ac2[v >> 2][(v >> 1) & 1];
            const int r0 = 8 * (v & 1);
            *(LAS f32x4*)sl = (f32x4){t[r0], t[r0 + 1], t[r0 + 2], t[r0 + 3]};
            *(LAS f32x4*)(sl + 1024) = (f32x4){t[r0 + 4], t[r0 + 5], t[r0 + 6], t[r0 + 7]};
        }
        WAIT_BAR("lgkmcnt(0)");
        STAMP(16);
        STAMP(17);
        STAMP_OUT(18);
    } else {
        u64 epoch;
        asm volatile("s_load_dwordx2 %0, %1, 0x0" : "=s"(epoch) : "s"(p.ws) : "memory");
        u32x4 xvo;
#pragma unroll
        for (int j = 0; j < 4; ++j) xvo[j] = (unsigned)((8 * j + (lane >> 3)) * D + ((((lane & 7) ^ (lane >> 4)) ^ (4 * (j & 1))) * 8)) * 2u - 1024u * j;
        const u16* xb = sptr(p.x, (mt * 128 + wq * 32) * D);
        auto group_x = [&](int si) { glds16x4(sptr(xb, kst(si) * 64), xvo, lb + ring(si) + wq * 4096); };
        group_x(0);
        group_x(1); group_w(1, 0); group_w(1, 1);
        const int cbi = L_CONV + chn * C_CH;
        LAS char* cb = sm + cbi;
        unsigned kvo[2], uso;
#pragma unroll
        for (int rd = 0; rd < 2; ++rd) kvo[rd] = (unsigned)((lane >> 3) * D + (((lane & 7) ^ (((lane >> 4) & 1) | (((2 * wq + rd) & 3) << 1))) * 8)) * 2u;
        uso = WS_U + (unsigned)(kc0 + wq * 16 + (lane >> 3)) * 8192u + (unsigned)(1984 - kr0 + (lane & 7) * 8) * 2u;
        int kro = lb + L_KVO + wq * 2048 + (lane >> 3) * 128 + (((lane & 7) ^ (lane >> 3)) * 16);
        const int cc = 2 * par + ((lane >> 2) & 1);
        int zo = cbi + C_U + par * C_US + (248 + (lane < 36 ? lane : 35)) * 16;
        zo = lane < 8 ? cbi + cc * C_HS + (lane & 3) * 16 : zo;
        u32x4 w2o = w2_off();
        const unsigned fo = lane * 16u;
        unsigned ao = (wq < 2 ? WS_CSUM + (unsigned)(nt * 2 + wq) * 1024u : WS_STATS + mt * 1024u) + fo;
        asm volatile("" : "+v"(zo), "+v"(w2o), "+v"(kvo[0]), "+v"(kvo[1]), "+v"(uso), "+v"(kro), "+v"(ao));
        asm volatile("s_waitcnt vmcnt(16) lgkmcnt(0)" : "+s"(epoch) :: "memory");
        const unsigned tag32 = (unsigned)(epoch ^ TAG_SALT);
        u32x4 bval, ug[2] = {{tag32, 0u, tag32, 0u}, {tag32, 0u, tag32, 0u}};
        unsigned dbits = *(LAS u16*)(sm + wave * 1024 + 240 + (dch & 7) * 2);
        {
            const u32x4 h0 = lds128(sm + wave * 1024 + 224);
            const bool s1 = !(cc & 1), s2 = !(cc & 2);
            const unsigned a0 = s1 ? 0u : h0[0], a1 = s1 ? h0[0] : h0[1], a2 = s1 ? h0[1] : h0[2], a3 = s1 ? h0[2] : h0[3];
            const unsigned lm = (lane == 3 || lane == 7) ? 0xffffffffu : 0u;
            bval = (u32x4){(s2 ? 0u : a0) & lm, (s2 ? 0u : a1) & lm, (s2 ? a0 : a2) & lm, (s2 ? a1 : a3) & lm};
            asm volatile("" : "+v"(bval), "+v"(dbits), "+v"(ug[0]), "+v"(ug[1]));
        }
        auto kv_store = [&](int rd) {
            LAS unsigned* kp = (LAS unsigned*)(lp(kro) + rd * 1024);
            ug[0][1] = kp[0]; asm volatile("" ::: "memory"); ug[0][3] = kp[1]; asm volatile("" ::: "memory");
            ug[1][1] = kp[2]; asm volatile("" ::: "memory"); ug[1][3] = kp[3];
            __builtin_amdgcn_raw_buffer_store_b128(ug[0], wsr, uso, rd * 65536, 16);
            __builtin_amdgcn_raw_buffer_store_b128(ug[1], wsr, uso, rd * 65536 + 4096, 16);
        };
        WAIT_BAR("vmcnt(12) lgkmcnt(0)");
        STAMP(1);
        group_x(2); group_w(2, 0); group_w(2, 1);
#pragma unroll
        for (int j = 0; j < 2; ++j) {
            glds16(sptr(p.k, (kr0 + (2 * wq + j) * 8) * D + kc0), kvo[j], lb + L_KV + (2 * wq + j) * 1024);
            glds16(sptr(p.v, (kr0 + (2 * wq + j) * 8) * D + kc0), kvo[j], lb + L_KV + 8192 + (2 * wq + j) * 1024);
        }
        STAMPG(2); WAIT_BAR("vmcnt(16)"); STAMPG(3);
#pragma unroll
        for (int w = 1; w < 5; ++w) {
            group_x(w + 2); group_w(w + 2, 0); group_w(w + 2, 1);
            if (w == 3 && lane < 32) *(LAS unsigned*)(sm + L_CTL + lane * 4) = 0u;
            if (w == 4) kv_store(0);
            if (w == 2) STAMP(2);
            STAMPG(2 + 2 * w); WAIT_BAR("vmcnt(12) lgkmcnt(0)"); STAMPG(3 + 2 * w);
            if (w == 2) STAMP(3);
        }
#pragma unroll
        for (int i = 0; i < 3; ++i) pair_w2(w2o, 2 * i, w2i + i * 4096);
        STAMPG(12); WAIT_BAR("vmcnt(12)"); STAMPG(13);
        kv_store(1);
        asm volatile("" ::: "memory");
        if (lane == 0) __hip_atomic_fetch_add((LAS unsigned*)(sm + L_CTL + 12), 1u, __ATOMIC_RELAXED, __HIP_MEMORY_SCOPE_WORKGROUP);
        if (wq < 3) {
            u32x4 av;
#pragma unroll 1
            for (unsigned spins = 0; spins < SPIN_CAP; ++spins) {
                asm volatile("" ::: "memory");
                av = __builtin_amdgcn_raw_buffer_load_b128(wsr, ao, 0, 16);
                if (__all(((av[0] ^ tag32) | (av[2] ^ tag32)) == 0u)) break;
            }
            *(LAS u32x2*)(sm + L_CSUM + wq * 512 + lane * 8) = (u32x2){av[1], av[3]};
            asm volatile("" ::: "memory");
            if (lane == 0) __hip_atomic_fetch_add((LAS unsigned*)(sm + L_CTL + 64), 1u, __ATOMIC_RELAXED, __HIP_MEMORY_SCOPE_WORKGROUP);
        }
        STAMP(4);
#pragma unroll
        for (int i = 0; i < 3; ++i) pair_w2(w2o, 6 + 2 * i, w2i + L_SLOT + i * 4096);
        wait_lds_eq(sm + L_CTL + 12, 8u);
        STAMP(5);
        __builtin_amdgcn_s_setprio(2);
        const unsigned ubase = WS_U + (unsigned)dch * 8192u;
        glds16x2c(p.ws + ubase + par * 2048, fo, lb + cbi + C_U + par * 2048);
        glds16x2c(p.ws + ubase + 4096 + par * 2048, fo, lb + cbi + C_U + C_US + par * 2048);
        glds4c(p.ws + ubase + (1 - par) * 2048, (lane & 1) * 4u, lb + L_UNX + wq * 256);
        {
            const u32x4 fvo = {fo, fo, fo, lane == 63 ? 62u * 16u : fo};
#pragma unroll
            for (int c = 0; c < 2; ++c) glds16x4(sptr(p.hk, dch * S + 4 * par + 2 * c + 2), fvo, lb + cbi + (2 * par + c) * C_HS + 64);
        }
        *(LAS u32x4*)(sm + zo) = bval;
        WAIT_VM(13);
        __hip_atomic_store((LAS unsigned*)(sm + L_CTL + 112 + wq * 4), 1u, __ATOMIC_RELAXED, __HIP_MEMORY_SCOPE_WORKGROUP);
        WAIT_VM(8);
        {
            u32x4 ua[2], ub[2]; u32x2 un[2];
#pragma unroll
            for (int rd = 0; rd < 2; ++rd) {
                const int x8 = 128 * par + 64 * rd + lane;
                ua[rd] = lds128(cb + C_U + x8 * 16); ub[rd] = lds128(cb + C_U + C_US + x8 * 16);
                un[rd] = *(LAS u32x2*)((rd == 1 && lane == 63) ? sm + L_UNX + wq * 256 : cb + C_U + x8 * 16 + 16);
            }
#pragma unroll 1
            for (unsigned spins = 0; spins < SPIN_CAP; ++spins) {
                unsigned df = 0;
#pragma unroll
                for (int rd = 0; rd < 2; ++rd) df |= (ua[rd][0] ^ tag32) | (ua[rd][2] ^ tag32) | (ub[rd][0] ^ tag32) | (ub[rd][2] ^ tag32) | (un[rd][0] ^ tag32);
                if (__all(df == 0u)) break;
                __builtin_amdgcn_s_sleep(4);
                asm volatile("" ::: "memory");
#pragma unroll
                for (int rd = 0; rd < 2; ++rd) {
                    const int x8 = 128 * par + 64 * rd + lane;
                    const unsigned uo = ubase + (unsigned)x8 * 16u;
                    ua[rd] = __builtin_amdgcn_raw_buffer_load_b128(wsr, uo, 0, 16);
                    ub[rd] = __builtin_amdgcn_raw_buffer_load_b128(wsr, uo + 4096u, 0, 16);
                    un[rd] = __builtin_amdgcn_raw_buffer_load_b64(wsr, x8 == 255 ? uo : uo + 16u, 0, 16);
                }
                asm volatile("" : "+v"(ua[0]), "+v"(ub[0]), "+v"(un[0]), "+v"(ua[1]), "+v"(ub[1]), "+v"(un[1]));
            }
#pragma unroll
            for (int rd = 0; rd < 2; ++rd) {
                const int x8 = 128 * par + 64 * rd + lane;
                const unsigned nx = x8 == 255 ? 0u : un[rd][1];
                *(LAS u32x4*)(cb + C_U + x8 * 16) = (u32x4){__builtin_amdgcn_alignbit(ua[rd][3], ua[rd][1], 16), __builtin_amdgcn_alignbit(ub[rd][1], ua[rd][3], 16),
                                                            __builtin_amdgcn_alignbit(ub[rd][3], ub[rd][1], 16), __builtin_amdgcn_alignbit(nx, ub[rd][3], 16)};
                *(LAS u32x4*)(cb + C_U + C_US + x8 * 16) = (u32x4){ua[rd][1], ua[rd][3], ub[rd][1], ub[rd][3]};
            }
        }
        asm volatile("s_waitcnt vmcnt(0) lgkmcnt(0)" ::: "memory");
        __hip_atomic_store((LAS unsigned*)(sm + L_CTL + 16 + wq * 4), 1u, __ATOMIC_RELAXED, __HIP_MEMORY_SCOPE_WORKGROUP);
        u32x4 lw2[8], xv[2];
        {
            const __amdgpu_buffer_rsrc_t wr2 = __builtin_amdgcn_make_buffer_rsrc((void*)p.w2, 0, F * D * 2, 0x00020000);
#pragma unroll
            for (int j = 0; j < 8; ++j) {
                int t = 24 + (j >> 2) * 2 + rot2; t = t >= 28 ? t - 28 : t;
                lw2[j] = __builtin_amdgcn_raw_buffer_load_b128(wr2, w2o[0], ((wq * 448 + t * 16 + (j & 3) * 8) * D + n64 * 64) * 2, 0);
            }
        }
        const int lf = fresh_lane();
        const int srow = 64 * m64 + 32 * (wq & 1) + (lf & 31);
        const int dd = 64 * n64 + 32 * (wq >> 1) + 16 * (lf >> 5);
        const unsigned xoff = (unsigned)(srow * D + dd) * 2u;
        {
            const __amdgpu_buffer_rsrc_t xr = __builtin_amdgcn_make_buffer_rsrc((void*)p.x, 0, S * D * 2, 0x00020000);
            xv[0] = __builtin_amdgcn_raw_buffer_load_b128(xr, xoff, 0, 0);
            xv[1] = __builtin_amdgcn_raw_buffer_load_b128(xr, xoff, 16, 0);
        }
        SCHED();
        wait_lds(sm + L_CTL + 16 + (wq ^ 1) * 4);
        STAMP(7);
        STAMP(6);
        const int n16 = lane & 15, kg = lane >> 4;
        f32x4 ca[4];
#pragma unroll
        for (int j = 0; j < 4; ++j) ca[j] = (f32x4){0.f, 0.f, 0.f, 0.f};
        int apo = lb + cbi + (n16 & 3) * C_HS + ((n16 >> 2) + kg) * 16;
        int bpo = lb + cbi + C_U + (n16 & 1) * C_US + (252 - 4 * (n16 >> 1) + kg) * 16 - 7 * 512;
        u32x4 fa[2][8], fb[2][8];
#pragma unroll
        for (int i = 0; i < 8; ++i) { fa[0][i] = lds128(lp(apo) + i * 512); fb[0][i] = lds128(lp(bpo) + (7 - i) * 512); }
        const bool odd = lf & 1;
        int ysrc = (2 * (lf >> 3) + (lf & 1) + 16 * ((lf >> 1) & 3)) * 4;
        unsigned yso = WS_Y + (unsigned)dch * 8192u + lf * 16u;
        f32x4 kd[4];
        {
            const float dval = bflo(dbits);
            const int sl0 = 32 * ((lf & 15) >> 1) + 8 * (lf >> 4) + 4 * (lf & 1);
#pragma unroll
            for (int j = 0; j < 4; ++j) {
                const int tj = (j == 0 ? 0 : j == 1 ? 3 : j == 2 ? 4 : 7) ^ par;
                const u32x2 kvr = *(LAS u32x2*)(cb + C_U + C_US + (2044 - 256 * tj - sl0) * 2);
                const unsigned ka = pk(bfhi(kvr[1]) * dval, bflo(kvr[1]) * dval), kb = pk(bfhi(kvr[0]) * dval, bflo(kvr[0]) * dval);
                kd[j] = (f32x4){bflo(ka), bfhi(ka), bflo(kb), bfhi(kb)};
                asm volatile("" : "+v"(kd[j]));
            }
            asm volatile("" : "+v"(ysrc), "+v"(yso));
        }
        wait_lds(sm + L_CTL + 32 + wq * 4);
        STAMP(8);
        auto conv_all = [&](auto pc_) {
            constexpr int P = decltype(pc_)::value;
            static_for([&](auto rc) {
                constexpr int r = decltype(rc)::value, c = r & 1, n = c ^ 1;
                static_for([&](auto kc) {
                    constexpr int k = decltype(kc)::value;
                    constexpr int T[4] = {P, 3 - P, 4 + P, 7 - P};
                    constexpr int j = k <= T[0] ? 0 : k <= T[0] + T[1] + 1 ? 1 : k <= T[0] + T[1] + T[2] + 2 ? 2 : 3;
                    constexpr int pp = k - (j == 0 ? 0 : j == 1 ? T[0] + 1 : j == 2 ? T[0] + T[1] + 2 : T[0] + T[1] + T[2] + 3);
                    ca[j] = mfma16(fa[c][pp], fb[c][T[j] - pp], ca[j]);
                    SCHED();
                    if constexpr (r < 7 && k < 2 * (T[3] + 1)) {
                        if constexpr (k & 1) fb[n][k >> 1] = lds128(lp(bpo) + (r + 1) * 64 + (7 - (k >> 1)) * 512);
                        else fa[n][k >> 1] = lds128(lp(apo) + (r + 1) * 64 + (k >> 1) * 512);
                    }
                    SCHED();
                }, std::make_integer_sequence<int, 18>{});
                asm volatile("" : "+v"(apo), "+v"(bpo));
            }, std::make_integer_sequence<int, 8>{});
        };
        if (par) conv_all(std::integral_constant<int, 1>{}); else conv_all(std::integral_constant<int, 0>{});
        SCHED();
        STAMP(9);
        {
            float r0[4], r1[4];
#pragma unroll
            for (int j = 0; j < 4; ++j) { r0[j] = dpp_xor1(odd ? ca[j][0] : ca[j][2]); r1[j] = dpp_xor1(odd ? ca[j][1] : ca[j][3]); }
            unsigned rp[4][2];
#pragma unroll
            for (int j = 0; j < 4; ++j) {
                const unsigned ya = pk(odd ? r0[j] : ca[j][0], odd ? ca[j][2] : r0[j]), yb = pk(odd ? r1[j] : ca[j][1], odd ? ca[j][3] : r1[j]);
                rp[j][0] = __builtin_amdgcn_ds_bpermute(ysrc, pk(add1(bflo1(ya), kd[j][0]), add1(bfhi1(ya), kd[j][1])));
                rp[j][1] = __builtin_amdgcn_ds_bpermute(ysrc, pk(add1(bflo1(yb), kd[j][2]), add1(bfhi1(yb), kd[j][3])));
            }
#pragma unroll
            for (int j = 0; j < 4; ++j)
                __builtin_amdgcn_raw_buffer_store_b128((u32x4){vmov1(tag32), rp[j][0], vmov1(tag32), rp[j][1]}, wsr, yso,
                                                       ((j == 0 ? 0 : j == 1 ? 3 : j == 2 ? 4 : 7) ^ par) * 1024, 16);
        }
        STAMP(10);
        WAIT_LGKM0();
        if (lane == 0) __hip_atomic_fetch_add((LAS unsigned*)(sm + L_CTL + 8), 1u, __ATOMIC_RELAXED, __HIP_MEMORY_SCOPE_WORKGROUP);
#pragma unroll 1
        for (unsigned spins = 0; spins < (1u << 22); ++spins) {
            if (__builtin_amdgcn_readfirstlane(__hip_atomic_load((LAS unsigned*)(sm + L_CTL + 8), __ATOMIC_RELAXED, __HIP_MEMORY_SCOPE_WORKGROUP)) == 4u) break;
            __builtin_amdgcn_s_sleep(1);
        }
        asm volatile("" ::: "memory");
#pragma unroll
        for (int j = 0; j < 8; ++j) *(LAS u32x4*)(sm + latei + j * 1024 + lf * 16) = lw2[j];
        asm volatile("" ::: "memory");
        __hip_atomic_store((LAS unsigned*)(sm + L_CTL + 80 + wq * 4), 1u, __ATOMIC_RELAXED, __HIP_MEMORY_SCOPE_WORKGROUP);
        STAMP(11);
        __builtin_amdgcn_s_sleep(YSLEEP);
        u32x2 yv[16], stv;
        const int yl = L_CONV + 32768 + wq * 4096;
        const unsigned yg = (unsigned)(lf >> 3) * 8192u + (unsigned)(lf & 7) * 16u;
        const u32x4 yvo = {yg, yg + (65536u - 1024u), yg + 2 * (65536u - 1024u), yg + 3 * (65536u - 1024u)};
        const char* ybase = p.ws + WS_Y + (unsigned)(64 * n64 + 32 * (wq >> 1)) * 8192u + (unsigned)(64 * m64 + 32 * (wq & 1)) * 4u;
#pragma unroll 1
        for (unsigned spins = 0; spins < SPIN_CAP; ++spins) {
            glds16x4c(ybase, yvo, lb + yl);
            stv = __builtin_amdgcn_raw_buffer_load_b64(wsr, WS_STATS + (unsigned)srow * 8u, 0, 16);
            WAIT_VM(0);
#pragma unroll
            for (int i = 0; i < 16; ++i) yv[i] = *(LAS u32x2*)(sm + yl + ((lf >> 5) * 16 + i) * 128 + ((lf & 31) >> 1) * 8);
            unsigned df = stv[0] ^ tag32;
#pragma unroll
            for (int i = 0; i < 16; ++i) df |= yv[i][0] ^ tag32;
            if (__all(df == 0u)) break;
            __builtin_amdgcn_s_sleep(8);
        }
        STAMP(14);
        __builtin_amdgcn_s_setprio(0);
        f32x2 sf[8], xf[8];
        {
            const float nmb = -bflo(stv[1]);
            float ri = __builtin_amdgcn_rcpf(bfhi(stv[1]));
            asm volatile("s_nop 1" : "+v"(ri));
            const unsigned ysh = (srow & 1) ? 0u : 16u;
#pragma unroll
            for (int i = 0; i < 8; ++i) {
                const unsigned xd = xv[i >> 2][i & 3];
                xf[i] = (f32x2){bflo1(xd), bfhi1(xd)};
                const unsigned tp = pk(add1(xf[i][0], nmb), add1(xf[i][1], nmb));
                const unsigned np = pk(mul1(bflo1(tp), ri), mul1(bfhi1(tp), ri));
                const unsigned sp = pk(mul1(bflo1(np), bfhi1(yv[2 * i][1] << ysh)), mul1(bfhi1(np), bfhi1(yv[2 * i + 1][1] << ysh)));
                sf[i] = (f32x2){bflo1(sp), bfhi1(sp)};
                asm volatile("" : "+v"(sf[i]), "+v"(xf[i]));
            }
        }
        const int rm = lb + L_W2 + (wq < 3 ? wq * 4096 : L_SLOT) + (lf >> 5) * 2048 + (lf & 31) * 16;
        STAMP(15);
        WAIT_BAR("lgkmcnt(0)");
        STAMP(16);
        f32x4 o[4];
#pragma unroll
        for (int j = 0; j < 4; ++j) {
            LAS char* rp = lp(rm) + j * 512;
            o[j] = (*(LAS f32x4*)rp + *(LAS f32x4*)(rp + 12288)) + (*(LAS f32x4*)(rp + 24576) + *(LAS f32x4*)(rp + 36864));
        }
        f32x2 ov[8], ev[8];
#pragma unroll
        for (int i = 0; i < 8; ++i) {
            const unsigned op = pk(o[i >> 1][(i & 1) * 2], o[i >> 1][(i & 1) * 2 + 1]);
            ov[i] = (f32x2){bflo(op), bfhi(op)};
            const f32x2 t = ov[i] * (f32x2){-1.44269504088896341f, -1.44269504088896341f};
            ev[i] = (f32x2){__builtin_amdgcn_exp2f(t[0]), __builtin_amdgcn_exp2f(t[1])};
        }
        SCHED();
        unsigned gp[8];
#pragma unroll
        for (int i = 0; i < 8; ++i) {
            const f32x2 dn = ev[i] + (f32x2){1.f, 1.f};
            const f32x2 g = ov[i] * (f32x2){__builtin_amdgcn_rcpf(dn[0]), __builtin_amdgcn_rcpf(dn[1])};
            gp[i] = pk(g[0], g[1]);
        }
        SCHED();
        unsigned mp2[8];
#pragma unroll
        for (int i = 0; i < 8; ++i) {
            const f32x2 m = sf[i] * (f32x2){bflo(gp[i]), bfhi(gp[i])};
            mp2[i] = pk(m[0], m[1]);
        }
        SCHED();
        u32x4 st[2];
#pragma unroll
        for (int i = 0; i < 8; ++i) {
            const f32x2 r = xf[i] + (f32x2){bflo(mp2[i]), bfhi(mp2[i])};
            st[i >> 2][i & 3] = pk(r[0], r[1]);
        }
        const __amdgpu_buffer_rsrc_t orr = __builtin_amdgcn_make_buffer_rsrc((void*)p.out, 0, S * D * 2, 0x00020000);
        __builtin_amdgcn_raw_buffer_store_b128(st[0], orr, xoff, 0, 0);
        __builtin_amdgcn_raw_buffer_store_b128(st[1], orr, xoff, 16, 0);
        STAMP(17);
        STAMP_OUT(18);
    }
}

extern "C" void kernel_launch(void* const* d_in, const int* in_sizes, int n_in,
                              void* d_out, int out_size, void* d_ws, size_t ws_size,
                              hipStream_t stream) {
    Params p;
    p.x = (const u16*)d_in[0]; p.k = (const u16*)d_in[1]; p.v = (const u16*)d_in[2];
    p.sk = (const u16*)d_in[3]; p.hk = (const u16*)d_in[4]; p.dv = (const u16*)d_in[5];
    p.w1 = (const u16*)d_in[7]; p.w3 = (const u16*)d_in[8]; p.w2 = (const u16*)d_in[9];
    p.out = (u16*)d_out; p.ws = (char*)d_ws;
    (void)hipFuncSetAttribute((const void*)fused_kernel, hipFuncAttributeMaxDynamicSharedMemorySize, L_TOTAL);
    fused_kernel<<<256, 512, L_TOTAL, stream>>>(p);
}
```

```cpp
#include <hip/hip_runtime.h>
#include <stdint.h>
#include <utility>

typedef unsigned short u16;
typedef unsigned long long u64;
typedef short s16x4 __attribute__((ext_vector_type(4)));
typedef short s16x8 __attribute__((ext_vector_type(8)));
typedef __bf16 bf16x2 __attribute__((ext_vector_type(2)));
typedef __bf16 bf16x8 __attribute__((ext_vector_type(8)));
typedef float f32x2 __attribute__((ext_vector_type(2)));
typedef float f32x4 __attribute__((ext_vector_type(4)));
typedef float f32x16 __attribute__((ext_vector_type(16)));
typedef unsigned u32x2 __attribute__((ext_vector_type(2)));
typedef unsigned u32x4 __attribute__((ext_vector_type(4)));
typedef unsigned u32x16 __attribute__((ext_vector_type(16)));
#define LAS __attribute__((address_space(3)))
#define GAS __attribute__((address_space(1)))
#define RLX_AGENT __ATOMIC_RELAXED, __HIP_MEMORY_SCOPE_AGENT

constexpr int S = 2048, D = 448, F = 1792;

constexpr unsigned WS_EPOCH = 0;
constexpr unsigned WS_FH = 1024;
constexpr unsigned WS_STATS = 8192;
constexpr unsigned WS_CSUM = 24576;
constexpr unsigned WS_U = 65536;
constexpr unsigned WS_Y = WS_U + D * S * 4;
constexpr unsigned WS_H = WS_Y + D * S * 4;
constexpr unsigned WS_END = WS_H + 64 * 112 * 1024;
constexpr unsigned WS_DBG = WS_END;

constexpr int L_KV = 0;
constexpr int L_CTL = 0;
constexpr int L_CSUM = 256;
constexpr int L_STAT = 1280;
constexpr int L_KVO = 8192;
constexpr int L_UNX = 6144;
constexpr int L_SLOT = 49152;
constexpr int L_CONV = 12288;
constexpr int C_CH = 26112, C_HS = 4160, C_U = 16640, C_US = 4640;
constexpr int L_W2 = 16384 + L_SLOT;
constexpr int L_TOTAL = 163840;
__host__ __device__ constexpr int ring(int stage) { return 16384 + (stage % 3) * L_SLOT; }
constexpr unsigned CODE_PREFETCH = 24576;
#define POLL_SLEEP 4
#define HDEPTH 6
#define ROT2 (8 * ml)
#define MPRIO 1
#define HPF 0
#define YSLEEP 16
#define TOUCH 1
#define TOUCH0 4
#define HGROW 4
#define ROT1 ((2 * ml + nl) % 6)

constexpr u64 TAG_SALT = 0x9E3779B97F4A7C15ull;
constexpr unsigned SPIN_CAP = 1u << 18;

struct Params {
    const u16* x; const u16* k; const u16* v; const u16* sk; const u16* hk; const u16* dv;
    const u16* w1; const u16* w3; const u16* w2; u16* out; char* ws;
};

__device__ __forceinline__ float bflo(unsigned p) { return __uint_as_float(p << 16); }
__device__ __forceinline__ float bfhi(unsigned p) { return __uint_as_float(p & 0xffff0000u); }
__device__ __forceinline__ float bflo1(unsigned p) { float r; asm("v_lshlrev_b32 %0, 16, %1" : "=v"(r) : "v"(p)); return r; }
__device__ __forceinline__ float bfhi1(unsigned p) { float r; asm("v_and_b32 %0, 0xffff0000, %1" : "=v"(r) : "v"(p)); return r; }
__device__ __forceinline__ float mul1(float a, float b) { float r; asm("v_mul_f32 %0, %1, %2" : "=v"(r) : "v"(a), "v"(b)); return r; }
__device__ __forceinline__ void fmac1(float& d, float a, float b) { asm("v_fmac_f32 %0, %1, %2" : "+v"(d) : "v"(a), "v"(b)); }
__device__ __forceinline__ float dpp_xor1(float v) {
    return __int_as_float(__builtin_amdgcn_update_dpp(0, __float_as_int(v), 0xB1, 0xf, 0xf, true));
}
__device__ __forceinline__ float add1(float a, float b) { float r; asm("v_add_f32 %0, %1, %2" : "=v"(r) : "v"(a), "v"(b)); return r; }
__device__ __forceinline__ unsigned vmov1(unsigned s) { unsigned r; asm volatile("v_mov_b32 %0, %1" : "=v"(r) : "s"(s)); return r; }
__device__ __forceinline__ unsigned pk(float a, float b) {
    f32x2 f = {a, b};
    return __builtin_bit_cast(unsigned, __builtin_convertvector(f, bf16x2));
}
__device__ __forceinline__ float rbf(float a) { return bflo(pk(a, a)); }
__device__ __forceinline__ float dot2(unsigned a, unsigned b, float c) {
    return __builtin_amdgcn_fdot2_f32_bf16(__builtin_bit_cast(bf16x2, a), __builtin_bit_cast(bf16x2, b), c, false);
}
__device__ __forceinline__ void glds16(const void* base, unsigned voff, unsigned ldst) {
    u64 t;
    asm volatile("s_mov_b32 m0, %3\n\ts_mov_b64 %0, %2\n\tglobal_load_lds_dwordx4 %1, %0" : "=&s"(t) : "v"(voff), "s"(base), "s"(ldst));
}
__device__ __forceinline__ void glds16x4(const void* base, u32x4 vo, unsigned ldst) {
    u64 t;
    asm volatile("s_mov_b32 m0, %6\n\ts_mov_b64 %0, %5\n\tglobal_load_lds_dwordx4 %1, %0\n\tglobal_load_lds_dwordx4 %2, %0 offset:1024\n\t"
                 "global_load_lds_dwordx4 %3, %0 offset:2048\n\tglobal_load_lds_dwordx4 %4, %0 offset:3072"
                 : "=&s"(t) : "v"(vo[0]), "v"(vo[1]), "v"(vo[2]), "v"(vo[3]), "s"(base), "s"(ldst));
}
__device__ __forceinline__ void glds16x4c(const void* base, u32x4 vo, unsigned ldst) {
    u64 t;
    asm volatile("s_mov_b32 m0, %6\n\ts_mov_b64 %0, %5\n\tglobal_load_lds_dwordx4 %1, %0 sc1\n\tglobal_load_lds_dwordx4 %2, %0 offset:1024 sc1\n\t"
                 "global_load_lds_dwordx4 %3, %0 offset:2048 sc1\n\tglobal_load_lds_dwordx4 %4, %0 offset:3072 sc1"
                 : "=&s"(t) : "v"(vo[0]), "v"(vo[1]), "v"(vo[2]), "v"(vo[3]), "s"(base), "s"(ldst) : "memory");
}
__device__ __forceinline__ void glds16x2c(const void* base, unsigned voff, unsigned ldst) {
    u64 t;
    asm volatile("s_mov_b32 m0, %3\n\ts_mov_b64 %0, %2\n\tglobal_load_lds_dwordx4 %1, %0 sc1\n\tglobal_load_lds_dwordx4 %1, %0 offset:1024 sc1"
                 : "=&s"(t) : "v"(voff), "s"(base), "s"(ldst));
}
__device__ __forceinline__ void glds16c(const void* base, unsigned voff, unsigned ldst) {
    u64 t;
    asm volatile("s_mov_b32 m0, %3\n\ts_mov_b64 %0, %2\n\tglobal_load_lds_dwordx4 %1, %0 sc1" : "=&s"(t) : "v"(voff), "s"(base), "s"(ldst));
}
__device__ __forceinline__ void glds4c(const void* base, unsigned voff, unsigned ldst) {
    u64 t;
    asm volatile("s_mov_b32 m0, %3\n\ts_mov_b64 %0, %2\n\tglobal_load_lds_dword %1, %0 sc1" : "=&s"(t) : "v"(voff), "s"(base), "s"(ldst));
}
__device__ __forceinline__ const u16* sptr(const u16* base, int off) {
    const u16* r = base + __builtin_amdgcn_readfirstlane(off);
    asm("" : "+s"(r));
    return r;
}
__device__ __forceinline__ u64 vsel(bool c, const void* a, u64 b) {
    u64 va = (u64)a;
    asm("" : "+v"(va), "+v"(b));
    return c ? va : b;
}
__device__ __forceinline__ LAS char* lp(int a) { return (LAS char*)(size_t)(unsigned)a; }
__device__ __forceinline__ u32x4 lds128(LAS char* p) { return *(LAS u32x4*)p; }
__device__ __forceinline__ u32x2 ldstr(LAS char* p) {
    return __builtin_bit_cast(u32x2, __builtin_amdgcn_ds_read_tr16_b64_v4i16((LAS s16x4*)p));
}
__device__ __forceinline__ u32x4 ldstr2(LAS char* p, int off) {
    u32x2 a = ldstr(p), b = ldstr(p + off);
    return (u32x4){a.x, a.y, b.x, b.y};
}
__device__ __forceinline__ f32x16 mfma32(u32x4 a, u32x4 b, f32x16 c) {
    return __builtin_amdgcn_mfma_f32_32x32x16_bf16(__builtin_bit_cast(bf16x8, a), __builtin_bit_cast(bf16x8, b), c, 0, 0, 0);
}
__device__ __forceinline__ f32x4 mfma16(u32x4 a, u32x4 b, f32x4 c) {
    return __builtin_amdgcn_mfma_f32_16x16x32_bf16(__builtin_bit_cast(bf16x8, a), __builtin_bit_cast(bf16x8, b), c, 0, 0, 0);
}
__device__ __forceinline__ float silu_f(float g) {
    return g * __builtin_amdgcn_rcpf(1.f + __builtin_amdgcn_exp2f(g * -1.44269504088896341f));
}
template <class Fn, int... I>
__device__ __forceinline__ void static_for(Fn&& fn, std::integer_sequence<int, I...>) { (fn(std::integral_constant<int, I>{}), ...); }
#define WAIT_VM(n) asm volatile("s_waitcnt vmcnt(" #n ")" ::: "memory")
#define WAIT_LGKM0() asm volatile("s_waitcnt lgkmcnt(0)" ::: "memory")
#define WAIT_BAR(w) asm volatile("s_waitcnt " w "\n\ts_barrier" ::: "memory")
#define SCHED() __builtin_amdgcn_sched_barrier(0)
#define CAS __attribute__((address_space(4)))
#define NSTAMP 18

__device__ __forceinline__ void wait_lds(LAS char* word) {
#pragma unroll 1
    for (unsigned spins = 0; spins < (SPIN_CAP << 4); ++spins) {
        if (__builtin_amdgcn_readfirstlane(__hip_atomic_load((LAS unsigned*)word, __ATOMIC_RELAXED, __HIP_MEMORY_SCOPE_WORKGROUP))) break;
        __builtin_amdgcn_s_sleep(1);
    }
    asm volatile("" ::: "memory");
}
__device__ __forceinline__ void wait_lds_eq(LAS char* word, unsigned want) {
#pragma unroll 1
    for (unsigned spins = 0; spins < (1u << 22); ++spins) {
        if ((unsigned)__builtin_amdgcn_readfirstlane(__hip_atomic_load((LAS unsigned*)word, __ATOMIC_RELAXED, __HIP_MEMORY_SCOPE_WORKGROUP)) == want) break;
        __builtin_amdgcn_s_sleep(1);
    }
    asm volatile("" ::: "memory");
}
__device__ __forceinline__ void arrive_publish(LAS char* ctr, char* ws, unsigned off, u64 tag, int lane) {
    if (lane == 0) {
        const unsigned old = __hip_atomic_fetch_add((LAS unsigned*)ctr, 1u, __ATOMIC_RELAXED, __HIP_MEMORY_SCOPE_WORKGROUP);
        if (old == 3u) __hip_atomic_store((GAS u64*)(ws + off), tag, RLX_AGENT);
    }
}

#define STAMP(i)
#define STAMPG(i)
#define STAMPA(i)
#define STAMP_DECL()
#define STAMP_OUT(n)

__global__ __launch_bounds__(512, 2) void fused_kernel(Params p) {
    extern __shared__ __attribute__((aligned(16))) char smem_raw[];
    u64 pc;
    asm volatile("s_getpc_b64 %0" : "=s"(pc));
    LAS char* sm = (LAS char*)smem_raw;
    const int tid = threadIdx.x, lane = tid & 63;
    const int wave = __builtin_amdgcn_readfirstlane(tid >> 6);
    const int xcd = blockIdx.x & 7, idx = blockIdx.x >> 3;
    const __amdgpu_buffer_rsrc_t wsr = __builtin_amdgcn_make_buffer_rsrc(p.ws, 0, WS_END, 0x00020000);
    asm volatile("" :: "s"(p.x), "s"(p.k), "s"(p.v), "s"(p.sk), "s"(p.hk), "s"(p.dv), "s"(p.w1), "s"(p.w3), "s"(p.w2), "s"(p.out), "s"(p.ws));
    STAMP_DECL();
    STAMP(0);

    if (idx < 4) {
        unsigned pf = 0;
        if (idx == 0 && wave == 7) {
            const GAS char* code = (const GAS char*)(pc & ~(u64)255);
#pragma unroll
            for (unsigned i = 0; i < CODE_PREFETCH / 8192; ++i) pf ^= *(const GAS unsigned*)(code + i * 8192 + lane * 128);
        }
        const int ca = (xcd >> 1) * 4 + idx, cmat = ca >= 7, cnt = 7 * (xcd & 1) + (ca >= 7 ? ca - 7 : ca);
        const bool job = ca < 14;
        u32x4 xr[7], wr[14];
        const int row = (8 * (xcd >> 1) + 4 * (xcd & 1) + idx) * 64 + wave * 8 + (lane >> 3);
#pragma unroll
        for (int i = 0; i < 7; ++i) xr[i] = *(const u32x4*)(p.x + (size_t)row * D + i * 64 + (lane & 7) * 8);
        if (job) {
            asm volatile("s_barrier" ::: "memory");
            const u16* wp = (cmat ? p.w3 : p.w1) + (size_t)(wave * 4 + (lane >> 4)) * F + cnt * 128 + (lane & 15) * 8;
#pragma unroll
            for (int i = 0; i < 14; ++i) wr[i] = *(const u32x4*)(wp + (size_t)(32 * i) * F);
        }
        const unsigned pf2 = *(const unsigned*)((const char*)p.hk + (size_t)((xcd * 4 + idx) * 448 + wave * 56 + (lane < 56 ? lane : 0)) * 128);
        u64 epoch;
        asm volatile("s_load_dwordx2 %0, %1, 0x0\n\ts_waitcnt lgkmcnt(0)" : "=s"(epoch) : "s"(p.ws) : "memory");
        const u64 tag = epoch ^ TAG_SALT;
        const unsigned tag32 = (unsigned)tag;
        {
            float sum = 0.f;
#pragma unroll
            for (int i = 0; i < 7; ++i)
#pragma unroll
                for (int j = 0; j < 4; ++j) sum = dot2(xr[i][j], 0x3f803f80u, sum);
            sum += __shfl_xor(sum, 1); sum += __shfl_xor(sum, 2); sum += __shfl_xor(sum, 4);
            const float mean = sum * (1.f / D);
            float sq = 0.f;
#pragma unroll
            for (int i = 0; i < 7; ++i)
#pragma unroll
                for (int j = 0; j < 4; ++j) {
                    const float a = bflo(xr[i][j]) - mean, b = bfhi(xr[i][j]) - mean;
                    sq = fmaf(a, a, sq); sq = fmaf(b, b, sq);
                }
            sq += __shfl_xor(sq, 1); sq += __shfl_xor(sq, 2); sq += __shfl_xor(sq, 4);
            const u32x2 st = {tag32, pk(mean, __builtin_amdgcn_sqrtf(rbf(rbf(sq * (1.f / D)) + 1e-5f)))};
            __builtin_amdgcn_raw_buffer_store_b64(st, wsr, (lane & 7) == 0 ? WS_STATS + (unsigned)row * 8u : 0xfffffff0u, 0, 16);
        }
        STAMPA(2);
        if (job) {
            float c[8];
#pragma unroll
            for (int j = 0; j < 8; ++j) c[j] = 0.f;
#pragma unroll
            for (int i = 0; i < 14; ++i)
#pragma unroll
                for (int j = 0; j < 4; ++j) { c[2 * j] += bflo(wr[i][j]); c[2 * j + 1] += bfhi(wr[i][j]); }
            LAS char* cp = sm + ((wave * 4 + (lane >> 4)) * 136 + (lane & 15) * 8) * 4;
            *(LAS f32x4*)cp = (f32x4){c[0], c[1], c[2], c[3]};
            *(LAS f32x4*)(cp + 16) = (f32x4){c[4], c[5], c[6], c[7]};
        }
        STAMPA(3);
        if (job) {
            WAIT_BAR("lgkmcnt(0)");
            float t = 0.f;
#pragma unroll
            for (int i = 0; i < 8; ++i) t += *(LAS float*)(sm + ((i * 4 + (tid & 3)) * 136 + (tid >> 2)) * 4);
            t += __shfl_xor(t, 1); t += __shfl_xor(t, 2);
            __builtin_amdgcn_raw_buffer_store_b64((u32x2){tag32, __float_as_uint(t)}, wsr,
                (tid & 3) == 0 ? WS_CSUM + (unsigned)((cnt * 2 + cmat) * 128 + (tid >> 2)) * 8u : 0xfffffff0u, 0, 16);
        }
        STAMP(1);
        asm volatile("" :: "v"(pf), "v"(pf2));
        STAMP_OUT(4);
        if (blockIdx.x == 0 && wave == 7) {
            __builtin_amdgcn_s_sleep(127);
            GAS u64* gp = (GAS u64*)(p.ws + WS_STATS) + lane * 8;
            GAS u64* fp = (GAS u64*)(p.ws + WS_FH) + (lane >> 4) * 16 + ((lane & 15) < 14 ? (lane & 15) : 0);
#pragma unroll 1
            for (unsigned spins = 0; spins < SPIN_CAP; ++spins) {
                bool ok = true;
#pragma unroll
                for (int m = 0; m < 4; ++m)
                    ok &= ((unsigned)__hip_atomic_load(gp + m * 512, RLX_AGENT) == tag32) & (__hip_atomic_load(fp + m * 64, RLX_AGENT) == tag);
                if (__all(ok)) break;
                __builtin_amdgcn_s_sleep(32);
            }
            if (lane == 0) __hip_atomic_store((GAS u64*)(p.ws + WS_EPOCH), epoch + 1, RLX_AGENT);
        }
        return;
    }

    const int gi = idx - 4, ml = gi & 3, nl = gi >> 2;
    const int mt = 4 * (xcd >> 1) + ml, nt = 7 * (xcd & 1) + nl;
    const int rot1 = ROT1;
    const int m64 = 4 * xcd + ml, n64 = nl;
    const int gpair = xcd * 28 + gi;
    const int r32 = lane & 31, hh = lane >> 5, blk = (lane >> 4) & 1, q4 = (lane & 15) >> 2, p4 = lane & 3;
    const unsigned lb = (unsigned)(size_t)sm;
    const int wq = wave & 3;
    u32x4 wvo;
    wvo[0] = (unsigned)((lane >> 4) * F + (((lane & 15) ^ ((lane >> 4) << 2)) * 8)) * 2u;
#pragma unroll
    for (int j = 1; j < 4; ++j) wvo[j] = wvo[0] + j * (8 * F - 1024);
    const u16* w1b = sptr(p.w1, wq * 16 * F + nt * 128);
    const u16* w3b = sptr(p.w3, wq * 16 * F + nt * 128);
    auto kst = [&](int si) { if (si == 0) return 0; const int s = si + rot1; return s >= 7 ? s - 6 : s; };
    auto group_w = [&](int si, int m) { glds16x4(sptr(m ? w3b : w1b, kst(si) * 64 * F), wvo, lb + ring(si) + 16384 + m * 16384 + wq * 4096); };
    const int kr0 = (gpair & 31) * 64, kc0 = (gpair >> 5) * 64;
    const int chn = wq >> 1, par = wq & 1, dch = 2 * gpair + chn;
    if (wave < 4) group_w(0, 0);
    {
        const bool isk = lane >= 8 && lane < 14;
        u64 mb_ = vsel(lane == 15, p.dv, vsel(lane == 14, p.hk, vsel(isk, p.k, (u64)p.sk)));
        unsigned mo = (unsigned)((kc0 + wq * 16) * 8 + (lane & 7) * 16);
        mo = isk ? (unsigned)((kr0 ? kr0 - 3 + ((lane - 8) >> 1) : 0) * D + kc0 + wq * 16 + (lane & 1) * 8) * 2u : mo;
        mo = lane == 14 ? (unsigned)dch * 4096u : mo;
        mo = lane == 15 ? (unsigned)(dch & ~7) * 2u : mo;
        const u64 ma = mb_ + mo;
        asm volatile("s_mov_b32 m0, %1\n\ts_nop 0\n\tglobal_load_lds_dwordx4 %0, off" :: "v"(ma), "s"(lb + wave * 1024));
    }
    if (wave < 4) group_w(0, 1);
    SCHED();
    const int rot2 = ROT2;
    const int w2i = L_W2 + wq * 12288;
    auto fresh_lane = [&]() { int l = lane; asm volatile("" : "+v"(l)); return l; };
    const int latei = L_CONV + wq * 8192;
    auto pair_w2 = [&](u32x4 vo, int i, int dst) {
        int t = i + rot2; t = t >= 28 ? t - 28 : t;
        glds16x4(sptr(p.w2, (wq * 448 + t * 16) * D + n64 * 64), vo, lb + dst);
    };
    auto w2_off = [&]() {
        const int l = fresh_lane();
        const unsigned o = (unsigned)((l >> 3) * D + (((l & 7) ^ (((l >> 4) & 1) << 2)) * 8)) * 2u;
        return (u32x4){o, o + (16 * D - 1024), o + 2 * (16 * D - 1024), o + 3 * (16 * D - 1024)};
    };

    if (wave < 4) {
        u64 epoch;
        asm volatile("s_load_dwordx2 %0, %1, 0x0" : "=s"(epoch) : "s"(p.ws) : "memory");
        __builtin_amdgcn_s_setprio(MPRIO);
        const int wm = wave >> 1, wn = wave & 1;
        int xa[4], wa0, wa1;
#pragma unroll
        for (int i = 0; i < 4; ++i) xa[i] = lb + ring(0) + (((64 * wm + r32) * 128 + ((hh ^ ((r32 >> 1) & 7)) * 16)) ^ (i * 32));
        wa0 = 16384 + (8 * hh + q4) * 256 + (((8 * wn + 2 * blk + (p4 >> 1)) ^ (q4 << 2)) * 16) + 8 * (p4 & 1);
        wa1 = (wa0 ^ 64) + lb + ring(0); wa0 += lb + ring(0);
        f32x16 acc[2][2][2];
#pragma unroll
        for (int i = 0; i < 8; ++i)
#pragma unroll
            for (int r = 0; r < 16; ++r) acc[i >> 2][(i >> 1) & 1][i & 1][r] = 0.f;
        u32x4 fx[2][2], fw[2][2][2];
        auto load_frag = [&](int g, int j) {
            const int pp = g & 3, b = g & 1;
            if (j == 0) {
                if (g >= 4) {
                    const int d = ring(g >> 2) - ring((g >> 2) - 1);
                    if (pp == 0) { wa0 += d; wa1 += d; asm volatile("" : "+v"(wa0), "+v"(wa1)); }
                    xa[pp] += d; asm volatile("" : "+v"(xa[pp]));
                }
                fw[b][0][0] = ldstr2(lp(wa0) + pp * 4096, 1024);
            }
            if (j == 1) { fx[b][0] = lds128(lp(xa[pp])); fx[b][1] = lds128(lp(xa[pp]) + 4096); }
            if (j == 2) fw[b][0][1] = ldstr2(lp(wa0) + pp * 4096 + 16384, 1024);
            if (j == 3) fw[b][1][0] = ldstr2(lp(wa1) + pp * 4096, 1024);
            if (j == 4) fw[b][1][1] = ldstr2(lp(wa1) + pp * 4096 + 16384, 1024);
        };
        int ka0[2], kam[2], kwo;
#pragma unroll
        for (int rd = 0; rd < 2; ++rd) {
            const int ck = 2 * wq + rd;
            const int bq = 2 * (lane >> 4) + (p4 >> 1), bm = bq ? bq - 1 : 0;
            ka0[rd] = lb + L_KV + (8 * bq + q4) * 128 + ((ck ^ (((q4 >> 1) & 1) | ((bq & 3) << 1))) * 16) + 8 * (p4 & 1);
            kam[rd] = lb + L_KV + (8 * bm + 4 + q4) * 128 + ((ck ^ (((q4 >> 1) & 1) | ((bm & 3) << 1))) * 16) + 8 * (p4 & 1);
        }
        kwo = lb + L_KVO + wq * 2048 + (lane & 7) * 128 + (((7 - (lane >> 3)) ^ (lane & 7)) * 16);
        asm volatile("" : "+v"(ka0[0]), "+v"(ka0[1]), "+v"(kam[0]), "+v"(kam[1]), "+v"(kwo));
        WAIT_VM(4);
        u32x2 skv[2], hal[2];
#pragma unroll
        for (int rd = 0; rd < 2; ++rd) {
            LAS char* mp = sm + wave * 1024;
            skv[rd] = *(LAS u32x2*)(mp + (rd * 8 + (lane & 7)) * 8);
            const unsigned hm = kr0 ? 0xffffffffu : 0u;
            const unsigned a = *(LAS u16*)(mp + 128 + rd * 16 + (lane & 7) * 2), b = *(LAS u16*)(mp + 160 + rd * 16 + (lane & 7) * 2),
                           c = *(LAS u16*)(mp + 192 + rd * 16 + (lane & 7) * 2);
            hal[rd] = (u32x2){(a << 16) & hm, (b | (c << 16)) & hm};
            asm volatile("" : "+v"(skv[rd]), "+v"(hal[rd]));
        }
        asm volatile("s_waitcnt vmcnt(0) lgkmcnt(0)\n\ts_barrier" : "+s"(epoch) :: "memory");
        const u64 tag = epoch ^ TAG_SALT;
        STAMP(1);
        u32x2 kt[2][3], vt[2][2];
        float cf[4], kk[11], ko[8];
        u32x4 kpk;
        auto fill = [&](auto kc) {
            constexpr int k = decltype(kc)::value;
            if constexpr (k < 5) {
#pragma unroll
                for (int rd = 0; rd < 2; ++rd) {
                    if constexpr (k == 0) kt[rd][0] = ldstr(lp(kam[rd]));
                    else if constexpr (k < 3) kt[rd][k] = ldstr(lp(ka0[rd]) + (k - 1) * 512);
                    else vt[rd][k - 3] = ldstr(lp(ka0[rd]) + 8192 + (k - 3) * 512);
                }
            } else if constexpr (k >= 6 && k < 6 + 2 * 16) {
                constexpr int rd = (k - 6) / 16, s = (k - 6) % 16;
                if constexpr (s == 0) { cf[0] = bflo1(skv[rd][0]); cf[1] = bfhi1(skv[rd][0]); cf[2] = bflo1(skv[rd][1]); cf[3] = bfhi1(skv[rd][1]); }
                else if constexpr (s == 1) {
                    const unsigned a = lane < 8 ? hal[rd][0] : kt[rd][0][0], b = lane < 8 ? hal[rd][1] : kt[rd][0][1];
                    kk[0] = bfhi1(a); kk[1] = bflo1(b); kk[2] = bfhi1(b);
                } else if constexpr (s < 4) {
                    const u32x2 t = kt[rd][s - 1];
                    kk[4 * s - 5] = bflo1(t[0]); kk[4 * s - 4] = bfhi1(t[0]); kk[4 * s - 3] = bflo1(t[1]); kk[4 * s - 2] = bfhi1(t[1]);
                } else if constexpr (s < 12) {
                    constexpr int tp = (s - 4) >> 1, o0 = ((s - 4) & 1) * 4;
#pragma unroll
                    for (int i = o0; i < o0 + 4; ++i) {
                        if (tp == 0) ko[i] = mul1(kk[i], cf[3]);
                        else fmac1(ko[i], kk[i + tp], cf[3 - tp]);
                    }
                } else {
                    constexpr int j = s - 12;
                    const unsigned v = vt[rd][j >> 1][j & 1];
                    kpk[3 - j] = pk(mul1(ko[2 * j + 1], bfhi1(v)), mul1(ko[2 * j], bflo1(v)));
                    if constexpr (j == 3) *(LAS u32x4*)(lp(kwo) + rd * 1024) = kpk;
                }
            }
        };
#pragma unroll
        for (int j = 0; j < 5; ++j) load_frag(0, j);
        f32x4 cs[2][2][4];
        unsigned tch[4];
        unsigned tco = (unsigned)(gi * 4 + wq + (lane < 28 ? lane : 27) * 448) * 128u;
        asm volatile("" : "+v"(tco));
        static_for([&](auto gc) {
            constexpr int g = decltype(gc)::value, b = g & 1, pp = g & 3;
            SCHED();
            if (g == 11) STAMP(2);
            if (pp == 3 && g < 27) { STAMPG(2 + 2 * (g >> 2)); WAIT_BAR("lgkmcnt(0)"); STAMPG(3 + 2 * (g >> 2)); }
            if (g == 27) {
                WAIT_LGKM0();
                if (lane == 0) __hip_atomic_fetch_add((LAS unsigned*)(sm + L_CTL + 12), 1u, __ATOMIC_RELAXED, __HIP_MEMORY_SCOPE_WORKGROUP);
            }
            if (g == 11) STAMP(3);
            static_for([&](auto ic) {
                constexpr int i = decltype(ic)::value, ft = i >> 2, m = (i >> 1) & 1, sb = i & 1;
                acc[sb][ft][m] = mfma32(fw[b][ft][m], fx[b][sb], acc[sb][ft][m]);
                SCHED();
                if (g < 27 && i < 5) load_frag(g + 1, i);
                if constexpr (TOUCH && i == 4 && pp == 2 && g >= TOUCH0 && g < TOUCH0 + 16) {
                    u64 t;
                    asm volatile("s_mov_b64 %1, %3\n\tglobal_load_dword %0, %2, %1" : "=v"(tch[(g - TOUCH0) >> 2]), "=&s"(t)
                                 : "v"(tco), "s"((const char*)p.w2 + ((g - TOUCH0) >> 2) * (112 * 128)));
                }
                if constexpr (i >= 5 && g >= 7) fill(std::integral_constant<int, (g - 7) * 3 + i - 5>{});
                SCHED();
            }, std::make_integer_sequence<int, 8>{});
        }, std::make_integer_sequence<int, 28>{});
        STAMP(4);
        wait_lds_eq(sm + L_CTL + 64, 3u);
        unsigned sa[2];
#pragma unroll
        for (int i = 0; i < 8; ++i) cs[i >> 2][0][i & 3] = *(LAS f32x4*)(sm + L_CSUM + (64 * wn + 32 * (i >> 2) + 8 * (i & 3) + 4 * hh) * 4);
        sa[0] = *(LAS unsigned*)(sm + L_STAT + (64 * wm + r32) * 4);
        sa[1] = *(LAS unsigned*)(sm + L_STAT + (64 * wm + 32 + r32) * 4);
#pragma unroll
        for (int i = 0; i < 8; ++i) cs[i >> 2][1][i & 3] = *(LAS f32x4*)(sm + L_CSUM + 512 + (64 * wn + 32 * (i >> 2) + 8 * (i & 3) + 4 * hh) * 4);
        const int lw = fresh_lane();
        const int w2a0 = lb + w2i + (4 * (lw >> 5) + ((lw & 15) >> 2)) * 128 + (((2 * ((lw >> 4) & 1) + ((lw & 3) >> 1)) ^ (((lw >> 3) & 1) << 2)) * 16) + 8 * (lw & 1);
        const int w2a1 = w2a0 ^ 64;
        u32x4 w2r[12][2];
        auto park = [&](int h) {
#pragma unroll
            for (int i = 0; i < 6; ++i) {
                w2r[6 * h + i][0] = ldstr2(lp(w2a0) + h * L_SLOT + i * 2048, 1024);
                w2r[6 * h + i][1] = ldstr2(lp(w2a1) + h * L_SLOT + i * 2048, 1024);
            }
            asm volatile("" ::: "memory");
        };
#pragma unroll
        for (int sb = 0; sb < 2; ++sb) {
            const float ri = __builtin_amdgcn_rcpf(bfhi(sa[sb])) * -1.44269504088896341f, nm = -bflo(sa[sb]), d2 = bfhi(sa[sb]) * bfhi(sa[sb]);
            const f32x2 ri2 = {ri, ri}, nm2 = {nm, nm}, dd2 = {d2, d2};
#pragma unroll
            for (int ft = 0; ft < 2; ++ft) {
#pragma unroll
                for (int t2 = 0; t2 < 2; ++t2) {
                    f32x2 gu[4], ee[4];
#pragma unroll
                    for (int j = 0; j < 4; ++j) {
                        const int r = 8 * t2 + 2 * j;
                        const f32x2 g = __builtin_elementwise_fma(nm2, (f32x2){cs[ft][0][r >> 2][r & 3], cs[ft][0][r >> 2][(r & 3) + 1]}, (f32x2){acc[sb][ft][0][r], acc[sb][ft][0][r + 1]});
                        const f32x2 u = __builtin_elementwise_fma(nm2, (f32x2){cs[ft][1][r >> 2][r & 3], cs[ft][1][r >> 2][(r & 3) + 1]}, (f32x2){acc[sb][ft][1][r], acc[sb][ft][1][r + 1]});
                        const f32x2 t = g * ri2;
                        ee[j] = (f32x2){__builtin_amdgcn_exp2f(t[0]), __builtin_amdgcn_exp2f(t[1])};
                        gu[j] = g * u;
                    }
                    u32x4 st;
#pragma unroll
                    for (int j = 0; j < 4; ++j) {
                        const f32x2 dn = __builtin_elementwise_fma(ee[j], dd2, dd2);
                        const f32x2 hv = gu[j] * (f32x2){__builtin_amdgcn_rcpf(dn[0]), __builtin_amdgcn_rcpf(dn[1])};
                        st[j] = pk(hv[0], hv[1]);
                    }
                    const unsigned fi = (unsigned)((4 * mt + 2 * wm + sb) * 112 + 8 * nt + 4 * wn + 2 * ft + t2);
                    __builtin_amdgcn_raw_buffer_store_b128(st, wsr, WS_H + fi * 1024u + lane * 16u, 0, 16);
                }
            }
        }
        SCHED();
        STAMP(5);
        const u32x4 w2o = w2_off();
        wait_lds(sm + L_CTL + 112 + wq * 4);
        park(0);
        park(1);
        __hip_atomic_store((LAS unsigned*)(sm + L_CTL + 32 + wq * 4), 1u, __ATOMIC_RELAXED, __HIP_MEMORY_SCOPE_WORKGROUP);
        WAIT_VM(0);
        if (TOUCH) asm volatile("" :: "v"(tch[0]), "v"(tch[1]), "v"(tch[2]), "v"(tch[3]));
        arrive_publish(sm + L_CTL, p.ws, WS_FH + mt * 128 + nt * 8, tag, lane);
        STAMP(6);
        WAIT_LGKM0();
#pragma unroll
        for (int i = 0; i < 12; ++i) asm volatile("" : "+v"(w2r[i][0]), "+v"(w2r[i][1]));
#pragma unroll
        for (int i = 0; i < 3; ++i) pair_w2(w2o, 12 + 2 * i, w2i + i * 4096);
#pragma unroll
        for (int i = 0; i < 3; ++i) pair_w2(w2o, 18 + 2 * i, w2i + L_SLOT + i * 4096);
        STAMP(7);
        const unsigned need = ((2u << ((28 * wq + 27) >> 3)) - 1u) & ~((1u << ((28 * wq) >> 3)) - 1u);
        unsigned hpf[2] = {0u, 0u};
        if (wave == 0) {
            GAS u64* fp = (GAS u64*)(p.ws + WS_FH + (m64 >> 1) * 128) + (lw < 14 ? lw : 0);
#pragma unroll 1
            for (unsigned spins = 0; spins < SPIN_CAP; ++spins) {
                unsigned m = (unsigned)__ballot(__hip_atomic_load(fp, RLX_AGENT) == tag) & 0x3fffu;
                if (spins == SPIN_CAP - 1) m = 0x3fffu;
                __hip_atomic_store((LAS unsigned*)(sm + L_CTL + 4), m, __ATOMIC_RELAXED, __HIP_MEMORY_SCOPE_WORKGROUP);
                if (m == 0x3fffu) break;
                __builtin_amdgcn_s_sleep(POLL_SLEEP);
            }
            __builtin_amdgcn_fence(__ATOMIC_ACQUIRE, "wavefront");
        } else {
            WAIT_VM(0);
            int pp[2]; unsigned po[2];
#pragma unroll
            for (int k = 0; k < 2; ++k) {
                const int e = (wq - 1) * 90 + k * 45 + (lw < 45 ? lw : 44), pr = e / 19;
                int ln = nl * 19 + (e - pr * 19); ln = ln < 128 ? ln : 127;
                pp[k] = pr < 14 ? pr : 13;
                po[k] = WS_H + (unsigned)((2 * m64 + (ln >> 6)) * 112 + 8 * pp[k]) * 1024u + (unsigned)(ln & 63) * 128u;
            }
            unsigned done = 0;
#pragma unroll 1
            for (unsigned spins = 0; spins < (1u << 22); ++spins) {
                const unsigned m = __builtin_amdgcn_readfirstlane(__hip_atomic_load((LAS unsigned*)(sm + L_CTL + 4), __ATOMIC_RELAXED, __HIP_MEMORY_SCOPE_WORKGROUP));
                const unsigned fresh = m & ~done;
                if (HPF && fresh) {
#pragma unroll
                    for (int k = 0; k < 2; ++k) {
                        const unsigned o = ((fresh >> pp[k]) & 1u) ? po[k] : 0xfffffff0u;
                        asm volatile("buffer_load_dword %0, %1, %2, 0 offen sc1" : "+v"(hpf[k]) : "v"(o), "s"(wsr) : "memory");
                    }
                }
                done = m;
                if ((m & need) == need) break;
                __builtin_amdgcn_s_sleep(1);
            }
            asm volatile("" ::: "memory");
        }
        STAMP(11);
        const unsigned hb = WS_H + (unsigned)(2 * m64 * 112 + 28 * wq) * 1024u + lw * 16u;
        u32x4 hf[28][2];
        auto load_h = [&](int i, int j) {
            int t = i + rot2; t = t >= 28 ? t - 28 : t;
            hf[i][j] = __builtin_amdgcn_raw_buffer_load_b128(wsr, hb + j * (112u * 1024u), t * 1024, 16);
        };
        constexpr int HD = HDEPTH, HG = HGROW;
#pragma unroll
        for (int i = 0; i < HD; ++i) { load_h(i, 0); load_h(i, 1); }
        STAMP(14);
        f32x16 ac2[2][2];
#pragma unroll
        for (int i = 0; i < 4; ++i)
#pragma unroll
            for (int r = 0; r < 16; ++r) ac2[i >> 1][i & 1][r] = 0.f;
        u32x4 a0 = w2r[0][0], a1 = w2r[0][1];
        static_for([&](auto ic) {
            constexpr int i = decltype(ic)::value, n = i + 1;
            SCHED();
            if (i == 23) wait_lds(sm + L_CTL + 80 + wq * 4);
            u32x4 b0, b1;
            if constexpr (n < 12) { b0 = w2r[n][0]; b1 = w2r[n][1]; }
            else if constexpr (n < 28) {
                constexpr int so = n < 24 ? ((n - 12) / 6) * L_SLOT + ((n - 12) % 6) * 2048 : (n - 24) * 2048;
                const int lo = n < 24 ? 0 : latei - w2i;
                b0 = ldstr2(lp(w2a0 + lo) + so, 1024);
                b1 = ldstr2(lp(w2a1 + lo) + so, 1024);
            }
            SCHED();
            ac2[0][0] = mfma32(a0, hf[i][0], ac2[0][0]);
            SCHED();
            constexpr int l0 = i < HG ? HD + 2 * i : HD + HG + i;
            if constexpr (l0 < 28) load_h(l0, 0);
            SCHED();
            ac2[1][0] = mfma32(a1, hf[i][0], ac2[1][0]);
            SCHED();
            if constexpr (l0 < 28) load_h(l0, 1);
            SCHED();
            ac2[0][1] = mfma32(a0, hf[i][1], ac2[0][1]);
            SCHED();
            if constexpr (i < HG) load_h(l0 + 1, 0);
            SCHED();
            ac2[1][1] = mfma32(a1, hf[i][1], ac2[1][1]);
            SCHED();
            if constexpr (i < HG) load_h(l0 + 1, 1);
            a0 = b0; a1 = b1;
        }, std::make_integer_sequence<int, 28>{});
        SCHED();
        asm volatile("" :: "v"(hpf[0]), "v"(hpf[1]));
        STAMP(15);
        WAIT_LGKM0();
#pragma unroll
        for (int v = 0; v < 8; ++v) {
            LAS char* sl = sm + w2i + (v < 6 ? v * 2048 : L_SLOT + (v - 6) * 2048) + lw * 16;
            const f32x16& t = ac2[v >> 2][(v >> 1) & 1];
            const int r0 = 8 * (v & 1);
            *(LAS f32x4*)sl = (f32x4){t[r0], t[r0 + 1], t[r0 + 2], t[r0 + 3]};
            *(LAS f32x4*)(sl + 1024) = (f32x4){t[r0 + 4], t[r0 + 5], t[r0 + 6], t[r0 + 7]};
        }
        WAIT_BAR("lgkmcnt(0)");
        STAMP(16);
        STAMP(17);
        STAMP_OUT(18);
    } else {
        u64 epoch;
        asm volatile("s_load_dwordx2 %0, %1, 0x0" : "=s"(epoch) : "s"(p.ws) : "memory");
        u32x4 xvo;
#pragma unroll
        for (int j = 0; j < 4; ++j) xvo[j] = (unsigned)((8 * j + (lane >> 3)) * D + ((((lane & 7) ^ (lane >> 4)) ^ (4 * (j & 1))) * 8)) * 2u - 1024u * j;
        const u16* xb = sptr(p.x, (mt * 128 + wq * 32) * D);
        auto group_x = [&](int si) { glds16x4(sptr(xb, kst(si) * 64), xvo, lb + ring(si) + wq * 4096); };
        group_x(0);
        group_x(1); group_w(1, 0); group_w(1, 1);
        const int cbi = L_CONV + chn * C_CH;
        LAS char* cb = sm + cbi;
        unsigned kvo[2], uso;
#pragma unroll
        for (int rd = 0; rd < 2; ++rd) kvo[rd] = (unsigned)((lane >> 3) * D + (((lane & 7) ^ (((lane >> 4) & 1) | (((2 * wq + rd) & 3) << 1))) * 8)) * 2u;
        uso = WS_U + (unsigned)(kc0 + wq * 16 + (lane >> 3)) * 8192u + (unsigned)(1984 - kr0 + (lane & 7) * 8) * 2u;
        int kro = lb + L_KVO + wq * 2048 + (lane >> 3) * 128 + (((lane & 7) ^ (lane >> 3)) * 16);
        const int cc = 2 * par + ((lane >> 2) & 1);
        int zo = cbi + C_U + par * C_US + (248 + (lane < 36 ? lane : 35)) * 16;
        zo = lane < 8 ? cbi + cc * C_HS + (lane & 3) * 16 : zo;
        u32x4 w2o = w2_off();
        const unsigned fo = lane * 16u;
        unsigned ao = (wq < 2 ? WS_CSUM + (unsigned)(nt * 2 + wq) * 1024u : WS_STATS + mt * 1024u) + fo;
        asm volatile("" : "+v"(zo), "+v"(w2o), "+v"(kvo[0]), "+v"(kvo[1]), "+v"(uso), "+v"(kro), "+v"(ao));
        asm volatile("s_waitcnt vmcnt(16) lgkmcnt(0)" : "+s"(epoch) :: "memory");
        const unsigned tag32 = (unsigned)(epoch ^ TAG_SALT);
        u32x4 bval, ug[2] = {{tag32, 0u, tag32, 0u}, {tag32, 0u, tag32, 0u}};
        unsigned dbits = *(LAS u16*)(sm + wave * 1024 + 240 + (dch & 7) * 2);
        {
            const u32x4 h0 = lds128(sm + wave * 1024 + 224);
            const bool s1 = !(cc & 1), s2 = !(cc & 2);
            const unsigned a0 = s1 ? 0u : h0[0], a1 = s1 ? h0[0] : h0[1], a2 = s1 ? h0[1] : h0[2], a3 = s1 ? h0[2] : h0[3];
            const unsigned lm = (lane == 3 || lane == 7) ? 0xffffffffu : 0u;
            bval = (u32x4){(s2 ? 0u : a0) & lm, (s2 ? 0u : a1) & lm, (s2 ? a0 : a2) & lm, (s2 ? a1 : a3) & lm};
            asm volatile("" : "+v"(bval), "+v"(dbits), "+v"(ug[0]), "+v"(ug[1]));
        }
        auto kv_store = [&](int rd) {
            LAS unsigned* kp = (LAS unsigned*)(lp(kro) + rd * 1024);
            ug[0][1] = kp[0]; asm volatile("" ::: "memory"); ug[0][3] = kp[1]; asm volatile("" ::: "memory");
            ug[1][1] = kp[2]; asm volatile("" ::: "memory"); ug[1][3] = kp[3];
            __builtin_amdgcn_raw_buffer_store_b128(ug[0], wsr, uso, rd * 65536, 16);
            __builtin_amdgcn_raw_buffer_store_b128(ug[1], wsr, uso, rd * 65536 + 4096, 16);
        };
        WAIT_BAR("vmcnt(12) lgkmcnt(0)");
        STAMP(1);
        group_x(2); group_w(2, 0); group_w(2, 1);
#pragma unroll
        for (int j = 0; j < 2; ++j) {
            glds16(sptr(p.k, (kr0 + (2 * wq + j) * 8) * D + kc0), kvo[j], lb + L_KV + (2 * wq + j) * 1024);
            glds16(sptr(p.v, (kr0 + (2 * wq + j) * 8) * D + kc0), kvo[j], lb + L_KV + 8192 + (2 * wq + j) * 1024);
        }
        STAMPG(2); WAIT_BAR("vmcnt(16)"); STAMPG(3);
#pragma unroll
        for (int w = 1; w < 5; ++w) {
            group_x(w + 2); group_w(w + 2, 0); group_w(w + 2, 1);
            if (w == 3 && lane < 32) *(LAS unsigned*)(sm + L_CTL + lane * 4) = 0u;
            if (w == 4) kv_store(0);
            if (w == 2) STAMP(2);
            STAMPG(2 + 2 * w); WAIT_BAR("vmcnt(12) lgkmcnt(0)"); STAMPG(3 + 2 * w);
            if (w == 2) STAMP(3);
        }
#pragma unroll
        for (int i = 0; i < 3; ++i) pair_w2(w2o, 2 * i, w2i + i * 4096);
        STAMPG(12); WAIT_BAR("vmcnt(12)"); STAMPG(13);
        kv_store(1);
        asm volatile("" ::: "memory");
        if (lane == 0) __hip_atomic_fetch_add((LAS unsigned*)(sm + L_CTL + 12), 1u, __ATOMIC_RELAXED, __HIP_MEMORY_SCOPE_WORKGROUP);
        if (wq < 3) {
            u32x4 av;
#pragma unroll 1
            for (unsigned spins = 0; spins < SPIN_CAP; ++spins) {
                asm volatile("" ::: "memory");
                av = __builtin_amdgcn_raw_buffer_load_b128(wsr, ao, 0, 16);
                if (__all(((av[0] ^ tag32) | (av[2] ^ tag32)) == 0u)) break;
            }
            *(LAS u32x2*)(sm + L_CSUM + wq * 512 + lane * 8) = (u32x2){av[1], av[3]};
            asm volatile("" ::: "memory");
            if (lane == 0) __hip_atomic_fetch_add((LAS unsigned*)(sm + L_CTL + 64), 1u, __ATOMIC_RELAXED, __HIP_MEMORY_SCOPE_WORKGROUP);
        }
        STAMP(4);
#pragma unroll
        for (int i = 0; i < 3; ++i) pair_w2(w2o, 6 + 2 * i, w2i + L_SLOT + i * 4096);
        wait_lds_eq(sm + L_CTL + 12, 8u);
        STAMP(5);
        __builtin_amdgcn_s_setprio(2);
        const unsigned ubase = WS_U + (unsigned)dch * 8192u;
        glds16x2c(p.ws + ubase + par * 2048, fo, lb + cbi + C_U + par * 2048);
        glds16x2c(p.ws + ubase + 4096 + par * 2048, fo, lb + cbi + C_U + C_US + par * 2048);
        glds4c(p.ws + ubase + (1 - par) * 2048, (lane & 1) * 4u, lb + L_UNX + wq * 256);
        {
            const u32x4 fvo = {fo, fo, fo, lane == 63 ? 62u * 16u : fo};
#pragma unroll
            for (int c = 0; c < 2; ++c) glds16x4(sptr(p.hk, dch * S + 4 * par + 2 * c + 2), fvo, lb + cbi + (2 * par + c) * C_HS + 64);
        }
        *(LAS u32x4*)(sm + zo) = bval;
        WAIT_VM(13);
        __hip_atomic_store((LAS unsigned*)(sm + L_CTL + 112 + wq * 4), 1u, __ATOMIC_RELAXED, __HIP_MEMORY_SCOPE_WORKGROUP);
        WAIT_VM(8);
        {
            u32x4 ua[2], ub[2]; u32x2 un[2];
#pragma unroll
            for (int rd = 0; rd < 2; ++rd) {
                const int x8 = 128 * par + 64 * rd + lane;
                ua[rd] = lds128(cb + C_U + x8 * 16); ub[rd] = lds128(cb + C_U + C_US + x8 * 16);
                un[rd] = *(LAS u32x2*)((rd == 1 && lane == 63) ? sm + L_UNX + wq * 256 : cb + C_U + x8 * 16 + 16);
            }
#pragma unroll 1
            for (unsigned spins = 0; spins < SPIN_CAP; ++spins) {
                unsigned df = 0;
#pragma unroll
                for (int rd = 0; rd < 2; ++rd) df |= (ua[rd][0] ^ tag32) | (ua[rd][2] ^ tag32) | (ub[rd][0] ^ tag32) | (ub[rd][2] ^ tag32) | (un[rd][0] ^ tag32);
                if (__all(df == 0u)) break;
                __builtin_amdgcn_s_sleep(4);
                asm volatile("" ::: "memory");
#pragma unroll
                for (int rd = 0; rd < 2; ++rd) {
                    const int x8 = 128 * par + 64 * rd + lane;
                    const unsigned uo = ubase + (unsigned)x8 * 16u;
                    ua[rd] = __builtin_amdgcn_raw_buffer_load_b128(wsr, uo, 0, 16);
                    ub[rd] = __builtin_amdgcn_raw_buffer_load_b128(wsr, uo + 4096u, 0, 16);
                    un[rd] = __builtin_amdgcn_raw_buffer_load_b64(wsr, x8 == 255 ? uo : uo + 16u, 0, 16);
                }
                asm volatile("" : "+v"(ua[0]), "+v"(ub[0]), "+v"(un[0]), "+v"(ua[1]), "+v"(ub[1]), "+v"(un[1]));
            }
#pragma unroll
            for (int rd = 0; rd < 2; ++rd) {
                const int x8 = 128 * par + 64 * rd + lane;
                const unsigned nx = x8 == 255 ? 0u : un[rd][1];
                *(LAS u32x4*)(cb + C_U + x8 * 16) = (u32x4){__builtin_amdgcn_alignbit(ua[rd][3], ua[rd][1], 16), __builtin_amdgcn_alignbit(ub[rd][1], ua[rd][3], 16),
                                                            __builtin_amdgcn_alignbit(ub[rd][3], ub[rd][1], 16), __builtin_amdgcn_alignbit(nx, ub[rd][3], 16)};
                *(LAS u32x4*)(cb + C_U + C_US + x8 * 16) = (u32x4){ua[rd][1], ua[rd][3], ub[rd][1], ub[rd][3]};
            }
        }
        asm volatile("s_waitcnt vmcnt(0) lgkmcnt(0)" ::: "memory");
        __hip_atomic_store((LAS unsigned*)(sm + L_CTL + 16 + wq * 4), 1u, __ATOMIC_RELAXED, __HIP_MEMORY_SCOPE_WORKGROUP);
        u32x4 lw2[8], xv[2];
        {
            const __amdgpu_buffer_rsrc_t wr2 = __builtin_amdgcn_make_buffer_rsrc((void*)p.w2, 0, F * D * 2, 0x00020000);
#pragma unroll
            for (int j = 0; j < 8; ++j) {
                int t = 24 + (j >> 2) * 2 + rot2; t = t >= 28 ? t - 28 : t;
                lw2[j] = __builtin_amdgcn_raw_buffer_load_b128(wr2, w2o[0], ((wq * 448 + t * 16 + (j & 3) * 8) * D + n64 * 64) * 2, 0);
            }
        }
        const int lf = fresh_lane();
        const int srow = 64 * m64 + 32 * (wq & 1) + (lf & 31);
        const int dd = 64 * n64 + 32 * (wq >> 1) + 16 * (lf >> 5);
        const unsigned xoff = (unsigned)(srow * D + dd) * 2u;
        {
            const __amdgpu_buffer_rsrc_t xr = __builtin_amdgcn_make_buffer_rsrc((void*)p.x, 0, S * D * 2, 0x00020000);
            xv[0] = __builtin_amdgcn_raw_buffer_load_b128(xr, xoff, 0, 0);
            xv[1] = __builtin_amdgcn_raw_buffer_load_b128(xr, xoff, 16, 0);
        }
        SCHED();
        wait_lds(sm + L_CTL + 16 + (wq ^ 1) * 4);
        STAMP(7);
        STAMP(6);
        const int n16 = lane & 15, kg = lane >> 4;
        f32x4 ca[4];
#pragma unroll
        for (int j = 0; j < 4; ++j) ca[j] = (f32x4){0.f, 0.f, 0.f, 0.f};
        int apo = lb + cbi + (n16 & 3) * C_HS + ((n16 >> 2) + kg) * 16;
        int bpo = lb + cbi + C_U + (n16 & 1) * C_US + (252 - 4 * (n16 >> 1) + kg) * 16 - 7 * 512;
        u32x4 fa[2][8], fb[2][8];
#pragma unroll
        for (int i = 0; i < 8; ++i) { fa[0][i] = lds128(lp(apo) + i * 512); fb[0][i] = lds128(lp(bpo) + (7 - i) * 512); }
        const bool odd = lf & 1;
        int ysrc = (2 * (lf >> 3) + (lf & 1) + 16 * ((lf >> 1) & 3)) * 4;
        unsigned yso = WS_Y + (unsigned)dch * 8192u + lf * 16u;
        f32x4 kd[4];
        {
            const float dval = bflo(dbits);
            const int sl0 = 32 * ((lf & 15) >> 1) + 8 * (lf >> 4) + 4 * (lf & 1);
#pragma unroll
            for (int j = 0; j < 4; ++j) {
                const int tj = (j == 0 ? 0 : j == 1 ? 3 : j == 2 ? 4 : 7) ^ par;
                const u32x2 kvr = *(LAS u32x2*)(cb + C_U + C_US + (2044 - 256 * tj - sl0) * 2);
                const unsigned ka = pk(bfhi(kvr[1]) * dval, bflo(kvr[1]) * dval), kb = pk(bfhi(kvr[0]) * dval, bflo(kvr[0]) * dval);
                kd[j] = (f32x4){bflo(ka), bfhi(ka), bflo(kb), bfhi(kb)};
                asm volatile("" : "+v"(kd[j]));
            }
            asm volatile("" : "+v"(ysrc), "+v"(yso));
        }
        wait_lds(sm + L_CTL + 32 + wq * 4);
        STAMP(8);
        auto conv_all = [&](auto pc_) {
            constexpr int P = decltype(pc_)::value;
            static_for([&](auto rc) {
                constexpr int r = decltype(rc)::value, c = r & 1, n = c ^ 1;
                static_for([&](auto kc) {
                    constexpr int k = decltype(kc)::value;
                    constexpr int T[4] = {P, 3 - P, 4 + P, 7 - P};
                    constexpr int j = k <= T[0] ? 0 : k <= T[0] + T[1] + 1 ? 1 : k <= T[0] + T[1] + T[2] + 2 ? 2 : 3;
                    constexpr int pp = k - (j == 0 ? 0 : j == 1 ? T[0] + 1 : j == 2 ? T[0] + T[1] + 2 : T[0] + T[1] + T[2] + 3);
                    ca[j] = mfma16(fa[c][pp], fb[c][T[j] - pp], ca[j]);
                    SCHED();
                    if constexpr (r < 7 && k < 2 * (T[3] + 1)) {
                        if constexpr (k & 1) fb[n][k >> 1] = lds128(lp(bpo) + (r + 1) * 64 + (7 - (k >> 1)) * 512);
                        else fa[n][k >> 1] = lds128(lp(apo) + (r + 1) * 64 + (k >> 1) * 512);
                    }
                    SCHED();
                }, std::make_integer_sequence<int, 18>{});
                asm volatile("" : "+v"(apo), "+v"(bpo));
            }, std::make_integer_sequence<int, 8>{});
        };
        if (par) conv_all(std::integral_constant<int, 1>{}); else conv_all(std::integral_constant<int, 0>{});
        SCHED();
        STAMP(9);
        {
            float r0[4], r1[4];
#pragma unroll
            for (int j = 0; j < 4; ++j) { r0[j] = dpp_xor1(odd ? ca[j][0] : ca[j][2]); r1[j] = dpp_xor1(odd ? ca[j][1] : ca[j][3]); }
            unsigned rp[4][2];
#pragma unroll
            for (int j = 0; j < 4; ++j) {
                const unsigned ya = pk(odd ? r0[j] : ca[j][0], odd ? ca[j][2] : r0[j]), yb = pk(odd ? r1[j] : ca[j][1], odd ? ca[j][3] : r1[j]);
                rp[j][0] = __builtin_amdgcn_ds_bpermute(ysrc, pk(add1(bflo1(ya), kd[j][0]), add1(bfhi1(ya), kd[j][1])));
                rp[j][1] = __builtin_amdgcn_ds_bpermute(ysrc, pk(add1(bflo1(yb), kd[j][2]), add1(bfhi1(yb), kd[j][3])));
            }
#pragma unroll
            for (int j = 0; j < 4; ++j)
                __builtin_amdgcn_raw_buffer_store_b128((u32x4){vmov1(tag32), rp[j][0], vmov1(tag32), rp[j][1]}, wsr, yso,
                                                       ((j == 0 ? 0 : j == 1 ? 3 : j == 2 ? 4 : 7) ^ par) * 1024, 16);
        }
        STAMP(10);
        WAIT_LGKM0();
        if (lane == 0) __hip_atomic_fetch_add((LAS unsigned*)(sm + L_CTL + 8), 1u, __ATOMIC_RELAXED, __HIP_MEMORY_SCOPE_WORKGROUP);
#pragma unroll 1
        for (unsigned spins = 0; spins < (1u << 22); ++spins) {
            if (__builtin_amdgcn_readfirstlane(__hip_atomic_load((LAS unsigned*)(sm + L_CTL + 8), __ATOMIC_RELAXED, __HIP_MEMORY_SCOPE_WORKGROUP)) == 4u) break;
            __builtin_amdgcn_s_sleep(1);
        }
        asm volatile("" ::: "memory");
#pragma unroll
        for (int j = 0; j < 8; ++j) *(LAS u32x4*)(sm + latei + j * 1024 + lf * 16) = lw2[j];
        asm volatile("" ::: "memory");
        __hip_atomic_store((LAS unsigned*)(sm + L_CTL + 80 + wq * 4), 1u, __ATOMIC_RELAXED, __HIP_MEMORY_SCOPE_WORKGROUP);
        STAMP(11);
        __builtin_amdgcn_s_sleep(YSLEEP);
        u32x2 yv[16], stv;
        const int yl = L_CONV + 32768 + wq * 4096;
        const unsigned yg = (unsigned)(lf >> 3) * 8192u + (unsigned)(lf & 7) * 16u;
        const u32x4 yvo = {yg, yg + (65536u - 1024u), yg + 2 * (65536u - 1024u), yg + 3 * (65536u - 1024u)};
        const char* ybase = p.ws + WS_Y + (unsigned)(64 * n64 + 32 * (wq >> 1)) * 8192u + (unsigned)(64 * m64 + 32 * (wq & 1)) * 4u;
#pragma unroll 1
        for (unsigned spins = 0; spins < SPIN_CAP; ++spins) {
            glds16x4c(ybase, yvo, lb + yl);
            stv = __builtin_amdgcn_raw_buffer_load_b64(wsr, WS_STATS + (unsigned)srow * 8u, 0, 16);
            WAIT_VM(0);
#pragma unroll
            for (int i = 0; i < 16; ++i) yv[i] = *(LAS u32x2*)(sm + yl + ((lf >> 5) * 16 + i) * 128 + ((lf & 31) >> 1) * 8);
            unsigned df = stv[0] ^ tag32;
#pragma unroll
            for (int i = 0; i < 16; ++i) df |= yv[i][0] ^ tag32;
            if (__all(df == 0u)) break;
            __builtin_amdgcn_s_sleep(8);
        }
        STAMP(14);
        __builtin_amdgcn_s_setprio(0);
        f32x2 sf[8], xf[8];
        {
            const float nmb = -bflo(stv[1]);
            float ri = __builtin_amdgcn_rcpf(bfhi(stv[1]));
            asm volatile("s_nop 1" : "+v"(ri));
            const unsigned ysh = (srow & 1) ? 0u : 16u;
#pragma unroll
            for (int i = 0; i < 8; ++i) {
                const unsigned xd = xv[i >> 2][i & 3];
                xf[i] = (f32x2){bflo1(xd), bfhi1(xd)};
                const unsigned tp = pk(add1(xf[i][0], nmb), add1(xf[i][1], nmb));
                const unsigned np = pk(mul1(bflo1(tp), ri), mul1(bfhi1(tp), ri));
                sf[i] = (f32x2){mul1(bflo1(np), bfhi1(yv[2 * i][1] << ysh)), mul1(bfhi1(np), bfhi1(yv[2 * i + 1][1] << ysh))};
                asm volatile("" : "+v"(sf[i]), "+v"(xf[i]));
            }
        }
        const int rm = lb + L_W2 + (wq < 3 ? wq * 4096 : L_SLOT) + (lf >> 5) * 2048 + (lf & 31) * 16;
        STAMP(15);
        WAIT_BAR("lgkmcnt(0)");
        STAMP(16);
        f32x4 o[4];
#pragma unroll
        for (int j = 0; j < 4; ++j) {
            LAS char* rp = lp(rm) + j * 512;
            o[j] = (*(LAS f32x4*)rp + *(LAS f32x4*)(rp + 12288)) + (*(LAS f32x4*)(rp + 24576) + *(LAS f32x4*)(rp + 36864));
        }
        f32x2 so[8], ev[8];
#pragma unroll
        for (int i = 0; i < 8; ++i) {
            const f32x2 ov = {o[i >> 1][(i & 1) * 2], o[i >> 1][(i & 1) * 2 + 1]};
            const f32x2 t = ov * (f32x2){-1.44269504088896341f, -1.44269504088896341f};
            ev[i] = (f32x2){__builtin_amdgcn_exp2f(t[0]), __builtin_amdgcn_exp2f(t[1])};
            so[i] = sf[i] * ov;
        }
        SCHED();
        u32x4 st[2];
#pragma unroll
        for (int i = 0; i < 8; ++i) {
            const f32x2 dn = ev[i] + (f32x2){1.f, 1.f};
            const f32x2 r = __builtin_elementwise_fma(so[i], (f32x2){__builtin_amdgcn_rcpf(dn[0]), __builtin_amdgcn_rcpf(dn[1])}, xf[i]);
            st[i >> 2][i & 3] = pk(r[0], r[1]);
        }
        const __amdgpu_buffer_rsrc_t orr = __builtin_amdgcn_make_buffer_rsrc((void*)p.out, 0, S * D * 2, 0x00020000);
        __builtin_amdgcn_raw_buffer_store_b128(st[0], orr, xoff, 0, 0);
        __builtin_amdgcn_raw_buffer_store_b128(st[1], orr, xoff, 16, 0);
        STAMP(17);
        STAMP_OUT(18);
    }
}

extern "C" void kernel_launch(void* const* d_in, const int* in_sizes, int n_in,
                              void* d_out, int out_size, void* d_ws, size_t ws_size,
                              hipStream_t stream) {
    Params p;
    p.x = (const u16*)d_in[0]; p.k = (const u16*)d_in[1]; p.v = (const u16*)d_in[2];
    p.sk = (const u16*)d_in[3]; p.hk = (const u16*)d_in[4]; p.dv = (const u16*)d_in[5];
    p.w1 = (const u16*)d_in[7]; p.w3 = (const u16*)d_in[8]; p.w2 = (const u16*)d_in[9];
    p.out = (u16*)d_out; p.ws = (char*)d_ws;
    (void)hipFuncSetAttribute((const void*)fused_kernel, hipFuncAttributeMaxDynamicSharedMemorySize, L_TOTAL);
    fused_kernel<<<256, 512, L_TOTAL, stream>>>(p);
}
```

```cpp
#include <hip/hip_runtime.h>
#include <stdint.h>
#include <utility>

typedef unsigned short u16;
typedef unsigned long long u64;
typedef short s16x4 __attribute__((ext_vector_type(4)));
typedef short s16x8 __attribute__((ext_vector_type(8)));
typedef __bf16 bf16x2 __attribute__((ext_vector_type(2)));
typedef __bf16 bf16x8 __attribute__((ext_vector_type(8)));
typedef float f32x2 __attribute__((ext_vector_type(2)));
typedef float f32x4 __attribute__((ext_vector_type(4)));
typedef float f32x16 __attribute__((ext_vector_type(16)));
typedef unsigned u32x2 __attribute__((ext_vector_type(2)));
typedef unsigned u32x4 __attribute__((ext_vector_type(4)));
typedef unsigned u32x16 __attribute__((ext_vector_type(16)));
#define LAS __attribute__((address_space(3)))
#define GAS __attribute__((address_space(1)))
#define RLX_AGENT __ATOMIC_RELAXED, __HIP_MEMORY_SCOPE_AGENT

constexpr int S = 2048, D = 448, F = 1792;

constexpr unsigned WS_EPOCH = 0;
constexpr unsigned WS_FH = 1024;
constexpr unsigned WS_STATS = 8192;
constexpr unsigned WS_CSUM = 24576;
constexpr unsigned WS_U = 65536;
constexpr unsigned WS_Y = WS_U + D * S * 4;
constexpr unsigned WS_H = WS_Y + D * S * 4;
constexpr unsigned WS_END = WS_H + 64 * 112 * 1024;
constexpr unsigned WS_DBG = WS_END;

constexpr int L_KV = 0;
constexpr int L_CTL = 0;
constexpr int L_CSUM = 256;
constexpr int L_STAT = 1280;
constexpr int L_KVO = 8192;
constexpr int L_UNX = 6144;
constexpr int L_SLOT = 49152;
constexpr int L_CONV = 12288;
constexpr int C_CH = 26112, C_HS = 4160, C_U = 16640, C_US = 4640;
constexpr int L_W2 = 16384 + L_SLOT;
constexpr int L_TOTAL = 163840;
__host__ __device__ constexpr int ring(int stage) { return 16384 + (stage % 3) * L_SLOT; }
constexpr unsigned CODE_PREFETCH = 36864;
#define POLL_SLEEP 4
#define HDEPTH 6
#define ROT2 (8 * ml)
#define MPRIO 1
#define HPF 0
#define YSLEEP 16
#define TOUCH 1
#define TOUCH0 4
#define HGROW 4
#define ROT1 ((2 * ml + nl) % 6)

constexpr u64 TAG_SALT = 0x9E3779B97F4A7C15ull;
constexpr unsigned SPIN_CAP = 1u << 18;

struct Params {
    const u16* x; const u16* k; const u16* v; const u16* sk; const u16* hk; const u16* dv;
    const u16* w1; const u16* w3; const u16* w2; u16* out; char* ws;
};

__device__ __forceinline__ float bflo(unsigned p) { return __uint_as_float(p << 16); }
__device__ __forceinline__ float bfhi(unsigned p) { return __uint_as_float(p & 0xffff0000u); }
__device__ __forceinline__ float bflo1(unsigned p) { float r; asm("v_lshlrev_b32 %0, 16, %1" : "=v"(r) : "v"(p)); return r; }
__device__ __forceinline__ float bfhi1(unsigned p) { float r; asm("v_and_b32 %0, 0xffff0000, %1" : "=v"(r) : "v"(p)); return r; }
__device__ __forceinline__ float mul1(float a, float b) { float r; asm("v_mul_f32 %0, %1, %2" : "=v"(r) : "v"(a), "v"(b)); return r; }
__device__ __forceinline__ void fmac1(float& d, float a, float b) { asm("v_fmac_f32 %0, %1, %2" : "+v"(d) : "v"(a), "v"(b)); }
__device__ __forceinline__ float dpp_xor1(float v) {
    return __int_as_float(__builtin_amdgcn_update_dpp(0, __float_as_int(v), 0xB1, 0xf, 0xf, true));
}
__device__ __forceinline__ float add1(float a, float b) { float r; asm("v_add_f32 %0, %1, %2" : "=v"(r) : "v"(a), "v"(b)); return r; }
__device__ __forceinline__ unsigned vmov1(unsigned s) { unsigned r; asm volatile("v_mov_b32 %0, %1" : "=v"(r) : "s"(s)); return r; }
__device__ __forceinline__ unsigned pk(float a, float b) {
    f32x2 f = {a, b};
    return __builtin_bit_cast(unsigned, __builtin_convertvector(f, bf16x2));
}
__device__ __forceinline__ float rbf(float a) { return bflo(pk(a, a)); }
__device__ __forceinline__ float dot2(unsigned a, unsigned b, float c) {
    return __builtin_amdgcn_fdot2_f32_bf16(__builtin_bit_cast(bf16x2, a), __builtin_bit_cast(bf16x2, b), c, false);
}
__device__ __forceinline__ void glds16(const void* base, unsigned voff, unsigned ldst) {
    u64 t;
    asm volatile("s_mov_b32 m0, %3\n\ts_mov_b64 %0, %2\n\tglobal_load_lds_dwordx4 %1, %0" : "=&s"(t) : "v"(voff), "s"(base), "s"(ldst));
}
__device__ __forceinline__ void glds16x4(const void* base, u32x4 vo, unsigned ldst) {
    u64 t;
    asm volatile("s_mov_b32 m0, %6\n\ts_mov_b64 %0, %5\n\tglobal_load_lds_dwordx4 %1, %0\n\tglobal_load_lds_dwordx4 %2, %0 offset:1024\n\t"
                 "global_load_lds_dwordx4 %3, %0 offset:2048\n\tglobal_load_lds_dwordx4 %4, %0 offset:3072"
                 : "=&s"(t) : "v"(vo[0]), "v"(vo[1]), "v"(vo[2]), "v"(vo[3]), "s"(base), "s"(ldst));
}
__device__ __forceinline__ void glds16x4c(const void* base, u32x4 vo, unsigned ldst) {
    u64 t;
    asm volatile("s_mov_b32 m0, %6\n\ts_mov_b64 %0, %5\n\tglobal_load_lds_dwordx4 %1, %0 sc1\n\tglobal_load_lds_dwordx4 %2, %0 offset:1024 sc1\n\t"
                 "global_load_lds_dwordx4 %3, %0 offset:2048 sc1\n\tglobal_load_lds_dwordx4 %4, %0 offset:3072 sc1"
                 : "=&s"(t) : "v"(vo[0]), "v"(vo[1]), "v"(vo[2]), "v"(vo[3]), "s"(base), "s"(ldst) : "memory");
}
__device__ __forceinline__ void glds16x2c(const void* base, unsigned voff, unsigned ldst) {
    u64 t;
    asm volatile("s_mov_b32 m0, %3\n\ts_mov_b64 %0, %2\n\tglobal_load_lds_dwordx4 %1, %0 sc1\n\tglobal_load_lds_dwordx4 %1, %0 offset:1024 sc1"
                 : "=&s"(t) : "v"(voff), "s"(base), "s"(ldst));
}
__device__ __forceinline__ void glds16c(const void* base, unsigned voff, unsigned ldst) {
    u64 t;
    asm volatile("s_mov_b32 m0, %3\n\ts_mov_b64 %0, %2\n\tglobal_load_lds_dwordx4 %1, %0 sc1" : "=&s"(t) : "v"(voff), "s"(base), "s"(ldst));
}
__device__ __forceinline__ void glds4c(const void* base, unsigned voff, unsigned ldst) {
    u64 t;
    asm volatile("s_mov_b32 m0, %3\n\ts_mov_b64 %0, %2\n\tglobal_load_lds_dword %1, %0 sc1" : "=&s"(t) : "v"(voff), "s"(base), "s"(ldst));
}
__device__ __forceinline__ const u16* sptr(const u16* base, int off) {
    const u16* r = base + __builtin_amdgcn_readfirstlane(off);
    asm("" : "+s"(r));
    return r;
}
__device__ __forceinline__ u64 vsel(bool c, const void* a, u64 b) {
    u64 va = (u64)a;
    asm("" : "+v"(va), "+v"(b));
    return c ? va : b;
}
__device__ __forceinline__ LAS char* lp(int a) { return (LAS char*)(size_t)(unsigned)a; }
__device__ __forceinline__ u32x4 lds128(LAS char* p) { return *(LAS u32x4*)p; }
__device__ __forceinline__ u32x2 ldstr(LAS char* p) {
    return __builtin_bit_cast(u32x2, __builtin_amdgcn_ds_read_tr16_b64_v4i16((LAS s16x4*)p));
}
__device__ __forceinline__ u32x4 ldstr2(LAS char* p, int off) {
    u32x2 a = ldstr(p), b = ldstr(p + off);
    return (u32x4){a.x, a.y, b.x, b.y};
}
__device__ __forceinline__ f32x16 mfma32(u32x4 a, u32x4 b, f32x16 c) {
    return __builtin_amdgcn_mfma_f32_32x32x16_bf16(__builtin_bit_cast(bf16x8, a), __builtin_bit_cast(bf16x8, b), c, 0, 0, 0);
}
__device__ __forceinline__ f32x4 mfma16(u32x4 a, u32x4 b, f32x4 c) {
    return __builtin_amdgcn_mfma_f32_16x16x32_bf16(__builtin_bit_cast(bf16x8, a), __builtin_bit_cast(bf16x8, b), c, 0, 0, 0);
}
__device__ __forceinline__ float silu_f(float g) {
    return g * __builtin_amdgcn_rcpf(1.f + __builtin_amdgcn_exp2f(g * -1.44269504088896341f));
}
template <class Fn, int... I>
__device__ __forceinline__ void static_for(Fn&& fn, std::integer_sequence<int, I...>) { (fn(std::integral_constant<int, I>{}), ...); }
#define WAIT_VM(n) asm volatile("s_waitcnt vmcnt(" #n ")" ::: "memory")
#define WAIT_LGKM0() asm volatile("s_waitcnt lgkmcnt(0)" ::: "memory")
#define WAIT_BAR(w) asm volatile("s_waitcnt " w "\n\ts_barrier" ::: "memory")
#define SCHED() __builtin_amdgcn_sched_barrier(0)
#define CAS __attribute__((address_space(4)))
#define NSTAMP 18

__device__ __forceinline__ void wait_lds(LAS char* word) {
#pragma unroll 1
    for (unsigned spins = 0; spins < (SPIN_CAP << 4); ++spins) {
        if (__builtin_amdgcn_readfirstlane(__hip_atomic_load((LAS unsigned*)word, __ATOMIC_RELAXED, __HIP_MEMORY_SCOPE_WORKGROUP))) break;
        __builtin_amdgcn_s_sleep(1);
    }
    asm volatile("" ::: "memory");
}
__device__ __forceinline__ void wait_lds_eq(LAS char* word, unsigned want) {
#pragma unroll 1
    for (unsigned spins = 0; spins < (1u << 22); ++spins) {
        if ((unsigned)__builtin_amdgcn_readfirstlane(__hip_atomic_load((LAS unsigned*)word, __ATOMIC_RELAXED, __HIP_MEMORY_SCOPE_WORKGROUP)) == want) break;
        __builtin_amdgcn_s_sleep(1);
    }
    asm volatile("" ::: "memory");
}
__device__ __forceinline__ void arrive_publish(LAS char* ctr, char* ws, unsigned off, u64 tag, int lane) {
    if (lane == 0) {
        const unsigned old = __hip_atomic_fetch_add((LAS unsigned*)ctr, 1u, __ATOMIC_RELAXED, __HIP_MEMORY_SCOPE_WORKGROUP);
        if (old == 3u) __hip_atomic_store((GAS u64*)(ws + off), tag, RLX_AGENT);
    }
}

#define STAMP(i)
#define STAMPG(i)
#define STAMPA(i)
#define STAMP_DECL()
#define STAMP_OUT(n)

__global__ __launch_bounds__(512, 2) void fused_kernel(Params p) {
    extern __shared__ __attribute__((aligned(16))) char smem_raw[];
    u64 pc;
    asm volatile("s_getpc_b64 %0" : "=s"(pc));
    LAS char* sm = (LAS char*)smem_raw;
    const int tid = threadIdx.x, lane = tid & 63;
    const int wave = __builtin_amdgcn_readfirstlane(tid >> 6);
    const int xcd = blockIdx.x & 7, idx = blockIdx.x >> 3;
    if (idx == 0 && wave < 5) {
        const unsigned o = (unsigned)tid * 128u;
        asm volatile("s_mov_b32 m0, %2\n\ts_nop 0\n\tglobal_load_lds_dword %0, %1" :: "v"(o < CODE_PREFETCH ? o : 0u), "s"(pc & ~(u64)255),
                     "s"((unsigned)(size_t)sm + 32768u));
    }
    SCHED();
    const __amdgpu_buffer_rsrc_t wsr = __builtin_amdgcn_make_buffer_rsrc(p.ws, 0, WS_END, 0x00020000);
    asm volatile("" :: "s"(p.x), "s"(p.k), "s"(p.v), "s"(p.sk), "s"(p.hk), "s"(p.dv), "s"(p.w1), "s"(p.w3), "s"(p.w2), "s"(p.out), "s"(p.ws));
    STAMP_DECL();
    STAMP(0);

    if (idx < 4) {
        const int ca = (xcd >> 1) * 4 + idx, cmat = ca >= 7, cnt = 7 * (xcd & 1) + (ca >= 7 ? ca - 7 : ca);
        const bool job = ca < 14;
        u32x4 xr[7], wr[14];
        const int row = (8 * (xcd >> 1) + 4 * (xcd & 1) + idx) * 64 + wave * 8 + (lane >> 3);
#pragma unroll
        for (int i = 0; i < 7; ++i) xr[i] = *(const u32x4*)(p.x + (size_t)row * D + i * 64 + (lane & 7) * 8);
        if (job) {
            asm volatile("s_barrier" ::: "memory");
            const u16* wp = (cmat ? p.w3 : p.w1) + (size_t)(wave * 4 + (lane >> 4)) * F + cnt * 128 + (lane & 15) * 8;
#pragma unroll
            for (int i = 0; i < 14; ++i) wr[i] = *(const u32x4*)(wp + (size_t)(32 * i) * F);
        }
        const unsigned pf2 = *(const unsigned*)((const char*)p.hk + (size_t)((xcd * 4 + idx) * 448 + wave * 56 + (lane < 56 ? lane : 0)) * 128);
        u64 epoch;
        asm volatile("s_load_dwordx2 %0, %1, 0x0\n\ts_waitcnt lgkmcnt(0)" : "=s"(epoch) : "s"(p.ws) : "memory");
        const u64 tag = epoch ^ TAG_SALT;
        const unsigned tag32 = (unsigned)tag;
        {
            float sum = 0.f;
#pragma unroll
            for (int i = 0; i < 7; ++i)
#pragma unroll
                for (int j = 0; j < 4; ++j) sum = dot2(xr[i][j], 0x3f803f80u, sum);
            sum += __shfl_xor(sum, 1); sum += __shfl_xor(sum, 2); sum += __shfl_xor(sum, 4);
            const float mean = sum * (1.f / D);
            float sq = 0.f;
#pragma unroll
            for (int i = 0; i < 7; ++i)
#pragma unroll
                for (int j = 0; j < 4; ++j) {
                    const float a = bflo(xr[i][j]) - mean, b = bfhi(xr[i][j]) - mean;
                    sq = fmaf(a, a, sq); sq = fmaf(b, b, sq);
                }
            sq += __shfl_xor(sq, 1); sq += __shfl_xor(sq, 2); sq += __shfl_xor(sq, 4);
            const u32x2 st = {tag32, pk(mean, __builtin_amdgcn_sqrtf(rbf(rbf(sq * (1.f / D)) + 1e-5f)))};
            __builtin_amdgcn_raw_buffer_store_b64(st, wsr, (lane & 7) == 0 ? WS_STATS + (unsigned)row * 8u : 0xfffffff0u, 0, 16);
        }
        STAMPA(2);
        if (job) {
            float c[8];
#pragma unroll
            for (int j = 0; j < 8; ++j) c[j] = 0.f;
#pragma unroll
            for (int i = 0; i < 14; ++i)
#pragma unroll
                for (int j = 0; j < 4; ++j) { c[2 * j] += bflo(wr[i][j]); c[2 * j + 1] += bfhi(wr[i][j]); }
            LAS char* cp = sm + ((wave * 4 + (lane >> 4)) * 136 + (lane & 15) * 8) * 4;
            *(LAS f32x4*)cp = (f32x4){c[0], c[1], c[2], c[3]};
            *(LAS f32x4*)(cp + 16) = (f32x4){c[4], c[5], c[6], c[7]};
        }
        STAMPA(3);
        if (job) {
            WAIT_BAR("lgkmcnt(0)");
            float t = 0.f;
#pragma unroll
            for (int i = 0; i < 8; ++i) t += *(LAS float*)(sm + ((i * 4 + (tid & 3)) * 136 + (tid >> 2)) * 4);
            t += __shfl_xor(t, 1); t += __shfl_xor(t, 2);
            __builtin_amdgcn_raw_buffer_store_b64((u32x2){tag32, __float_as_uint(t)}, wsr,
                (tid & 3) == 0 ? WS_CSUM + (unsigned)((cnt * 2 + cmat) * 128 + (tid >> 2)) * 8u : 0xfffffff0u, 0, 16);
        }
        STAMP(1);
        asm volatile("" :: "v"(pf2));
        STAMP_OUT(4);
        if (blockIdx.x == 0 && wave == 7) {
            __builtin_amdgcn_s_sleep(127);
            GAS u64* gp = (GAS u64*)(p.ws + WS_STATS) + lane * 8;
            GAS u64* fp = (GAS u64*)(p.ws + WS_FH) + (lane >> 4) * 16 + ((lane & 15) < 14 ? (lane & 15) : 0);
#pragma unroll 1
            for (unsigned spins = 0; spins < SPIN_CAP; ++spins) {
                bool ok = true;
#pragma unroll
                for (int m = 0; m < 4; ++m)
                    ok &= ((unsigned)__hip_atomic_load(gp + m * 512, RLX_AGENT) == tag32) & (__hip_atomic_load(fp + m * 64, RLX_AGENT) == tag);
                if (__all(ok)) break;
                __builtin_amdgcn_s_sleep(32);
            }
            if (lane == 0) __hip_atomic_store((GAS u64*)(p.ws + WS_EPOCH), epoch + 1, RLX_AGENT);
        }
        return;
    }

    const int gi = idx - 4, ml = gi & 3, nl = gi >> 2;
    const int mt = 4 * (xcd >> 1) + ml, nt = 7 * (xcd & 1) + nl;
    const int rot1 = ROT1;
    const int m64 = 4 * xcd + ml, n64 = nl;
    const int gpair = xcd * 28 + gi;
    const int r32 = lane & 31, hh = lane >> 5, blk = (lane >> 4) & 1, q4 = (lane & 15) >> 2, p4 = lane & 3;
    const unsigned lb = (unsigned)(size_t)sm;
    const int wq = wave & 3;
    u32x4 wvo;
    wvo[0] = (unsigned)((lane >> 4) * F + (((lane & 15) ^ ((lane >> 4) << 2)) * 8)) * 2u;
#pragma unroll
    for (int j = 1; j < 4; ++j) wvo[j] = wvo[0] + j * (8 * F - 1024);
    const u16* w1b = sptr(p.w1, wq * 16 * F + nt * 128);
    const u16* w3b = sptr(p.w3, wq * 16 * F + nt * 128);
    auto kst = [&](int si) { if (si == 0) return 0; const int s = si + rot1; return s >= 7 ? s - 6 : s; };
    auto group_w = [&](int si, int m) { glds16x4(sptr(m ? w3b : w1b, kst(si) * 64 * F), wvo, lb + ring(si) + 16384 + m * 16384 + wq * 4096); };
    const int kr0 = (gpair & 31) * 64, kc0 = (gpair >> 5) * 64;
    const int chn = wq >> 1, par = wq & 1, dch = 2 * gpair + chn;
    if (wave < 4) group_w(0, 0);
    {
        const bool isk = lane >= 8 && lane < 14;
        u64 mb_ = vsel(lane == 15, p.dv, vsel(lane == 14, p.hk, vsel(isk, p.k, (u64)p.sk)));
        unsigned mo = (unsigned)((kc0 + wq * 16) * 8 + (lane & 7) * 16);
        mo = isk ? (unsigned)((kr0 ? kr0 - 3 + ((lane - 8) >> 1) : 0) * D + kc0 + wq * 16 + (lane & 1) * 8) * 2u : mo;
        mo = lane == 14 ? (unsigned)dch * 4096u : mo;
        mo = lane == 15 ? (unsigned)(dch & ~7) * 2u : mo;
        const u64 ma = mb_ + mo;
        asm volatile("s_mov_b32 m0, %1\n\ts_nop 0\n\tglobal_load_lds_dwordx4 %0, off" :: "v"(ma), "s"(lb + wave * 1024));
    }
    if (wave < 4) group_w(0, 1);
    SCHED();
    const int rot2 = ROT2;
    const int w2i = L_W2 + wq * 12288;
    auto fresh_lane = [&]() { int l = lane; asm volatile("" : "+v"(l)); return l; };
    const int latei = L_CONV + wq * 8192;
    auto pair_w2 = [&](u32x4 vo, int i, int dst) {
        int t = i + rot2; t = t >= 28 ? t - 28 : t;
        glds16x4(sptr(p.w2, (wq * 448 + t * 16) * D + n64 * 64), vo, lb + dst);
    };
    auto w2_off = [&]() {
        const int l = fresh_lane();
        const unsigned o = (unsigned)((l >> 3) * D + (((l & 7) ^ (((l >> 4) & 1) << 2)) * 8)) * 2u;
        return (u32x4){o, o + (16 * D - 1024), o + 2 * (16 * D - 1024), o + 3 * (16 * D - 1024)};
    };

    if (wave < 4) {
        u64 epoch;
        asm volatile("s_load_dwordx2 %0, %1, 0x0" : "=s"(epoch) : "s"(p.ws) : "memory");
        __builtin_amdgcn_s_setprio(MPRIO);
        const int wm = wave >> 1, wn = wave & 1;
        int xa[4], wa0, wa1;
#pragma unroll
        for (int i = 0; i < 4; ++i) xa[i] = lb + ring(0) + (((64 * wm + r32) * 128 + ((hh ^ ((r32 >> 1) & 7)) * 16)) ^ (i * 32));
        wa0 = 16384 + (8 * hh + q4) * 256 + (((8 * wn + 2 * blk + (p4 >> 1)) ^ (q4 << 2)) * 16) + 8 * (p4 & 1);
        wa1 = (wa0 ^ 64) + lb + ring(0); wa0 += lb + ring(0);
        f32x16 acc[2][2][2];
#pragma unroll
        for (int i = 0; i < 8; ++i)
#pragma unroll
            for (int r = 0; r < 16; ++r) acc[i >> 2][(i >> 1) & 1][i & 1][r] = 0.f;
        u32x4 fx[2][2], fw[2][2][2];
        auto load_frag = [&](int g, int j) {
            const int pp = g & 3, b = g & 1;
            if (j == 0) {
                if (g >= 4) {
                    const int d = ring(g >> 2) - ring((g >> 2) - 1);
                    if (pp == 0) { wa0 += d; wa1 += d; asm volatile("" : "+v"(wa0), "+v"(wa1)); }
                    xa[pp] += d; asm volatile("" : "+v"(xa[pp]));
                }
                fw[b][0][0] = ldstr2(lp(wa0) + pp * 4096, 1024);
            }
            if (j == 1) { fx[b][0] = lds128(lp(xa[pp])); fx[b][1] = lds128(lp(xa[pp]) + 4096); }
            if (j == 2) fw[b][0][1] = ldstr2(lp(wa0) + pp * 4096 + 16384, 1024);
            if (j == 3) fw[b][1][0] = ldstr2(lp(wa1) + pp * 4096, 1024);
            if (j == 4) fw[b][1][1] = ldstr2(lp(wa1) + pp * 4096 + 16384, 1024);
        };
        int ka0[2], kam[2], kwo;
#pragma unroll
        for (int rd = 0; rd < 2; ++rd) {
            const int ck = 2 * wq + rd;
            const int bq = 2 * (lane >> 4) + (p4 >> 1), bm = bq ? bq - 1 : 0;
            ka0[rd] = lb + L_KV + (8 * bq + q4) * 128 + ((ck ^ (((q4 >> 1) & 1) | ((bq & 3) << 1))) * 16) + 8 * (p4 & 1);
            kam[rd] = lb + L_KV + (8 * bm + 4 + q4) * 128 + ((ck ^ (((q4 >> 1) & 1) | ((bm & 3) << 1))) * 16) + 8 * (p4 & 1);
        }
        kwo = lb + L_KVO + wq * 2048 + (lane & 7) * 128 + (((7 - (lane >> 3)) ^ (lane & 7)) * 16);
        asm volatile("" : "+v"(ka0[0]), "+v"(ka0[1]), "+v"(kam[0]), "+v"(kam[1]), "+v"(kwo));
        WAIT_VM(4);
        u32x2 skv[2], hal[2];
#pragma unroll
        for (int rd = 0; rd < 2; ++rd) {
            LAS char* mp = sm + wave * 1024;
            skv[rd] = *(LAS u32x2*)(mp + (rd * 8 + (lane & 7)) * 8);
            const unsigned hm = kr0 ? 0xffffffffu : 0u;
            const unsigned a = *(LAS u16*)(mp + 128 + rd * 16 + (lane & 7) * 2), b = *(LAS u16*)(mp + 160 + rd * 16 + (lane & 7) * 2),
                           c = *(LAS u16*)(mp + 192 + rd * 16 + (lane & 7) * 2);
            hal[rd] = (u32x2){(a << 16) & hm, (b | (c << 16)) & hm};
            asm volatile("" : "+v"(skv[rd]), "+v"(hal[rd]));
        }
        asm volatile("s_waitcnt vmcnt(0) lgkmcnt(0)\n\ts_barrier" : "+s"(epoch) :: "memory");
        const u64 tag = epoch ^ TAG_SALT;
        STAMP(1);
        u32x2 kt[2][3], vt[2][2];
        float cf[4], kk[11], ko[8];
        u32x4 kpk;
        auto fill = [&](auto kc) {
            constexpr int k = decltype(kc)::value;
            if constexpr (k < 5) {
#pragma unroll
                for (int rd = 0; rd < 2; ++rd) {
                    if constexpr (k == 0) kt[rd][0] = ldstr(lp(kam[rd]));
                    else if constexpr (k < 3) kt[rd][k] = ldstr(lp(ka0[rd]) + (k - 1) * 512);
                    else vt[rd][k - 3] = ldstr(lp(ka0[rd]) + 8192 + (k - 3) * 512);
                }
            } else if constexpr (k >= 6 && k < 6 + 2 * 16) {
                constexpr int rd = (k - 6) / 16, s = (k - 6) % 16;
                if constexpr (s == 0) { cf[0] = bflo1(skv[rd][0]); cf[1] = bfhi1(skv[rd][0]); cf[2] = bflo1(skv[rd][1]); cf[3] = bfhi1(skv[rd][1]); }
                else if constexpr (s == 1) {
                    const unsigned a = lane < 8 ? hal[rd][0] : kt[rd][0][0], b = lane < 8 ? hal[rd][1] : kt[rd][0][1];
                    kk[0] = bfhi1(a); kk[1] = bflo1(b); kk[2] = bfhi1(b);
                } else if constexpr (s < 4) {
                    const u32x2 t = kt[rd][s - 1];
                    kk[4 * s - 5] = bflo1(t[0]); kk[4 * s - 4] = bfhi1(t[0]); kk[4 * s - 3] = bflo1(t[1]); kk[4 * s - 2] = bfhi1(t[1]);
                } else if constexpr (s < 12) {
                    constexpr int tp = (s - 4) >> 1, o0 = ((s - 4) & 1) * 4;
#pragma unroll
                    for (int i = o0; i < o0 + 4; ++i) {
                        if (tp == 0) ko[i] = mul1(kk[i], cf[3]);
                        else fmac1(ko[i], kk[i + tp], cf[3 - tp]);
                    }
                } else {
                    constexpr int j = s - 12;
                    const unsigned v = vt[rd][j >> 1][j & 1];
                    kpk[3 - j] = pk(mul1(ko[2 * j + 1], bfhi1(v)), mul1(ko[2 * j], bflo1(v)));
                    if constexpr (j == 3) *(LAS u32x4*)(lp(kwo) + rd * 1024) = kpk;
                }
            }
        };
#pragma unroll
        for (int j = 0; j < 5; ++j) load_frag(0, j);
        f32x4 cs[2][2][4];
        unsigned tch[4];
        unsigned tco = (unsigned)(gi * 4 + wq + (lane < 28 ? lane : 27) * 448) * 128u;
        asm volatile("" : "+v"(tco));
        static_for([&](auto gc) {
            constexpr int g = decltype(gc)::value, b = g & 1, pp = g & 3;
            SCHED();
            if (g == 11) STAMP(2);
            if (pp == 3 && g < 27) { STAMPG(2 + 2 * (g >> 2)); WAIT_BAR("lgkmcnt(0)"); STAMPG(3 + 2 * (g >> 2)); }
            if (g == 27) {
                WAIT_LGKM0();
                if (lane == 0) __hip_atomic_fetch_add((LAS unsigned*)(sm + L_CTL + 12), 1u, __ATOMIC_RELAXED, __HIP_MEMORY_SCOPE_WORKGROUP);
            }
            if (g == 11) STAMP(3);
            static_for([&](auto ic) {
                constexpr int i = decltype(ic)::value, ft = i >> 2, m = (i >> 1) & 1, sb = i & 1;
                acc[sb][ft][m] = mfma32(fw[b][ft][m], fx[b][sb], acc[sb][ft][m]);
                SCHED();
                if (g < 27 && i < 5) load_frag(g + 1, i);
                if constexpr (TOUCH && i == 4 && pp == 2 && g >= TOUCH0 && g < TOUCH0 + 16) {
                    u64 t;
                    asm volatile("s_mov_b64 %1, %3\n\tglobal_load_dword %0, %2, %1" : "=v"(tch[(g - TOUCH0) >> 2]), "=&s"(t)
                                 : "v"(tco), "s"((const char*)p.w2 + ((g - TOUCH0) >> 2) * (112 * 128)));
                }
                if constexpr (i >= 5 && g >= 7) fill(std::integral_constant<int, (g - 7) * 3 + i - 5>{});
                SCHED();
            }, std::make_integer_sequence<int, 8>{});
        }, std::make_integer_sequence<int, 28>{});
        STAMP(4);
        wait_lds_eq(sm + L_CTL + 64, 3u);
        unsigned sa[2];
#pragma unroll
        for (int i = 0; i < 8; ++i) cs[i >> 2][0][i & 3] = *(LAS f32x4*)(sm + L_CSUM + (64 * wn + 32 * (i >> 2) + 8 * (i & 3) + 4 * hh) * 4);
        sa[0] = *(LAS unsigned*)(sm + L_STAT + (64 * wm + r32) * 4);
        sa[1] = *(LAS unsigned*)(sm + L_STAT + (64 * wm + 32 + r32) * 4);
#pragma unroll
        for (int i = 0; i < 8; ++i) cs[i >> 2][1][i & 3] = *(LAS f32x4*)(sm + L_CSUM + 512 + (64 * wn + 32 * (i >> 2) + 8 * (i & 3) + 4 * hh) * 4);
        const int lw = fresh_lane();
        const int w2a0 = lb + w2i + (4 * (lw >> 5) + ((lw & 15) >> 2)) * 128 + (((2 * ((lw >> 4) & 1) + ((lw & 3) >> 1)) ^ (((lw >> 3) & 1) << 2)) * 16) + 8 * (lw & 1);
        const int w2a1 = w2a0 ^ 64;
        u32x4 w2r[12][2];
        auto park = [&](int h) {
#pragma unroll
            for (int i = 0; i < 6; ++i) {
                w2r[6 * h + i][0] = ldstr2(lp(w2a0) + h * L_SLOT + i * 2048, 1024);
                w2r[6 * h + i][1] = ldstr2(lp(w2a1) + h * L_SLOT + i * 2048, 1024);
            }
            asm volatile("" ::: "memory");
        };
#pragma unroll
        for (int sb = 0; sb < 2; ++sb) {
            const float ri = __builtin_amdgcn_rcpf(bfhi(sa[sb])) * -1.44269504088896341f, nm = -bflo(sa[sb]), d2 = bfhi(sa[sb]) * bfhi(sa[sb]);
            const f32x2 ri2 = {ri, ri}, nm2 = {nm, nm}, dd2 = {d2, d2};
#pragma unroll
            for (int ft = 0; ft < 2; ++ft) {
#pragma unroll
                for (int t2 = 0; t2 < 2; ++t2) {
                    f32x2 gu[4], ee[4];
#pragma unroll
                    for (int j = 0; j < 4; ++j) {
                        const int r = 8 * t2 + 2 * j;
                        const f32x2 g = __builtin_elementwise_fma(nm2, (f32x2){cs[ft][0][r >> 2][r & 3], cs[ft][0][r >> 2][(r & 3) + 1]}, (f32x2){acc[sb][ft][0][r], acc[sb][ft][0][r + 1]});
                        const f32x2 u = __builtin_elementwise_fma(nm2, (f32x2){cs[ft][1][r >> 2][r & 3], cs[ft][1][r >> 2][(r & 3) + 1]}, (f32x2){acc[sb][ft][1][r], acc[sb][ft][1][r + 1]});
                        const f32x2 t = g * ri2;
                        ee[j] = (f32x2){__builtin_amdgcn_exp2f(t[0]), __builtin_amdgcn_exp2f(t[1])};
                        gu[j] = g * u;
                    }
                    u32x4 st;
#pragma unroll
                    for (int j = 0; j < 4; ++j) {
                        const f32x2 dn = __builtin_elementwise_fma(ee[j], dd2, dd2);
                        const f32x2 hv = gu[j] * (f32x2){__builtin_amdgcn_rcpf(dn[0]), __builtin_amdgcn_rcpf(dn[1])};
                        st[j] = pk(hv[0], hv[1]);
                    }
                    const unsigned fi = (unsigned)((4 * mt + 2 * wm + sb) * 112 + 8 * nt + 4 * wn + 2 * ft + t2);
                    __builtin_amdgcn_raw_buffer_store_b128(st, wsr, WS_H + fi * 1024u + lane * 16u, 0, 16);
                }
            }
        }
        SCHED();
        STAMP(5);
        const u32x4 w2o = w2_off();
        wait_lds(sm + L_CTL + 112 + wq * 4);
        park(0);
        park(1);
        __hip_atomic_store((LAS unsigned*)(sm + L_CTL + 32 + wq * 4), 1u, __ATOMIC_RELAXED, __HIP_MEMORY_SCOPE_WORKGROUP);
        WAIT_VM(0);
        if (TOUCH) asm volatile("" :: "v"(tch[0]), "v"(tch[1]), "v"(tch[2]), "v"(tch[3]));
        arrive_publish(sm + L_CTL, p.ws, WS_FH + mt * 128 + nt * 8, tag, lane);
        STAMP(6);
        WAIT_LGKM0();
#pragma unroll
        for (int i = 0; i < 12; ++i) asm volatile("" : "+v"(w2r[i][0]), "+v"(w2r[i][1]));
#pragma unroll
        for (int i = 0; i < 3; ++i) pair_w2(w2o, 12 + 2 * i, w2i + i * 4096);
#pragma unroll
        for (int i = 0; i < 3; ++i) pair_w2(w2o, 18 + 2 * i, w2i + L_SLOT + i * 4096);
        STAMP(7);
        const unsigned need = ((2u << ((28 * wq + 27) >> 3)) - 1u) & ~((1u << ((28 * wq) >> 3)) - 1u);
        unsigned hpf[2] = {0u, 0u};
        if (wave == 0) {
            GAS u64* fp = (GAS u64*)(p.ws + WS_FH + (m64 >> 1) * 128) + (lw < 14 ? lw : 0);
#pragma unroll 1
            for (unsigned spins = 0; spins < SPIN_CAP; ++spins) {
                unsigned m = (unsigned)__ballot(__hip_atomic_load(fp, RLX_AGENT) == tag) & 0x3fffu;
                if (spins == SPIN_CAP - 1) m = 0x3fffu;
                __hip_atomic_store((LAS unsigned*)(sm + L_CTL + 4), m, __ATOMIC_RELAXED, __HIP_MEMORY_SCOPE_WORKGROUP);
                if (m == 0x3fffu) break;
                __builtin_amdgcn_s_sleep(POLL_SLEEP);
            }
            __builtin_amdgcn_fence(__ATOMIC_ACQUIRE, "wavefront");
        } else {
            WAIT_VM(0);
            int pp[2]; unsigned po[2];
#pragma unroll
            for (int k = 0; k < 2; ++k) {
                const int e = (wq - 1) * 90 + k * 45 + (lw < 45 ? lw : 44), pr = e / 19;
                int ln = nl * 19 + (e - pr * 19); ln = ln < 128 ? ln : 127;
                pp[k] = pr < 14 ? pr : 13;
                po[k] = WS_H + (unsigned)((2 * m64 + (ln >> 6)) * 112 + 8 * pp[k]) * 1024u + (unsigned)(ln & 63) * 128u;
            }
            unsigned done = 0;
#pragma unroll 1
            for (unsigned spins = 0; spins < (1u << 22); ++spins) {
                const unsigned m = __builtin_amdgcn_readfirstlane(__hip_atomic_load((LAS unsigned*)(sm + L_CTL + 4), __ATOMIC_RELAXED, __HIP_MEMORY_SCOPE_WORKGROUP));
                const unsigned fresh = m & ~done;
                if (HPF && fresh) {
#pragma unroll
                    for (int k = 0; k < 2; ++k) {
                        const unsigned o = ((fresh >> pp[k]) & 1u) ? po[k] : 0xfffffff0u;
                        asm volatile("buffer_load_dword %0, %1, %2, 0 offen sc1" : "+v"(hpf[k]) : "v"(o), "s"(wsr) : "memory");
                    }
                }
                done = m;
                if ((m & need) == need) break;
                __builtin_amdgcn_s_sleep(1);
            }
            asm volatile("" ::: "memory");
        }
        STAMP(11);
        const unsigned hb = WS_H + (unsigned)(2 * m64 * 112 + 28 * wq) * 1024u + lw * 16u;
        u32x4 hf[28][2];
        auto load_h = [&](int i, int j) {
            int t = i + rot2; t = t >= 28 ? t - 28 : t;
            hf[i][j] = __builtin_amdgcn_raw_buffer_load_b128(wsr, hb + j * (112u * 1024u), t * 1024, 16);
        };
        constexpr int HD = HDEPTH, HG = HGROW;
#pragma unroll
        for (int i = 0; i < HD; ++i) { load_h(i, 0); load_h(i, 1); }
        STAMP(14);
        f32x16 ac2[2][2];
#pragma unroll
        for (int i = 0; i < 4; ++i)
#pragma unroll
            for (int r = 0; r < 16; ++r) ac2[i >> 1][i & 1][r] = 0.f;
        u32x4 a0 = w2r[0][0], a1 = w2r[0][1];
        static_for([&](auto ic) {
            constexpr int i = decltype(ic)::value, n = i + 1;
            SCHED();
            if (i == 23) wait_lds(sm + L_CTL + 80 + wq * 4);
            u32x4 b0, b1;
            if constexpr (n < 12) { b0 = w2r[n][0]; b1 = w2r[n][1]; }
            else if constexpr (n < 28) {
                constexpr int so = n < 24 ? ((n - 12) / 6) * L_SLOT + ((n - 12) % 6) * 2048 : (n - 24) * 2048;
                const int lo = n < 24 ? 0 : latei - w2i;
                b0 = ldstr2(lp(w2a0 + lo) + so, 1024);
                b1 = ldstr2(lp(w2a1 + lo) + so, 1024);
            }
            SCHED();
            ac2[0][0] = mfma32(a0, hf[i][0], ac2[0][0]);
            SCHED();
            constexpr int l0 = i < HG ? HD + 2 * i : HD + HG + i;
            if constexpr (l0 < 28) load_h(l0, 0);
            SCHED();
            ac2[1][0] = mfma32(a1, hf[i][0], ac2[1][0]);
            SCHED();
            if constexpr (l0 < 28) load_h(l0, 1);
            SCHED();
            ac2[0][1] = mfma32(a0, hf[i][1], ac2[0][1]);
            SCHED();
            if constexpr (i < HG) load_h(l0 + 1, 0);
            SCHED();
            ac2[1][1] = mfma32(a1, hf[i][1], ac2[1][1]);
            SCHED();
            if constexpr (i < HG) load_h(l0 + 1, 1);
            a0 = b0; a1 = b1;
        }, std::make_integer_sequence<int, 28>{});
        SCHED();
        asm volatile("" :: "v"(hpf[0]), "v"(hpf[1]));
        STAMP(15);
        WAIT_LGKM0();
#pragma unroll
        for (int v = 0; v < 8; ++v) {
            LAS char* sl = sm + w2i + (v < 6 ? v * 2048 : L_SLOT + (v - 6) * 2048) + lw * 16;
            const f32x16& t = ac2[v >> 2][(v >> 1) & 1];
            const int r0 = 8 * (v & 1);
            *(LAS f32x4*)sl = (f32x4){t[r0], t[r0 + 1], t[r0 + 2], t[r0 + 3]};
            *(LAS f32x4*)(sl + 1024) = (f32x4){t[r0 + 4], t[r0 + 5], t[r0 + 6], t[r0 + 7]};
        }
        WAIT_BAR("lgkmcnt(0)");
        STAMP(16);
        STAMP(17);
        STAMP_OUT(18);
    } else {
        u64 epoch;
        asm volatile("s_load_dwordx2 %0, %1, 0x0" : "=s"(epoch) : "s"(p.ws) : "memory");
        u32x4 xvo;
#pragma unroll
        for (int j = 0; j < 4; ++j) xvo[j] = (unsigned)((8 * j + (lane >> 3)) * D + ((((lane & 7) ^ (lane >> 4)) ^ (4 * (j & 1))) * 8)) * 2u - 1024u * j;
        const u16* xb = sptr(p.x, (mt * 128 + wq * 32) * D);
        auto group_x = [&](int si) { glds16x4(sptr(xb, kst(si) * 64), xvo, lb + ring(si) + wq * 4096); };
        group_x(0);
        group_x(1); group_w(1, 0); group_w(1, 1);
        const int cbi = L_CONV + chn * C_CH;
        LAS char* cb = sm + cbi;
        unsigned kvo[2], uso;
#pragma unroll
        for (int rd = 0; rd < 2; ++rd) kvo[rd] = (unsigned)((lane >> 3) * D + (((lane & 7) ^ (((lane >> 4) & 1) | (((2 * wq + rd) & 3) << 1))) * 8)) * 2u;
        uso = WS_U + (unsigned)(kc0 + wq * 16 + (lane >> 3)) * 8192u + (unsigned)(1984 - kr0 + (lane & 7) * 8) * 2u;
        int kro = lb + L_KVO + wq * 2048 + (lane >> 3) * 128 + (((lane & 7) ^ (lane >> 3)) * 16);
        const int cc = 2 * par + ((lane >> 2) & 1);
        int zo = cbi + C_U + par * C_US + (248 + (lane < 36 ? lane : 35)) * 16;
        zo = lane < 8 ? cbi + cc * C_HS + (lane & 3) * 16 : zo;
        u32x4 w2o = w2_off();
        const unsigned fo = lane * 16u;
        unsigned ao = (wq < 2 ? WS_CSUM + (unsigned)(nt * 2 + wq) * 1024u : WS_STATS + mt * 1024u) + fo;
        asm volatile("" : "+v"(zo), "+v"(w2o), "+v"(kvo[0]), "+v"(kvo[1]), "+v"(uso), "+v"(kro), "+v"(ao));
        asm volatile("s_waitcnt vmcnt(16) lgkmcnt(0)" : "+s"(epoch) :: "memory");
        const unsigned tag32 = (unsigned)(epoch ^ TAG_SALT);
        u32x4 bval, ug[2] = {{tag32, 0u, tag32, 0u}, {tag32, 0u, tag32, 0u}};
        unsigned dbits = *(LAS u16*)(sm + wave * 1024 + 240 + (dch & 7) * 2);
        {
            const u32x4 h0 = lds128(sm + wave * 1024 + 224);
            const bool s1 = !(cc & 1), s2 = !(cc & 2);
            const unsigned a0 = s1 ? 0u : h0[0], a1 = s1 ? h0[0] : h0[1], a2 = s1 ? h0[1] : h0[2], a3 = s1 ? h0[2] : h0[3];
            const unsigned lm = (lane == 3 || lane == 7) ? 0xffffffffu : 0u;
            bval = (u32x4){(s2 ? 0u : a0) & lm, (s2 ? 0u : a1) & lm, (s2 ? a0 : a2) & lm, (s2 ? a1 : a3) & lm};
            asm volatile("" : "+v"(bval), "+v"(dbits), "+v"(ug[0]), "+v"(ug[1]));
        }
        auto kv_store = [&](int rd) {
            LAS unsigned* kp = (LAS unsigned*)(lp(kro) + rd * 1024);
            ug[0][1] = kp[0]; asm volatile("" ::: "memory"); ug[0][3] = kp[1]; asm volatile("" ::: "memory");
            ug[1][1] = kp[2]; asm volatile("" ::: "memory"); ug[1][3] = kp[3];
            __builtin_amdgcn_raw_buffer_store_b128(ug[0], wsr, uso, rd * 65536, 16);
            __builtin_amdgcn_raw_buffer_store_b128(ug[1], wsr, uso, rd * 65536 + 4096, 16);
        };
        WAIT_BAR("vmcnt(12) lgkmcnt(0)");
        STAMP(1);
        group_x(2); group_w(2, 0); group_w(2, 1);
#pragma unroll
        for (int j = 0; j < 2; ++j) {
            glds16(sptr(p.k, (kr0 + (2 * wq + j) * 8) * D + kc0), kvo[j], lb + L_KV + (2 * wq + j) * 1024);
            glds16(sptr(p.v, (kr0 + (2 * wq + j) * 8) * D + kc0), kvo[j], lb + L_KV + 8192 + (2 * wq + j) * 1024);
        }
        STAMPG(2); WAIT_BAR("vmcnt(16)"); STAMPG(3);
#pragma unroll
        for (int w = 1; w < 5; ++w) {
            group_x(w + 2); group_w(w + 2, 0); group_w(w + 2, 1);
            if (w == 3 && lane < 32) *(LAS unsigned*)(sm + L_CTL + lane * 4) = 0u;
            if (w == 4) kv_store(0);
            if (w == 2) STAMP(2);
            STAMPG(2 + 2 * w); WAIT_BAR("vmcnt(12) lgkmcnt(0)"); STAMPG(3 + 2 * w);
            if (w == 2) STAMP(3);
        }
#pragma unroll
        for (int i = 0; i < 3; ++i) pair_w2(w2o, 2 * i, w2i + i * 4096);
        STAMPG(12); WAIT_BAR("vmcnt(12)"); STAMPG(13);
        kv_store(1);
        asm volatile("" ::: "memory");
        if (lane == 0) __hip_atomic_fetch_add((LAS unsigned*)(sm + L_CTL + 12), 1u, __ATOMIC_RELAXED, __HIP_MEMORY_SCOPE_WORKGROUP);
        if (wq < 3) {
            u32x4 av;
#pragma unroll 1
            for (unsigned spins = 0; spins < SPIN_CAP; ++spins) {
                asm volatile("" ::: "memory");
                av = __builtin_amdgcn_raw_buffer_load_b128(wsr, ao, 0, 16);
                if (__all(((av[0] ^ tag32) | (av[2] ^ tag32)) == 0u)) break;
            }
            *(LAS u32x2*)(sm + L_CSUM + wq * 512 + lane * 8) = (u32x2){av[1], av[3]};
            asm volatile("" ::: "memory");
            if (lane == 0) __hip_atomic_fetch_add((LAS unsigned*)(sm + L_CTL + 64), 1u, __ATOMIC_RELAXED, __HIP_MEMORY_SCOPE_WORKGROUP);
        }
        STAMP(4);
#pragma unroll
        for (int i = 0; i < 3; ++i) pair_w2(w2o, 6 + 2 * i, w2i + L_SLOT + i * 4096);
        wait_lds_eq(sm + L_CTL + 12, 8u);
        STAMP(5);
        __builtin_amdgcn_s_setprio(2);
        const unsigned ubase = WS_U + (unsigned)dch * 8192u;
        glds16x2c(p.ws + ubase + par * 2048, fo, lb + cbi + C_U + par * 2048);
        glds16x2c(p.ws + ubase + 4096 + par * 2048, fo, lb + cbi + C_U + C_US + par * 2048);
        glds4c(p.ws + ubase + (1 - par) * 2048, (lane & 1) * 4u, lb + L_UNX + wq * 256);
        {
            const u32x4 fvo = {fo, fo, fo, lane == 63 ? 62u * 16u : fo};
#pragma unroll
            for (int c = 0; c < 2; ++c) glds16x4(sptr(p.hk, dch * S + 4 * par + 2 * c + 2), fvo, lb + cbi + (2 * par + c) * C_HS + 64);
        }
        *(LAS u32x4*)(sm + zo) = bval;
        WAIT_VM(13);
        __hip_atomic_store((LAS unsigned*)(sm + L_CTL + 112 + wq * 4), 1u, __ATOMIC_RELAXED, __HIP_MEMORY_SCOPE_WORKGROUP);
        WAIT_VM(8);
        {
            u32x4 ua[2], ub[2]; u32x2 un[2];
#pragma unroll
            for (int rd = 0; rd < 2; ++rd) {
                const int x8 = 128 * par + 64 * rd + lane;
                ua[rd] = lds128(cb + C_U + x8 * 16); ub[rd] = lds128(cb + C_U + C_US + x8 * 16);
                un[rd] = *(LAS u32x2*)((rd == 1 && lane == 63) ? sm + L_UNX + wq * 256 : cb + C_U + x8 * 16 + 16);
            }
#pragma unroll 1
            for (unsigned spins = 0; spins < SPIN_CAP; ++spins) {
                unsigned df = 0;
#pragma unroll
                for (int rd = 0; rd < 2; ++rd) df |= (ua[rd][0] ^ tag32) | (ua[rd][2] ^ tag32) | (ub[rd][0] ^ tag32) | (ub[rd][2] ^ tag32) | (un[rd][0] ^ tag32);
                if (__all(df == 0u)) break;
                __builtin_amdgcn_s_sleep(4);
                asm volatile("" ::: "memory");
#pragma unroll
                for (int rd = 0; rd < 2; ++rd) {
                    const int x8 = 128 * par + 64 * rd + lane;
                    const unsigned uo = ubase + (unsigned)x8 * 16u;
                    ua[rd] = __builtin_amdgcn_raw_buffer_load_b128(wsr, uo, 0, 16);
                    ub[rd] = __builtin_amdgcn_raw_buffer_load_b128(wsr, uo + 4096u, 0, 16);
                    un[rd] = __builtin_amdgcn_raw_buffer_load_b64(wsr, x8 == 255 ? uo : uo + 16u, 0, 16);
                }
                asm volatile("" : "+v"(ua[0]), "+v"(ub[0]), "+v"(un[0]), "+v"(ua[1]), "+v"(ub[1]), "+v"(un[1]));
            }
#pragma unroll
            for (int rd = 0; rd < 2; ++rd) {
                const int x8 = 128 * par + 64 * rd + lane;
                const unsigned nx = x8 == 255 ? 0u : un[rd][1];
                *(LAS u32x4*)(cb + C_U + x8 * 16) = (u32x4){__builtin_amdgcn_alignbit(ua[rd][3], ua[rd][1], 16), __builtin_amdgcn_alignbit(ub[rd][1], ua[rd][3], 16),
                                                            __builtin_amdgcn_alignbit(ub[rd][3], ub[rd][1], 16), __builtin_amdgcn_alignbit(nx, ub[rd][3], 16)};
                *(LAS u32x4*)(cb + C_U + C_US + x8 * 16) = (u32x4){ua[rd][1], ua[rd][3], ub[rd][1], ub[rd][3]};
            }
        }
        asm volatile("s_waitcnt vmcnt(0) lgkmcnt(0)" ::: "memory");
        __hip_atomic_store((LAS unsigned*)(sm + L_CTL + 16 + wq * 4), 1u, __ATOMIC_RELAXED, __HIP_MEMORY_SCOPE_WORKGROUP);
        u32x4 lw2[8], xv[2];
        {
            const __amdgpu_buffer_rsrc_t wr2 = __builtin_amdgcn_make_buffer_rsrc((void*)p.w2, 0, F * D * 2, 0x00020000);
#pragma unroll
            for (int j = 0; j < 8; ++j) {
                int t = 24 + (j >> 2) * 2 + rot2; t = t >= 28 ? t - 28 : t;
                lw2[j] = __builtin_amdgcn_raw_buffer_load_b128(wr2, w2o[0], ((wq * 448 + t * 16 + (j & 3) * 8) * D + n64 * 64) * 2, 0);
            }
        }
        const int lf = fresh_lane();
        const int srow = 64 * m64 + 32 * (wq & 1) + (lf & 31);
        const int dd = 64 * n64 + 32 * (wq >> 1) + 16 * (lf >> 5);
        const unsigned xoff = (unsigned)(srow * D + dd) * 2u;
        {
            const __amdgpu_buffer_rsrc_t xr = __builtin_amdgcn_make_buffer_rsrc((void*)p.x, 0, S * D * 2, 0x00020000);
            xv[0] = __builtin_amdgcn_raw_buffer_load_b128(xr, xoff, 0, 0);
            xv[1] = __builtin_amdgcn_raw_buffer_load_b128(xr, xoff, 16, 0);
        }
        SCHED();
        wait_lds(sm + L_CTL + 16 + (wq ^ 1) * 4);
        STAMP(7);
        STAMP(6);
        const int n16 = lane & 15, kg = lane >> 4;
        f32x4 ca[4];
#pragma unroll
        for (int j = 0; j < 4; ++j) ca[j] = (f32x4){0.f, 0.f, 0.f, 0.f};
        int apo = lb + cbi + (n16 & 3) * C_HS + ((n16 >> 2) + kg) * 16;
        int bpo = lb + cbi + C_U + (n16 & 1) * C_US + (252 - 4 * (n16 >> 1) + kg) * 16 - 7 * 512;
        u32x4 fa[2][8], fb[2][8];
#pragma unroll
        for (int i = 0; i < 8; ++i) { fa[0][i] = lds128(lp(apo) + i * 512); fb[0][i] = lds128(lp(bpo) + (7 - i) * 512); }
        const bool odd = lf & 1;
        int ysrc = (2 * (lf >> 3) + (lf & 1) + 16 * ((lf >> 1) & 3)) * 4;
        unsigned yso = WS_Y + (unsigned)dch * 8192u + lf * 16u;
        f32x4 kd[4];
        {
            const float dval = bflo(dbits);
            const int sl0 = 32 * ((lf & 15) >> 1) + 8 * (lf >> 4) + 4 * (lf & 1);
#pragma unroll
            for (int j = 0; j < 4; ++j) {
                const int tj = (j == 0 ? 0 : j == 1 ? 3 : j == 2 ? 4 : 7) ^ par;
                const u32x2 kvr = *(LAS u32x2*)(cb + C_U + C_US + (2044 - 256 * tj - sl0) * 2);
                const unsigned ka = pk(bfhi(kvr[1]) * dval, bflo(kvr[1]) * dval), kb = pk(bfhi(kvr[0]) * dval, bflo(kvr[0]) * dval);
                kd[j] = (f32x4){bflo(ka), bfhi(ka), bflo(kb), bfhi(kb)};
                asm volatile("" : "+v"(kd[j]));
            }
            asm volatile("" : "+v"(ysrc), "+v"(yso));
        }
        wait_lds(sm + L_CTL + 32 + wq * 4);
        STAMP(8);
        auto conv_all = [&](auto pc_) {
            constexpr int P = decltype(pc_)::value;
            static_for([&](auto rc) {
                constexpr int r = decltype(rc)::value, c = r & 1, n = c ^ 1;
                static_for([&](auto kc) {
                    constexpr int k = decltype(kc)::value;
                    constexpr int T[4] = {P, 3 - P, 4 + P, 7 - P};
                    constexpr int j = k <= T[0] ? 0 : k <= T[0] + T[1] + 1 ? 1 : k <= T[0] + T[1] + T[2] + 2 ? 2 : 3;
                    constexpr int pp = k - (j == 0 ? 0 : j == 1 ? T[0] + 1 : j == 2 ? T[0] + T[1] + 2 : T[0] + T[1] + T[2] + 3);
                    ca[j] = mfma16(fa[c][pp], fb[c][T[j] - pp], ca[j]);
                    SCHED();
                    if constexpr (r < 7 && k < 2 * (T[3] + 1)) {
                        if constexpr (k & 1) fb[n][k >> 1] = lds128(lp(bpo) + (r + 1) * 64 + (7 - (k >> 1)) * 512);
                        else fa[n][k >> 1] = lds128(lp(apo) + (r + 1) * 64 + (k >> 1) * 512);
                    }
                    SCHED();
                }, std::make_integer_sequence<int, 18>{});
                asm volatile("" : "+v"(apo), "+v"(bpo));
            }, std::make_integer_sequence<int, 8>{});
        };
        if (par) conv_all(std::integral_constant<int, 1>{}); else conv_all(std::integral_constant<int, 0>{});
        SCHED();
        STAMP(9);
        {
            float r0[4], r1[4];
#pragma unroll
            for (int j = 0; j < 4; ++j) { r0[j] = dpp_xor1(odd ? ca[j][0] : ca[j][2]); r1[j] = dpp_xor1(odd ? ca[j][1] : ca[j][3]); }
            unsigned rp[4][2];
#pragma unroll
            for (int j = 0; j < 4; ++j) {
                const unsigned ya = pk(odd ? r0[j] : ca[j][0], odd ? ca[j][2] : r0[j]), yb = pk(odd ? r1[j] : ca[j][1], odd ? ca[j][3] : r1[j]);
                rp[j][0] = __builtin_amdgcn_ds_bpermute(ysrc, pk(add1(bflo1(ya), kd[j][0]), add1(bfhi1(ya), kd[j][1])));
                rp[j][1] = __builtin_amdgcn_ds_bpermute(ysrc, pk(add1(bflo1(yb), kd[j][2]), add1(bfhi1(yb), kd[j][3])));
            }
#pragma unroll
            for (int j = 0; j < 4; ++j)
                __builtin_amdgcn_raw_buffer_store_b128((u32x4){vmov1(tag32), rp[j][0], vmov1(tag32), rp[j][1]}, wsr, yso,
                                                       ((j == 0 ? 0 : j == 1 ? 3 : j == 2 ? 4 : 7) ^ par) * 1024, 16);
        }
        STAMP(10);
        WAIT_LGKM0();
        if (lane == 0) __hip_atomic_fetch_add((LAS unsigned*)(sm + L_CTL + 8), 1u, __ATOMIC_RELAXED, __HIP_MEMORY_SCOPE_WORKGROUP);
#pragma unroll 1
        for (unsigned spins = 0; spins < (1u << 22); ++spins) {
            if (__builtin_amdgcn_readfirstlane(__hip_atomic_load((LAS unsigned*)(sm + L_CTL + 8), __ATOMIC_RELAXED, __HIP_MEMORY_SCOPE_WORKGROUP)) == 4u) break;
            __builtin_amdgcn_s_sleep(1);
        }
        asm volatile("" ::: "memory");
#pragma unroll
        for (int j = 0; j < 8; ++j) *(LAS u32x4*)(sm + latei + j * 1024 + lf * 16) = lw2[j];
        asm volatile("" ::: "memory");
        __hip_atomic_store((LAS unsigned*)(sm + L_CTL + 80 + wq * 4), 1u, __ATOMIC_RELAXED, __HIP_MEMORY_SCOPE_WORKGROUP);
        STAMP(11);
        __builtin_amdgcn_s_sleep(YSLEEP);
        u32x2 yv[16], stv;
        const int yl = L_CONV + 32768 + wq * 4096;
        const unsigned yg = (unsigned)(lf >> 3) * 8192u + (unsigned)(lf & 7) * 16u;
        const u32x4 yvo = {yg, yg + (65536u - 1024u), yg + 2 * (65536u - 1024u), yg + 3 * (65536u - 1024u)};
        const char* ybase = p.ws + WS_Y + (unsigned)(64 * n64 + 32 * (wq >> 1)) * 8192u + (unsigned)(64 * m64 + 32 * (wq & 1)) * 4u;
#pragma unroll 1
        for (unsigned spins = 0; spins < SPIN_CAP; ++spins) {
            glds16x4c(ybase, yvo, lb + yl);
            stv = __builtin_amdgcn_raw_buffer_load_b64(wsr, WS_STATS + (unsigned)srow * 8u, 0, 16);
            WAIT_VM(0);
#pragma unroll
            for (int i = 0; i < 16; ++i) yv[i] = *(LAS u32x2*)(sm + yl + ((lf >> 5) * 16 + i) * 128 + ((lf & 31) >> 1) * 8);
            unsigned df = stv[0] ^ tag32;
#pragma unroll
            for (int i = 0; i < 16; ++i) df |= yv[i][0] ^ tag32;
            if (__all(df == 0u)) break;
            __builtin_amdgcn_s_sleep(8);
        }
        STAMP(14);
        __builtin_amdgcn_s_setprio(0);
        f32x2 sf[8], xf[8];
        {
            const float nmb = -bflo(stv[1]);
            float ri = __builtin_amdgcn_rcpf(bfhi(stv[1]));
            asm volatile("s_nop 1" : "+v"(ri));
            const unsigned ysh = (srow & 1) ? 0u : 16u;
#pragma unroll
            for (int i = 0; i < 8; ++i) {
                const unsigned xd = xv[i >> 2][i & 3];
                xf[i] = (f32x2){bflo1(xd), bfhi1(xd)};
                const unsigned tp = pk(add1(xf[i][0], nmb), add1(xf[i][1], nmb));
                const unsigned np = pk(mul1(bflo1(tp), ri), mul1(bfhi1(tp), ri));
                sf[i] = (f32x2){mul1(bflo1(np), bfhi1(yv[2 * i][1] << ysh)), mul1(bfhi1(np), bfhi1(yv[2 * i + 1][1] << ysh))};
                asm volatile("" : "+v"(sf[i]), "+v"(xf[i]));
            }
        }
        const int rm = lb + L_W2 + (wq < 3 ? wq * 4096 : L_SLOT) + (lf >> 5) * 2048 + (lf & 31) * 16;
        STAMP(15);
        WAIT_BAR("lgkmcnt(0)");
        STAMP(16);
        f32x4 o[4];
#pragma unroll
        for (int j = 0; j < 4; ++j) {
            LAS char* rp = lp(rm) + j * 512;
            o[j] = (*(LAS f32x4*)rp + *(LAS f32x4*)(rp + 12288)) + (*(LAS f32x4*)(rp + 24576) + *(LAS f32x4*)(rp + 36864));
        }
        f32x2 so[8], ev[8];
#pragma unroll
        for (int i = 0; i < 8; ++i) {
            const f32x2 ov = {o[i >> 1][(i & 1) * 2], o[i >> 1][(i & 1) * 2 + 1]};
            const f32x2 t = ov * (f32x2){-1.44269504088896341f, -1.44269504088896341f};
            ev[i] = (f32x2){__builtin_amdgcn_exp2f(t[0]), __builtin_amdgcn_exp2f(t[1])};
            so[i] = sf[i] * ov;
        }
        SCHED();
        u32x4 st[2];
#pragma unroll
        for (int i = 0; i < 8; ++i) {
            const f32x2 dn = ev[i] + (f32x2){1.f, 1.f};
            const f32x2 r = __builtin_elementwise_fma(so[i], (f32x2){__builtin_amdgcn_rcpf(dn[0]), __builtin_amdgcn_rcpf(dn[1])}, xf[i]);
            st[i >> 2][i & 3] = pk(r[0], r[1]);
        }
        const __amdgpu_buffer_rsrc_t orr = __builtin_amdgcn_make_buffer_rsrc((void*)p.out, 0, S * D * 2, 0x00020000);
        __builtin_amdgcn_raw_buffer_store_b128(st[0], orr, xoff, 0, 0);
        __builtin_amdgcn_raw_buffer_store_b128(st[1], orr, xoff, 16, 0);
        STAMP(17);
        STAMP_OUT(18);
    }
}

extern "C" void kernel_launch(void* const* d_in, const int* in_sizes, int n_in,
                              void* d_out, int out_size, void* d_ws, size_t ws_size,
                              hipStream_t stream) {
    Params p;
    p.x = (const u16*)d_in[0]; p.k = (const u16*)d_in[1]; p.v = (const u16*)d_in[2];
    p.sk = (const u16*)d_in[3]; p.hk = (const u16*)d_in[4]; p.dv = (const u16*)d_in[5];
    p.w1 = (const u16*)d_in[7]; p.w3 = (const u16*)d_in[8]; p.w2 = (const u16*)d_in[9];
    p.out = (u16*)d_out; p.ws = (char*)d_ws;
    (void)hipFuncSetAttribute((const void*)fused_kernel, hipFuncAttributeMaxDynamicSharedMemorySize, L_TOTAL);
    fused_kernel<<<256, 512, L_TOTAL, stream>>>(p);
}
```

```cpp
#include <hip/hip_runtime.h>
#include <stdint.h>
#include <utility>

typedef unsigned short u16;
typedef unsigned long long u64;
typedef short s16x4 __attribute__((ext_vector_type(4)));
typedef short s16x8 __attribute__((ext_vector_type(8)));
typedef __bf16 bf16x2 __attribute__((ext_vector_type(2)));
typedef __bf16 bf16x8 __attribute__((ext_vector_type(8)));
typedef float f32x2 __attribute__((ext_vector_type(2)));
typedef float f32x4 __attribute__((ext_vector_type(4)));
typedef float f32x16 __attribute__((ext_vector_type(16)));
typedef unsigned u32x2 __attribute__((ext_vector_type(2)));
typedef unsigned u32x4 __attribute__((ext_vector_type(4)));
typedef unsigned u32x16 __attribute__((ext_vector_type(16)));
#define LAS __attribute__((address_space(3)))
#define GAS __attribute__((address_space(1)))
#define RLX_AGENT __ATOMIC_RELAXED, __HIP_MEMORY_SCOPE_AGENT

constexpr int S = 2048, D = 448, F = 1792;

constexpr unsigned WS_EPOCH = 0;
constexpr unsigned WS_FH = 65536;
constexpr unsigned WS_STATS = 8192;
constexpr unsigned WS_CSUM = 24576;
constexpr unsigned WS_U = 262144;
constexpr unsigned WS_Y = WS_U + D * S * 4;
constexpr unsigned WS_H = WS_Y + D * S * 4;
constexpr unsigned WS_END = WS_H + 64 * 112 * 1024;
constexpr unsigned WS_DBG = WS_END;

constexpr int L_KV = 0;
constexpr int L_CTL = 0;
constexpr int L_CSUM = 256;
constexpr int L_STAT = 1280;
constexpr int L_RAW = 2048;
constexpr int L_KVO = 8192;
constexpr int L_UNX = 6144;
constexpr int L_SLOT = 49152;
constexpr int L_CONV = 12288;
constexpr int C_CH = 26112, C_HS = 4160, C_U = 16640, C_US = 4640;
constexpr int L_W2 = 16384 + L_SLOT;
constexpr int L_TOTAL = 163840;
__host__ __device__ constexpr int ring(int stage) { return 16384 + (stage % 3) * L_SLOT; }
constexpr unsigned CODE_PREFETCH = 36864;
#define POLL_SLEEP 4
#define HDEPTH 6
#define ROT2 (8 * ml)
#define MPRIO 1
#define EPARK 0
#define YSLEEP 16
#define TOUCH 1
#define TOUCH0 4
#define HGROW 4
#define PROBE 0
#define ROT1 ((2 * ml + nl) % 6)

constexpr u64 TAG_SALT = 0x9E3779B97F4A7C15ull;
constexpr unsigned SPIN_CAP = 1u << 18;

struct Params {
    const u16* x; const u16* k; const u16* v; const u16* sk; const u16* hk; const u16* dv;
    const u16* w1; const u16* w3; const u16* w2; u16* out; char* ws;
};

__device__ __forceinline__ float bflo(unsigned p) { return __uint_as_float(p << 16); }
__device__ __forceinline__ float bfhi(unsigned p) { return __uint_as_float(p & 0xffff0000u); }
__device__ __forceinline__ float bflo1(unsigned p) { float r; asm("v_lshlrev_b32 %0, 16, %1" : "=v"(r) : "v"(p)); return r; }
__device__ __forceinline__ float bfhi1(unsigned p) { float r; asm("v_and_b32 %0, 0xffff0000, %1" : "=v"(r) : "v"(p)); return r; }
__device__ __forceinline__ float mul1(float a, float b) { float r; asm("v_mul_f32 %0, %1, %2" : "=v"(r) : "v"(a), "v"(b)); return r; }
__device__ __forceinline__ void fmac1(float& d, float a, float b) { asm("v_fmac_f32 %0, %1, %2" : "+v"(d) : "v"(a), "v"(b)); }
__device__ __forceinline__ float dpp_xor1(float v) {
    return __int_as_float(__builtin_amdgcn_update_dpp(0, __float_as_int(v), 0xB1, 0xf, 0xf, true));
}
__device__ __forceinline__ float add1(float a, float b) { float r; asm("v_add_f32 %0, %1, %2" : "=v"(r) : "v"(a), "v"(b)); return r; }
__device__ __forceinline__ unsigned vmov1(unsigned s) { unsigned r; asm volatile("v_mov_b32 %0, %1" : "=v"(r) : "s"(s)); return r; }
__device__ __forceinline__ unsigned pk(float a, float b) {
    f32x2 f = {a, b};
    return __builtin_bit_cast(unsigned, __builtin_convertvector(f, bf16x2));
}
__device__ __forceinline__ float rbf(float a) { return bflo(pk(a, a)); }
__device__ __forceinline__ float dot2(unsigned a, unsigned b, float c) {
    return __builtin_amdgcn_fdot2_f32_bf16(__builtin_bit_cast(bf16x2, a), __builtin_bit_cast(bf16x2, b), c, false);
}
__device__ __forceinline__ void glds16(const void* base, unsigned voff, unsigned ldst) {
    u64 t;
    asm volatile("s_mov_b32 m0, %3\n\ts_mov_b64 %0, %2\n\tglobal_load_lds_dwordx4 %1, %0" : "=&s"(t) : "v"(voff), "s"(base), "s"(ldst));
}
__device__ __forceinline__ void glds16x4(const void* base, u32x4 vo, unsigned ldst) {
    u64 t;
    asm volatile("s_mov_b32 m0, %6\n\ts_mov_b64 %0, %5\n\tglobal_load_lds_dwordx4 %1, %0\n\tglobal_load_lds_dwordx4 %2, %0 offset:1024\n\t"
                 "global_load_lds_dwordx4 %3, %0 offset:2048\n\tglobal_load_lds_dwordx4 %4, %0 offset:3072"
                 : "=&s"(t) : "v"(vo[0]), "v"(vo[1]), "v"(vo[2]), "v"(vo[3]), "s"(base), "s"(ldst));
}
__device__ __forceinline__ void glds16x4c(const void* base, u32x4 vo, unsigned ldst) {
    u64 t;
    asm volatile("s_mov_b32 m0, %6\n\ts_mov_b64 %0, %5\n\tglobal_load_lds_dwordx4 %1, %0 sc1\n\tglobal_load_lds_dwordx4 %2, %0 offset:1024 sc1\n\t"
                 "global_load_lds_dwordx4 %3, %0 offset:2048 sc1\n\tglobal_load_lds_dwordx4 %4, %0 offset:3072 sc1"
                 : "=&s"(t) : "v"(vo[0]), "v"(vo[1]), "v"(vo[2]), "v"(vo[3]), "s"(base), "s"(ldst) : "memory");
}
__device__ __forceinline__ void glds16x2c(const void* base, unsigned voff, unsigned ldst) {
    u64 t;
    asm volatile("s_mov_b32 m0, %3\n\ts_mov_b64 %0, %2\n\tglobal_load_lds_dwordx4 %1, %0 sc1\n\tglobal_load_lds_dwordx4 %1, %0 offset:1024 sc1"
                 : "=&s"(t) : "v"(voff), "s"(base), "s"(ldst));
}
__device__ __forceinline__ void glds16c(const void* base, unsigned voff, unsigned ldst) {
    u64 t;
    asm volatile("s_mov_b32 m0, %3\n\ts_mov_b64 %0, %2\n\tglobal_load_lds_dwordx4 %1, %0 sc1" : "=&s"(t) : "v"(voff), "s"(base), "s"(ldst));
}
__device__ __forceinline__ void glds4c(const void* base, unsigned voff, unsigned ldst) {
    u64 t;
    asm volatile("s_mov_b32 m0, %3\n\ts_mov_b64 %0, %2\n\tglobal_load_lds_dword %1, %0 sc1" : "=&s"(t) : "v"(voff), "s"(base), "s"(ldst));
}
__device__ __forceinline__ const u16* sptr(const u16* base, int off) {
    const u16* r = base + __builtin_amdgcn_readfirstlane(off);
    asm("" : "+s"(r));
    return r;
}
__device__ __forceinline__ u64 vsel(bool c, const void* a, u64 b) {
    u64 va = (u64)a;
    asm("" : "+v"(va), "+v"(b));
    return c ? va : b;
}
__device__ __forceinline__ LAS char* lp(int a) { return (LAS char*)(size_t)(unsigned)a; }
__device__ __forceinline__ u32x4 lds128(LAS char* p) { return *(LAS u32x4*)p; }
__device__ __forceinline__ u32x2 ldstr(LAS char* p) {
    return __builtin_bit_cast(u32x2, __builtin_amdgcn_ds_read_tr16_b64_v4i16((LAS s16x4*)p));
}
__device__ __forceinline__ u32x4 ldstr2(LAS char* p, int off) {
    u32x2 a = ldstr(p), b = ldstr(p + off);
    return (u32x4){a.x, a.y, b.x, b.y};
}
__device__ __forceinline__ f32x16 mfma32(u32x4 a, u32x4 b, f32x16 c) {
    return __builtin_amdgcn_mfma_f32_32x32x16_bf16(__builtin_bit_cast(bf16x8, a), __builtin_bit_cast(bf16x8, b), c, 0, 0, 0);
}
__device__ __forceinline__ f32x4 mfma16(u32x4 a, u32x4 b, f32x4 c) {
    return __builtin_amdgcn_mfma_f32_16x16x32_bf16(__builtin_bit_cast(bf16x8, a), __builtin_bit_cast(bf16x8, b), c, 0, 0, 0);
}
__device__ __forceinline__ float silu_f(float g) {
    return g * __builtin_amdgcn_rcpf(1.f + __builtin_amdgcn_exp2f(g * -1.44269504088896341f));
}
template <class Fn, int... I>
__device__ __forceinline__ void static_for(Fn&& fn, std::integer_sequence<int, I...>) { (fn(std::integral_constant<int, I>{}), ...); }
#define WAIT_VM(n) asm volatile("s_waitcnt vmcnt(" #n ")" ::: "memory")
#define WAIT_LGKM0() asm volatile("s_waitcnt lgkmcnt(0)" ::: "memory")
#define WAIT_BAR(w) asm volatile("s_waitcnt " w "\n\ts_barrier" ::: "memory")
#define SCHED() __builtin_amdgcn_sched_barrier(0)
#define CAS __attribute__((address_space(4)))
#define NSTAMP 18

__device__ __forceinline__ void wait_lds(LAS char* word) {
#pragma unroll 1
    for (unsigned spins = 0; spins < (SPIN_CAP << 4); ++spins) {
        if (__builtin_amdgcn_readfirstlane(__hip_atomic_load((LAS unsigned*)word, __ATOMIC_RELAXED, __HIP_MEMORY_SCOPE_WORKGROUP))) break;
        __builtin_amdgcn_s_sleep(1);
    }
    asm volatile("" ::: "memory");
}
__device__ __forceinline__ void wait_lds_eq(LAS char* word, unsigned want) {
#pragma unroll 1
    for (unsigned spins = 0; spins < (1u << 22); ++spins) {
        if ((unsigned)__builtin_amdgcn_readfirstlane(__hip_atomic_load((LAS unsigned*)word, __ATOMIC_RELAXED, __HIP_MEMORY_SCOPE_WORKGROUP)) == want) break;
        __builtin_amdgcn_s_sleep(1);
    }
    asm volatile("" ::: "memory");
}
#define STAMP(i) do { if ((i) == PROBE && tid == 0 && idx >= 4) { u64 e_; asm volatile("s_load_dwordx2 %0, %1, 0x0\n\ts_waitcnt lgkmcnt(0)" : "=s"(e_) : "s"(p.ws) : "memory"); \
    __hip_atomic_store((GAS u64*)(p.ws + 196608 + (blockIdx.x - 32) * 128), e_ ^ TAG_SALT, RLX_AGENT); } } while (0)
#define STAMPG(i)
#define STAMPA(i)
#define STAMP_DECL()
#define STAMP_OUT(n)

__global__ __launch_bounds__(512, 2) void fused_kernel(Params p) {
    extern __shared__ __attribute__((aligned(16))) char smem_raw[];
    u64 pc;
    asm volatile("s_getpc_b64 %0" : "=s"(pc));
    LAS char* sm = (LAS char*)smem_raw;
    const int tid = threadIdx.x, lane = tid & 63;
    const int wave = __builtin_amdgcn_readfirstlane(tid >> 6);
    const int xcd = blockIdx.x & 7, idx = blockIdx.x >> 3;
    if (idx == 0 && wave < 5) {
        const unsigned o = (unsigned)tid * 128u;
        asm volatile("s_mov_b32 m0, %2\n\ts_nop 0\n\tglobal_load_lds_dword %0, %1" :: "v"(o < CODE_PREFETCH ? o : 0u), "s"(pc & ~(u64)255),
                     "s"((unsigned)(size_t)sm + 32768u));
    }
    SCHED();
    const __amdgpu_buffer_rsrc_t wsr = __builtin_amdgcn_make_buffer_rsrc(p.ws, 0, WS_END, 0x00020000);
    asm volatile("" :: "s"(p.x), "s"(p.k), "s"(p.v), "s"(p.sk), "s"(p.hk), "s"(p.dv), "s"(p.w1), "s"(p.w3), "s"(p.w2), "s"(p.out), "s"(p.ws));
    STAMP_DECL();
    STAMP(0);

    if (idx < 4) {
        const int ca = (xcd >> 1) * 4 + idx, cmat = ca >= 7, cnt = 7 * (xcd & 1) + (ca >= 7 ? ca - 7 : ca);
        const bool job = ca < 14;
        u32x4 xr[7], wr[14];
        const int row = (8 * (xcd >> 1) + 4 * (xcd & 1) + idx) * 64 + wave * 8 + (lane >> 3);
#pragma unroll
        for (int i = 0; i < 7; ++i) xr[i] = *(const u32x4*)(p.x + (size_t)row * D + i * 64 + (lane & 7) * 8);
        const unsigned pf2 = *(const unsigned*)((const char*)p.hk + (size_t)((xcd * 4 + idx) * 448 + wave * 56 + (lane < 56 ? lane : 0)) * 128);
        u64 epoch;
        asm volatile("s_load_dwordx2 %0, %1, 0x0\n\ts_waitcnt lgkmcnt(0)" : "=s"(epoch) : "s"(p.ws) : "memory");
        const u64 tag = epoch ^ TAG_SALT;
        const unsigned tag32 = (unsigned)tag;
        {
            float s4[4] = {0.f, 0.f, 0.f, 0.f};
#pragma unroll
            for (int i = 0; i < 7; ++i)
#pragma unroll
                for (int j = 0; j < 4; ++j) s4[j] = dot2(xr[i][j], 0x3f803f80u, s4[j]);
            float sum = (s4[0] + s4[1]) + (s4[2] + s4[3]);
            sum += __shfl_xor(sum, 1); sum += __shfl_xor(sum, 2); sum += __shfl_xor(sum, 4);
            const float mean = sum * (1.f / D);
            f32x2 q4[4] = {{0.f, 0.f}, {0.f, 0.f}, {0.f, 0.f}, {0.f, 0.f}};
#pragma unroll
            for (int i = 0; i < 7; ++i)
#pragma unroll
                for (int j = 0; j < 4; ++j) {
                    const f32x2 d = (f32x2){bflo(xr[i][j]), bfhi(xr[i][j])} - (f32x2){mean, mean};
                    q4[j] = __builtin_elementwise_fma(d, d, q4[j]);
                }
            const f32x2 q2 = (q4[0] + q4[1]) + (q4[2] + q4[3]);
            float sq = q2[0] + q2[1];
            sq += __shfl_xor(sq, 1); sq += __shfl_xor(sq, 2); sq += __shfl_xor(sq, 4);
            const u32x2 st = {tag32, pk(mean, __builtin_amdgcn_sqrtf(rbf(rbf(sq * (1.f / D)) + 1e-5f)))};
            __builtin_amdgcn_raw_buffer_store_b64(st, wsr, (lane & 7) == 0 ? WS_STATS + (unsigned)row * 8u : 0xfffffff0u, 0, 16);
        }
        STAMPA(2);
        if (job) {
            SCHED();
            const u16* wp = (cmat ? p.w3 : p.w1) + (size_t)(wave * 4 + (lane >> 4)) * F + cnt * 128 + (lane & 15) * 8;
#pragma unroll
            for (int i = 0; i < 14; ++i) wr[i] = *(const u32x4*)(wp + (size_t)(32 * i) * F);
        }
        if (job) {
            float c[8];
#pragma unroll
            for (int j = 0; j < 8; ++j) c[j] = 0.f;
#pragma unroll
            for (int i = 0; i < 14; ++i)
#pragma unroll
                for (int j = 0; j < 4; ++j) { c[2 * j] += bflo(wr[i][j]); c[2 * j + 1] += bfhi(wr[i][j]); }
            LAS char* cp = sm + ((wave * 4 + (lane >> 4)) * 136 + (lane & 15) * 8) * 4;
            *(LAS f32x4*)cp = (f32x4){c[0], c[1], c[2], c[3]};
            *(LAS f32x4*)(cp + 16) = (f32x4){c[4], c[5], c[6], c[7]};
        }
        STAMPA(3);
        if (job) {
            WAIT_BAR("lgkmcnt(0)");
            float t = 0.f;
#pragma unroll
            for (int i = 0; i < 8; ++i) t += *(LAS float*)(sm + ((i * 4 + (tid & 3)) * 136 + (tid >> 2)) * 4);
            t += __shfl_xor(t, 1); t += __shfl_xor(t, 2);
            __builtin_amdgcn_raw_buffer_store_b64((u32x2){tag32, __float_as_uint(t)}, wsr,
                (tid & 3) == 0 ? WS_CSUM + (unsigned)((cnt * 2 + cmat) * 128 + (tid >> 2)) * 8u : 0xfffffff0u, 0, 16);
        }
        STAMP(1);
        asm volatile("" :: "v"(pf2));
        STAMP_OUT(4);
        if (blockIdx.x == 30 && wave == 7) {
            GAS u64* pp = (GAS u64*)(p.ws + 196608) + (lane < 56 ? lane : 0) * 16;
#pragma unroll 1
            for (unsigned spins = 0; spins < SPIN_CAP; ++spins) {
                bool ok = true;
#pragma unroll
                for (int m = 0; m < 4; ++m) ok &= __hip_atomic_load(pp + m * 56 * 16, RLX_AGENT) == tag;
                if (__all(ok)) break;
                __builtin_amdgcn_s_sleep(2);
            }
            const u64 t0 = __builtin_amdgcn_s_memrealtime();
            while (__builtin_amdgcn_s_memrealtime() - t0 < 2000ull) __builtin_amdgcn_s_sleep(1);
        }
        if (blockIdx.x == 30 && wave == 7) {
            GAS u64* gp = (GAS u64*)(p.ws + WS_STATS) + lane * 8;
            const int fl = lane < 56 ? lane : 0;
            GAS u64* fp = (GAS u64*)(p.ws + WS_FH) + ((fl / 14) * 56 + (fl % 14) * 2) * 16;
#pragma unroll 1
            for (unsigned spins = 0; spins < SPIN_CAP; ++spins) {
                bool ok = true;
#pragma unroll
                for (int m = 0; m < 4; ++m)
                    ok &= ((unsigned)__hip_atomic_load(gp + m * 512, RLX_AGENT) == tag32) & (__hip_atomic_load(fp + m * 224 * 16, RLX_AGENT) == tag);
                if (__all(ok)) break;
                __builtin_amdgcn_s_sleep(32);
            }
            if (lane == 0) __hip_atomic_store((GAS u64*)(p.ws + WS_EPOCH), epoch + 1, RLX_AGENT);
        }
        return;
    }

    const int gi = idx - 4, ml = gi & 3, nl = gi >> 2;
    const int mt = 4 * (xcd >> 1) + ml, nt = 7 * (xcd & 1) + nl;
    const int rot1 = ROT1;
    const int m64 = 4 * xcd + ml, n64 = nl;
    const int gpair = xcd * 28 + gi;
    const int r32 = lane & 31, hh = lane >> 5, blk = (lane >> 4) & 1, q4 = (lane & 15) >> 2, p4 = lane & 3;
    const unsigned lb = (unsigned)(size_t)sm;
    const int wq = wave & 3;
    u32x4 wvo;
    wvo[0] = (unsigned)((lane >> 4) * F + (((lane & 15) ^ ((lane >> 4) << 2)) * 8)) * 2u;
#pragma unroll
    for (int j = 1; j < 4; ++j) wvo[j] = wvo[0] + j * (8 * F - 1024);
    const u16* w1b = sptr(p.w1, wq * 16 * F + nt * 128);
    const u16* w3b = sptr(p.w3, wq * 16 * F + nt * 128);
    auto kst = [&](int si) { if (si == 0) return 0; const int s = si + rot1; return s >= 7 ? s - 6 : s; };
    auto group_w = [&](int si, int m) { glds16x4(sptr(m ? w3b : w1b, kst(si) * 64 * F), wvo, lb + ring(si) + 16384 + m * 16384 + wq * 4096); };
    const int kr0 = (gpair & 31) * 64, kc0 = (gpair >> 5) * 64;
    const int chn = wq >> 1, par = wq & 1, dch = 2 * gpair + chn;
    if (wave < 4) group_w(0, 0);
    {
        const bool isk = lane >= 8 && lane < 14;
        u64 mb_ = vsel(lane == 15, p.dv, vsel(lane == 14, p.hk, vsel(isk, p.k, (u64)p.sk)));
        unsigned mo = (unsigned)((kc0 + wq * 16) * 8 + (lane & 7) * 16);
        mo = isk ? (unsigned)((kr0 ? kr0 - 3 + ((lane - 8) >> 1) : 0) * D + kc0 + wq * 16 + (lane & 1) * 8) * 2u : mo;
        mo = lane == 14 ? (unsigned)dch * 4096u : mo;
        mo = lane == 15 ? (unsigned)(dch & ~7) * 2u : mo;
        const u64 ma = mb_ + mo;
        asm volatile("s_mov_b32 m0, %1\n\ts_nop 0\n\tglobal_load_lds_dwordx4 %0, off" :: "v"(ma), "s"(lb + wave * 1024));
    }
    if (wave < 4) group_w(0, 1);
    SCHED();
    const int rot2 = ROT2;
    const int w2i = L_W2 + wq * 12288;
    auto fresh_lane = [&]() { int l = lane; asm volatile("" : "+v"(l)); return l; };
    const int latei = L_CONV + wq * 8192;
    auto pair_w2 = [&](u32x4 vo, int i, int dst) {
        int t = i + rot2; t = t >= 28 ? t - 28 : t;
        glds16x4(sptr(p.w2, (wq * 448 + t * 16) * D + n64 * 64), vo, lb + dst);
    };
    auto w2_off = [&]() {
        const int l = fresh_lane();
        const unsigned o = (unsigned)((l >> 3) * D + (((l & 7) ^ (((l >> 4) & 1) << 2)) * 8)) * 2u;
        return (u32x4){o, o + (16 * D - 1024), o + 2 * (16 * D - 1024), o + 3 * (16 * D - 1024)};
    };

    if (wave < 4) {
        u64 epoch;
        asm volatile("s_load_dwordx2 %0, %1, 0x0" : "=s"(epoch) : "s"(p.ws) : "memory");
        __builtin_amdgcn_s_setprio(MPRIO);
        const int wm = wave >> 1, wn = wave & 1;
        int xa[4], wa0, wa1;
#pragma unroll
        for (int i = 0; i < 4; ++i) xa[i] = lb + ring(0) + (((64 * wm + r32) * 128 + ((hh ^ ((r32 >> 1) & 7)) * 16)) ^ (i * 32));
        wa0 = 16384 + (8 * hh + q4) * 256 + (((8 * wn + 2 * blk + (p4 >> 1)) ^ (q4 << 2)) * 16) + 8 * (p4 & 1);
        wa1 = (wa0 ^ 64) + lb + ring(0); wa0 += lb + ring(0);
        f32x16 acc[2][2][2];
#pragma unroll
        for (int i = 0; i < 8; ++i)
#pragma unroll
            for (int r = 0; r < 16; ++r) acc[i >> 2][(i >> 1) & 1][i & 1][r] = 0.f;
        u32x4 fx[2][2], fw[2][2][2];
        auto load_frag = [&](int g, int j) {
            const int pp = g & 3, b = g & 1;
            if (j == 0) {
                if (g >= 4) {
                    const int d = ring(g >> 2) - ring((g >> 2) - 1);
                    if (pp == 0) { wa0 += d; wa1 += d; asm volatile("" : "+v"(wa0), "+v"(wa1)); }
                    xa[pp] += d; asm volatile("" : "+v"(xa[pp]));
                }
                fw[b][0][0] = ldstr2(lp(wa0) + pp * 4096, 1024);
            }
            if (j == 1) { fx[b][0] = lds128(lp(xa[pp])); fx[b][1] = lds128(lp(xa[pp]) + 4096); }
            if (j == 2) fw[b][0][1] = ldstr2(lp(wa0) + pp * 4096 + 16384, 1024);
            if (j == 3) fw[b][1][0] = ldstr2(lp(wa1) + pp * 4096, 1024);
            if (j == 4) fw[b][1][1] = ldstr2(lp(wa1) + pp * 4096 + 16384, 1024);
        };
        int ka0[2], kam[2], kwo;
#pragma unroll
        for (int rd = 0; rd < 2; ++rd) {
            const int ck = 2 * wq + rd;
            const int bq = 2 * (lane >> 4) + (p4 >> 1), bm = bq ? bq - 1 : 0;
            ka0[rd] = lb + L_KV + (8 * bq + q4) * 128 + ((ck ^ (((q4 >> 1) & 1) | ((bq & 3) << 1))) * 16) + 8 * (p4 & 1);
            kam[rd] = lb + L_KV + (8 * bm + 4 + q4) * 128 + ((ck ^ (((q4 >> 1) & 1) | ((bm & 3) << 1))) * 16) + 8 * (p4 & 1);
        }
        kwo = lb + L_KVO + wq * 2048 + (lane & 7) * 128 + (((7 - (lane >> 3)) ^ (lane & 7)) * 16);
        asm volatile("" : "+v"(ka0[0]), "+v"(ka0[1]), "+v"(kam[0]), "+v"(kam[1]), "+v"(kwo));
        WAIT_VM(4);
        u32x2 skv[2], hal[2];
#pragma unroll
        for (int rd = 0; rd < 2; ++rd) {
            LAS char* mp = sm + wave * 1024;
            skv[rd] = *(LAS u32x2*)(mp + (rd * 8 + (lane & 7)) * 8);
            const unsigned hm = kr0 ? 0xffffffffu : 0u;
            const unsigned a = *(LAS u16*)(mp + 128 + rd * 16 + (lane & 7) * 2), b = *(LAS u16*)(mp + 160 + rd * 16 + (lane & 7) * 2),
                           c = *(LAS u16*)(mp + 192 + rd * 16 + (lane & 7) * 2);
            hal[rd] = (u32x2){(a << 16) & hm, (b | (c << 16)) & hm};
            asm volatile("" : "+v"(skv[rd]), "+v"(hal[rd]));
        }
        asm volatile("s_waitcnt vmcnt(0) lgkmcnt(0)\n\ts_barrier" : "+s"(epoch) :: "memory");
        const u64 tag = epoch ^ TAG_SALT;
        STAMP(1);
        u32x2 kt[2][3], vt[2][2];
        float cf[4], kk[11], ko[8];
        u32x4 kpk;
        auto fill = [&](auto kc) {
            constexpr int k = decltype(kc)::value;
            if constexpr (k < 5) {
#pragma unroll
                for (int rd = 0; rd < 2; ++rd) {
                    if constexpr (k == 0) kt[rd][0] = ldstr(lp(kam[rd]));
                    else if constexpr (k < 3) kt[rd][k] = ldstr(lp(ka0[rd]) + (k - 1) * 512);
                    else vt[rd][k - 3] = ldstr(lp(ka0[rd]) + 8192 + (k - 3) * 512);
                }
            } else if constexpr (k >= 6 && k < 6 + 2 * 16) {
                constexpr int rd = (k - 6) / 16, s = (k - 6) % 16;
                if constexpr (s == 0) { cf[0] = bflo1(skv[rd][0]); cf[1] = bfhi1(skv[rd][0]); cf[2] = bflo1(skv[rd][1]); cf[3] = bfhi1(skv[rd][1]); }
                else if constexpr (s == 1) {
                    const unsigned a = lane < 8 ? hal[rd][0] : kt[rd][0][0], b = lane < 8 ? hal[rd][1] : kt[rd][0][1];
                    kk[0] = bfhi1(a); kk[1] = bflo1(b); kk[2] = bfhi1(b);
                } else if constexpr (s < 4) {
                    const u32x2 t = kt[rd][s - 1];
                    kk[4 * s - 5] = bflo1(t[0]); kk[4 * s - 4] = bfhi1(t[0]); kk[4 * s - 3] = bflo1(t[1]); kk[4 * s - 2] = bfhi1(t[1]);
                } else if constexpr (s < 12) {
                    constexpr int tp = (s - 4) >> 1, o0 = ((s - 4) & 1) * 4;
#pragma unroll
                    for (int i = o0; i < o0 + 4; ++i) {
                        if (tp == 0) ko[i] = mul1(kk[i], cf[3]);
                        else fmac1(ko[i], kk[i + tp], cf[3 - tp]);
                    }
                } else {
                    constexpr int j = s - 12;
                    const unsigned v = vt[rd][j >> 1][j & 1];
                    kpk[3 - j] = pk(mul1(ko[2 * j + 1], bfhi1(v)), mul1(ko[2 * j], bflo1(v)));
                    if constexpr (j == 3) *(LAS u32x4*)(lp(kwo) + rd * 1024) = kpk;
                }
            }
        };
#pragma unroll
        for (int j = 0; j < 5; ++j) load_frag(0, j);
        f32x4 cs[2][2][4];
        unsigned tch[4];
        unsigned tco = (unsigned)(gi * 4 + wq + (lane < 28 ? lane : 27) * 448) * 128u;
        asm volatile("" : "+v"(tco));
        static_for([&](auto gc) {
            constexpr int g = decltype(gc)::value, b = g & 1, pp = g & 3;
            SCHED();
            if (g == 11) STAMP(2);
            if (pp == 3 && g < 27) { STAMPG(2 + 2 * (g >> 2)); WAIT_BAR("lgkmcnt(0)"); STAMPG(3 + 2 * (g >> 2)); }
            if (g == 27) {
                WAIT_LGKM0();
                if (lane == 0) __hip_atomic_fetch_add((LAS unsigned*)(sm + L_CTL + 12), 1u, __ATOMIC_RELAXED, __HIP_MEMORY_SCOPE_WORKGROUP);
            }
            if (g == 11) STAMP(3);
            static_for([&](auto ic) {
                constexpr int i = decltype(ic)::value, ft = i >> 2, m = (i >> 1) & 1, sb = i & 1;
                acc[sb][ft][m] = mfma32(fw[b][ft][m], fx[b][sb], acc[sb][ft][m]);
                SCHED();
                if (g < 27 && i < 5) load_frag(g + 1, i);
                if constexpr (TOUCH && i == 4 && pp == 2 && g >= TOUCH0 && g < TOUCH0 + 16) {
                    u64 t;
                    asm volatile("s_mov_b64 %1, %3\n\tglobal_load_dword %0, %2, %1" : "=v"(tch[(g - TOUCH0) >> 2]), "=&s"(t)
                                 : "v"(tco), "s"((const char*)p.w2 + ((g - TOUCH0) >> 2) * (112 * 128)));
                }
                if constexpr (i >= 5 && g >= 7) fill(std::integral_constant<int, (g - 7) * 3 + i - 5>{});
                SCHED();
            }, std::make_integer_sequence<int, 8>{});
        }, std::make_integer_sequence<int, 28>{});
        STAMP(4);
        wait_lds_eq(sm + L_CTL + 64, 3u);
        unsigned sa[2];
#pragma unroll
        for (int i = 0; i < 8; ++i) cs[i >> 2][0][i & 3] = *(LAS f32x4*)(sm + L_CSUM + (64 * wn + 32 * (i >> 2) + 8 * (i & 3) + 4 * hh) * 4);
        sa[0] = *(LAS unsigned*)(sm + L_STAT + (64 * wm + r32) * 4);
        sa[1] = *(LAS unsigned*)(sm + L_STAT + (64 * wm + 32 + r32) * 4);
#pragma unroll
        for (int i = 0; i < 8; ++i) cs[i >> 2][1][i & 3] = *(LAS f32x4*)(sm + L_CSUM + 512 + (64 * wn + 32 * (i >> 2) + 8 * (i & 3) + 4 * hh) * 4);
        const int lw = fresh_lane();
        const int w2a0 = lb + w2i + (4 * (lw >> 5) + ((lw & 15) >> 2)) * 128 + (((2 * ((lw >> 4) & 1) + ((lw & 3) >> 1)) ^ (((lw >> 3) & 1) << 2)) * 16) + 8 * (lw & 1);
        const int w2a1 = w2a0 ^ 64;
        u32x4 w2r[12][2];
        auto park = [&](int i) {
            w2r[i][0] = ldstr2(lp(w2a0) + (i / 6) * L_SLOT + (i % 6) * 2048, 1024);
            w2r[i][1] = ldstr2(lp(w2a1) + (i / 6) * L_SLOT + (i % 6) * 2048, 1024);
        };
#pragma unroll
        for (int sb = 0; sb < 2; ++sb) {
            const float ri = __builtin_amdgcn_rcpf(bfhi(sa[sb])) * -1.44269504088896341f, nm = -bflo(sa[sb]), d2 = bfhi(sa[sb]) * bfhi(sa[sb]);
            const f32x2 ri2 = {ri, ri}, nm2 = {nm, nm}, dd2 = {d2, d2};
#pragma unroll
            for (int ft = 0; ft < 2; ++ft) {
#pragma unroll
                for (int t2 = 0; t2 < 2; ++t2) {
                    const int grp = sb * 4 + ft * 2 + t2;
                    if (EPARK && grp == 2) { SCHED(); wait_lds(sm + L_CTL + 48 + wq * 4); }
                    if (EPARK && grp >= 2) { SCHED(); park(grp - 2); }
                    f32x2 gu[4], ee[4];
#pragma unroll
                    for (int j = 0; j < 4; ++j) {
                        const int r = 8 * t2 + 2 * j;
                        const f32x2 g = __builtin_elementwise_fma(nm2, (f32x2){cs[ft][0][r >> 2][r & 3], cs[ft][0][r >> 2][(r & 3) + 1]}, (f32x2){acc[sb][ft][0][r], acc[sb][ft][0][r + 1]});
                        const f32x2 u = __builtin_elementwise_fma(nm2, (f32x2){cs[ft][1][r >> 2][r & 3], cs[ft][1][r >> 2][(r & 3) + 1]}, (f32x2){acc[sb][ft][1][r], acc[sb][ft][1][r + 1]});
                        const f32x2 t = g * ri2;
                        ee[j] = (f32x2){__builtin_amdgcn_exp2f(t[0]), __builtin_amdgcn_exp2f(t[1])};
                        gu[j] = g * u;
                    }
                    u32x4 st;
#pragma unroll
                    for (int j = 0; j < 4; ++j) {
                        const f32x2 dn = __builtin_elementwise_fma(ee[j], dd2, dd2);
                        const f32x2 hv = gu[j] * (f32x2){__builtin_amdgcn_rcpf(dn[0]), __builtin_amdgcn_rcpf(dn[1])};
                        st[j] = pk(hv[0], hv[1]);
                    }
                    const unsigned fi = (unsigned)((4 * mt + 2 * wm + sb) * 112 + 8 * nt + 4 * wn + 2 * ft + t2);
                    __builtin_amdgcn_raw_buffer_store_b128(st, wsr, WS_H + fi * 1024u + lane * 16u, 0, 16);
                }
            }
        }
        SCHED();
        STAMP(5);
        const u32x4 w2o = w2_off();
        GAS u64* myflag = (GAS u64*)(p.ws + WS_FH + ((2 * mt + wm) * 28 + 2 * nt + wn) * 128);
        if (!__builtin_amdgcn_readfirstlane(__hip_atomic_load((LAS unsigned*)(sm + L_CTL + 112 + wq * 4), __ATOMIC_RELAXED, __HIP_MEMORY_SCOPE_WORKGROUP))) {
            WAIT_VM(0);
            if (lane == 0) __hip_atomic_store(myflag, tag, RLX_AGENT);
            wait_lds(sm + L_CTL + 112 + wq * 4);
        }
        asm volatile("" ::: "memory");
#pragma unroll
        for (int i = 6; i < 12; ++i) park(i);
        WAIT_VM(0);
        if (TOUCH) asm volatile("" :: "v"(tch[0]), "v"(tch[1]), "v"(tch[2]), "v"(tch[3]));
        if (lane == 0) __hip_atomic_store(myflag, tag, RLX_AGENT);
        STAMP(6);
        if (!EPARK) {
#pragma unroll
            for (int i = 0; i < 6; ++i) park(i);
        }
        asm volatile("" ::: "memory");
        __hip_atomic_store((LAS unsigned*)(sm + L_CTL + 32 + wq * 4), 1u, __ATOMIC_RELAXED, __HIP_MEMORY_SCOPE_WORKGROUP);
        WAIT_LGKM0();
#pragma unroll
        for (int i = 0; i < 12; ++i) asm volatile("" : "+v"(w2r[i][0]), "+v"(w2r[i][1]));
#pragma unroll
        for (int i = 0; i < 3; ++i) pair_w2(w2o, 12 + 2 * i, w2i + i * 4096);
#pragma unroll
        for (int i = 0; i < 3; ++i) pair_w2(w2o, 18 + 2 * i, w2i + L_SLOT + i * 4096);
        STAMP(7);
        {
            GAS u64* fp = (GAS u64*)(p.ws + WS_FH + (m64 * 28 + 7 * wq + (lw < 7 ? lw : 0)) * 128);
#pragma unroll 1
            for (unsigned spins = 0; spins < SPIN_CAP; ++spins) {
                if (__all(__hip_atomic_load(fp, RLX_AGENT) == tag)) break;
                __builtin_amdgcn_s_sleep(POLL_SLEEP);
            }
            __builtin_amdgcn_fence(__ATOMIC_ACQUIRE, "wavefront");
        }
        STAMP(11);
        const unsigned hb = WS_H + (unsigned)(2 * m64 * 112 + 28 * wq) * 1024u + lw * 16u;
        u32x4 hf[28][2];
        auto load_h = [&](int i, int j) {
            int t = i + rot2; t = t >= 28 ? t - 28 : t;
            hf[i][j] = __builtin_amdgcn_raw_buffer_load_b128(wsr, hb + j * (112u * 1024u), t * 1024, 16);
        };
        constexpr int HD = HDEPTH, HG = HGROW;
#pragma unroll
        for (int i = 0; i < HD; ++i) { load_h(i, 0); load_h(i, 1); }
        STAMP(14);
        f32x16 ac2[2][2];
#pragma unroll
        for (int i = 0; i < 4; ++i)
#pragma unroll
            for (int r = 0; r < 16; ++r) ac2[i >> 1][i & 1][r] = 0.f;
        u32x4 a0 = w2r[0][0], a1 = w2r[0][1];
        static_for([&](auto ic) {
            constexpr int i = decltype(ic)::value, n = i + 1;
            SCHED();
            if (i == 23) wait_lds(sm + L_CTL + 80 + wq * 4);
            u32x4 b0, b1;
            if constexpr (n < 12) { b0 = w2r[n][0]; b1 = w2r[n][1]; }
            else if constexpr (n < 28) {
                constexpr int so = n < 24 ? ((n - 12) / 6) * L_SLOT + ((n - 12) % 6) * 2048 : (n - 24) * 2048;
                const int lo = n < 24 ? 0 : latei - w2i;
                b0 = ldstr2(lp(w2a0 + lo) + so, 1024);
                b1 = ldstr2(lp(w2a1 + lo) + so, 1024);
            }
            SCHED();
            ac2[0][0] = mfma32(a0, hf[i][0], ac2[0][0]);
            SCHED();
            constexpr int l0 = i < HG ? HD + 2 * i : HD + HG + i;
            if constexpr (l0 < 28) load_h(l0, 0);
            SCHED();
            ac2[1][0] = mfma32(a1, hf[i][0], ac2[1][0]);
            SCHED();
            if constexpr (l0 < 28) load_h(l0, 1);
            SCHED();
            ac2[0][1] = mfma32(a0, hf[i][1], ac2[0][1]);
            SCHED();
            if constexpr (i < HG) load_h(l0 + 1, 0);
            SCHED();
            ac2[1][1] = mfma32(a1, hf[i][1], ac2[1][1]);
            SCHED();
            if constexpr (i < HG) load_h(l0 + 1, 1);
            a0 = b0; a1 = b1;
        }, std::make_integer_sequence<int, 28>{});
        SCHED();
        STAMP(15);
        WAIT_LGKM0();
#pragma unroll
        for (int v = 0; v < 8; ++v) {
            LAS char* sl = sm + w2i + (v < 6 ? v * 2048 : L_SLOT + (v - 6) * 2048) + lw * 16;
            const f32x16& t = ac2[v >> 2][(v >> 1) & 1];
            const int r0 = 8 * (v & 1);
            *(LAS f32x4*)sl = (f32x4){t[r0], t[r0 + 1], t[r0 + 2], t[r0 + 3]};
            *(LAS f32x4*)(sl + 1024) = (f32x4){t[r0 + 4], t[r0 + 5], t[r0 + 6], t[r0 + 7]};
        }
        WAIT_BAR("lgkmcnt(0)");
        STAMP(16);
        STAMP(17);
        STAMP_OUT(18);
    } else {
        u64 epoch;
        asm volatile("s_load_dwordx2 %0, %1, 0x0" : "=s"(epoch) : "s"(p.ws) : "memory");
        u32x4 xvo;
#pragma unroll
        for (int j = 0; j < 4; ++j) xvo[j] = (unsigned)((8 * j + (lane >> 3)) * D + ((((lane & 7) ^ (lane >> 4)) ^ (4 * (j & 1))) * 8)) * 2u - 1024u * j;
        const u16* xb = sptr(p.x, (mt * 128 + wq * 32) * D);
        auto group_x = [&](int si) { glds16x4(sptr(xb, kst(si) * 64), xvo, lb + ring(si) + wq * 4096); };
        group_x(0);
        group_x(1); group_w(1, 0); group_w(1, 1);
        const int cbi = L_CONV + chn * C_CH;
        LAS char* cb = sm + cbi;
        unsigned kvo[2], uso;
#pragma unroll
        for (int rd = 0; rd < 2; ++rd) kvo[rd] = (unsigned)((lane >> 3) * D + (((lane & 7) ^ (((lane >> 4) & 1) | (((2 * wq + rd) & 3) << 1))) * 8)) * 2u;
        uso = WS_U + (unsigned)(kc0 + wq * 16 + (lane >> 3)) * 8192u + (unsigned)(1984 - kr0 + (lane & 7) * 8) * 2u;
        int kro = lb + L_KVO + wq * 2048 + (lane >> 3) * 128 + (((lane & 7) ^ (lane >> 3)) * 16);
        const int cc = 2 * par + ((lane >> 2) & 1);
        int zo = cbi + C_U + par * C_US + (248 + (lane < 36 ? lane : 35)) * 16;
        zo = lane < 8 ? cbi + cc * C_HS + (lane & 3) * 16 : zo;
        u32x4 w2o = w2_off();
        const unsigned fo = lane * 16u;
        unsigned ao = (wq < 2 ? WS_CSUM + (unsigned)(nt * 2 + wq) * 1024u : WS_STATS + mt * 1024u) + fo;
        asm volatile("" : "+v"(zo), "+v"(w2o), "+v"(kvo[0]), "+v"(kvo[1]), "+v"(uso), "+v"(kro), "+v"(ao));
        asm volatile("s_waitcnt vmcnt(16) lgkmcnt(0)" : "+s"(epoch) :: "memory");
        const unsigned tag32 = (unsigned)(epoch ^ TAG_SALT);
        u32x4 bval, ug[2] = {{tag32, 0u, tag32, 0u}, {tag32, 0u, tag32, 0u}};
        unsigned dbits = *(LAS u16*)(sm + wave * 1024 + 240 + (dch & 7) * 2);
        {
            const u32x4 h0 = lds128(sm + wave * 1024 + 224);
            const bool s1 = !(cc & 1), s2 = !(cc & 2);
            const unsigned a0 = s1 ? 0u : h0[0], a1 = s1 ? h0[0] : h0[1], a2 = s1 ? h0[1] : h0[2], a3 = s1 ? h0[2] : h0[3];
            const unsigned lm = (lane == 3 || lane == 7) ? 0xffffffffu : 0u;
            bval = (u32x4){(s2 ? 0u : a0) & lm, (s2 ? 0u : a1) & lm, (s2 ? a0 : a2) & lm, (s2 ? a1 : a3) & lm};
            asm volatile("" : "+v"(bval), "+v"(dbits), "+v"(ug[0]), "+v"(ug[1]));
        }
        auto kv_store = [&](int rd) {
            LAS unsigned* kp = (LAS unsigned*)(lp(kro) + rd * 1024);
            ug[0][1] = kp[0]; asm volatile("" ::: "memory"); ug[0][3] = kp[1]; asm volatile("" ::: "memory");
            ug[1][1] = kp[2]; asm volatile("" ::: "memory"); ug[1][3] = kp[3];
            __builtin_amdgcn_raw_buffer_store_b128(ug[0], wsr, uso, rd * 65536, 16);
            __builtin_amdgcn_raw_buffer_store_b128(ug[1], wsr, uso, rd * 65536 + 4096, 16);
        };
        WAIT_BAR("vmcnt(12) lgkmcnt(0)");
        STAMP(1);
        group_x(2); group_w(2, 0); group_w(2, 1);
#pragma unroll
        for (int j = 0; j < 2; ++j) {
            glds16(sptr(p.k, (kr0 + (2 * wq + j) * 8) * D + kc0), kvo[j], lb + L_KV + (2 * wq + j) * 1024);
            glds16(sptr(p.v, (kr0 + (2 * wq + j) * 8) * D + kc0), kvo[j], lb + L_KV + 8192 + (2 * wq + j) * 1024);
        }
        STAMPG(2); WAIT_BAR("vmcnt(16)"); STAMPG(3);
#pragma unroll
        for (int w = 1; w < 5; ++w) {
            group_x(w + 2); group_w(w + 2, 0); group_w(w + 2, 1);
            if (w == 3 && lane < 32) *(LAS unsigned*)(sm + L_CTL + lane * 4) = 0u;
            if (w == 4) kv_store(0);
            if (w == 2) STAMP(2);
            STAMPG(2 + 2 * w); WAIT_BAR("vmcnt(12) lgkmcnt(0)"); STAMPG(3 + 2 * w);
            if (w == 2) STAMP(3);
        }
        glds16c(p.ws, ao, lb + L_RAW + wq * 1024);
#pragma unroll
        for (int i = 0; i < 3; ++i) pair_w2(w2o, 2 * i, w2i + i * 4096);
        STAMPG(12); WAIT_BAR("vmcnt(12)"); STAMPG(13);
        kv_store(1);
        asm volatile("" ::: "memory");
        if (lane == 0) __hip_atomic_fetch_add((LAS unsigned*)(sm + L_CTL + 12), 1u, __ATOMIC_RELAXED, __HIP_MEMORY_SCOPE_WORKGROUP);
        if (wq < 3) {
            u32x4 av = lds128(sm + L_RAW + wq * 1024 + lane * 16);
#pragma unroll 1
            for (unsigned spins = 0; spins < SPIN_CAP; ++spins) {
                if (__all(((av[0] ^ tag32) | (av[2] ^ tag32)) == 0u)) break;
                asm volatile("" ::: "memory");
                av = __builtin_amdgcn_raw_buffer_load_b128(wsr, ao, 0, 16);
                asm volatile("" : "+v"(av));
            }
            *(LAS u32x2*)(sm + L_CSUM + wq * 512 + lane * 8) = (u32x2){av[1], av[3]};
            asm volatile("" ::: "memory");
            if (lane == 0) __hip_atomic_fetch_add((LAS unsigned*)(sm + L_CTL + 64), 1u, __ATOMIC_RELAXED, __HIP_MEMORY_SCOPE_WORKGROUP);
        }
        STAMP(4);
#pragma unroll
        for (int i = 0; i < 3; ++i) pair_w2(w2o, 6 + 2 * i, w2i + L_SLOT + i * 4096);
        WAIT_VM(12);
        __hip_atomic_store((LAS unsigned*)(sm + L_CTL + 48 + wq * 4), 1u, __ATOMIC_RELAXED, __HIP_MEMORY_SCOPE_WORKGROUP);
        wait_lds_eq(sm + L_CTL + 12, 8u);
        STAMP(5);
        __builtin_amdgcn_s_setprio(2);
        const unsigned ubase = WS_U + (unsigned)dch * 8192u;
        glds16x2c(p.ws + ubase + par * 2048, fo, lb + cbi + C_U + par * 2048);
        glds16x2c(p.ws + ubase + 4096 + par * 2048, fo, lb + cbi + C_U + C_US + par * 2048);
        glds4c(p.ws + ubase + (1 - par) * 2048, (lane & 1) * 4u, lb + L_UNX + wq * 256);
        {
            const u32x4 fvo = {fo, fo, fo, lane == 63 ? 62u * 16u : fo};
#pragma unroll
            for (int c = 0; c < 2; ++c) glds16x4(sptr(p.hk, dch * S + 4 * par + 2 * c + 2), fvo, lb + cbi + (2 * par + c) * C_HS + 64);
        }
        *(LAS u32x4*)(sm + zo) = bval;
        WAIT_VM(13);
        __hip_atomic_store((LAS unsigned*)(sm + L_CTL + 112 + wq * 4), 1u, __ATOMIC_RELAXED, __HIP_MEMORY_SCOPE_WORKGROUP);
        WAIT_VM(8);
        {
            u32x4 ua[2], ub[2]; u32x2 un[2];
#pragma unroll
            for (int rd = 0; rd < 2; ++rd) {
                const int x8 = 128 * par + 64 * rd + lane;
                ua[rd] = lds128(cb + C_U + x8 * 16); ub[rd] = lds128(cb + C_U + C_US + x8 * 16);
                un[rd] = *(LAS u32x2*)((rd == 1 && lane == 63) ? sm + L_UNX + wq * 256 : cb + C_U + x8 * 16 + 16);
            }
#pragma unroll 1
            for (unsigned spins = 0; spins < SPIN_CAP; ++spins) {
                unsigned df = 0;
#pragma unroll
                for (int rd = 0; rd < 2; ++rd) df |= (ua[rd][0] ^ tag32) | (ua[rd][2] ^ tag32) | (ub[rd][0] ^ tag32) | (ub[rd][2] ^ tag32) | (un[rd][0] ^ tag32);
                if (__all(df == 0u)) break;
                __builtin_amdgcn_s_sleep(4);
                asm volatile("" ::: "memory");
#pragma unroll
                for (int rd = 0; rd < 2; ++rd) {
                    const int x8 = 128 * par + 64 * rd + lane;
                    const unsigned uo = ubase + (unsigned)x8 * 16u;
                    ua[rd] = __builtin_amdgcn_raw_buffer_load_b128(wsr, uo, 0, 16);
                    ub[rd] = __builtin_amdgcn_raw_buffer_load_b128(wsr, uo + 4096u, 0, 16);
                    un[rd] = __builtin_amdgcn_raw_buffer_load_b64(wsr, x8 == 255 ? uo : uo + 16u, 0, 16);
                }
                asm volatile("" : "+v"(ua[0]), "+v"(ub[0]), "+v"(un[0]), "+v"(ua[1]), "+v"(ub[1]), "+v"(un[1]));
            }
#pragma unroll
            for (int rd = 0; rd < 2; ++rd) {
                const int x8 = 128 * par + 64 * rd + lane;
                const unsigned nx = x8 == 255 ? 0u : un[rd][1];
                *(LAS u32x4*)(cb + C_U + x8 * 16) = (u32x4){__builtin_amdgcn_alignbit(ua[rd][3], ua[rd][1], 16), __builtin_amdgcn_alignbit(ub[rd][1], ua[rd][3], 16),
                                                            __builtin_amdgcn_alignbit(ub[rd][3], ub[rd][1], 16), __builtin_amdgcn_alignbit(nx, ub[rd][3], 16)};
                *(LAS u32x4*)(cb + C_U + C_US + x8 * 16) = (u32x4){ua[rd][1], ua[rd][3], ub[rd][1], ub[rd][3]};
            }
        }
        asm volatile("s_waitcnt vmcnt(0) lgkmcnt(0)" ::: "memory");
        __hip_atomic_store((LAS unsigned*)(sm + L_CTL + 16 + wq * 4), 1u, __ATOMIC_RELAXED, __HIP_MEMORY_SCOPE_WORKGROUP);
        u32x4 lw2[8], xv[2];
        {
            const __amdgpu_buffer_rsrc_t wr2 = __builtin_amdgcn_make_buffer_rsrc((void*)p.w2, 0, F * D * 2, 0x00020000);
#pragma unroll
            for (int j = 0; j < 8; ++j) {
                int t = 24 + (j >> 2) * 2 + rot2; t = t >= 28 ? t - 28 : t;
                lw2[j] = __builtin_amdgcn_raw_buffer_load_b128(wr2, w2o[0], ((wq * 448 + t * 16 + (j & 3) * 8) * D + n64 * 64) * 2, 0);
            }
        }
        const int lf = fresh_lane();
        const int srow = 64 * m64 + 32 * (wq & 1) + (lf & 31);
        const int dd = 64 * n64 + 32 * (wq >> 1) + 16 * (lf >> 5);
        const unsigned xoff = (unsigned)(srow * D + dd) * 2u;
        {
            const __amdgpu_buffer_rsrc_t xr = __builtin_amdgcn_make_buffer_rsrc((void*)p.x, 0, S * D * 2, 0x00020000);
            xv[0] = __builtin_amdgcn_raw_buffer_load_b128(xr, xoff, 0, 0);
            xv[1] = __builtin_amdgcn_raw_buffer_load_b128(xr, xoff, 16, 0);
        }
        SCHED();
        wait_lds(sm + L_CTL + 16 + (wq ^ 1) * 4);
        STAMP(7);
        STAMP(6);
        const int n16 = lane & 15, kg = lane >> 4;
        f32x4 ca[4];
#pragma unroll
        for (int j = 0; j < 4; ++j) ca[j] = (f32x4){0.f, 0.f, 0.f, 0.f};
        int apo = lb + cbi + (n16 & 3) * C_HS + ((n16 >> 2) + kg) * 16;
        int bpo = lb + cbi + C_U + (n16 & 1) * C_US + (252 - 4 * (n16 >> 1) + kg) * 16 - 7 * 512;
        u32x4 fa[2][8], fb[2][8];
#pragma unroll
        for (int i = 0; i < 8; ++i) { fa[0][i] = lds128(lp(apo) + i * 512); fb[0][i] = lds128(lp(bpo) + (7 - i) * 512); }
        const bool odd = lf & 1;
        int ysrc = (2 * (lf >> 3) + (lf & 1) + 16 * ((lf >> 1) & 3)) * 4;
        unsigned yso = WS_Y + (unsigned)dch * 8192u + lf * 16u;
        f32x4 kd[4];
        {
            const float dval = bflo(dbits);
            const int sl0 = 32 * ((lf & 15) >> 1) + 8 * (lf >> 4) + 4 * (lf & 1);
#pragma unroll
            for (int j = 0; j < 4; ++j) {
                const int tj = (j == 0 ? 0 : j == 1 ? 3 : j == 2 ? 4 : 7) ^ par;
                const u32x2 kvr = *(LAS u32x2*)(cb + C_U + C_US + (2044 - 256 * tj - sl0) * 2);
                const unsigned ka = pk(bfhi(kvr[1]) * dval, bflo(kvr[1]) * dval), kb = pk(bfhi(kvr[0]) * dval, bflo(kvr[0]) * dval);
                kd[j] = (f32x4){bflo(ka), bfhi(ka), bflo(kb), bfhi(kb)};
                asm volatile("" : "+v"(kd[j]));
            }
            asm volatile("" : "+v"(ysrc), "+v"(yso));
        }
        wait_lds(sm + L_CTL + 32 + wq * 4);
        STAMP(8);
        auto conv_all = [&](auto pc_) {
            constexpr int P = decltype(pc_)::value;
            static_for([&](auto rc) {
                constexpr int r = decltype(rc)::value, c = r & 1, n = c ^ 1;
                static_for([&](auto kc) {
                    constexpr int k = decltype(kc)::value;
                    constexpr int T[4] = {P, 3 - P, 4 + P, 7 - P};
                    constexpr int j = k <= T[0] ? 0 : k <= T[0] + T[1] + 1 ? 1 : k <= T[0] + T[1] + T[2] + 2 ? 2 : 3;
                    constexpr int pp = k - (j == 0 ? 0 : j == 1 ? T[0] + 1 : j == 2 ? T[0] + T[1] + 2 : T[0] + T[1] + T[2] + 3);
                    ca[j] = mfma16(fa[c][pp], fb[c][T[j] - pp], ca[j]);
                    SCHED();
                    if constexpr (r < 7 && k < 2 * (T[3] + 1)) {
                        if constexpr (k & 1) fb[n][k >> 1] = lds128(lp(bpo) + (r + 1) * 64 + (7 - (k >> 1)) * 512);
                        else fa[n][k >> 1] = lds128(lp(apo) + (r + 1) * 64 + (k >> 1) * 512);
                    }
                    SCHED();
                }, std::make_integer_sequence<int, 18>{});
                asm volatile("" : "+v"(apo), "+v"(bpo));
            }, std::make_integer_sequence<int, 8>{});
        };
        if (par) conv_all(std::integral_constant<int, 1>{}); else conv_all(std::integral_constant<int, 0>{});
        SCHED();
        STAMP(9);
        {
            float r0[4], r1[4];
#pragma unroll
            for (int j = 0; j < 4; ++j) { r0[j] = dpp_xor1(odd ? ca[j][0] : ca[j][2]); r1[j] = dpp_xor1(odd ? ca[j][1] : ca[j][3]); }
            unsigned rp[4][2];
#pragma unroll
            for (int j = 0; j < 4; ++j) {
                const unsigned ya = pk(odd ? r0[j] : ca[j][0], odd ? ca[j][2] : r0[j]), yb = pk(odd ? r1[j] : ca[j][1], odd ? ca[j][3] : r1[j]);
                rp[j][0] = __builtin_amdgcn_ds_bpermute(ysrc, pk(add1(bflo1(ya), kd[j][0]), add1(bfhi1(ya), kd[j][1])));
                rp[j][1] = __builtin_amdgcn_ds_bpermute(ysrc, pk(add1(bflo1(yb), kd[j][2]), add1(bfhi1(yb), kd[j][3])));
            }
#pragma unroll
            for (int j = 0; j < 4; ++j)
                __builtin_amdgcn_raw_buffer_store_b128((u32x4){vmov1(tag32), rp[j][0], vmov1(tag32), rp[j][1]}, wsr, yso,
                                                       ((j == 0 ? 0 : j == 1 ? 3 : j == 2 ? 4 : 7) ^ par) * 1024, 16);
        }
        STAMP(10);
        WAIT_LGKM0();
        if (lane == 0) __hip_atomic_fetch_add((LAS unsigned*)(sm + L_CTL + 8), 1u, __ATOMIC_RELAXED, __HIP_MEMORY_SCOPE_WORKGROUP);
#pragma unroll 1
        for (unsigned spins = 0; spins < (1u << 22); ++spins) {
            if (__builtin_amdgcn_readfirstlane(__hip_atomic_load((LAS unsigned*)(sm + L_CTL + 8), __ATOMIC_RELAXED, __HIP_MEMORY_SCOPE_WORKGROUP)) == 4u) break;
            __builtin_amdgcn_s_sleep(1);
        }
        asm volatile("" ::: "memory");
#pragma unroll
        for (int j = 0; j < 8; ++j) *(LAS u32x4*)(sm + latei + j * 1024 + lf * 16) = lw2[j];
        asm volatile("" ::: "memory");
        __hip_atomic_store((LAS unsigned*)(sm + L_CTL + 80 + wq * 4), 1u, __ATOMIC_RELAXED, __HIP_MEMORY_SCOPE_WORKGROUP);
        STAMP(11);
        __builtin_amdgcn_s_sleep(YSLEEP);
        u32x2 yv[16], stv;
        const int yl = L_CONV + 32768 + wq * 4096;
        const unsigned yg = (unsigned)(lf >> 3) * 8192u + (unsigned)(lf & 7) * 16u;
        const u32x4 yvo = {yg, yg + (65536u - 1024u), yg + 2 * (65536u - 1024u), yg + 3 * (65536u - 1024u)};
        const char* ybase = p.ws + WS_Y + (unsigned)(64 * n64 + 32 * (wq >> 1)) * 8192u + (unsigned)(64 * m64 + 32 * (wq & 1)) * 4u;
#pragma unroll 1
        for (unsigned spins = 0; spins < SPIN_CAP; ++spins) {
            glds16x4c(ybase, yvo, lb + yl);
            stv = __builtin_amdgcn_raw_buffer_load_b64(wsr, WS_STATS + (unsigned)srow * 8u, 0, 16);
            WAIT_VM(0);
#pragma unroll
            for (int i = 0; i < 16; ++i) yv[i] = *(LAS u32x2*)(sm + yl + ((lf >> 5) * 16 + i) * 128 + ((lf & 31) >> 1) * 8);
            unsigned df = stv[0] ^ tag32;
#pragma unroll
            for (int i = 0; i < 16; ++i) df |= yv[i][0] ^ tag32;
            if (__all(df == 0u)) break;
            __builtin_amdgcn_s_sleep(8);
        }
        STAMP(14);
        __builtin_amdgcn_s_setprio(0);
        f32x2 sf[8], xf[8];
        {
            const float nmb = -bflo(stv[1]);
            float ri = __builtin_amdgcn_rcpf(bfhi(stv[1]));
            asm volatile("s_nop 1" : "+v"(ri));
            const unsigned ysh = (srow & 1) ? 0u : 16u;
#pragma unroll
            for (int i = 0; i < 8; ++i) {
                const unsigned xd = xv[i >> 2][i & 3];
                xf[i] = (f32x2){bflo1(xd), bfhi1(xd)};
                const unsigned tp = pk(add1(xf[i][0], nmb), add1(xf[i][1], nmb));
                const unsigned np = pk(mul1(bflo1(tp), ri), mul1(bfhi1(tp), ri));
                sf[i] = (f32x2){mul1(bflo1(np), bfhi1(yv[2 * i][1] << ysh)), mul1(bfhi1(np), bfhi1(yv[2 * i + 1][1] << ysh))};
                asm volatile("" : "+v"(sf[i]), "+v"(xf[i]));
            }
        }
        const int rm = lb + L_W2 + (wq < 3 ? wq * 4096 : L_SLOT) + (lf >> 5) * 2048 + (lf & 31) * 16;
        STAMP(15);
        WAIT_BAR("lgkmcnt(0)");
        STAMP(16);
        f32x4 o[4];
#pragma unroll
        for (int j = 0; j < 4; ++j) {
            LAS char* rp = lp(rm) + j * 512;
            o[j] = (*(LAS f32x4*)rp + *(LAS f32x4*)(rp + 12288)) + (*(LAS f32x4*)(rp + 24576) + *(LAS f32x4*)(rp + 36864));
        }
        f32x2 so[8], ev[8];
#pragma unroll
        for (int i = 0; i < 8; ++i) {
            const f32x2 ov = {o[i >> 1][(i & 1) * 2], o[i >> 1][(i & 1) * 2 + 1]};
            const f32x2 t = ov * (f32x2){-1.44269504088896341f, -1.44269504088896341f};
            ev[i] = (f32x2){__builtin_amdgcn_exp2f(t[0]), __builtin_amdgcn_exp2f(t[1])};
            so[i] = sf[i] * ov;
        }
        SCHED();
        u32x4 st[2];
#pragma unroll
        for (int i = 0; i < 8; ++i) {
            const f32x2 dn = ev[i] + (f32x2){1.f, 1.f};
            const f32x2 r = __builtin_elementwise_fma(so[i], (f32x2){__builtin_amdgcn_rcpf(dn[0]), __builtin_amdgcn_rcpf(dn[1])}, xf[i]);
            st[i >> 2][i & 3] = pk(r[0], r[1]);
        }
        const __amdgpu_buffer_rsrc_t orr = __builtin_amdgcn_make_buffer_rsrc((void*)p.out, 0, S * D * 2, 0x00020000);
        __builtin_amdgcn_raw_buffer_store_b128(st[0], orr, xoff, 0, 0);
        __builtin_amdgcn_raw_buffer_store_b128(st[1], orr, xoff, 16, 0);
        STAMP(17);
        STAMP_OUT(18);
    }
}

extern "C" void kernel_launch(void* const* d_in, const int* in_sizes, int n_in,
                              void* d_out, int out_size, void* d_ws, size_t ws_size,
                              hipStream_t stream) {
    Params p;
    p.x = (const u16*)d_in[0]; p.k = (const u16*)d_in[1]; p.v = (const u16*)d_in[2];
    p.sk = (const u16*)d_in[3]; p.hk = (const u16*)d_in[4]; p.dv = (const u16*)d_in[5];
    p.w1 = (const u16*)d_in[7]; p.w3 = (const u16*)d_in[8]; p.w2 = (const u16*)d_in[9];
    p.out = (u16*)d_out; p.ws = (char*)d_ws;
    (void)hipFuncSetAttribute((const void*)fused_kernel, hipFuncAttributeMaxDynamicSharedMemorySize, L_TOTAL);
    fused_kernel<<<256, 512, L_TOTAL, stream>>>(p);
}
```

```cpp
#include <hip/hip_runtime.h>
#include <stdint.h>
#include <utility>

typedef unsigned short u16;
typedef unsigned long long u64;
typedef short s16x4 __attribute__((ext_vector_type(4)));
typedef short s16x8 __attribute__((ext_vector_type(8)));
typedef __bf16 bf16x2 __attribute__((ext_vector_type(2)));
typedef __bf16 bf16x8 __attribute__((ext_vector_type(8)));
typedef float f32x2 __attribute__((ext_vector_type(2)));
typedef float f32x4 __attribute__((ext_vector_type(4)));
typedef float f32x16 __attribute__((ext_vector_type(16)));
typedef unsigned u32x2 __attribute__((ext_vector_type(2)));
typedef unsigned u32x4 __attribute__((ext_vector_type(4)));
typedef unsigned u32x16 __attribute__((ext_vector_type(16)));
#define LAS __attribute__((address_space(3)))
#define GAS __attribute__((address_space(1)))
#define RLX_AGENT __ATOMIC_RELAXED, __HIP_MEMORY_SCOPE_AGENT

constexpr int S = 2048, D = 448, F = 1792;

constexpr unsigned WS_EPOCH = 0;
constexpr unsigned WS_FH = 65536;
constexpr unsigned WS_STATS = 8192;
constexpr unsigned WS_CSUM = 24576;
constexpr unsigned WS_U = 262144;
constexpr unsigned WS_Y = WS_U + D * S * 4;
constexpr unsigned WS_H = WS_Y + D * S * 4;
constexpr unsigned WS_END = WS_H + 64 * 112 * 1024;
constexpr unsigned WS_DBG = WS_END;

constexpr int L_KV = 0;
constexpr int L_CTL = 0;
constexpr int L_CSUM = 256;
constexpr int L_STAT = 1280;
constexpr int L_RAW = 2048;
constexpr int L_KVO = 8192;
constexpr int L_UNX = 6144;
constexpr int L_SLOT = 49152;
constexpr int L_CONV = 12288;
constexpr int C_CH = 26112, C_HS = 4160, C_U = 16640, C_US = 4640;
constexpr int L_W2 = 16384 + L_SLOT;
constexpr int L_TOTAL = 163840;
__host__ __device__ constexpr int ring(int stage) { return 16384 + (stage % 3) * L_SLOT; }
constexpr unsigned CODE_PREFETCH = 36864;
#define POLL_SLEEP 4
#define HDEPTH 6
#define ROT2 (8 * ml)
#define MPRIO 1
#define EPARK 0
#define YSLEEP 16
#define TOUCH 1
#define TOUCH0 4
#define HGROW 4
#define ROTM 6
#define KVW 0
#define MWARM 0
#define URETRY 4
#define KVNT " nt"
#define ROT1 ((2 * ml + nl) % ROTM)

constexpr u64 TAG_SALT = 0x9E3779B97F4A7C15ull;
constexpr unsigned SPIN_CAP = 1u << 18;

struct Params {
    const u16* x; const u16* k; const u16* v; const u16* sk; const u16* hk; const u16* dv;
    const u16* w1; const u16* w3; const u16* w2; u16* out; char* ws;
};

__device__ __forceinline__ float bflo(unsigned p) { return __uint_as_float(p << 16); }
__device__ __forceinline__ float bfhi(unsigned p) { return __uint_as_float(p & 0xffff0000u); }
__device__ __forceinline__ float bflo1(unsigned p) { float r; asm("v_lshlrev_b32 %0, 16, %1" : "=v"(r) : "v"(p)); return r; }
__device__ __forceinline__ float bfhi1(unsigned p) { float r; asm("v_and_b32 %0, 0xffff0000, %1" : "=v"(r) : "v"(p)); return r; }
__device__ __forceinline__ float mul1(float a, float b) { float r; asm("v_mul_f32 %0, %1, %2" : "=v"(r) : "v"(a), "v"(b)); return r; }
__device__ __forceinline__ void fmac1(float& d, float a, float b) { asm("v_fmac_f32 %0, %1, %2" : "+v"(d) : "v"(a), "v"(b)); }
__device__ __forceinline__ float dpp_xor1(float v) {
    return __int_as_float(__builtin_amdgcn_update_dpp(0, __float_as_int(v), 0xB1, 0xf, 0xf, true));
}
__device__ __forceinline__ float add1(float a, float b) { float r; asm("v_add_f32 %0, %1, %2" : "=v"(r) : "v"(a), "v"(b)); return r; }
__device__ __forceinline__ unsigned vmov1(unsigned s) { unsigned r; asm volatile("v_mov_b32 %0, %1" : "=v"(r) : "s"(s)); return r; }
__device__ __forceinline__ unsigned pk(float a, float b) {
    f32x2 f = {a, b};
    return __builtin_bit_cast(unsigned, __builtin_convertvector(f, bf16x2));
}
__device__ __forceinline__ float rbf(float a) { return bflo(pk(a, a)); }
__device__ __forceinline__ float dot2(unsigned a, unsigned b, float c) {
    return __builtin_amdgcn_fdot2_f32_bf16(__builtin_bit_cast(bf16x2, a), __builtin_bit_cast(bf16x2, b), c, false);
}
__device__ __forceinline__ void glds16(const void* base, unsigned voff, unsigned ldst) {
    u64 t;
    asm volatile("s_mov_b32 m0, %3\n\ts_mov_b64 %0, %2\n\tglobal_load_lds_dwordx4 %1, %0" KVNT : "=&s"(t) : "v"(voff), "s"(base), "s"(ldst));
}
__device__ __forceinline__ void glds16x4(const void* base, u32x4 vo, unsigned ldst) {
    u64 t;
    asm volatile("s_mov_b32 m0, %6\n\ts_mov_b64 %0, %5\n\tglobal_load_lds_dwordx4 %1, %0\n\tglobal_load_lds_dwordx4 %2, %0 offset:1024\n\t"
                 "global_load_lds_dwordx4 %3, %0 offset:2048\n\tglobal_load_lds_dwordx4 %4, %0 offset:3072"
                 : "=&s"(t) : "v"(vo[0]), "v"(vo[1]), "v"(vo[2]), "v"(vo[3]), "s"(base), "s"(ldst));
}
__device__ __forceinline__ void glds16x4c(const void* base, u32x4 vo, unsigned ldst) {
    u64 t;
    asm volatile("s_mov_b32 m0, %6\n\ts_mov_b64 %0, %5\n\tglobal_load_lds_dwordx4 %1, %0 sc1\n\tglobal_load_lds_dwordx4 %2, %0 offset:1024 sc1\n\t"
                 "global_load_lds_dwordx4 %3, %0 offset:2048 sc1\n\tglobal_load_lds_dwordx4 %4, %0 offset:3072 sc1"
                 : "=&s"(t) : "v"(vo[0]), "v"(vo[1]), "v"(vo[2]), "v"(vo[3]), "s"(base), "s"(ldst) : "memory");
}
__device__ __forceinline__ void glds16x2c(const void* base, unsigned voff, unsigned ldst) {
    u64 t;
    asm volatile("s_mov_b32 m0, %3\n\ts_mov_b64 %0, %2\n\tglobal_load_lds_dwordx4 %1, %0 sc1\n\tglobal_load_lds_dwordx4 %1, %0 offset:1024 sc1"
                 : "=&s"(t) : "v"(voff), "s"(base), "s"(ldst));
}
__device__ __forceinline__ void glds16c(const void* base, unsigned voff, unsigned ldst) {
    u64 t;
    asm volatile("s_mov_b32 m0, %3\n\ts_mov_b64 %0, %2\n\tglobal_load_lds_dwordx4 %1, %0 sc1" : "=&s"(t) : "v"(voff), "s"(base), "s"(ldst));
}
__device__ __forceinline__ void glds4c(const void* base, unsigned voff, unsigned ldst) {
    u64 t;
    asm volatile("s_mov_b32 m0, %3\n\ts_mov_b64 %0, %2\n\tglobal_load_lds_dword %1, %0 sc1" : "=&s"(t) : "v"(voff), "s"(base), "s"(ldst));
}
__device__ __forceinline__ const u16* sptr(const u16* base, int off) {
    const u16* r = base + __builtin_amdgcn_readfirstlane(off);
    asm("" : "+s"(r));
    return r;
}
__device__ __forceinline__ u64 vsel(bool c, const void* a, u64 b) {
    u64 va = (u64)a;
    asm("" : "+v"(va), "+v"(b));
    return c ? va : b;
}
__device__ __forceinline__ LAS char* lp(int a) { return (LAS char*)(size_t)(unsigned)a; }
__device__ __forceinline__ u32x4 lds128(LAS char* p) { return *(LAS u32x4*)p; }
__device__ __forceinline__ u32x2 ldstr(LAS char* p) {
    return __builtin_bit_cast(u32x2, __builtin_amdgcn_ds_read_tr16_b64_v4i16((LAS s16x4*)p));
}
__device__ __forceinline__ u32x4 ldstr2(LAS char* p, int off) {
    u32x2 a = ldstr(p), b = ldstr(p + off);
    return (u32x4){a.x, a.y, b.x, b.y};
}
__device__ __forceinline__ f32x16 mfma32(u32x4 a, u32x4 b, f32x16 c) {
    return __builtin_amdgcn_mfma_f32_32x32x16_bf16(__builtin_bit_cast(bf16x8, a), __builtin_bit_cast(bf16x8, b), c, 0, 0, 0);
}
__device__ __forceinline__ f32x4 mfma16(u32x4 a, u32x4 b, f32x4 c) {
    return __builtin_amdgcn_mfma_f32_16x16x32_bf16(__builtin_bit_cast(bf16x8, a), __builtin_bit_cast(bf16x8, b), c, 0, 0, 0);
}
__device__ __forceinline__ float silu_f(float g) {
    return g * __builtin_amdgcn_rcpf(1.f + __builtin_amdgcn_exp2f(g * -1.44269504088896341f));
}
template <class Fn, int... I>
__device__ __forceinline__ void static_for(Fn&& fn, std::integer_sequence<int, I...>) { (fn(std::integral_constant<int, I>{}), ...); }
#define WAIT_VM(n) asm volatile("s_waitcnt vmcnt(" #n ")" ::: "memory")
#define WAIT_LGKM0() asm volatile("s_waitcnt lgkmcnt(0)" ::: "memory")
#define WAIT_BAR(w) asm volatile("s_waitcnt " w "\n\ts_barrier" ::: "memory")
#define SCHED() __builtin_amdgcn_sched_barrier(0)
#define CAS __attribute__((address_space(4)))
#define NSTAMP 18

__device__ __forceinline__ void wait_lds(LAS char* word) {
#pragma unroll 1
    for (unsigned spins = 0; spins < (SPIN_CAP << 4); ++spins) {
        if (__builtin_amdgcn_readfirstlane(__hip_atomic_load((LAS unsigned*)word, __ATOMIC_RELAXED, __HIP_MEMORY_SCOPE_WORKGROUP))) break;
        __builtin_amdgcn_s_sleep(1);
    }
    asm volatile("" ::: "memory");
}
__device__ __forceinline__ void wait_lds_eq(LAS char* word, unsigned want) {
#pragma unroll 1
    for (unsigned spins = 0; spins < (1u << 22); ++spins) {
        if ((unsigned)__builtin_amdgcn_readfirstlane(__hip_atomic_load((LAS unsigned*)word, __ATOMIC_RELAXED, __HIP_MEMORY_SCOPE_WORKGROUP)) == want) break;
        __builtin_amdgcn_s_sleep(1);
    }
    asm volatile("" ::: "memory");
}
#define STAMP(i)
#define STAMPG(i)
#define STAMPA(i)
#define STAMP_DECL()
#define STAMP_OUT(n)

__global__ __launch_bounds__(512, 2) void fused_kernel(Params p) {
    extern __shared__ __attribute__((aligned(16))) char smem_raw[];
    u64 pc;
    asm volatile("s_getpc_b64 %0" : "=s"(pc));
    LAS char* sm = (LAS char*)smem_raw;
    const int tid = threadIdx.x, lane = tid & 63;
    const int wave = __builtin_amdgcn_readfirstlane(tid >> 6);
    const int xcd = blockIdx.x & 7, idx = blockIdx.x >> 3;
    if (idx == 0 && wave < 5) {
        const unsigned o = (unsigned)tid * 128u;
        asm volatile("s_mov_b32 m0, %2\n\ts_nop 0\n\tglobal_load_lds_dword %0, %1" :: "v"(o < CODE_PREFETCH ? o : 0u), "s"(pc & ~(u64)255),
                     "s"((unsigned)(size_t)sm + 32768u));
    }
    SCHED();
    const __amdgpu_buffer_rsrc_t wsr = __builtin_amdgcn_make_buffer_rsrc(p.ws, 0, WS_END, 0x00020000);
    asm volatile("" :: "s"(p.x), "s"(p.k), "s"(p.v), "s"(p.sk), "s"(p.hk), "s"(p.dv), "s"(p.w1), "s"(p.w3), "s"(p.w2), "s"(p.out), "s"(p.ws));
    STAMP_DECL();
    STAMP(0);

    if (idx < 4) {
        const int ca = (xcd >> 1) * 4 + idx, cmat = ca >= 7, cnt = 7 * (xcd & 1) + (ca >= 7 ? ca - 7 : ca);
        const bool job = ca < 14;
        u32x4 xr[7], wr[14];
        const int row = (8 * (xcd >> 1) + 4 * (xcd & 1) + idx) * 64 + wave * 8 + (lane >> 3);
#pragma unroll
        for (int i = 0; i < 7; ++i) xr[i] = *(const u32x4*)(p.x + (size_t)row * D + i * 64 + (lane & 7) * 8);
        u64 epoch;
        asm volatile("s_load_dwordx2 %0, %1, 0x0\n\ts_waitcnt lgkmcnt(0)" : "=s"(epoch) : "s"(p.ws) : "memory");
        const u64 tag = epoch ^ TAG_SALT;
        const unsigned tag32 = (unsigned)tag;
        {
            float s4[4] = {0.f, 0.f, 0.f, 0.f};
#pragma unroll
            for (int i = 0; i < 7; ++i)
#pragma unroll
                for (int j = 0; j < 4; ++j) s4[j] = dot2(xr[i][j], 0x3f803f80u, s4[j]);
            float sum = (s4[0] + s4[1]) + (s4[2] + s4[3]);
            sum += __shfl_xor(sum, 1); sum += __shfl_xor(sum, 2); sum += __shfl_xor(sum, 4);
            const float mean = sum * (1.f / D);
            f32x2 q4[4] = {{0.f, 0.f}, {0.f, 0.f}, {0.f, 0.f}, {0.f, 0.f}};
#pragma unroll
            for (int i = 0; i < 7; ++i)
#pragma unroll
                for (int j = 0; j < 4; ++j) {
                    const f32x2 d = (f32x2){bflo(xr[i][j]), bfhi(xr[i][j])} - (f32x2){mean, mean};
                    q4[j] = __builtin_elementwise_fma(d, d, q4[j]);
                }
            const f32x2 q2 = (q4[0] + q4[1]) + (q4[2] + q4[3]);
            float sq = q2[0] + q2[1];
            sq += __shfl_xor(sq, 1); sq += __shfl_xor(sq, 2); sq += __shfl_xor(sq, 4);
            const u32x2 st = {tag32, pk(mean, __builtin_amdgcn_sqrtf(rbf(rbf(sq * (1.f / D)) + 1e-5f)))};
            __builtin_amdgcn_raw_buffer_store_b64(st, wsr, (lane & 7) == 0 ? WS_STATS + (unsigned)row * 8u : 0xfffffff0u, 0, 16);
        }
        STAMPA(2);
        if (job) {
            SCHED();
            const u16* wp = (cmat ? p.w3 : p.w1) + (size_t)(wave * 4 + (lane >> 4)) * F + cnt * 128 + (lane & 15) * 8;
#pragma unroll
            for (int i = 0; i < 14; ++i) wr[i] = *(const u32x4*)(wp + (size_t)(32 * i) * F);
        }
        if (job) {
            float c[8];
#pragma unroll
            for (int j = 0; j < 8; ++j) c[j] = 0.f;
#pragma unroll
            for (int i = 0; i < 14; ++i)
#pragma unroll
                for (int j = 0; j < 4; ++j) { c[2 * j] += bflo(wr[i][j]); c[2 * j + 1] += bfhi(wr[i][j]); }
            LAS char* cp = sm + ((wave * 4 + (lane >> 4)) * 136 + (lane & 15) * 8) * 4;
            *(LAS f32x4*)cp = (f32x4){c[0], c[1], c[2], c[3]};
            *(LAS f32x4*)(cp + 16) = (f32x4){c[4], c[5], c[6], c[7]};
        }
        STAMPA(3);
        if (job) {
            WAIT_BAR("lgkmcnt(0)");
            float t = 0.f;
#pragma unroll
            for (int i = 0; i < 8; ++i) t += *(LAS float*)(sm + ((i * 4 + (tid & 3)) * 136 + (tid >> 2)) * 4);
            t += __shfl_xor(t, 1); t += __shfl_xor(t, 2);
            __builtin_amdgcn_raw_buffer_store_b64((u32x2){tag32, __float_as_uint(t)}, wsr,
                (tid & 3) == 0 ? WS_CSUM + (unsigned)((cnt * 2 + cmat) * 128 + (tid >> 2)) * 8u : 0xfffffff0u, 0, 16);
        }
        STAMP(1);
        SCHED();
        if (!job) __builtin_amdgcn_s_sleep(80);
        const unsigned pf2 = *(const unsigned*)((const char*)p.hk + (size_t)((xcd * 4 + idx) * 448 + wave * 56 + (lane < 56 ? lane : 0)) * 128);
        asm volatile("" :: "v"(pf2));
        STAMP_OUT(4);
        if (blockIdx.x == 0 && wave == 7) {
            __builtin_amdgcn_s_sleep(127);
            __builtin_amdgcn_s_sleep(100);
            GAS u64* gp = (GAS u64*)(p.ws + WS_STATS) + lane * 8;
            const int fl = lane < 56 ? lane : 0;
            GAS u64* fp = (GAS u64*)(p.ws + WS_FH) + ((fl / 14) * 56 + (fl % 14) * 2) * 16;
#pragma unroll 1
            for (unsigned spins = 0; spins < SPIN_CAP; ++spins) {
                bool ok = true;
#pragma unroll
                for (int m = 0; m < 4; ++m)
                    ok &= ((unsigned)__hip_atomic_load(gp + m * 512, RLX_AGENT) == tag32) & (__hip_atomic_load(fp + m * 224 * 16, RLX_AGENT) == tag);
                if (__all(ok)) break;
                __builtin_amdgcn_s_sleep(32);
            }
            if (lane == 0) __hip_atomic_store((GAS u64*)(p.ws + WS_EPOCH), epoch + 1, RLX_AGENT);
        }
        return;
    }

    const int gi = idx - 4, ml = gi & 3, nl = gi >> 2;
    const int mt = 4 * (xcd >> 1) + ml, nt = 7 * (xcd & 1) + nl;
    const int rot1 = ROT1;
    const int m64 = 4 * xcd + ml, n64 = nl;
    const int gpair = xcd * 28 + gi;
    const int r32 = lane & 31, hh = lane >> 5, blk = (lane >> 4) & 1, q4 = (lane & 15) >> 2, p4 = lane & 3;
    const unsigned lb = (unsigned)(size_t)sm;
    const int wq = wave & 3;
    u32x4 wvo;
    wvo[0] = (unsigned)((lane >> 4) * F + (((lane & 15) ^ ((lane >> 4) << 2)) * 8)) * 2u;
#pragma unroll
    for (int j = 1; j < 4; ++j) wvo[j] = wvo[0] + j * (8 * F - 1024);
    const u16* w1b = sptr(p.w1, wq * 16 * F + nt * 128);
    const u16* w3b = sptr(p.w3, wq * 16 * F + nt * 128);
    auto kst = [&](int si) { if (ROTM == 6 && si == 0) return 0; const int s = si + rot1; return s >= 7 ? s - ROTM : s; };
    auto group_w = [&](int si, int m) { glds16x4(sptr(m ? w3b : w1b, kst(si) * 64 * F), wvo, lb + ring(si) + 16384 + m * 16384 + wq * 4096); };
    const int kr0 = (gpair & 31) * 64, kc0 = (gpair >> 5) * 64;
    const int chn = wq >> 1, par = wq & 1, dch = 2 * gpair + chn;
    if (wave < 4) group_w(0, 0);
    {
        const bool isk = lane >= 8 && lane < 14;
        u64 mb_ = vsel(lane == 15, p.dv, vsel(lane == 14, p.hk, vsel(isk, p.k, (u64)p.sk)));
        unsigned mo = (unsigned)((kc0 + wq * 16) * 8 + (lane & 7) * 16);
        mo = isk ? (unsigned)((kr0 ? kr0 - 3 + ((lane - 8) >> 1) : 0) * D + kc0 + wq * 16 + (lane & 1) * 8) * 2u : mo;
        mo = lane == 14 ? (unsigned)dch * 4096u : mo;
        mo = lane == 15 ? (unsigned)(dch & ~7) * 2u : mo;
        const u64 ma = mb_ + mo;
        asm volatile("s_mov_b32 m0, %1\n\ts_nop 0\n\tglobal_load_lds_dwordx4 %0, off" :: "v"(ma), "s"(lb + wave * 1024));
    }
    if (wave < 4) group_w(0, 1);
    SCHED();
    const int rot2 = ROT2;
    const int w2i = L_W2 + wq * 12288;
    auto fresh_lane = [&]() { int l = lane; asm volatile("" : "+v"(l)); return l; };
    const int latei = L_CONV + wq * 8192;
    auto pair_w2 = [&](u32x4 vo, int i, int dst) {
        int t = i + rot2; t = t >= 28 ? t - 28 : t;
        glds16x4(sptr(p.w2, (wq * 448 + t * 16) * D + n64 * 64), vo, lb + dst);
    };
    auto w2_off = [&]() {
        const int l = fresh_lane();
        const unsigned o = (unsigned)((l >> 3) * D + (((l & 7) ^ (((l >> 4) & 1) << 2)) * 8)) * 2u;
        return (u32x4){o, o + (16 * D - 1024), o + 2 * (16 * D - 1024), o + 3 * (16 * D - 1024)};
    };

    if (wave < 4) {
        u64 epoch;
        asm volatile("s_load_dwordx2 %0, %1, 0x0" : "=s"(epoch) : "s"(p.ws) : "memory");
        __builtin_amdgcn_s_setprio(MPRIO);
        const int wm = wave >> 1, wn = wave & 1;
        int xa[4], wa0, wa1;
#pragma unroll
        for (int i = 0; i < 4; ++i) xa[i] = lb + ring(0) + (((64 * wm + r32) * 128 + ((hh ^ ((r32 >> 1) & 7)) * 16)) ^ (i * 32));
        wa0 = 16384 + (8 * hh + q4) * 256 + (((8 * wn + 2 * blk + (p4 >> 1)) ^ (q4 << 2)) * 16) + 8 * (p4 & 1);
        wa1 = (wa0 ^ 64) + lb + ring(0); wa0 += lb + ring(0);
        f32x16 acc[2][2][2];
#pragma unroll
        for (int i = 0; i < 8; ++i)
#pragma unroll
            for (int r = 0; r < 16; ++r) acc[i >> 2][(i >> 1) & 1][i & 1][r] = 0.f;
        u32x4 fx[2][2], fw[2][2][2];
        auto load_frag = [&](int g, int j) {
            const int pp = g & 3, b = g & 1;
            if (j == 0) {
                if (g >= 4) {
                    const int d = ring(g >> 2) - ring((g >> 2) - 1);
                    if (pp == 0) { wa0 += d; wa1 += d; asm volatile("" : "+v"(wa0), "+v"(wa1)); }
                    xa[pp] += d; asm volatile("" : "+v"(xa[pp]));
                }
                fw[b][0][0] = ldstr2(lp(wa0) + pp * 4096, 1024);
            }
            if (j == 1) { fx[b][0] = lds128(lp(xa[pp])); fx[b][1] = lds128(lp(xa[pp]) + 4096); }
            if (j == 2) fw[b][0][1] = ldstr2(lp(wa0) + pp * 4096 + 16384, 1024);
            if (j == 3) fw[b][1][0] = ldstr2(lp(wa1) + pp * 4096, 1024);
            if (j == 4) fw[b][1][1] = ldstr2(lp(wa1) + pp * 4096 + 16384, 1024);
        };
        int ka0[2], kam[2], kwo;
#pragma unroll
        for (int rd = 0; rd < 2; ++rd) {
            const int ck = 2 * wq + rd;
            const int bq = 2 * (lane >> 4) + (p4 >> 1), bm = bq ? bq - 1 : 0;
            ka0[rd] = lb + L_KV + (8 * bq + q4) * 128 + ((ck ^ (((q4 >> 1) & 1) | ((bq & 3) << 1))) * 16) + 8 * (p4 & 1);
            kam[rd] = lb + L_KV + (8 * bm + 4 + q4) * 128 + ((ck ^ (((q4 >> 1) & 1) | ((bm & 3) << 1))) * 16) + 8 * (p4 & 1);
        }
        kwo = lb + L_KVO + wq * 2048 + (lane & 7) * 128 + (((7 - (lane >> 3)) ^ (lane & 7)) * 16);
        asm volatile("" : "+v"(ka0[0]), "+v"(ka0[1]), "+v"(kam[0]), "+v"(kam[1]), "+v"(kwo));
        WAIT_VM(4);
        u32x2 skv[2], hal[2];
#pragma unroll
        for (int rd = 0; rd < 2; ++rd) {
            LAS char* mp = sm + wave * 1024;
            skv[rd] = *(LAS u32x2*)(mp + (rd * 8 + (lane & 7)) * 8);
            const unsigned hm = kr0 ? 0xffffffffu : 0u;
            const unsigned a = *(LAS u16*)(mp + 128 + rd * 16 + (lane & 7) * 2), b = *(LAS u16*)(mp + 160 + rd * 16 + (lane & 7) * 2),
                           c = *(LAS u16*)(mp + 192 + rd * 16 + (lane & 7) * 2);
            hal[rd] = (u32x2){(a << 16) & hm, (b | (c << 16)) & hm};
            asm volatile("" : "+v"(skv[rd]), "+v"(hal[rd]));
        }
        asm volatile("s_waitcnt vmcnt(0) lgkmcnt(0)\n\ts_barrier" : "+s"(epoch) :: "memory");
        const u64 tag = epoch ^ TAG_SALT;
        STAMP(1);
        u32x2 kt[2][3], vt[2][2];
        float cf[4], kk[11], ko[8];
        u32x4 kpk;
        auto fill = [&](auto kc) {
            constexpr int k = decltype(kc)::value;
            if constexpr (k < 4) {
                constexpr int rd = k >> 1;
                if constexpr ((k & 1) == 0) { kt[rd][0] = ldstr(lp(kam[rd])); kt[rd][1] = ldstr(lp(ka0[rd])); kt[rd][2] = ldstr(lp(ka0[rd]) + 512); }
                else { vt[rd][0] = ldstr(lp(ka0[rd]) + 8192); vt[rd][1] = ldstr(lp(ka0[rd]) + 8192 + 512); }
            } else if constexpr (k < 4 + 2 * 16) {
                constexpr int rd = (k - 4) / 16, s = (k - 4) % 16;
                if constexpr (s == 0) { cf[0] = bflo1(skv[rd][0]); cf[1] = bfhi1(skv[rd][0]); cf[2] = bflo1(skv[rd][1]); cf[3] = bfhi1(skv[rd][1]); }
                else if constexpr (s == 1) {
                    const unsigned a = lane < 8 ? hal[rd][0] : kt[rd][0][0], b = lane < 8 ? hal[rd][1] : kt[rd][0][1];
                    kk[0] = bfhi1(a); kk[1] = bflo1(b); kk[2] = bfhi1(b);
                } else if constexpr (s < 4) {
                    const u32x2 t = kt[rd][s - 1];
                    kk[4 * s - 5] = bflo1(t[0]); kk[4 * s - 4] = bfhi1(t[0]); kk[4 * s - 3] = bflo1(t[1]); kk[4 * s - 2] = bfhi1(t[1]);
                } else if constexpr (s < 12) {
                    constexpr int tp = (s - 4) >> 1, o0 = ((s - 4) & 1) * 4;
#pragma unroll
                    for (int i = o0; i < o0 + 4; ++i) {
                        if (tp == 0) ko[i] = mul1(kk[i], cf[3]);
                        else fmac1(ko[i], kk[i + tp], cf[3 - tp]);
                    }
                } else {
                    constexpr int j = s - 12;
                    const unsigned v = vt[rd][j >> 1][j & 1];
                    kpk[3 - j] = pk(mul1(ko[2 * j + 1], bfhi1(v)), mul1(ko[2 * j], bflo1(v)));
                    if constexpr (j == 3) *(LAS u32x4*)(lp(kwo) + rd * 1024) = kpk;
                }
            }
        };
#pragma unroll
        for (int j = 0; j < 5; ++j) load_frag(0, j);
        f32x4 cs[2][2][4];
        unsigned tch[4], tch0 = 0;
        unsigned tc0 = (unsigned)((gpair * 4 + wq) * 14 + (lane < 14 ? lane : 13)) * 128u;
        unsigned tco = (unsigned)(gi * 4 + wq + (lane < 28 ? lane : 27) * 448) * 128u;
        asm volatile("" : "+v"(tco));
        static_for([&](auto gc) {
            constexpr int g = decltype(gc)::value, b = g & 1, pp = g & 3;
            SCHED();
            if (g == 11) STAMP(2);
            if (pp == 3 && g < 27) { STAMPG(2 + 2 * (g >> 2)); WAIT_BAR("lgkmcnt(0)"); STAMPG(3 + 2 * (g >> 2)); }
            if (g == 27) {
                WAIT_LGKM0();
                if (lane == 0) __hip_atomic_fetch_add((LAS unsigned*)(sm + L_CTL + 12), 1u, __ATOMIC_RELAXED, __HIP_MEMORY_SCOPE_WORKGROUP);
            }
            if (g == 11) STAMP(3);
            static_for([&](auto ic) {
                constexpr int i = decltype(ic)::value, ft = i >> 2, m = (i >> 1) & 1, sb = i & 1;
                acc[sb][ft][m] = mfma32(fw[b][ft][m], fx[b][sb], acc[sb][ft][m]);
                SCHED();
                if (g < 27 && i < 5) load_frag(g + 1, i);
                if constexpr (MWARM && i == 6 && g == MWARM - 1) {
                    u64 t;
                    asm volatile("s_mov_b64 %1, %3\n\tglobal_load_dword %0, %2, %1" : "=v"(tch0), "=&s"(t) : "v"(tc0), "s"(p.w2));
                }
                if constexpr (TOUCH && i == 4 && pp == 2 && g >= TOUCH0 && g < TOUCH0 + 16) {
                    u64 t;
                    asm volatile("s_mov_b64 %1, %3\n\tglobal_load_dword %0, %2, %1" : "=v"(tch[(g - TOUCH0) >> 2]), "=&s"(t)
                                 : "v"(tco), "s"((const char*)p.w2 + ((g - TOUCH0) >> 2) * (112 * 128)));
                }
                if constexpr (i >= 5 && g >= 7 + 4 * KVW) fill(std::integral_constant<int, (g - 7 - 4 * KVW) * 3 + i - 5>{});
                SCHED();
            }, std::make_integer_sequence<int, 8>{});
        }, std::make_integer_sequence<int, 28>{});
        STAMP(4);
        wait_lds_eq(sm + L_CTL + 64, 3u);
        unsigned sa[2];
#pragma unroll
        for (int i = 0; i < 8; ++i) cs[i >> 2][0][i & 3] = *(LAS f32x4*)(sm + L_CSUM + (64 * wn + 32 * (i >> 2) + 8 * (i & 3) + 4 * hh) * 4);
        sa[0] = *(LAS unsigned*)(sm + L_STAT + (64 * wm + r32) * 4);
        sa[1] = *(LAS unsigned*)(sm + L_STAT + (64 * wm + 32 + r32) * 4);
#pragma unroll
        for (int i = 0; i < 8; ++i) cs[i >> 2][1][i & 3] = *(LAS f32x4*)(sm + L_CSUM + 512 + (64 * wn + 32 * (i >> 2) + 8 * (i & 3) + 4 * hh) * 4);
        const int lw = fresh_lane();
        const int w2a0 = lb + w2i + (4 * (lw >> 5) + ((lw & 15) >> 2)) * 128 + (((2 * ((lw >> 4) & 1) + ((lw & 3) >> 1)) ^ (((lw >> 3) & 1) << 2)) * 16) + 8 * (lw & 1);
        const int w2a1 = w2a0 ^ 64;
        u32x4 w2r[12][2];
        auto park = [&](int i) {
            w2r[i][0] = ldstr2(lp(w2a0) + (i / 6) * L_SLOT + (i % 6) * 2048, 1024);
            w2r[i][1] = ldstr2(lp(w2a1) + (i / 6) * L_SLOT + (i % 6) * 2048, 1024);
        };
#pragma unroll
        for (int sb = 0; sb < 2; ++sb) {
            const float ri = __builtin_amdgcn_rcpf(bfhi(sa[sb])) * -1.44269504088896341f, nm = -bflo(sa[sb]), d2 = bfhi(sa[sb]) * bfhi(sa[sb]);
            const f32x2 ri2 = {ri, ri}, nm2 = {nm, nm}, dd2 = {d2, d2};
#pragma unroll
            for (int ft = 0; ft < 2; ++ft) {
#pragma unroll
                for (int t2 = 0; t2 < 2; ++t2) {
                    const int grp = sb * 4 + ft * 2 + t2;
                    if (EPARK && grp == 2) { SCHED(); wait_lds(sm + L_CTL + 48 + wq * 4); }
                    if (EPARK && grp >= 2) { SCHED(); park(grp - 2); }
                    f32x2 gu[4], ee[4];
#pragma unroll
                    for (int j = 0; j < 4; ++j) {
                        const int r = 8 * t2 + 2 * j;
                        const f32x2 g = __builtin_elementwise_fma(nm2, (f32x2){cs[ft][0][r >> 2][r & 3], cs[ft][0][r >> 2][(r & 3) + 1]}, (f32x2){acc[sb][ft][0][r], acc[sb][ft][0][r + 1]});
                        const f32x2 u = __builtin_elementwise_fma(nm2, (f32x2){cs[ft][1][r >> 2][r & 3], cs[ft][1][r >> 2][(r & 3) + 1]}, (f32x2){acc[sb][ft][1][r], acc[sb][ft][1][r + 1]});
                        const f32x2 t = g * ri2;
                        ee[j] = (f32x2){__builtin_amdgcn_exp2f(t[0]), __builtin_amdgcn_exp2f(t[1])};
                        gu[j] = g * u;
                    }
                    u32x4 st;
#pragma unroll
                    for (int j = 0; j < 4; ++j) {
                        const f32x2 dn = __builtin_elementwise_fma(ee[j], dd2, dd2);
                        const f32x2 hv = gu[j] * (f32x2){__builtin_amdgcn_rcpf(dn[0]), __builtin_amdgcn_rcpf(dn[1])};
                        st[j] = pk(hv[0], hv[1]);
                    }
                    const unsigned fi = (unsigned)((4 * mt + 2 * wm + sb) * 112 + 8 * nt + 4 * wn + 2 * ft + t2);
                    __builtin_amdgcn_raw_buffer_store_b128(st, wsr, WS_H + fi * 1024u + lane * 16u, 0, 16);
                }
            }
        }
        SCHED();
        STAMP(5);
        const u32x4 w2o = w2_off();
        GAS u64* myflag = (GAS u64*)(p.ws + WS_FH + ((2 * mt + wm) * 28 + 2 * nt + wn) * 128);
        if (!__builtin_amdgcn_readfirstlane(__hip_atomic_load((LAS unsigned*)(sm + L_CTL + 112 + wq * 4), __ATOMIC_RELAXED, __HIP_MEMORY_SCOPE_WORKGROUP))) {
            WAIT_VM(0);
            if (lane == 0) __hip_atomic_store(myflag, tag, RLX_AGENT);
            wait_lds(sm + L_CTL + 112 + wq * 4);
        }
        asm volatile("" ::: "memory");
#pragma unroll
        for (int i = 6; i < 12; ++i) park(i);
        WAIT_VM(0);
        if (TOUCH) asm volatile("" :: "v"(tch[0]), "v"(tch[1]), "v"(tch[2]), "v"(tch[3]));
        asm volatile("" :: "v"(tch0));
        if (lane == 0) __hip_atomic_store(myflag, tag, RLX_AGENT);
        STAMP(6);
        if (!EPARK) {
#pragma unroll
            for (int i = 0; i < 6; ++i) park(i);
        }
        asm volatile("" ::: "memory");
        __hip_atomic_store((LAS unsigned*)(sm + L_CTL + 32 + wq * 4), 1u, __ATOMIC_RELAXED, __HIP_MEMORY_SCOPE_WORKGROUP);
        WAIT_LGKM0();
#pragma unroll
        for (int i = 0; i < 12; ++i) asm volatile("" : "+v"(w2r[i][0]), "+v"(w2r[i][1]));
#pragma unroll
        for (int i = 0; i < 3; ++i) pair_w2(w2o, 12 + 2 * i, w2i + i * 4096);
#pragma unroll
        for (int i = 0; i < 3; ++i) pair_w2(w2o, 18 + 2 * i, w2i + L_SLOT + i * 4096);
        STAMP(7);
        {
            GAS u64* fp = (GAS u64*)(p.ws + WS_FH + (m64 * 28 + 7 * wq + (lw < 7 ? lw : 0)) * 128);
#pragma unroll 1
            for (unsigned spins = 0; spins < SPIN_CAP; ++spins) {
                if (__all(__hip_atomic_load(fp, RLX_AGENT) == tag)) break;
                __builtin_amdgcn_s_sleep(POLL_SLEEP);
            }
            __builtin_amdgcn_fence(__ATOMIC_ACQUIRE, "wavefront");
        }
        STAMP(11);
        const unsigned hb = WS_H + (unsigned)(2 * m64 * 112 + 28 * wq) * 1024u + lw * 16u;
        u32x4 hf[28][2];
        auto load_h = [&](int i, int j) {
            int t = i + rot2; t = t >= 28 ? t - 28 : t;
            hf[i][j] = __builtin_amdgcn_raw_buffer_load_b128(wsr, hb + j * (112u * 1024u), t * 1024, 16);
        };
        constexpr int HD = HDEPTH, HG = HGROW;
#pragma unroll
        for (int i = 0; i < HD; ++i) { load_h(i, 0); load_h(i, 1); }
        STAMP(14);
        f32x16 ac2[2][2];
#pragma unroll
        for (int i = 0; i < 4; ++i)
#pragma unroll
            for (int r = 0; r < 16; ++r) ac2[i >> 1][i & 1][r] = 0.f;
        u32x4 a0 = w2r[0][0], a1 = w2r[0][1];
        static_for([&](auto ic) {
            constexpr int i = decltype(ic)::value, n = i + 1;
            SCHED();
            if (i == 23) wait_lds(sm + L_CTL + 80 + wq * 4);
            u32x4 b0, b1;
            if constexpr (n < 12) { b0 = w2r[n][0]; b1 = w2r[n][1]; }
            else if constexpr (n < 28) {
                constexpr int so = n < 24 ? ((n - 12) / 6) * L_SLOT + ((n - 12) % 6) * 2048 : (n - 24) * 2048;
                const int lo = n < 24 ? 0 : latei - w2i;
                b0 = ldstr2(lp(w2a0 + lo) + so, 1024);
                b1 = ldstr2(lp(w2a1 + lo) + so, 1024);
            }
            SCHED();
            ac2[0][0] = mfma32(a0, hf[i][0], ac2[0][0]);
            SCHED();
            constexpr int l0 = i < HG ? HD + 2 * i : HD + HG + i;
            if constexpr (l0 < 28) load_h(l0, 0);
            SCHED();
            ac2[1][0] = mfma32(a1, hf[i][0], ac2[1][0]);
            SCHED();
            if constexpr (l0 < 28) load_h(l0, 1);
            SCHED();
            ac2[0][1] = mfma32(a0, hf[i][1], ac2[0][1]);
            SCHED();
            if constexpr (i < HG) load_h(l0 + 1, 0);
            SCHED();
            ac2[1][1] = mfma32(a1, hf[i][1], ac2[1][1]);
            SCHED();
            if constexpr (i < HG) load_h(l0 + 1, 1);
            a0 = b0; a1 = b1;
        }, std::make_integer_sequence<int, 28>{});
        SCHED();
        STAMP(15);
        WAIT_LGKM0();
#pragma unroll
        for (int v = 0; v < 8; ++v) {
            LAS char* sl = sm + w2i + (v < 6 ? v * 2048 : L_SLOT + (v - 6) * 2048) + lw * 16;
            const f32x16& t = ac2[v >> 2][(v >> 1) & 1];
            const int r0 = 8 * (v & 1);
            *(LAS f32x4*)sl = (f32x4){t[r0], t[r0 + 1], t[r0 + 2], t[r0 + 3]};
            *(LAS f32x4*)(sl + 1024) = (f32x4){t[r0 + 4], t[r0 + 5], t[r0 + 6], t[r0 + 7]};
        }
        WAIT_BAR("lgkmcnt(0)");
        STAMP(16);
        STAMP(17);
        STAMP_OUT(18);
    } else {
        u64 epoch;
        asm volatile("s_load_dwordx2 %0, %1, 0x0" : "=s"(epoch) : "s"(p.ws) : "memory");
        u32x4 xvo;
#pragma unroll
        for (int j = 0; j < 4; ++j) xvo[j] = (unsigned)((8 * j + (lane >> 3)) * D + ((((lane & 7) ^ (lane >> 4)) ^ (4 * (j & 1))) * 8)) * 2u - 1024u * j;
        const u16* xb = sptr(p.x, (mt * 128 + wq * 32) * D);
        auto group_x = [&](int si) { glds16x4(sptr(xb, kst(si) * 64), xvo, lb + ring(si) + wq * 4096); };
        group_x(0);
        group_x(1); group_w(1, 0); group_w(1, 1);
        const int cbi = L_CONV + chn * C_CH;
        LAS char* cb = sm + cbi;
        unsigned kvo[2], uso;
#pragma unroll
        for (int rd = 0; rd < 2; ++rd) kvo[rd] = (unsigned)((lane >> 3) * D + (((lane & 7) ^ (((lane >> 4) & 1) | (((2 * wq + rd) & 3) << 1))) * 8)) * 2u;
        uso = WS_U + (unsigned)(kc0 + wq * 16 + (lane >> 3)) * 8192u + (unsigned)(1984 - kr0 + (lane & 7) * 8) * 2u;
        int kro = lb + L_KVO + wq * 2048 + (lane >> 3) * 128 + (((lane & 7) ^ (lane >> 3)) * 16);
        const int cc = 2 * par + ((lane >> 2) & 1);
        int zo = cbi + C_U + par * C_US + (248 + (lane < 36 ? lane : 35)) * 16;
        zo = lane < 8 ? cbi + cc * C_HS + (lane & 3) * 16 : zo;
        u32x4 w2o = w2_off();
        const unsigned fo = lane * 16u;
        unsigned ao = (wq < 2 ? WS_CSUM + (unsigned)(nt * 2 + wq) * 1024u : WS_STATS + mt * 1024u) + fo;
        asm volatile("" : "+v"(zo), "+v"(w2o), "+v"(kvo[0]), "+v"(kvo[1]), "+v"(uso), "+v"(kro), "+v"(ao));
        asm volatile("s_waitcnt vmcnt(16) lgkmcnt(0)" : "+s"(epoch) :: "memory");
        const unsigned tag32 = (unsigned)(epoch ^ TAG_SALT);
        u32x4 bval, ug[2] = {{tag32, 0u, tag32, 0u}, {tag32, 0u, tag32, 0u}};
        unsigned dbits = *(LAS u16*)(sm + wave * 1024 + 240 + (dch & 7) * 2);
        {
            const u32x4 h0 = lds128(sm + wave * 1024 + 224);
            const bool s1 = !(cc & 1), s2 = !(cc & 2);
            const unsigned a0 = s1 ? 0u : h0[0], a1 = s1 ? h0[0] : h0[1], a2 = s1 ? h0[1] : h0[2], a3 = s1 ? h0[2] : h0[3];
            const unsigned lm = (lane == 3 || lane == 7) ? 0xffffffffu : 0u;
            bval = (u32x4){(s2 ? 0u : a0) & lm, (s2 ? 0u : a1) & lm, (s2 ? a0 : a2) & lm, (s2 ? a1 : a3) & lm};
            asm volatile("" : "+v"(bval), "+v"(dbits), "+v"(ug[0]), "+v"(ug[1]));
        }
        auto kv_store = [&](int rd) {
            LAS unsigned* kp = (LAS unsigned*)(lp(kro) + rd * 1024);
            ug[0][1] = kp[0]; asm volatile("" ::: "memory"); ug[0][3] = kp[1]; asm volatile("" ::: "memory");
            ug[1][1] = kp[2]; asm volatile("" ::: "memory"); ug[1][3] = kp[3];
            __builtin_amdgcn_raw_buffer_store_b128(ug[0], wsr, uso, rd * 65536, 16);
            __builtin_amdgcn_raw_buffer_store_b128(ug[1], wsr, uso, rd * 65536 + 4096, 16);
        };
        WAIT_BAR("vmcnt(12) lgkmcnt(0)");
        STAMP(1);
#pragma unroll
        for (int w = 0; w < 5; ++w) {
            group_x(w + 2); group_w(w + 2, 0); group_w(w + 2, 1);
            if (w == KVW) {
#pragma unroll
                for (int j = 0; j < 2; ++j) {
                    glds16(sptr(p.k, (kr0 + (2 * wq + j) * 8) * D + kc0), kvo[j], lb + L_KV + (2 * wq + j) * 1024);
                    glds16(sptr(p.v, (kr0 + (2 * wq + j) * 8) * D + kc0), kvo[j], lb + L_KV + 8192 + (2 * wq + j) * 1024);
                }
            }
            if (w == 4 && lane < 32) *(LAS unsigned*)(sm + L_CTL + lane * 4) = 0u;
            if (w == 4 && KVW == 0) kv_store(0);
            if (w == 2) STAMP(2);
            STAMPG(2 + 2 * w);
            if (w == KVW) WAIT_BAR("vmcnt(16) lgkmcnt(0)"); else WAIT_BAR("vmcnt(12) lgkmcnt(0)");
            STAMPG(3 + 2 * w);
            if (w == 2) STAMP(3);
        }
        glds16c(p.ws, ao, lb + L_RAW + wq * 1024);
#pragma unroll
        for (int i = 0; i < 3; ++i) pair_w2(w2o, 2 * i, w2i + i * 4096);
        kv_store(KVW ? 0 : 1);
        STAMPG(12); WAIT_BAR("vmcnt(12) lgkmcnt(0)"); STAMPG(13);
        if (KVW) kv_store(1);
        asm volatile("" ::: "memory");
        if (lane == 0) __hip_atomic_fetch_add((LAS unsigned*)(sm + L_CTL + 12), 1u, __ATOMIC_RELAXED, __HIP_MEMORY_SCOPE_WORKGROUP);
        if (wq < 3) {
            u32x4 av = lds128(sm + L_RAW + wq * 1024 + lane * 16);
#pragma unroll 1
            for (unsigned spins = 0; spins < SPIN_CAP; ++spins) {
                if (__all(((av[0] ^ tag32) | (av[2] ^ tag32)) == 0u)) break;
                asm volatile("" ::: "memory");
                av = __builtin_amdgcn_raw_buffer_load_b128(wsr, ao, 0, 16);
                asm volatile("" : "+v"(av));
            }
            *(LAS u32x2*)(sm + L_CSUM + wq * 512 + lane * 8) = (u32x2){av[1], av[3]};
            asm volatile("" ::: "memory");
            if (lane == 0) __hip_atomic_fetch_add((LAS unsigned*)(sm + L_CTL + 64), 1u, __ATOMIC_RELAXED, __HIP_MEMORY_SCOPE_WORKGROUP);
        }
        STAMP(4);
#pragma unroll
        for (int i = 0; i < 3; ++i) pair_w2(w2o, 6 + 2 * i, w2i + L_SLOT + i * 4096);
        WAIT_VM(12);
        __hip_atomic_store((LAS unsigned*)(sm + L_CTL + 48 + wq * 4), 1u, __ATOMIC_RELAXED, __HIP_MEMORY_SCOPE_WORKGROUP);
        wait_lds_eq(sm + L_CTL + 12, 8u);
        STAMP(5);
        __builtin_amdgcn_s_setprio(2);
        const unsigned ubase = WS_U + (unsigned)dch * 8192u;
        glds16x2c(p.ws + ubase + par * 2048, fo, lb + cbi + C_U + par * 2048);
        glds16x2c(p.ws + ubase + 4096 + par * 2048, fo, lb + cbi + C_U + C_US + par * 2048);
        glds4c(p.ws + ubase + (1 - par) * 2048, (lane & 1) * 4u, lb + L_UNX + wq * 256);
        {
            const u32x4 fvo = {fo, fo, fo, lane == 63 ? 62u * 16u : fo};
#pragma unroll
            for (int c = 0; c < 2; ++c) glds16x4(sptr(p.hk, dch * S + 4 * par + 2 * c + 2), fvo, lb + cbi + (2 * par + c) * C_HS + 64);
        }
        *(LAS u32x4*)(sm + zo) = bval;
        WAIT_VM(13);
        __hip_atomic_store((LAS unsigned*)(sm + L_CTL + 112 + wq * 4), 1u, __ATOMIC_RELAXED, __HIP_MEMORY_SCOPE_WORKGROUP);
        WAIT_VM(8);
        {
            u32x4 ua[2], ub[2]; u32x2 un[2];
#pragma unroll
            for (int rd = 0; rd < 2; ++rd) {
                const int x8 = 128 * par + 64 * rd + lane;
                ua[rd] = lds128(cb + C_U + x8 * 16); ub[rd] = lds128(cb + C_U + C_US + x8 * 16);
                un[rd] = *(LAS u32x2*)((rd == 1 && lane == 63) ? sm + L_UNX + wq * 256 : cb + C_U + x8 * 16 + 16);
            }
#pragma unroll 1
            for (unsigned spins = 0; spins < SPIN_CAP; ++spins) {
                unsigned df = 0;
#pragma unroll
                for (int rd = 0; rd < 2; ++rd) df |= (ua[rd][0] ^ tag32) | (ua[rd][2] ^ tag32) | (ub[rd][0] ^ tag32) | (ub[rd][2] ^ tag32) | (un[rd][0] ^ tag32);
                if (__all(df == 0u)) break;
                __builtin_amdgcn_s_sleep(URETRY);
                asm volatile("" ::: "memory");
#pragma unroll
                for (int rd = 0; rd < 2; ++rd) {
                    const int x8 = 128 * par + 64 * rd + lane;
                    const unsigned uo = ubase + (unsigned)x8 * 16u;
                    ua[rd] = __builtin_amdgcn_raw_buffer_load_b128(wsr, uo, 0, 16);
                    ub[rd] = __builtin_amdgcn_raw_buffer_load_b128(wsr, uo + 4096u, 0, 16);
                    un[rd] = __builtin_amdgcn_raw_buffer_load_b64(wsr, x8 == 255 ? uo : uo + 16u, 0, 16);
                }
                asm volatile("" : "+v"(ua[0]), "+v"(ub[0]), "+v"(un[0]), "+v"(ua[1]), "+v"(ub[1]), "+v"(un[1]));
            }
#pragma unroll
            for (int rd = 0; rd < 2; ++rd) {
                const int x8 = 128 * par + 64 * rd + lane;
                const unsigned nx = x8 == 255 ? 0u : un[rd][1];
                *(LAS u32x4*)(cb + C_U + x8 * 16) = (u32x4){__builtin_amdgcn_alignbit(ua[rd][3], ua[rd][1], 16), __builtin_amdgcn_alignbit(ub[rd][1], ua[rd][3], 16),
                                                            __builtin_amdgcn_alignbit(ub[rd][3], ub[rd][1], 16), __builtin_amdgcn_alignbit(nx, ub[rd][3], 16)};
                *(LAS u32x4*)(cb + C_U + C_US + x8 * 16) = (u32x4){ua[rd][1], ua[rd][3], ub[rd][1], ub[rd][3]};
            }
        }
        asm volatile("s_waitcnt vmcnt(0) lgkmcnt(0)" ::: "memory");
        __hip_atomic_store((LAS unsigned*)(sm + L_CTL + 16 + wq * 4), 1u, __ATOMIC_RELAXED, __HIP_MEMORY_SCOPE_WORKGROUP);
        u32x4 lw2[8], xv[2];
        {
            const __amdgpu_buffer_rsrc_t wr2 = __builtin_amdgcn_make_buffer_rsrc((void*)p.w2, 0, F * D * 2, 0x00020000);
#pragma unroll
            for (int j = 0; j < 8; ++j) {
                int t = 24 + (j >> 2) * 2 + rot2; t = t >= 28 ? t - 28 : t;
                lw2[j] = __builtin_amdgcn_raw_buffer_load_b128(wr2, w2o[0], ((wq * 448 + t * 16 + (j & 3) * 8) * D + n64 * 64) * 2, 0);
            }
        }
        const int lf = fresh_lane();
        const int srow = 64 * m64 + 32 * (wq & 1) + (lf & 31);
        const int dd = 64 * n64 + 32 * (wq >> 1) + 16 * (lf >> 5);
        const unsigned xoff = (unsigned)(srow * D + dd) * 2u;
        {
            const __amdgpu_buffer_rsrc_t xr = __builtin_amdgcn_make_buffer_rsrc((void*)p.x, 0, S * D * 2, 0x00020000);
            xv[0] = __builtin_amdgcn_raw_buffer_load_b128(xr, xoff, 0, 0);
            xv[1] = __builtin_amdgcn_raw_buffer_load_b128(xr, xoff, 16, 0);
        }
        SCHED();
        wait_lds(sm + L_CTL + 16 + (wq ^ 1) * 4);
        STAMP(7);
        STAMP(6);
        const int n16 = lane & 15, kg = lane >> 4;
        f32x4 ca[4];
#pragma unroll
        for (int j = 0; j < 4; ++j) ca[j] = (f32x4){0.f, 0.f, 0.f, 0.f};
        int apo = lb + cbi + (n16 & 3) * C_HS + ((n16 >> 2) + kg) * 16;
        int bpo = lb + cbi + C_U + (n16 & 1) * C_US + (252 - 4 * (n16 >> 1) + kg) * 16 - 7 * 512;
        u32x4 fa[2][8], fb[2][8];
#pragma unroll
        for (int i = 0; i < 8; ++i) { fa[0][i] = lds128(lp(apo) + i * 512); fb[0][i] = lds128(lp(bpo) + (7 - i) * 512); }
        const bool odd = lf & 1;
        int ysrc = (2 * (lf >> 3) + (lf & 1) + 16 * ((lf >> 1) & 3)) * 4;
        unsigned yso = WS_Y + (unsigned)dch * 8192u + lf * 16u;
        f32x4 kd[4];
        {
            const float dval = bflo(dbits);
            const int sl0 = 32 * ((lf & 15) >> 1) + 8 * (lf >> 4) + 4 * (lf & 1);
#pragma unroll
            for (int j = 0; j < 4; ++j) {
                const int tj = (j == 0 ? 0 : j == 1 ? 3 : j == 2 ? 4 : 7) ^ par;
                const u32x2 kvr = *(LAS u32x2*)(cb + C_U + C_US + (2044 - 256 * tj - sl0) * 2);
                const unsigned ka = pk(bfhi(kvr[1]) * dval, bflo(kvr[1]) * dval), kb = pk(bfhi(kvr[0]) * dval, bflo(kvr[0]) * dval);
                kd[j] = (f32x4){bflo(ka), bfhi(ka), bflo(kb), bfhi(kb)};
                asm volatile("" : "+v"(kd[j]));
            }
            asm volatile("" : "+v"(ysrc), "+v"(yso));
        }
        wait_lds(sm + L_CTL + 32 + wq * 4);
        STAMP(8);
        auto conv_all = [&](auto pc_) {
            constexpr int P = decltype(pc_)::value;
            static_for([&](auto rc) {
                constexpr int r = decltype(rc)::value, c = r & 1, n = c ^ 1;
                static_for([&](auto kc) {
                    constexpr int k = decltype(kc)::value;
                    constexpr int T[4] = {P, 3 - P, 4 + P, 7 - P};
                    constexpr int j = k <= T[0] ? 0 : k <= T[0] + T[1] + 1 ? 1 : k <= T[0] + T[1] + T[2] + 2 ? 2 : 3;
                    constexpr int pp = k - (j == 0 ? 0 : j == 1 ? T[0] + 1 : j == 2 ? T[0] + T[1] + 2 : T[0] + T[1] + T[2] + 3);
                    ca[j] = mfma16(fa[c][pp], fb[c][T[j] - pp], ca[j]);
                    SCHED();
                    if constexpr (r < 7 && k < 2 * (T[3] + 1)) {
                        if constexpr (k & 1) fb[n][k >> 1] = lds128(lp(bpo) + (r + 1) * 64 + (7 - (k >> 1)) * 512);
                        else fa[n][k >> 1] = lds128(lp(apo) + (r + 1) * 64 + (k >> 1) * 512);
                    }
                    SCHED();
                }, std::make_integer_sequence<int, 18>{});
                asm volatile("" : "+v"(apo), "+v"(bpo));
            }, std::make_integer_sequence<int, 8>{});
        };
        if (par) conv_all(std::integral_constant<int, 1>{}); else conv_all(std::integral_constant<int, 0>{});
        SCHED();
        STAMP(9);
        {
            float r0[4], r1[4];
#pragma unroll
            for (int j = 0; j < 4; ++j) { r0[j] = dpp_xor1(odd ? ca[j][0] : ca[j][2]); r1[j] = dpp_xor1(odd ? ca[j][1] : ca[j][3]); }
            unsigned rp[4][2];
#pragma unroll
            for (int j = 0; j < 4; ++j) {
                const unsigned ya = pk(odd ? r0[j] : ca[j][0], odd ? ca[j][2] : r0[j]), yb = pk(odd ? r1[j] : ca[j][1], odd ? ca[j][3] : r1[j]);
                rp[j][0] = __builtin_amdgcn_ds_bpermute(ysrc, pk(add1(bflo1(ya), kd[j][0]), add1(bfhi1(ya), kd[j][1])));
                rp[j][1] = __builtin_amdgcn_ds_bpermute(ysrc, pk(add1(bflo1(yb), kd[j][2]), add1(bfhi1(yb), kd[j][3])));
            }
#pragma unroll
            for (int j = 0; j < 4; ++j)
                __builtin_amdgcn_raw_buffer_store_b128((u32x4){vmov1(tag32), rp[j][0], vmov1(tag32), rp[j][1]}, wsr, yso,
                                                       ((j == 0 ? 0 : j == 1 ? 3 : j == 2 ? 4 : 7) ^ par) * 1024, 16);
        }
        STAMP(10);
        WAIT_LGKM0();
        if (lane == 0) __hip_atomic_fetch_add((LAS unsigned*)(sm + L_CTL + 8), 1u, __ATOMIC_RELAXED, __HIP_MEMORY_SCOPE_WORKGROUP);
#pragma unroll 1
        for (unsigned spins = 0; spins < (1u << 22); ++spins) {
            if (__builtin_amdgcn_readfirstlane(__hip_atomic_load((LAS unsigned*)(sm + L_CTL + 8), __ATOMIC_RELAXED, __HIP_MEMORY_SCOPE_WORKGROUP)) == 4u) break;
            __builtin_amdgcn_s_sleep(1);
        }
        asm volatile("" ::: "memory");
#pragma unroll
        for (int j = 0; j < 8; ++j) *(LAS u32x4*)(sm + latei + j * 1024 + lf * 16) = lw2[j];
        asm volatile("" ::: "memory");
        __hip_atomic_store((LAS unsigned*)(sm + L_CTL + 80 + wq * 4), 1u, __ATOMIC_RELAXED, __HIP_MEMORY_SCOPE_WORKGROUP);
        STAMP(11);
        __builtin_amdgcn_s_sleep(YSLEEP);
        u32x2 yv[16], stv;
        const int yl = L_CONV + 32768 + wq * 4096;
        const unsigned yg = (unsigned)(lf >> 3) * 8192u + (unsigned)(lf & 7) * 16u;
        const u32x4 yvo = {yg, yg + (65536u - 1024u), yg + 2 * (65536u - 1024u), yg + 3 * (65536u - 1024u)};
        const char* ybase = p.ws + WS_Y + (unsigned)(64 * n64 + 32 * (wq >> 1)) * 8192u + (unsigned)(64 * m64 + 32 * (wq & 1)) * 4u;
#pragma unroll 1
        for (unsigned spins = 0; spins < SPIN_CAP; ++spins) {
            glds16x4c(ybase, yvo, lb + yl);
            stv = __builtin_amdgcn_raw_buffer_load_b64(wsr, WS_STATS + (unsigned)srow * 8u, 0, 16);
            WAIT_VM(0);
#pragma unroll
            for (int i = 0; i < 16; ++i) yv[i] = *(LAS u32x2*)(sm + yl + ((lf >> 5) * 16 + i) * 128 + ((lf & 31) >> 1) * 8);
            unsigned df = stv[0] ^ tag32;
#pragma unroll
            for (int i = 0; i < 16; ++i) df |= yv[i][0] ^ tag32;
            if (__all(df == 0u)) break;
            __builtin_amdgcn_s_sleep(8);
        }
        STAMP(14);
        __builtin_amdgcn_s_setprio(0);
        f32x2 sf[8], xf[8];
        {
            const float nmb = -bflo(stv[1]);
            float ri = __builtin_amdgcn_rcpf(bfhi(stv[1]));
            asm volatile("s_nop 1" : "+v"(ri));
            const unsigned ysh = (srow & 1) ? 0u : 16u;
#pragma unroll
            for (int i = 0; i < 8; ++i) {
                const unsigned xd = xv[i >> 2][i & 3];
                xf[i] = (f32x2){bflo1(xd), bfhi1(xd)};
                const unsigned tp = pk(add1(xf[i][0], nmb), add1(xf[i][1], nmb));
                const unsigned np = pk(mul1(bflo1(tp), ri), mul1(bfhi1(tp), ri));
                sf[i] = (f32x2){mul1(bflo1(np), bfhi1(yv[2 * i][1] << ysh)), mul1(bfhi1(np), bfhi1(yv[2 * i + 1][1] << ysh))};
                asm volatile("" : "+v"(sf[i]), "+v"(xf[i]));
            }
        }
        const int rm = lb + L_W2 + (wq < 3 ? wq * 4096 : L_SLOT) + (lf >> 5) * 2048 + (lf & 31) * 16;
        STAMP(15);
        WAIT_BAR("lgkmcnt(0)");
        STAMP(16);
        f32x4 o[4];
#pragma unroll
        for (int j = 0; j < 4; ++j) {
            LAS char* rp = lp(rm) + j * 512;
            o[j] = (*(LAS f32x4*)rp + *(LAS f32x4*)(rp + 12288)) + (*(LAS f32x4*)(rp + 24576) + *(LAS f32x4*)(rp + 36864));
        }
        f32x2 so[8], ev[8];
#pragma unroll
        for (int i = 0; i < 8; ++i) {
            const f32x2 ov = {o[i >> 1][(i & 1) * 2], o[i >> 1][(i & 1) * 2 + 1]};
            const f32x2 t = ov * (f32x2){-1.44269504088896341f, -1.44269504088896341f};
            ev[i] = (f32x2){__builtin_amdgcn_exp2f(t[0]), __builtin_amdgcn_exp2f(t[1])};
            so[i] = sf[i] * ov;
        }
        SCHED();
        u32x4 st[2];
#pragma unroll
        for (int i = 0; i < 8; ++i) {
            const f32x2 dn = ev[i] + (f32x2){1.f, 1.f};
            const f32x2 r = __builtin_elementwise_fma(so[i], (f32x2){__builtin_amdgcn_rcpf(dn[0]), __builtin_amdgcn_rcpf(dn[1])}, xf[i]);
            st[i >> 2][i & 3] = pk(r[0], r[1]);
        }
        const __amdgpu_buffer_rsrc_t orr = __builtin_amdgcn_make_buffer_rsrc((void*)p.out, 0, S * D * 2, 0x00020000);
        __builtin_amdgcn_raw_buffer_store_b128(st[0], orr, xoff, 0, 0);
        __builtin_amdgcn_raw_buffer_store_b128(st[1], orr, xoff, 16, 0);
        STAMP(17);
        STAMP_OUT(18);
    }
}

extern "C" void kernel_launch(void* const* d_in, const int* in_sizes, int n_in,
                              void* d_out, int out_size, void* d_ws, size_t ws_size,
                              hipStream_t stream) {
    Params p;
    p.x = (const u16*)d_in[0]; p.k = (const u16*)d_in[1]; p.v = (const u16*)d_in[2];
    p.sk = (const u16*)d_in[3]; p.hk = (const u16*)d_in[4]; p.dv = (const u16*)d_in[5];
    p.w1 = (const u16*)d_in[7]; p.w3 = (const u16*)d_in[8]; p.w2 = (const u16*)d_in[9];
    p.out = (u16*)d_out; p.ws = (char*)d_ws;
    (void)hipFuncSetAttribute((const void*)fused_kernel, hipFuncAttributeMaxDynamicSharedMemorySize, L_TOTAL);
    fused_kernel<<<256, 512, L_TOTAL, stream>>>(p);
}
```

```cpp
#include <hip/hip_runtime.h>
#include <stdint.h>
#include <utility>

typedef unsigned short u16;
typedef unsigned long long u64;
typedef short s16x4 __attribute__((ext_vector_type(4)));
typedef short s16x8 __attribute__((ext_vector_type(8)));
typedef __bf16 bf16x2 __attribute__((ext_vector_type(2)));
typedef __bf16 bf16x8 __attribute__((ext_vector_type(8)));
typedef float f32x2 __attribute__((ext_vector_type(2)));
typedef float f32x4 __attribute__((ext_vector_type(4)));
typedef float f32x16 __attribute__((ext_vector_type(16)));
typedef unsigned u32x2 __attribute__((ext_vector_type(2)));
typedef unsigned u32x4 __attribute__((ext_vector_type(4)));
typedef unsigned u32x16 __attribute__((ext_vector_type(16)));
#define LAS __attribute__((address_space(3)))
#define GAS __attribute__((address_space(1)))
#define RLX_AGENT __ATOMIC_RELAXED, __HIP_MEMORY_SCOPE_AGENT

constexpr int S = 2048, D = 448, F = 1792;

constexpr unsigned WS_EPOCH = 0;
constexpr unsigned WS_FH = 65536;
constexpr unsigned WS_STATS = 8192;
constexpr unsigned WS_CSUM = 24576;
constexpr unsigned WS_U = 262144;
constexpr unsigned WS_Y = WS_U + D * S * 4;
constexpr unsigned WS_H = WS_Y + D * S * 4;
constexpr unsigned WS_END = WS_H + 64 * 112 * 1024;
constexpr unsigned WS_DBG = WS_END;

constexpr int L_KV = 0;
constexpr int L_CTL = 0;
constexpr int L_CSUM = 256;
constexpr int L_STAT = 1280;
constexpr int L_RAW = 2048;
constexpr int L_KVO = 8192;
constexpr int L_UNX = 6144;
constexpr int L_SLOT = 49152;
constexpr int L_CONV = 12288;
constexpr int C_CH = 26112, C_HS = 4160, C_U = 16640, C_US = 4640;
constexpr int L_W2 = 16384 + L_SLOT;
constexpr int L_TOTAL = 163840;
__host__ __device__ constexpr int ring(int stage) { return 16384 + (stage % 3) * L_SLOT; }
constexpr unsigned CODE_PREFETCH = 36864;
#define POLL_SLEEP 4
#define HDEPTH 6
#define ROT2 (8 * ml)
#define MPRIO 1
#define EPARK 0
#define YSLEEP 16
#define TOUCH 1
#define TOUCH0 4
#define HGROW 4
#define ROT1 ((2 * ml + nl) % 6)

constexpr u64 TAG_SALT = 0x9E3779B97F4A7C15ull;
constexpr unsigned SPIN_CAP = 1u << 18;

struct Params {
    const u16* x; const u16* k; const u16* v; const u16* sk; const u16* hk; const u16* dv;
    const u16* w1; const u16* w3; const u16* w2; u16* out; char* ws;
};

__device__ __forceinline__ float bflo(unsigned p) { return __uint_as_float(p << 16); }
__device__ __forceinline__ float bfhi(unsigned p) { return __uint_as_float(p & 0xffff0000u); }
__device__ __forceinline__ float bflo1(unsigned p) { float r; asm("v_lshlrev_b32 %0, 16, %1" : "=v"(r) : "v"(p)); return r; }
__device__ __forceinline__ float bfhi1(unsigned p) { float r; asm("v_and_b32 %0, 0xffff0000, %1" : "=v"(r) : "v"(p)); return r; }
__device__ __forceinline__ float mul1(float a, float b) { float r; asm("v_mul_f32 %0, %1, %2" : "=v"(r) : "v"(a), "v"(b)); return r; }
__device__ __forceinline__ void fmac1(float& d, float a, float b) { asm("v_fmac_f32 %0, %1, %2" : "+v"(d) : "v"(a), "v"(b)); }
__device__ __forceinline__ float dpp_xor1(float v) {
    return __int_as_float(__builtin_amdgcn_update_dpp(0, __float_as_int(v), 0xB1, 0xf, 0xf, true));
}
__device__ __forceinline__ float add1(float a, float b) { float r; asm("v_add_f32 %0, %1, %2" : "=v"(r) : "v"(a), "v"(b)); return r; }
__device__ __forceinline__ unsigned vmov1(unsigned s) { unsigned r; asm volatile("v_mov_b32 %0, %1" : "=v"(r) : "s"(s)); return r; }
__device__ __forceinline__ unsigned pk(float a, float b) {
    f32x2 f = {a, b};
    return __builtin_bit_cast(unsigned, __builtin_convertvector(f, bf16x2));
}
__device__ __forceinline__ float rbf(float a) { return bflo(pk(a, a)); }
__device__ __forceinline__ float dot2(unsigned a, unsigned b, float c) {
    return __builtin_amdgcn_fdot2_f32_bf16(__builtin_bit_cast(bf16x2, a), __builtin_bit_cast(bf16x2, b), c, false);
}
__device__ __forceinline__ void glds16(const void* base, unsigned voff, unsigned ldst) {
    u64 t;
    asm volatile("s_mov_b32 m0, %3\n\ts_mov_b64 %0, %2\n\tglobal_load_lds_dwordx4 %1, %0" : "=&s"(t) : "v"(voff), "s"(base), "s"(ldst));
}
__device__ __forceinline__ void glds16x4(const void* base, u32x4 vo, unsigned ldst) {
    u64 t;
    asm volatile("s_mov_b32 m0, %6\n\ts_mov_b64 %0, %5\n\tglobal_load_lds_dwordx4 %1, %0\n\tglobal_load_lds_dwordx4 %2, %0 offset:1024\n\t"
                 "global_load_lds_dwordx4 %3, %0 offset:2048\n\tglobal_load_lds_dwordx4 %4, %0 offset:3072"
                 : "=&s"(t) : "v"(vo[0]), "v"(vo[1]), "v"(vo[2]), "v"(vo[3]), "s"(base), "s"(ldst));
}
__device__ __forceinline__ void glds16x4c(const void* base, u32x4 vo, unsigned ldst) {
    u64 t;
    asm volatile("s_mov_b32 m0, %6\n\ts_mov_b64 %0, %5\n\tglobal_load_lds_dwordx4 %1, %0 sc1\n\tglobal_load_lds_dwordx4 %2, %0 offset:1024 sc1\n\t"
                 "global_load_lds_dwordx4 %3, %0 offset:2048 sc1\n\tglobal_load_lds_dwordx4 %4, %0 offset:3072 sc1"
                 : "=&s"(t) : "v"(vo[0]), "v"(vo[1]), "v"(vo[2]), "v"(vo[3]), "s"(base), "s"(ldst) : "memory");
}
__device__ __forceinline__ void glds16x2c(const void* base, unsigned voff, unsigned ldst) {
    u64 t;
    asm volatile("s_mov_b32 m0, %3\n\ts_mov_b64 %0, %2\n\tglobal_load_lds_dwordx4 %1, %0 sc1\n\tglobal_load_lds_dwordx4 %1, %0 offset:1024 sc1"
                 : "=&s"(t) : "v"(voff), "s"(base), "s"(ldst));
}
__device__ __forceinline__ void glds16c(const void* base, unsigned voff, unsigned ldst) {
    u64 t;
    asm volatile("s_mov_b32 m0, %3\n\ts_mov_b64 %0, %2\n\tglobal_load_lds_dwordx4 %1, %0 sc1" : "=&s"(t) : "v"(voff), "s"(base), "s"(ldst));
}
__device__ __forceinline__ void glds4c(const void* base, unsigned voff, unsigned ldst) {
    u64 t;
    asm volatile("s_mov_b32 m0, %3\n\ts_mov_b64 %0, %2\n\tglobal_load_lds_dword %1, %0 sc1" : "=&s"(t) : "v"(voff), "s"(base), "s"(ldst));
}
__device__ __forceinline__ const u16* sptr(const u16* base, int off) {
    const u16* r = base + __builtin_amdgcn_readfirstlane(off);
    asm("" : "+s"(r));
    return r;
}
__device__ __forceinline__ u64 vsel(bool c, const void* a, u64 b) {
    u64 va = (u64)a;
    asm("" : "+v"(va), "+v"(b));
    return c ? va : b;
}
__device__ __forceinline__ LAS char* lp(int a) { return (LAS char*)(size_t)(unsigned)a; }
__device__ __forceinline__ u32x4 lds128(LAS char* p) { return *(LAS u32x4*)p; }
__device__ __forceinline__ u32x2 ldstr(LAS char* p) {
    return __builtin_bit_cast(u32x2, __builtin_amdgcn_ds_read_tr16_b64_v4i16((LAS s16x4*)p));
}
__device__ __forceinline__ u32x4 ldstr2(LAS char* p, int off) {
    u32x2 a = ldstr(p), b = ldstr(p + off);
    return (u32x4){a.x, a.y, b.x, b.y};
}
__device__ __forceinline__ f32x16 mfma32(u32x4 a, u32x4 b, f32x16 c) {
    return __builtin_amdgcn_mfma_f32_32x32x16_bf16(__builtin_bit_cast(bf16x8, a), __builtin_bit_cast(bf16x8, b), c, 0, 0, 0);
}
__device__ __forceinline__ f32x4 mfma16(u32x4 a, u32x4 b, f32x4 c) {
    return __builtin_amdgcn_mfma_f32_16x16x32_bf16(__builtin_bit_cast(bf16x8, a), __builtin_bit_cast(bf16x8, b), c, 0, 0, 0);
}
__device__ __forceinline__ float silu_f(float g) {
    return g * __builtin_amdgcn_rcpf(1.f + __builtin_amdgcn_exp2f(g * -1.44269504088896341f));
}
template <class Fn, int... I>
__device__ __forceinline__ void static_for(Fn&& fn, std::integer_sequence<int, I...>) { (fn(std::integral_constant<int, I>{}), ...); }
#define WAIT_VM(n) asm volatile("s_waitcnt vmcnt(" #n ")" ::: "memory")
#define WAIT_LGKM0() asm volatile("s_waitcnt lgkmcnt(0)" ::: "memory")
#define WAIT_BAR(w) asm volatile("s_waitcnt " w "\n\ts_barrier" ::: "memory")
#define SCHED() __builtin_amdgcn_sched_barrier(0)
#define CAS __attribute__((address_space(4)))
#define NSTAMP 18

__device__ __forceinline__ void wait_lds(LAS char* word) {
#pragma unroll 1
    for (unsigned spins = 0; spins < (SPIN_CAP << 4); ++spins) {
        if (__builtin_amdgcn_readfirstlane(__hip_atomic_load((LAS unsigned*)word, __ATOMIC_RELAXED, __HIP_MEMORY_SCOPE_WORKGROUP))) break;
        __builtin_amdgcn_s_sleep(1);
    }
    asm volatile("" ::: "memory");
}
__device__ __forceinline__ void wait_lds_eq(LAS char* word, unsigned want) {
#pragma unroll 1
    for (unsigned spins = 0; spins < (1u << 22); ++spins) {
        if ((unsigned)__builtin_amdgcn_readfirstlane(__hip_atomic_load((LAS unsigned*)word, __ATOMIC_RELAXED, __HIP_MEMORY_SCOPE_WORKGROUP)) == want) break;
        __builtin_amdgcn_s_sleep(1);
    }
    asm volatile("" ::: "memory");
}
#define STAMP(i)
#define STAMPG(i)
#define STAMPA(i)
#define STAMP_DECL()
#define STAMP_OUT(n)

__global__ __launch_bounds__(512, 2) void fused_kernel(Params p) {
    extern __shared__ __attribute__((aligned(16))) char smem_raw[];
    u64 pc;
    asm volatile("s_getpc_b64 %0" : "=s"(pc));
    LAS char* sm = (LAS char*)smem_raw;
    const int tid = threadIdx.x, lane = tid & 63;
    const int wave = __builtin_amdgcn_readfirstlane(tid >> 6);
    const int xcd = blockIdx.x & 7, idx = blockIdx.x >> 3;
    if (idx == 0 && wave < 5) {
        const unsigned o = (unsigned)tid * 128u;
        asm volatile("s_mov_b32 m0, %2\n\ts_nop 0\n\tglobal_load_lds_dword %0, %1" :: "v"(o < CODE_PREFETCH ? o : 0u), "s"(pc & ~(u64)255),
                     "s"((unsigned)(size_t)sm + 32768u));
    }
    SCHED();
    const __amdgpu_buffer_rsrc_t wsr = __builtin_amdgcn_make_buffer_rsrc(p.ws, 0, WS_END, 0x00020000);
    asm volatile("" :: "s"(p.x), "s"(p.k), "s"(p.v), "s"(p.sk), "s"(p.hk), "s"(p.dv), "s"(p.w1), "s"(p.w3), "s"(p.w2), "s"(p.out), "s"(p.ws));
    STAMP_DECL();
    STAMP(0);

    if (idx < 4) {
        const int ca = (xcd >> 1) * 4 + idx, cmat = ca >= 7, cnt = 7 * (xcd & 1) + (ca >= 7 ? ca - 7 : ca);
        const bool job = ca < 14;
        u32x4 xr[7], wr[14];
        const int row = (8 * (xcd >> 1) + 4 * (xcd & 1) + idx) * 64 + wave * 8 + (lane >> 3);
#pragma unroll
        for (int i = 0; i < 7; ++i) xr[i] = *(const u32x4*)(p.x + (size_t)row * D + i * 64 + (lane & 7) * 8);
        const unsigned pf2 = *(const unsigned*)((const char*)p.hk + (size_t)((xcd * 4 + idx) * 448 + wave * 56 + (lane < 56 ? lane : 0)) * 128);
        u64 epoch;
        asm volatile("s_load_dwordx2 %0, %1, 0x0\n\ts_waitcnt lgkmcnt(0)" : "=s"(epoch) : "s"(p.ws) : "memory");
        const u64 tag = epoch ^ TAG_SALT;
        const unsigned tag32 = (unsigned)tag;
        {
            float s4[4] = {0.f, 0.f, 0.f, 0.f};
#pragma unroll
            for (int i = 0; i < 7; ++i)
#pragma unroll
                for (int j = 0; j < 4; ++j) s4[j] = dot2(xr[i][j], 0x3f803f80u, s4[j]);
            float sum = (s4[0] + s4[1]) + (s4[2] + s4[3]);
            sum += __shfl_xor(sum, 1); sum += __shfl_xor(sum, 2); sum += __shfl_xor(sum, 4);
            const float mean = sum * (1.f / D);
            f32x2 q4[4] = {{0.f, 0.f}, {0.f, 0.f}, {0.f, 0.f}, {0.f, 0.f}};
#pragma unroll
            for (int i = 0; i < 7; ++i)
#pragma unroll
                for (int j = 0; j < 4; ++j) {
                    const f32x2 d = (f32x2){bflo(xr[i][j]), bfhi(xr[i][j])} - (f32x2){mean, mean};
                    q4[j] = __builtin_elementwise_fma(d, d, q4[j]);
                }
            const f32x2 q2 = (q4[0] + q4[1]) + (q4[2] + q4[3]);
            float sq = q2[0] + q2[1];
            sq += __shfl_xor(sq, 1); sq += __shfl_xor(sq, 2); sq += __shfl_xor(sq, 4);
            const u32x2 st = {tag32, pk(mean, __builtin_amdgcn_sqrtf(rbf(rbf(sq * (1.f / D)) + 1e-5f)))};
            __builtin_amdgcn_raw_buffer_store_b64(st, wsr, (lane & 7) == 0 ? WS_STATS + (unsigned)row * 8u : 0xfffffff0u, 0, 16);
        }
        STAMPA(2);
        if (job) {
            SCHED();
            const u16* wp = (cmat ? p.w3 : p.w1) + (size_t)(wave * 4 + (lane >> 4)) * F + cnt * 128 + (lane & 15) * 8;
#pragma unroll
            for (int i = 0; i < 14; ++i) wr[i] = *(const u32x4*)(wp + (size_t)(32 * i) * F);
        }
        if (job) {
            float c[8];
#pragma unroll
            for (int j = 0; j < 8; ++j) c[j] = 0.f;
#pragma unroll
            for (int i = 0; i < 14; ++i)
#pragma unroll
                for (int j = 0; j < 4; ++j) { c[2 * j] += bflo(wr[i][j]); c[2 * j + 1] += bfhi(wr[i][j]); }
            LAS char* cp = sm + ((wave * 4 + (lane >> 4)) * 136 + (lane & 15) * 8) * 4;
            *(LAS f32x4*)cp = (f32x4){c[0], c[1], c[2], c[3]};
            *(LAS f32x4*)(cp + 16) = (f32x4){c[4], c[5], c[6], c[7]};
        }
        STAMPA(3);
        if (job) {
            WAIT_BAR("lgkmcnt(0)");
            float t = 0.f;
#pragma unroll
            for (int i = 0; i < 8; ++i) t += *(LAS float*)(sm + ((i * 4 + (tid & 3)) * 136 + (tid >> 2)) * 4);
            t += __shfl_xor(t, 1); t += __shfl_xor(t, 2);
            __builtin_amdgcn_raw_buffer_store_b64((u32x2){tag32, __float_as_uint(t)}, wsr,
                (tid & 3) == 0 ? WS_CSUM + (unsigned)((cnt * 2 + cmat) * 128 + (tid >> 2)) * 8u : 0xfffffff0u, 0, 16);
        }
        STAMP(1);
        asm volatile("" :: "v"(pf2));
        STAMP_OUT(4);
        if (blockIdx.x == 0 && wave == 7) {
            __builtin_amdgcn_s_sleep(127);
            __builtin_amdgcn_s_sleep(100);
            GAS u64* gp = (GAS u64*)(p.ws + WS_STATS) + lane * 8;
            const int fl = lane < 56 ? lane : 0;
            GAS u64* fp = (GAS u64*)(p.ws + WS_FH) + ((fl / 14) * 56 + (fl % 14) * 2) * 16;
#pragma unroll 1
            for (unsigned spins = 0; spins < SPIN_CAP; ++spins) {
                bool ok = true;
#pragma unroll
                for (int m = 0; m < 4; ++m)
                    ok &= ((unsigned)__hip_atomic_load(gp + m * 512, RLX_AGENT) == tag32) & (__hip_atomic_load(fp + m * 224 * 16, RLX_AGENT) == tag);
                if (__all(ok)) break;
                __builtin_amdgcn_s_sleep(32);
            }
            if (lane == 0) __hip_atomic_store((GAS u64*)(p.ws + WS_EPOCH), epoch + 1, RLX_AGENT);
        }
        return;
    }

    const int gi = idx - 4, ml = gi & 3, nl = gi >> 2;
    const int mt = 4 * (xcd >> 1) + ml, nt = 7 * (xcd & 1) + nl;
    const int rot1 = ROT1;
    const int m64 = 4 * xcd + ml, n64 = nl;
    const int gpair = xcd * 28 + gi;
    const int r32 = lane & 31, hh = lane >> 5, blk = (lane >> 4) & 1, q4 = (lane & 15) >> 2, p4 = lane & 3;
    const unsigned lb = (unsigned)(size_t)sm;
    const int wq = wave & 3;
    u32x4 wvo;
    wvo[0] = (unsigned)((lane >> 4) * F + (((lane & 15) ^ ((lane >> 4) << 2)) * 8)) * 2u;
#pragma unroll
    for (int j = 1; j < 4; ++j) wvo[j] = wvo[0] + j * (8 * F - 1024);
    const u16* w1b = sptr(p.w1, wq * 16 * F + nt * 128);
    const u16* w3b = sptr(p.w3, wq * 16 * F + nt * 128);
    auto kst = [&](int si) { if (si == 0) return 0; const int s = si + rot1; return s >= 7 ? s - 6 : s; };
    auto group_w = [&](int si, int m) { glds16x4(sptr(m ? w3b : w1b, kst(si) * 64 * F), wvo, lb + ring(si) + 16384 + m * 16384 + wq * 4096); };
    const int kr0 = (gpair & 31) * 64, kc0 = (gpair >> 5) * 64;
    const int chn = wq >> 1, par = wq & 1, dch = 2 * gpair + chn;
    if (wave < 4) group_w(0, 0);
    {
        const bool isk = lane >= 8 && lane < 14;
        u64 mb_ = vsel(lane == 15, p.dv, vsel(lane == 14, p.hk, vsel(isk, p.k, (u64)p.sk)));
        unsigned mo = (unsigned)((kc0 + wq * 16) * 8 + (lane & 7) * 16);
        mo = isk ? (unsigned)((kr0 ? kr0 - 3 + ((lane - 8) >> 1) : 0) * D + kc0 + wq * 16 + (lane & 1) * 8) * 2u : mo;
        mo = lane == 14 ? (unsigned)dch * 4096u : mo;
        mo = lane == 15 ? (unsigned)(dch & ~7) * 2u : mo;
        const u64 ma = mb_ + mo;
        asm volatile("s_mov_b32 m0, %1\n\ts_nop 0\n\tglobal_load_lds_dwordx4 %0, off" :: "v"(ma), "s"(lb + wave * 1024));
    }
    if (wave < 4) group_w(0, 1);
    SCHED();
    const int rot2 = ROT2;
    const int w2i = L_W2 + wq * 12288;
    auto fresh_lane = [&]() { int l = lane; asm volatile("" : "+v"(l)); return l; };
    const int latei = L_CONV + wq * 8192;
    auto pair_w2 = [&](u32x4 vo, int i, int dst) {
        int t = i + rot2; t = t >= 28 ? t - 28 : t;
        glds16x4(sptr(p.w2, (wq * 448 + t * 16) * D + n64 * 64), vo, lb + dst);
    };
    auto w2_off = [&]() {
        const int l = fresh_lane();
        const unsigned o = (unsigned)((l >> 3) * D + (((l & 7) ^ (((l >> 4) & 1) << 2)) * 8)) * 2u;
        return (u32x4){o, o + (16 * D - 1024), o + 2 * (16 * D - 1024), o + 3 * (16 * D - 1024)};
    };

    if (wave < 4) {
        u64 epoch;
        asm volatile("s_load_dwordx2 %0, %1, 0x0" : "=s"(epoch) : "s"(p.ws) : "memory");
        __builtin_amdgcn_s_setprio(MPRIO);
        const int wm = wave >> 1, wn = wave & 1;
        int xa[4], wa0, wa1;
#pragma unroll
        for (int i = 0; i < 4; ++i) xa[i] = lb + ring(0) + (((64 * wm + r32) * 128 + ((hh ^ ((r32 >> 1) & 7)) * 16)) ^ (i * 32));
        wa0 = 16384 + (8 * hh + q4) * 256 + (((8 * wn + 2 * blk + (p4 >> 1)) ^ (q4 << 2)) * 16) + 8 * (p4 & 1);
        wa1 = (wa0 ^ 64) + lb + ring(0); wa0 += lb + ring(0);
        f32x16 acc[2][2][2];
#pragma unroll
        for (int i = 0; i < 8; ++i)
#pragma unroll
            for (int r = 0; r < 16; ++r) acc[i >> 2][(i >> 1) & 1][i & 1][r] = 0.f;
        u32x4 fx[2][2], fw[2][2][2];
        auto load_frag = [&](int g, int j) {
            const int pp = g & 3, b = g & 1;
            if (j == 0) {
                if (g >= 4) {
                    const int d = ring(g >> 2) - ring((g >> 2) - 1);
                    if (pp == 0) { wa0 += d; wa1 += d; asm volatile("" : "+v"(wa0), "+v"(wa1)); }
                    xa[pp] += d; asm volatile("" : "+v"(xa[pp]));
                }
                fw[b][0][0] = ldstr2(lp(wa0) + pp * 4096, 1024);
            }
            if (j == 1) { fx[b][0] = lds128(lp(xa[pp])); fx[b][1] = lds128(lp(xa[pp]) + 4096); }
            if (j == 2) fw[b][0][1] = ldstr2(lp(wa0) + pp * 4096 + 16384, 1024);
            if (j == 3) fw[b][1][0] = ldstr2(lp(wa1) + pp * 4096, 1024);
            if (j == 4) fw[b][1][1] = ldstr2(lp(wa1) + pp * 4096 + 16384, 1024);
        };
        int ka0[2], kam[2], kwo;
#pragma unroll
        for (int rd = 0; rd < 2; ++rd) {
            const int ck = 2 * wq + rd;
            const int bq = 2 * (lane >> 4) + (p4 >> 1), bm = bq ? bq - 1 : 0;
            ka0[rd] = lb + L_KV + (8 * bq + q4) * 128 + ((ck ^ (((q4 >> 1) & 1) | ((bq & 3) << 1))) * 16) + 8 * (p4 & 1);
            kam[rd] = lb + L_KV + (8 * bm + 4 + q4) * 128 + ((ck ^ (((q4 >> 1) & 1) | ((bm & 3) << 1))) * 16) + 8 * (p4 & 1);
        }
        kwo = lb + L_KVO + wq * 2048 + (lane & 7) * 128 + (((7 - (lane >> 3)) ^ (lane & 7)) * 16);
        asm volatile("" : "+v"(ka0[0]), "+v"(ka0[1]), "+v"(kam[0]), "+v"(kam[1]), "+v"(kwo));
        WAIT_VM(4);
        u32x2 skv[2], hal[2];
#pragma unroll
        for (int rd = 0; rd < 2; ++rd) {
            LAS char* mp = sm + wave * 1024;
            skv[rd] = *(LAS u32x2*)(mp + (rd * 8 + (lane & 7)) * 8);
            const unsigned hm = kr0 ? 0xffffffffu : 0u;
            const unsigned a = *(LAS u16*)(mp + 128 + rd * 16 + (lane & 7) * 2), b = *(LAS u16*)(mp + 160 + rd * 16 + (lane & 7) * 2),
                           c = *(LAS u16*)(mp + 192 + rd * 16 + (lane & 7) * 2);
            hal[rd] = (u32x2){(a << 16) & hm, (b | (c << 16)) & hm};
            asm volatile("" : "+v"(skv[rd]), "+v"(hal[rd]));
        }
        asm volatile("s_waitcnt vmcnt(0) lgkmcnt(0)\n\ts_barrier" : "+s"(epoch) :: "memory");
        const u64 tag = epoch ^ TAG_SALT;
        STAMP(1);
        u32x2 kt[2][3], vt[2][2];
        float cf[4], kk[11], ko[8];
        u32x4 kpk;
        auto fill = [&](auto kc) {
            constexpr int k = decltype(kc)::value;
            if constexpr (k < 5) {
#pragma unroll
                for (int rd = 0; rd < 2; ++rd) {
                    if constexpr (k == 0) kt[rd][0] = ldstr(lp(kam[rd]));
                    else if constexpr (k < 3) kt[rd][k] = ldstr(lp(ka0[rd]) + (k - 1) * 512);
                    else vt[rd][k - 3] = ldstr(lp(ka0[rd]) + 8192 + (k - 3) * 512);
                }
            } else if constexpr (k >= 6 && k < 6 + 2 * 16) {
                constexpr int rd = (k - 6) / 16, s = (k - 6) % 16;
                if constexpr (s == 0) { cf[0] = bflo1(skv[rd][0]); cf[1] = bfhi1(skv[rd][0]); cf[2] = bflo1(skv[rd][1]); cf[3] = bfhi1(skv[rd][1]); }
                else if constexpr (s == 1) {
                    const unsigned a = lane < 8 ? hal[rd][0] : kt[rd][0][0], b = lane < 8 ? hal[rd][1] : kt[rd][0][1];
                    kk[0] = bfhi1(a); kk[1] = bflo1(b); kk[2] = bfhi1(b);
                } else if constexpr (s < 4) {
                    const u32x2 t = kt[rd][s - 1];
                    kk[4 * s - 5] = bflo1(t[0]); kk[4 * s - 4] = bfhi1(t[0]); kk[4 * s - 3] = bflo1(t[1]); kk[4 * s - 2] = bfhi1(t[1]);
                } else if constexpr (s < 12) {
                    constexpr int tp = (s - 4) >> 1, o0 = ((s - 4) & 1) * 4;
#pragma unroll
                    for (int i = o0; i < o0 + 4; ++i) {
                        if (tp == 0) ko[i] = mul1(kk[i], cf[3]);
                        else fmac1(ko[i], kk[i + tp], cf[3 - tp]);
                    }
                } else {
                    constexpr int j = s - 12;
                    const unsigned v = vt[rd][j >> 1][j & 1];
                    kpk[3 - j] = pk(mul1(ko[2 * j + 1], bfhi1(v)), mul1(ko[2 * j], bflo1(v)));
                    if constexpr (j == 3) *(LAS u32x4*)(lp(kwo) + rd * 1024) = kpk;
                }
            }
        };
#pragma unroll
        for (int j = 0; j < 5; ++j) load_frag(0, j);
        f32x4 cs[2][2][4];
        unsigned tch[4];
        unsigned tco = (unsigned)(gi * 4 + wq + (lane < 28 ? lane : 27) * 448) * 128u;
        asm volatile("" : "+v"(tco));
        static_for([&](auto gc) {
            constexpr int g = decltype(gc)::value, b = g & 1, pp = g & 3;
            SCHED();
            if (g == 11) STAMP(2);
            if (pp == 3 && g < 27) { STAMPG(2 + 2 * (g >> 2)); WAIT_BAR("lgkmcnt(0)"); STAMPG(3 + 2 * (g >> 2)); }
            if (g == 27) {
                WAIT_LGKM0();
                if (lane == 0) __hip_atomic_fetch_add((LAS unsigned*)(sm + L_CTL + 12), 1u, __ATOMIC_RELAXED, __HIP_MEMORY_SCOPE_WORKGROUP);
            }
            if (g == 11) STAMP(3);
            static_for([&](auto ic) {
                constexpr int i = decltype(ic)::value, ft = i >> 2, m = (i >> 1) & 1, sb = i & 1;
                acc[sb][ft][m] = mfma32(fw[b][ft][m], fx[b][sb], acc[sb][ft][m]);
                SCHED();
                if (g < 27 && i < 5) load_frag(g + 1, i);
                if constexpr (TOUCH && i == 4 && pp == 2 && g >= TOUCH0 && g < TOUCH0 + 16) {
                    u64 t;
                    asm volatile("s_mov_b64 %1, %3\n\tglobal_load_dword %0, %2, %1" : "=v"(tch[(g - TOUCH0) >> 2]), "=&s"(t)
                                 : "v"(tco), "s"((const char*)p.w2 + ((g - TOUCH0) >> 2) * (112 * 128)));
                }
                if constexpr (i >= 5 && g >= 7) fill(std::integral_constant<int, (g - 7) * 3 + i - 5>{});
                SCHED();
            }, std::make_integer_sequence<int, 8>{});
        }, std::make_integer_sequence<int, 28>{});
        STAMP(4);
        wait_lds_eq(sm + L_CTL + 64, 3u);
        unsigned sa[2];
#pragma unroll
        for (int i = 0; i < 8; ++i) cs[i >> 2][0][i & 3] = *(LAS f32x4*)(sm + L_CSUM + (64 * wn + 32 * (i >> 2) + 8 * (i & 3) + 4 * hh) * 4);
        sa[0] = *(LAS unsigned*)(sm + L_STAT + (64 * wm + r32) * 4);
        sa[1] = *(LAS unsigned*)(sm + L_STAT + (64 * wm + 32 + r32) * 4);
#pragma unroll
        for (int i = 0; i < 8; ++i) cs[i >> 2][1][i & 3] = *(LAS f32x4*)(sm + L_CSUM + 512 + (64 * wn + 32 * (i >> 2) + 8 * (i & 3) + 4 * hh) * 4);
        const int lw = fresh_lane();
        const int w2a0 = lb + w2i + (4 * (lw >> 5) + ((lw & 15) >> 2)) * 128 + (((2 * ((lw >> 4) & 1) + ((lw & 3) >> 1)) ^ (((lw >> 3) & 1) << 2)) * 16) + 8 * (lw & 1);
        const int w2a1 = w2a0 ^ 64;
        u32x4 w2r[12][2];
        auto park = [&](int i) {
            w2r[i][0] = ldstr2(lp(w2a0) + (i / 6) * L_SLOT + (i % 6) * 2048, 1024);
            w2r[i][1] = ldstr2(lp(w2a1) + (i / 6) * L_SLOT + (i % 6) * 2048, 1024);
        };
#pragma unroll
        for (int sb = 0; sb < 2; ++sb) {
            const float ri = __builtin_amdgcn_rcpf(bfhi(sa[sb])) * -1.44269504088896341f, nm = -bflo(sa[sb]), d2 = bfhi(sa[sb]) * bfhi(sa[sb]);
            const f32x2 ri2 = {ri, ri}, nm2 = {nm, nm}, dd2 = {d2, d2};
#pragma unroll
            for (int ft = 0; ft < 2; ++ft) {
#pragma unroll
                for (int t2 = 0; t2 < 2; ++t2) {
                    const int grp = sb * 4 + ft * 2 + t2;
                    if (EPARK && grp == 2) { SCHED(); wait_lds(sm + L_CTL + 48 + wq * 4); }
                    if (EPARK && grp >= 2) { SCHED(); park(grp - 2); }
                    f32x2 gu[4], ee[4];
#pragma unroll
                    for (int j = 0; j < 4; ++j) {
                        const int r = 8 * t2 + 2 * j;
                        const f32x2 g = __builtin_elementwise_fma(nm2, (f32x2){cs[ft][0][r >> 2][r & 3], cs[ft][0][r >> 2][(r & 3) + 1]}, (f32x2){acc[sb][ft][0][r], acc[sb][ft][0][r + 1]});
                        const f32x2 u = __builtin_elementwise_fma(nm2, (f32x2){cs[ft][1][r >> 2][r & 3], cs[ft][1][r >> 2][(r & 3) + 1]}, (f32x2){acc[sb][ft][1][r], acc[sb][ft][1][r + 1]});
                        const f32x2 t = g * ri2;
                        ee[j] = (f32x2){__builtin_amdgcn_exp2f(t[0]), __builtin_amdgcn_exp2f(t[1])};
                        gu[j] = g * u;
                    }
                    u32x4 st;
#pragma unroll
                    for (int j = 0; j < 4; ++j) {
                        const f32x2 dn = __builtin_elementwise_fma(ee[j], dd2, dd2);
                        const f32x2 hv = gu[j] * (f32x2){__builtin_amdgcn_rcpf(dn[0]), __builtin_amdgcn_rcpf(dn[1])};
                        st[j] = pk(hv[0], hv[1]);
                    }
                    const unsigned fi = (unsigned)((4 * mt + 2 * wm + sb) * 112 + 8 * nt + 4 * wn + 2 * ft + t2);
                    __builtin_amdgcn_raw_buffer_store_b128(st, wsr, WS_H + fi * 1024u + lane * 16u, 0, 16);
                }
            }
        }
        SCHED();
        STAMP(5);
        const u32x4 w2o = w2_off();
        GAS u64* myflag = (GAS u64*)(p.ws + WS_FH + ((2 * mt + wm) * 28 + 2 * nt + wn) * 128);
        if (!__builtin_amdgcn_readfirstlane(__hip_atomic_load((LAS unsigned*)(sm + L_CTL + 112 + wq * 4), __ATOMIC_RELAXED, __HIP_MEMORY_SCOPE_WORKGROUP))) {
            WAIT_VM(0);
            if (lane == 0) __hip_atomic_store(myflag, tag, RLX_AGENT);
            wait_lds(sm + L_CTL + 112 + wq * 4);
        }
        asm volatile("" ::: "memory");
#pragma unroll
        for (int i = 6; i < 12; ++i) park(i);
        WAIT_VM(0);
        if (TOUCH) asm volatile("" :: "v"(tch[0]), "v"(tch[1]), "v"(tch[2]), "v"(tch[3]));
        if (lane == 0) __hip_atomic_store(myflag, tag, RLX_AGENT);
        STAMP(6);
        if (!EPARK) {
#pragma unroll
            for (int i = 0; i < 6; ++i) park(i);
        }
        asm volatile("" ::: "memory");
        __hip_atomic_store((LAS unsigned*)(sm + L_CTL + 32 + wq * 4), 1u, __ATOMIC_RELAXED, __HIP_MEMORY_SCOPE_WORKGROUP);
        WAIT_LGKM0();
#pragma unroll
        for (int i = 0; i < 12; ++i) asm volatile("" : "+v"(w2r[i][0]), "+v"(w2r[i][1]));
#pragma unroll
        for (int i = 0; i < 3; ++i) pair_w2(w2o, 12 + 2 * i, w2i + i * 4096);
#pragma unroll
        for (int i = 0; i < 3; ++i) pair_w2(w2o, 18 + 2 * i, w2i + L_SLOT + i * 4096);
        STAMP(7);
        {
            GAS u64* fp = (GAS u64*)(p.ws + WS_FH + (m64 * 28 + 7 * wq + (lw < 7 ? lw : 0)) * 128);
#pragma unroll 1
            for (unsigned spins = 0; spins < SPIN_CAP; ++spins) {
                if (__all(__hip_atomic_load(fp, RLX_AGENT) == tag)) break;
                __builtin_amdgcn_s_sleep(POLL_SLEEP);
            }
            __builtin_amdgcn_fence(__ATOMIC_ACQUIRE, "wavefront");
        }
        STAMP(11);
        const unsigned hb = WS_H + (unsigned)(2 * m64 * 112 + 28 * wq) * 1024u + lw * 16u;
        u32x4 hf[28][2];
        auto load_h = [&](int i, int j) {
            int t = i + rot2; t = t >= 28 ? t - 28 : t;
            hf[i][j] = __builtin_amdgcn_raw_buffer_load_b128(wsr, hb + j * (112u * 1024u), t * 1024, 16);
        };
        constexpr int HD = HDEPTH, HG = HGROW;
#pragma unroll
        for (int i = 0; i < HD; ++i) { load_h(i, 0); load_h(i, 1); }
        STAMP(14);
        f32x16 ac2[2][2];
#pragma unroll
        for (int i = 0; i < 4; ++i)
#pragma unroll
            for (int r = 0; r < 16; ++r) ac2[i >> 1][i & 1][r] = 0.f;
        u32x4 a0 = w2r[0][0], a1 = w2r[0][1];
        static_for([&](auto ic) {
            constexpr int i = decltype(ic)::value, n = i + 1;
            SCHED();
            if (i == 23) wait_lds(sm + L_CTL + 80 + wq * 4);
            u32x4 b0, b1;
            if constexpr (n < 12) { b0 = w2r[n][0]; b1 = w2r[n][1]; }
            else if constexpr (n < 28) {
                constexpr int so = n < 24 ? ((n - 12) / 6) * L_SLOT + ((n - 12) % 6) * 2048 : (n - 24) * 2048;
                const int lo = n < 24 ? 0 : latei - w2i;
                b0 = ldstr2(lp(w2a0 + lo) + so, 1024);
                b1 = ldstr2(lp(w2a1 + lo) + so, 1024);
            }
            SCHED();
            ac2[0][0] = mfma32(a0, hf[i][0], ac2[0][0]);
            SCHED();
            constexpr int l0 = i < HG ? HD + 2 * i : HD + HG + i;
            if constexpr (l0 < 28) load_h(l0, 0);
            SCHED();
            ac2[1][0] = mfma32(a1, hf[i][0], ac2[1][0]);
            SCHED();
            if constexpr (l0 < 28) load_h(l0, 1);
            SCHED();
            ac2[0][1] = mfma32(a0, hf[i][1], ac2[0][1]);
            SCHED();
            if constexpr (i < HG) load_h(l0 + 1, 0);
            SCHED();
            ac2[1][1] = mfma32(a1, hf[i][1], ac2[1][1]);
            SCHED();
            if constexpr (i < HG) load_h(l0 + 1, 1);
            a0 = b0; a1 = b1;
        }, std::make_integer_sequence<int, 28>{});
        SCHED();
        STAMP(15);
        WAIT_LGKM0();
#pragma unroll
        for (int v = 0; v < 8; ++v) {
            LAS char* sl = sm + w2i + (v < 6 ? v * 2048 : L_SLOT + (v - 6) * 2048) + lw * 16;
            const f32x16& t = ac2[v >> 2][(v >> 1) & 1];
            const int r0 = 8 * (v & 1);
            *(LAS f32x4*)sl = (f32x4){t[r0], t[r0 + 1], t[r0 + 2], t[r0 + 3]};
            *(LAS f32x4*)(sl + 1024) = (f32x4){t[r0 + 4], t[r0 + 5], t[r0 + 6], t[r0 + 7]};
        }
        WAIT_BAR("lgkmcnt(0)");
        STAMP(16);
        STAMP(17);
        STAMP_OUT(18);
    } else {
        u64 epoch;
        asm volatile("s_load_dwordx2 %0, %1, 0x0" : "=s"(epoch) : "s"(p.ws) : "memory");
        u32x4 xvo;
#pragma unroll
        for (int j = 0; j < 4; ++j) xvo[j] = (unsigned)((8 * j + (lane >> 3)) * D + ((((lane & 7) ^ (lane >> 4)) ^ (4 * (j & 1))) * 8)) * 2u - 1024u * j;
        const u16* xb = sptr(p.x, (mt * 128 + wq * 32) * D);
        auto group_x = [&](int si) { glds16x4(sptr(xb, kst(si) * 64), xvo, lb + ring(si) + wq * 4096); };
        group_x(0);
        group_x(1); group_w(1, 0); group_w(1, 1);
        const int cbi = L_CONV + chn * C_CH;
        LAS char* cb = sm + cbi;
        unsigned kvo[2], uso;
#pragma unroll
        for (int rd = 0; rd < 2; ++rd) kvo[rd] = (unsigned)((lane >> 3) * D + (((lane & 7) ^ (((lane >> 4) & 1) | (((2 * wq + rd) & 3) << 1))) * 8)) * 2u;
        uso = WS_U + (unsigned)(kc0 + wq * 16 + (lane >> 3)) * 8192u + (unsigned)(1984 - kr0 + (lane & 7) * 8) * 2u;
        int kro = lb + L_KVO + wq * 2048 + (lane >> 3) * 128 + (((lane & 7) ^ (lane >> 3)) * 16);
        const int cc = 2 * par + ((lane >> 2) & 1);
        int zo = cbi + C_U + par * C_US + (248 + (lane < 36 ? lane : 35)) * 16;
        zo = lane < 8 ? cbi + cc * C_HS + (lane & 3) * 16 : zo;
        u32x4 w2o = w2_off();
        const unsigned fo = lane * 16u;
        unsigned ao = (wq < 2 ? WS_CSUM + (unsigned)(nt * 2 + wq) * 1024u : WS_STATS + mt * 1024u) + fo;
        asm volatile("" : "+v"(zo), "+v"(w2o), "+v"(kvo[0]), "+v"(kvo[1]), "+v"(uso), "+v"(kro), "+v"(ao));
        asm volatile("s_waitcnt vmcnt(16) lgkmcnt(0)" : "+s"(epoch) :: "memory");
        const unsigned tag32 = (unsigned)(epoch ^ TAG_SALT);
        u32x4 bval, ug[2] = {{tag32, 0u, tag32, 0u}, {tag32, 0u, tag32, 0u}};
        unsigned dbits = *(LAS u16*)(sm + wave * 1024 + 240 + (dch & 7) * 2);
        {
            const u32x4 h0 = lds128(sm + wave * 1024 + 224);
            const bool s1 = !(cc & 1), s2 = !(cc & 2);
            const unsigned a0 = s1 ? 0u : h0[0], a1 = s1 ? h0[0] : h0[1], a2 = s1 ? h0[1] : h0[2], a3 = s1 ? h0[2] : h0[3];
            const unsigned lm = (lane == 3 || lane == 7) ? 0xffffffffu : 0u;
            bval = (u32x4){(s2 ? 0u : a0) & lm, (s2 ? 0u : a1) & lm, (s2 ? a0 : a2) & lm, (s2 ? a1 : a3) & lm};
            asm volatile("" : "+v"(bval), "+v"(dbits), "+v"(ug[0]), "+v"(ug[1]));
        }
        auto kv_store = [&](int rd) {
            LAS unsigned* kp = (LAS unsigned*)(lp(kro) + rd * 1024);
            ug[0][1] = kp[0]; asm volatile("" ::: "memory"); ug[0][3] = kp[1]; asm volatile("" ::: "memory");
            ug[1][1] = kp[2]; asm volatile("" ::: "memory"); ug[1][3] = kp[3];
            __builtin_amdgcn_raw_buffer_store_b128(ug[0], wsr, uso, rd * 65536, 16);
            __builtin_amdgcn_raw_buffer_store_b128(ug[1], wsr, uso, rd * 65536 + 4096, 16);
        };
        WAIT_BAR("vmcnt(12) lgkmcnt(0)");
        STAMP(1);
        group_x(2); group_w(2, 0); group_w(2, 1);
#pragma unroll
        for (int j = 0; j < 2; ++j) {
            glds16(sptr(p.k, (kr0 + (2 * wq + j) * 8) * D + kc0), kvo[j], lb + L_KV + (2 * wq + j) * 1024);
            glds16(sptr(p.v, (kr0 + (2 * wq + j) * 8) * D + kc0), kvo[j], lb + L_KV + 8192 + (2 * wq + j) * 1024);
        }
        STAMPG(2); WAIT_BAR("vmcnt(16)"); STAMPG(3);
#pragma unroll
        for (int w = 1; w < 5; ++w) {
            group_x(w + 2); group_w(w + 2, 0); group_w(w + 2, 1);
            if (w == 3 && lane < 32) *(LAS unsigned*)(sm + L_CTL + lane * 4) = 0u;
            if (w == 4) kv_store(0);
            if (w == 2) STAMP(2);
            STAMPG(2 + 2 * w); WAIT_BAR("vmcnt(12) lgkmcnt(0)"); STAMPG(3 + 2 * w);
            if (w == 2) STAMP(3);
        }
        glds16c(p.ws, ao, lb + L_RAW + wq * 1024);
#pragma unroll
        for (int i = 0; i < 3; ++i) pair_w2(w2o, 2 * i, w2i + i * 4096);
        STAMPG(12); WAIT_BAR("vmcnt(12)"); STAMPG(13);
        kv_store(1);
        asm volatile("" ::: "memory");
        if (lane == 0) __hip_atomic_fetch_add((LAS unsigned*)(sm + L_CTL + 12), 1u, __ATOMIC_RELAXED, __HIP_MEMORY_SCOPE_WORKGROUP);
        if (wq < 3) {
            u32x4 av = lds128(sm + L_RAW + wq * 1024 + lane * 16);
#pragma unroll 1
            for (unsigned spins = 0; spins < SPIN_CAP; ++spins) {
                if (__all(((av[0] ^ tag32) | (av[2] ^ tag32)) == 0u)) break;
                asm volatile("" ::: "memory");
                av = __builtin_amdgcn_raw_buffer_load_b128(wsr, ao, 0, 16);
                asm volatile("" : "+v"(av));
            }
            *(LAS u32x2*)(sm + L_CSUM + wq * 512 + lane * 8) = (u32x2){av[1], av[3]};
            asm volatile("" ::: "memory");
            if (lane == 0) __hip_atomic_fetch_add((LAS unsigned*)(sm + L_CTL + 64), 1u, __ATOMIC_RELAXED, __HIP_MEMORY_SCOPE_WORKGROUP);
        }
        STAMP(4);
#pragma unroll
        for (int i = 0; i < 3; ++i) pair_w2(w2o, 6 + 2 * i, w2i + L_SLOT + i * 4096);
        WAIT_VM(12);
        __hip_atomic_store((LAS unsigned*)(sm + L_CTL + 48 + wq * 4), 1u, __ATOMIC_RELAXED, __HIP_MEMORY_SCOPE_WORKGROUP);
        wait_lds_eq(sm + L_CTL + 12, 8u);
        STAMP(5);
        __builtin_amdgcn_s_setprio(2);
        const unsigned ubase = WS_U + (unsigned)dch * 8192u;
        glds16x2c(p.ws + ubase + par * 2048, fo, lb + cbi + C_U + par * 2048);
        glds16x2c(p.ws + ubase + 4096 + par * 2048, fo, lb + cbi + C_U + C_US + par * 2048);
        glds4c(p.ws + ubase + (1 - par) * 2048, (lane & 1) * 4u, lb + L_UNX + wq * 256);
        {
            const u32x4 fvo = {fo, fo, fo, lane == 63 ? 62u * 16u : fo};
#pragma unroll
            for (int c = 0; c < 2; ++c) glds16x4(sptr(p.hk, dch * S + 4 * par + 2 * c + 2), fvo, lb + cbi + (2 * par + c) * C_HS + 64);
        }
        *(LAS u32x4*)(sm + zo) = bval;
        WAIT_VM(13);
        __hip_atomic_store((LAS unsigned*)(sm + L_CTL + 112 + wq * 4), 1u, __ATOMIC_RELAXED, __HIP_MEMORY_SCOPE_WORKGROUP);
        WAIT_VM(8);
        {
            u32x4 ua[2], ub[2]; u32x2 un[2];
#pragma unroll
            for (int rd = 0; rd < 2; ++rd) {
                const int x8 = 128 * par + 64 * rd + lane;
                ua[rd] = lds128(cb + C_U + x8 * 16); ub[rd] = lds128(cb + C_U + C_US + x8 * 16);
                un[rd] = *(LAS u32x2*)((rd == 1 && lane == 63) ? sm + L_UNX + wq * 256 : cb + C_U + x8 * 16 + 16);
            }
#pragma unroll 1
            for (unsigned spins = 0; spins < SPIN_CAP; ++spins) {
                unsigned df = 0;
#pragma unroll
                for (int rd = 0; rd < 2; ++rd) df |= (ua[rd][0] ^ tag32) | (ua[rd][2] ^ tag32) | (ub[rd][0] ^ tag32) | (ub[rd][2] ^ tag32) | (un[rd][0] ^ tag32);
                if (__all(df == 0u)) break;
                __builtin_amdgcn_s_sleep(4);
                asm volatile("" ::: "memory");
#pragma unroll
                for (int rd = 0; rd < 2; ++rd) {
                    const int x8 = 128 * par + 64 * rd + lane;
                    const unsigned uo = ubase + (unsigned)x8 * 16u;
                    ua[rd] = __builtin_amdgcn_raw_buffer_load_b128(wsr, uo, 0, 16);
                    ub[rd] = __builtin_amdgcn_raw_buffer_load_b128(wsr, uo + 4096u, 0, 16);
                    un[rd] = __builtin_amdgcn_raw_buffer_load_b64(wsr, x8 == 255 ? uo : uo + 16u, 0, 16);
                }
                asm volatile("" : "+v"(ua[0]), "+v"(ub[0]), "+v"(un[0]), "+v"(ua[1]), "+v"(ub[1]), "+v"(un[1]));
            }
#pragma unroll
            for (int rd = 0; rd < 2; ++rd) {
                const int x8 = 128 * par + 64 * rd + lane;
                const unsigned nx = x8 == 255 ? 0u : un[rd][1];
                *(LAS u32x4*)(cb + C_U + x8 * 16) = (u32x4){__builtin_amdgcn_alignbit(ua[rd][3], ua[rd][1], 16), __builtin_amdgcn_alignbit(ub[rd][1], ua[rd][3], 16),
                                                            __builtin_amdgcn_alignbit(ub[rd][3], ub[rd][1], 16), __builtin_amdgcn_alignbit(nx, ub[rd][3], 16)};
                *(LAS u32x4*)(cb + C_U + C_US + x8 * 16) = (u32x4){ua[rd][1], ua[rd][3], ub[rd][1], ub[rd][3]};
            }
        }
        asm volatile("s_waitcnt vmcnt(0) lgkmcnt(0)" ::: "memory");
        __hip_atomic_store((LAS unsigned*)(sm + L_CTL + 16 + wq * 4), 1u, __ATOMIC_RELAXED, __HIP_MEMORY_SCOPE_WORKGROUP);
        u32x4 lw2[8], xv[2];
        {
            const __amdgpu_buffer_rsrc_t wr2 = __builtin_amdgcn_make_buffer_rsrc((void*)p.w2, 0, F * D * 2, 0x00020000);
#pragma unroll
            for (int j = 0; j < 8; ++j) {
                int t = 24 + (j >> 2) * 2 + rot2; t = t >= 28 ? t - 28 : t;
                lw2[j] = __builtin_amdgcn_raw_buffer_load_b128(wr2, w2o[0], ((wq * 448 + t * 16 + (j & 3) * 8) * D + n64 * 64) * 2, 0);
            }
        }
        const int lf = fresh_lane();
        const int srow = 64 * m64 + 32 * (wq & 1) + (lf & 31);
        const int dd = 64 * n64 + 32 * (wq >> 1) + 16 * (lf >> 5);
        const unsigned xoff = (unsigned)(srow * D + dd) * 2u;
        {
            const __amdgpu_buffer_rsrc_t xr = __builtin_amdgcn_make_buffer_rsrc((void*)p.x, 0, S * D * 2, 0x00020000);
            xv[0] = __builtin_amdgcn_raw_buffer_load_b128(xr, xoff, 0, 0);
            xv[1] = __builtin_amdgcn_raw_buffer_load_b128(xr, xoff, 16, 0);
        }
        SCHED();
        wait_lds(sm + L_CTL + 16 + (wq ^ 1) * 4);
        STAMP(7);
        STAMP(6);
        const int n16 = lane & 15, kg = lane >> 4;
        f32x4 ca[4];
#pragma unroll
        for (int j = 0; j < 4; ++j) ca[j] = (f32x4){0.f, 0.f, 0.f, 0.f};
        int apo = lb + cbi + (n16 & 3) * C_HS + ((n16 >> 2) + kg) * 16;
        int bpo = lb + cbi + C_U + (n16 & 1) * C_US + (252 - 4 * (n16 >> 1) + kg) * 16 - 7 * 512;
        u32x4 fa[2][8], fb[2][8];
#pragma unroll
        for (int i = 0; i < 8; ++i) { fa[0][i] = lds128(lp(apo) + i * 512); fb[0][i] = lds128(lp(bpo) + (7 - i) * 512); }
        const bool odd = lf & 1;
        int ysrc = (2 * (lf >> 3) + (lf & 1) + 16 * ((lf >> 1) & 3)) * 4;
        unsigned yso = WS_Y + (unsigned)dch * 8192u + lf * 16u;
        f32x4 kd[4];
        {
            const float dval = bflo(dbits);
            const int sl0 = 32 * ((lf & 15) >> 1) + 8 * (lf >> 4) + 4 * (lf & 1);
#pragma unroll
            for (int j = 0; j < 4; ++j) {
                const int tj = (j == 0 ? 0 : j == 1 ? 3 : j == 2 ? 4 : 7) ^ par;
                const u32x2 kvr = *(LAS u32x2*)(cb + C_U + C_US + (2044 - 256 * tj - sl0) * 2);
                const unsigned ka = pk(bfhi(kvr[1]) * dval, bflo(kvr[1]) * dval), kb = pk(bfhi(kvr[0]) * dval, bflo(kvr[0]) * dval);
                kd[j] = (f32x4){bflo(ka), bfhi(ka), bflo(kb), bfhi(kb)};
                asm volatile("" : "+v"(kd[j]));
            }
            asm volatile("" : "+v"(ysrc), "+v"(yso));
        }
        wait_lds(sm + L_CTL + 32 + wq * 4);
        STAMP(8);
        auto conv_all = [&](auto pc_) {
            constexpr int P = decltype(pc_)::value;
            static_for([&](auto rc) {
                constexpr int r = decltype(rc)::value, c = r & 1, n = c ^ 1;
                static_for([&](auto kc) {
                    constexpr int k = decltype(kc)::value;
                    constexpr int T[4] = {P, 3 - P, 4 + P, 7 - P};
                    constexpr int j = k <= T[0] ? 0 : k <= T[0] + T[1] + 1 ? 1 : k <= T[0] + T[1] + T[2] + 2 ? 2 : 3;
                    constexpr int pp = k - (j == 0 ? 0 : j == 1 ? T[0] + 1 : j == 2 ? T[0] + T[1] + 2 : T[0] + T[1] + T[2] + 3);
                    ca[j] = mfma16(fa[c][pp], fb[c][T[j] - pp], ca[j]);
                    SCHED();
                    if constexpr (r < 7 && k < 2 * (T[3] + 1)) {
                        if constexpr (k & 1) fb[n][k >> 1] = lds128(lp(bpo) + (r + 1) * 64 + (7 - (k >> 1)) * 512);
                        else fa[n][k >> 1] = lds128(lp(apo) + (r + 1) * 64 + (k >> 1) * 512);
                    }
                    SCHED();
                }, std::make_integer_sequence<int, 18>{});
                asm volatile("" : "+v"(apo), "+v"(bpo));
            }, std::make_integer_sequence<int, 8>{});
        };
        if (par) conv_all(std::integral_constant<int, 1>{}); else conv_all(std::integral_constant<int, 0>{});
        SCHED();
        STAMP(9);
        {
            float r0[4], r1[4];
#pragma unroll
            for (int j = 0; j < 4; ++j) { r0[j] = dpp_xor1(odd ? ca[j][0] : ca[j][2]); r1[j] = dpp_xor1(odd ? ca[j][1] : ca[j][3]); }
            unsigned rp[4][2];
#pragma unroll
            for (int j = 0; j < 4; ++j) {
                const unsigned ya = pk(odd ? r0[j] : ca[j][0], odd ? ca[j][2] : r0[j]), yb = pk(odd ? r1[j] : ca[j][1], odd ? ca[j][3] : r1[j]);
                rp[j][0] = __builtin_amdgcn_ds_bpermute(ysrc, pk(add1(bflo1(ya), kd[j][0]), add1(bfhi1(ya), kd[j][1])));
                rp[j][1] = __builtin_amdgcn_ds_bpermute(ysrc, pk(add1(bflo1(yb), kd[j][2]), add1(bfhi1(yb), kd[j][3])));
            }
#pragma unroll
            for (int j = 0; j < 4; ++j)
                __builtin_amdgcn_raw_buffer_store_b128((u32x4){vmov1(tag32), rp[j][0], vmov1(tag32), rp[j][1]}, wsr, yso,
                                                       ((j == 0 ? 0 : j == 1 ? 3 : j == 2 ? 4 : 7) ^ par) * 1024, 16);
        }
        STAMP(10);
        WAIT_LGKM0();
        if (lane == 0) __hip_atomic_fetch_add((LAS unsigned*)(sm + L_CTL + 8), 1u, __ATOMIC_RELAXED, __HIP_MEMORY_SCOPE_WORKGROUP);
#pragma unroll 1
        for (unsigned spins = 0; spins < (1u << 22); ++spins) {
            if (__builtin_amdgcn_readfirstlane(__hip_atomic_load((LAS unsigned*)(sm + L_CTL + 8), __ATOMIC_RELAXED, __HIP_MEMORY_SCOPE_WORKGROUP)) == 4u) break;
            __builtin_amdgcn_s_sleep(1);
        }
        asm volatile("" ::: "memory");
#pragma unroll
        for (int j = 0; j < 8; ++j) *(LAS u32x4*)(sm + latei + j * 1024 + lf * 16) = lw2[j];
        asm volatile("" ::: "memory");
        __hip_atomic_store((LAS unsigned*)(sm + L_CTL + 80 + wq * 4), 1u, __ATOMIC_RELAXED, __HIP_MEMORY_SCOPE_WORKGROUP);
        STAMP(11);
        __builtin_amdgcn_s_sleep(YSLEEP);
        u32x2 yv[16], stv;
        const int yl = L_CONV + 32768 + wq * 4096;
        const unsigned yg = (unsigned)(lf >> 3) * 8192u + (unsigned)(lf & 7) * 16u;
        const u32x4 yvo = {yg, yg + (65536u - 1024u), yg + 2 * (65536u - 1024u), yg + 3 * (65536u - 1024u)};
        const char* ybase = p.ws + WS_Y + (unsigned)(64 * n64 + 32 * (wq >> 1)) * 8192u + (unsigned)(64 * m64 + 32 * (wq & 1)) * 4u;
#pragma unroll 1
        for (unsigned spins = 0; spins < SPIN_CAP; ++spins) {
            glds16x4c(ybase, yvo, lb + yl);
            stv = __builtin_amdgcn_raw_buffer_load_b64(wsr, WS_STATS + (unsigned)srow * 8u, 0, 16);
            WAIT_VM(0);
#pragma unroll
            for (int i = 0; i < 16; ++i) yv[i] = *(LAS u32x2*)(sm + yl + ((lf >> 5) * 16 + i) * 128 + ((lf & 31) >> 1) * 8);
            unsigned df = stv[0] ^ tag32;
#pragma unroll
            for (int i = 0; i < 16; ++i) df |= yv[i][0] ^ tag32;
            if (__all(df == 0u)) break;
            __builtin_amdgcn_s_sleep(8);
        }
        STAMP(14);
        __builtin_amdgcn_s_setprio(0);
        f32x2 sf[8], xf[8];
        {
            const float nmb = -bflo(stv[1]);
            float ri = __builtin_amdgcn_rcpf(bfhi(stv[1]));
            asm volatile("s_nop 1" : "+v"(ri));
            const unsigned ysh = (srow & 1) ? 0u : 16u;
#pragma unroll
            for (int i = 0; i < 8; ++i) {
                const unsigned xd = xv[i >> 2][i & 3];
                xf[i] = (f32x2){bflo1(xd), bfhi1(xd)};
                const unsigned tp = pk(add1(xf[i][0], nmb), add1(xf[i][1], nmb));
                const unsigned np = pk(mul1(bflo1(tp), ri), mul1(bfhi1(tp), ri));
                sf[i] = (f32x2){mul1(bflo1(np), bfhi1(yv[2 * i][1] << ysh)), mul1(bfhi1(np), bfhi1(yv[2 * i + 1][1] << ysh))};
                asm volatile("" : "+v"(sf[i]), "+v"(xf[i]));
            }
        }
        const int rm = lb + L_W2 + (wq < 3 ? wq * 4096 : L_SLOT) + (lf >> 5) * 2048 + (lf & 31) * 16;
        STAMP(15);
        WAIT_BAR("lgkmcnt(0)");
        STAMP(16);
        f32x4 o[4];
#pragma unroll
        for (int j = 0; j < 4; ++j) {
            LAS char* rp = lp(rm) + j * 512;
            o[j] = (*(LAS f32x4*)rp + *(LAS f32x4*)(rp + 12288)) + (*(LAS f32x4*)(rp + 24576) + *(LAS f32x4*)(rp + 36864));
        }
        f32x2 so[8], ev[8];
#pragma unroll
        for (int i = 0; i < 8; ++i) {
            const f32x2 ov = {o[i >> 1][(i & 1) * 2], o[i >> 1][(i & 1) * 2 + 1]};
            const f32x2 t = ov * (f32x2){-1.44269504088896341f, -1.44269504088896341f};
            ev[i] = (f32x2){__builtin_amdgcn_exp2f(t[0]), __builtin_amdgcn_exp2f(t[1])};
            so[i] = sf[i] * ov;
        }
        SCHED();
        u32x4 st[2];
#pragma unroll
        for (int i = 0; i < 8; ++i) {
            const f32x2 dn = ev[i] + (f32x2){1.f, 1.f};
            const f32x2 r = __builtin_elementwise_fma(so[i], (f32x2){__builtin_amdgcn_rcpf(dn[0]), __builtin_amdgcn_rcpf(dn[1])}, xf[i]);
            st[i >> 2][i & 3] = pk(r[0], r[1]);
        }
        const __amdgpu_buffer_rsrc_t orr = __builtin_amdgcn_make_buffer_rsrc((void*)p.out, 0, S * D * 2, 0x00020000);
        __builtin_amdgcn_raw_buffer_store_b128(st[0], orr, xoff, 0, 0);
        __builtin_amdgcn_raw_buffer_store_b128(st[1], orr, xoff, 16, 0);
        STAMP(17);
        STAMP_OUT(18);
    }
}

extern "C" void kernel_launch(void* const* d_in, const int* in_sizes, int n_in,
                              void* d_out, int out_size, void* d_ws, size_t ws_size,
                              hipStream_t stream) {
    Params p;
    p.x = (const u16*)d_in[0]; p.k = (const u16*)d_in[1]; p.v = (const u16*)d_in[2];
    p.sk = (const u16*)d_in[3]; p.hk = (const u16*)d_in[4]; p.dv = (const u16*)d_in[5];
    p.w1 = (const u16*)d_in[7]; p.w3 = (const u16*)d_in[8]; p.w2 = (const u16*)d_in[9];
    p.out = (u16*)d_out; p.ws = (char*)d_ws;
    (void)hipFuncSetAttribute((const void*)fused_kernel, hipFuncAttributeMaxDynamicSharedMemorySize, L_TOTAL);
    fused_kernel<<<256, 512, L_TOTAL, stream>>>(p);
}
```

```cpp
#include <hip/hip_runtime.h>
#include <stdint.h>
#include <utility>

typedef unsigned short u16;
typedef unsigned long long u64;
typedef short s16x4 __attribute__((ext_vector_type(4)));
typedef short s16x8 __attribute__((ext_vector_type(8)));
typedef __bf16 bf16x2 __attribute__((ext_vector_type(2)));
typedef __bf16 bf16x8 __attribute__((ext_vector_type(8)));
typedef float f32x2 __attribute__((ext_vector_type(2)));
typedef float f32x4 __attribute__((ext_vector_type(4)));
typedef float f32x16 __attribute__((ext_vector_type(16)));
typedef unsigned u32x2 __attribute__((ext_vector_type(2)));
typedef unsigned u32x4 __attribute__((ext_vector_type(4)));
typedef unsigned u32x16 __attribute__((ext_vector_type(16)));
#define LAS __attribute__((address_space(3)))
#define GAS __attribute__((address_space(1)))
#define RLX_AGENT __ATOMIC_RELAXED, __HIP_MEMORY_SCOPE_AGENT

constexpr int S = 2048, D = 448, F = 1792;

constexpr unsigned WS_EPOCH = 0;
constexpr unsigned WS_FH = 65536;
constexpr unsigned WS_STATS = 8192;
constexpr unsigned WS_CSUM = 24576;
constexpr unsigned WS_U = 262144;
constexpr unsigned WS_Y = WS_U + D * S * 4;
constexpr unsigned WS_H = WS_Y + D * S * 4;
constexpr unsigned WS_END = WS_H + 64 * 112 * 1024;
constexpr unsigned WS_DBG = WS_END;

constexpr int L_KV = 0;
constexpr int L_CTL = 0;
constexpr int L_CSUM = 256;
constexpr int L_STAT = 1280;
constexpr int L_RAW = 2048;
constexpr int L_KVO = 8192;
constexpr int L_UNX = 6144;
constexpr int L_SLOT = 49152;
constexpr int L_CONV = 12288;
constexpr int C_CH = 26112, C_HS = 4160, C_U = 16640, C_US = 4640;
constexpr int L_W2 = 16384 + L_SLOT;
constexpr int L_TOTAL = 163840;
__host__ __device__ constexpr int ring(int stage) { return 16384 + (stage % 3) * L_SLOT; }
constexpr unsigned CODE_PREFETCH = 36864;
#define POLL_SLEEP 4
#define HDEPTH 6
#define ROT2 (8 * ml)
#define MPRIO 1
#define EPARK 0
#define YSLEEP 16
#define TOUCH 1
#define TOUCH0 4
#define HGROW 4
#define ROTM 6
#define KVW 0
#define MWARM 0
#define URETRY 4
#define KVNT " nt"
#define FLAGW 1
#define ROT1 ((2 * ml + nl) % ROTM)

constexpr u64 TAG_SALT = 0x9E3779B97F4A7C15ull;
constexpr unsigned SPIN_CAP = 1u << 18;

struct Params {
    const u16* x; const u16* k; const u16* v; const u16* sk; const u16* hk; const u16* dv;
    const u16* w1; const u16* w3; const u16* w2; u16* out; char* ws;
};

__device__ __forceinline__ float bflo(unsigned p) { return __uint_as_float(p << 16); }
__device__ __forceinline__ float bfhi(unsigned p) { return __uint_as_float(p & 0xffff0000u); }
__device__ __forceinline__ float bflo1(unsigned p) { float r; asm("v_lshlrev_b32 %0, 16, %1" : "=v"(r) : "v"(p)); return r; }
__device__ __forceinline__ float bfhi1(unsigned p) { float r; asm("v_and_b32 %0, 0xffff0000, %1" : "=v"(r) : "v"(p)); return r; }
__device__ __forceinline__ float mul1(float a, float b) { float r; asm("v_mul_f32 %0, %1, %2" : "=v"(r) : "v"(a), "v"(b)); return r; }
__device__ __forceinline__ void fmac1(float& d, float a, float b) { asm("v_fmac_f32 %0, %1, %2" : "+v"(d) : "v"(a), "v"(b)); }
__device__ __forceinline__ float dpp_xor1(float v) {
    return __int_as_float(__builtin_amdgcn_update_dpp(0, __float_as_int(v), 0xB1, 0xf, 0xf, true));
}
__device__ __forceinline__ float add1(float a, float b) { float r; asm("v_add_f32 %0, %1, %2" : "=v"(r) : "v"(a), "v"(b)); return r; }
__device__ __forceinline__ unsigned vmov1(unsigned s) { unsigned r; asm volatile("v_mov_b32 %0, %1" : "=v"(r) : "s"(s)); return r; }
__device__ __forceinline__ unsigned pk(float a, float b) {
    f32x2 f = {a, b};
    return __builtin_bit_cast(unsigned, __builtin_convertvector(f, bf16x2));
}
__device__ __forceinline__ float rbf(float a) { return bflo(pk(a, a)); }
__device__ __forceinline__ float dot2(unsigned a, unsigned b, float c) {
    return __builtin_amdgcn_fdot2_f32_bf16(__builtin_bit_cast(bf16x2, a), __builtin_bit_cast(bf16x2, b), c, false);
}
__device__ __forceinline__ void glds16(const void* base, unsigned voff, unsigned ldst) {
    u64 t;
    asm volatile("s_mov_b32 m0, %3\n\ts_mov_b64 %0, %2\n\tglobal_load_lds_dwordx4 %1, %0" KVNT : "=&s"(t) : "v"(voff), "s"(base), "s"(ldst));
}
__device__ __forceinline__ void glds16x4(const void* base, u32x4 vo, unsigned ldst) {
    u64 t;
    asm volatile("s_mov_b32 m0, %6\n\ts_mov_b64 %0, %5\n\tglobal_load_lds_dwordx4 %1, %0\n\tglobal_load_lds_dwordx4 %2, %0 offset:1024\n\t"
                 "global_load_lds_dwordx4 %3, %0 offset:2048\n\tglobal_load_lds_dwordx4 %4, %0 offset:3072"
                 : "=&s"(t) : "v"(vo[0]), "v"(vo[1]), "v"(vo[2]), "v"(vo[3]), "s"(base), "s"(ldst));
}
__device__ __forceinline__ void glds16x4c(const void* base, u32x4 vo, unsigned ldst) {
    u64 t;
    asm volatile("s_mov_b32 m0, %6\n\ts_mov_b64 %0, %5\n\tglobal_load_lds_dwordx4 %1, %0 sc1\n\tglobal_load_lds_dwordx4 %2, %0 offset:1024 sc1\n\t"
                 "global_load_lds_dwordx4 %3, %0 offset:2048 sc1\n\tglobal_load_lds_dwordx4 %4, %0 offset:3072 sc1"
                 : "=&s"(t) : "v"(vo[0]), "v"(vo[1]), "v"(vo[2]), "v"(vo[3]), "s"(base), "s"(ldst) : "memory");
}
__device__ __forceinline__ void glds16x2c(const void* base, unsigned voff, unsigned ldst) {
    u64 t;
    asm volatile("s_mov_b32 m0, %3\n\ts_mov_b64 %0, %2\n\tglobal_load_lds_dwordx4 %1, %0 sc1\n\tglobal_load_lds_dwordx4 %1, %0 offset:1024 sc1"
                 : "=&s"(t) : "v"(voff), "s"(base), "s"(ldst));
}
__device__ __forceinline__ void glds16c(const void* base, unsigned voff, unsigned ldst) {
    u64 t;
    asm volatile("s_mov_b32 m0, %3\n\ts_mov_b64 %0, %2\n\tglobal_load_lds_dwordx4 %1, %0 sc1" : "=&s"(t) : "v"(voff), "s"(base), "s"(ldst));
}
__device__ __forceinline__ void glds4c(const void* base, unsigned voff, unsigned ldst) {
    u64 t;
    asm volatile("s_mov_b32 m0, %3\n\ts_mov_b64 %0, %2\n\tglobal_load_lds_dword %1, %0 sc1" : "=&s"(t) : "v"(voff), "s"(base), "s"(ldst));
}
__device__ __forceinline__ const u16* sptr(const u16* base, int off) {
    const u16* r = base + __builtin_amdgcn_readfirstlane(off);
    asm("" : "+s"(r));
    return r;
}
__device__ __forceinline__ u64 vsel(bool c, const void* a, u64 b) {
    u64 va = (u64)a;
    asm("" : "+v"(va), "+v"(b));
    return c ? va : b;
}
__device__ __forceinline__ LAS char* lp(int a) { return (LAS char*)(size_t)(unsigned)a; }
__device__ __forceinline__ u32x4 lds128(LAS char* p) { return *(LAS u32x4*)p; }
__device__ __forceinline__ u32x2 ldstr(LAS char* p) {
    return __builtin_bit_cast(u32x2, __builtin_amdgcn_ds_read_tr16_b64_v4i16((LAS s16x4*)p));
}
__device__ __forceinline__ u32x4 ldstr2(LAS char* p, int off) {
    u32x2 a = ldstr(p), b = ldstr(p + off);
    return (u32x4){a.x, a.y, b.x, b.y};
}
__device__ __forceinline__ f32x16 mfma32(u32x4 a, u32x4 b, f32x16 c) {
    return __builtin_amdgcn_mfma_f32_32x32x16_bf16(__builtin_bit_cast(bf16x8, a), __builtin_bit_cast(bf16x8, b), c, 0, 0, 0);
}
__device__ __forceinline__ f32x4 mfma16(u32x4 a, u32x4 b, f32x4 c) {
    return __builtin_amdgcn_mfma_f32_16x16x32_bf16(__builtin_bit_cast(bf16x8, a), __builtin_bit_cast(bf16x8, b), c, 0, 0, 0);
}
__device__ __forceinline__ float silu_f(float g) {
    return g * __builtin_amdgcn_rcpf(1.f + __builtin_amdgcn_exp2f(g * -1.44269504088896341f));
}
template <class Fn, int... I>
__device__ __forceinline__ void static_for(Fn&& fn, std::integer_sequence<int, I...>) { (fn(std::integral_constant<int, I>{}), ...); }
#define WAIT_VM(n) asm volatile("s_waitcnt vmcnt(" #n ")" ::: "memory")
#define WAIT_LGKM0() asm volatile("s_waitcnt lgkmcnt(0)" ::: "memory")
#define WAIT_BAR(w) asm volatile("s_waitcnt " w "\n\ts_barrier" ::: "memory")
#define SCHED() __builtin_amdgcn_sched_barrier(0)
#define CAS __attribute__((address_space(4)))
#define NSTAMP 18

__device__ __forceinline__ void wait_lds(LAS char* word) {
#pragma unroll 1
    for (unsigned spins = 0; spins < (SPIN_CAP << 4); ++spins) {
        if (__builtin_amdgcn_readfirstlane(__hip_atomic_load((LAS unsigned*)word, __ATOMIC_RELAXED, __HIP_MEMORY_SCOPE_WORKGROUP))) break;
        __builtin_amdgcn_s_sleep(1);
    }
    asm volatile("" ::: "memory");
}
__device__ __forceinline__ void wait_lds_eq(LAS char* word, unsigned want) {
#pragma unroll 1
    for (unsigned spins = 0; spins < (1u << 22); ++spins) {
        if ((unsigned)__builtin_amdgcn_readfirstlane(__hip_atomic_load((LAS unsigned*)word, __ATOMIC_RELAXED, __HIP_MEMORY_SCOPE_WORKGROUP)) == want) break;
        __builtin_amdgcn_s_sleep(1);
    }
    asm volatile("" ::: "memory");
}
#define STAMP(i)
#define STAMPG(i)
#define STAMPA(i)
#define STAMP_DECL()
#define STAMP_OUT(n)

__global__ __launch_bounds__(512, 2) void fused_kernel(Params p) {
    extern __shared__ __attribute__((aligned(16))) char smem_raw[];
    u64 pc;
    asm volatile("s_getpc_b64 %0" : "=s"(pc));
    LAS char* sm = (LAS char*)smem_raw;
    const int tid = threadIdx.x, lane = tid & 63;
    const int wave = __builtin_amdgcn_readfirstlane(tid >> 6);
    const int xcd = blockIdx.x & 7, idx = blockIdx.x >> 3;
    if (idx == 0 && wave < 5) {
        const unsigned o = (unsigned)tid * 128u;
        asm volatile("s_mov_b32 m0, %2\n\ts_nop 0\n\tglobal_load_lds_dword %0, %1" :: "v"(o < CODE_PREFETCH ? o : 0u), "s"(pc & ~(u64)255),
                     "s"((unsigned)(size_t)sm + 32768u));
    }
    SCHED();
    const __amdgpu_buffer_rsrc_t wsr = __builtin_amdgcn_make_buffer_rsrc(p.ws, 0, WS_END, 0x00020000);
    asm volatile("" :: "s"(p.x), "s"(p.k), "s"(p.v), "s"(p.sk), "s"(p.hk), "s"(p.dv), "s"(p.w1), "s"(p.w3), "s"(p.w2), "s"(p.out), "s"(p.ws));
    STAMP_DECL();
    STAMP(0);

    if (idx < 4) {
        const int ca = (xcd >> 1) * 4 + idx, cmat = ca >= 7, cnt = 7 * (xcd & 1) + (ca >= 7 ? ca - 7 : ca);
        const bool job = ca < 14;
        u32x4 xr[7], wr[14];
        const int row = (8 * (xcd >> 1) + 4 * (xcd & 1) + idx) * 64 + wave * 8 + (lane >> 3);
#pragma unroll
        for (int i = 0; i < 7; ++i) xr[i] = *(const u32x4*)(p.x + (size_t)row * D + i * 64 + (lane & 7) * 8);
        u64 epoch;
        asm volatile("s_load_dwordx2 %0, %1, 0x0\n\ts_waitcnt lgkmcnt(0)" : "=s"(epoch) : "s"(p.ws) : "memory");
        const u64 tag = epoch ^ TAG_SALT;
        const unsigned tag32 = (unsigned)tag;
        {
            float s4[4] = {0.f, 0.f, 0.f, 0.f};
#pragma unroll
            for (int i = 0; i < 7; ++i)
#pragma unroll
                for (int j = 0; j < 4; ++j) s4[j] = dot2(xr[i][j], 0x3f803f80u, s4[j]);
            float sum = (s4[0] + s4[1]) + (s4[2] + s4[3]);
            sum += __shfl_xor(sum, 1); sum += __shfl_xor(sum, 2); sum += __shfl_xor(sum, 4);
            const float mean = sum * (1.f / D);
            f32x2 q4[4] = {{0.f, 0.f}, {0.f, 0.f}, {0.f, 0.f}, {0.f, 0.f}};
#pragma unroll
            for (int i = 0; i < 7; ++i)
#pragma unroll
                for (int j = 0; j < 4; ++j) {
                    const f32x2 d = (f32x2){bflo(xr[i][j]), bfhi(xr[i][j])} - (f32x2){mean, mean};
                    q4[j] = __builtin_elementwise_fma(d, d, q4[j]);
                }
            const f32x2 q2 = (q4[0] + q4[1]) + (q4[2] + q4[3]);
            float sq = q2[0] + q2[1];
            sq += __shfl_xor(sq, 1); sq += __shfl_xor(sq, 2); sq += __shfl_xor(sq, 4);
            const u32x2 st = {tag32, pk(mean, __builtin_amdgcn_sqrtf(rbf(rbf(sq * (1.f / D)) + 1e-5f)))};
            __builtin_amdgcn_raw_buffer_store_b64(st, wsr, (lane & 7) == 0 ? WS_STATS + (unsigned)row * 8u : 0xfffffff0u, 0, 16);
        }
        STAMPA(2);
        if (job) {
            SCHED();
            const u16* wp = (cmat ? p.w3 : p.w1) + (size_t)(wave * 4 + (lane >> 4)) * F + cnt * 128 + (lane & 15) * 8;
#pragma unroll
            for (int i = 0; i < 14; ++i) wr[i] = *(const u32x4*)(wp + (size_t)(32 * i) * F);
        }
        if (job) {
            float c[8];
#pragma unroll
            for (int j = 0; j < 8; ++j) c[j] = 0.f;
#pragma unroll
            for (int i = 0; i < 14; ++i)
#pragma unroll
                for (int j = 0; j < 4; ++j) { c[2 * j] += bflo(wr[i][j]); c[2 * j + 1] += bfhi(wr[i][j]); }
            LAS char* cp = sm + ((wave * 4 + (lane >> 4)) * 136 + (lane & 15) * 8) * 4;
            *(LAS f32x4*)cp = (f32x4){c[0], c[1], c[2], c[3]};
            *(LAS f32x4*)(cp + 16) = (f32x4){c[4], c[5], c[6], c[7]};
        }
        STAMPA(3);
        if (job) {
            WAIT_BAR("lgkmcnt(0)");
            float t = 0.f;
#pragma unroll
            for (int i = 0; i < 8; ++i) t += *(LAS float*)(sm + ((i * 4 + (tid & 3)) * 136 + (tid >> 2)) * 4);
            t += __shfl_xor(t, 1); t += __shfl_xor(t, 2);
            __builtin_amdgcn_raw_buffer_store_b64((u32x2){tag32, __float_as_uint(t)}, wsr,
                (tid & 3) == 0 ? WS_CSUM + (unsigned)((cnt * 2 + cmat) * 128 + (tid >> 2)) * 8u : 0xfffffff0u, 0, 16);
        }
        STAMP(1);
        SCHED();
        if (!job) __builtin_amdgcn_s_sleep(80);
        const unsigned pf2 = *(const unsigned*)((const char*)p.hk + (size_t)((xcd * 4 + idx) * 448 + wave * 56 + (lane < 56 ? lane : 0)) * 128);
        asm volatile("" :: "v"(pf2));
        STAMP_OUT(4);
        if (blockIdx.x == 0 && wave == 7) {
            __builtin_amdgcn_s_sleep(127);
            __builtin_amdgcn_s_sleep(100);
            GAS u64* gp = (GAS u64*)(p.ws + WS_STATS) + lane * 8;
            const int fl = lane < 56 ? lane : 0;
            GAS u64* fp = (GAS u64*)(p.ws + WS_FH) + ((fl / 14) * 56 + (fl % 14) * 2) * 16;
#pragma unroll 1
            for (unsigned spins = 0; spins < SPIN_CAP; ++spins) {
                bool ok = true;
#pragma unroll
                for (int m = 0; m < 4; ++m)
                    ok &= ((unsigned)__hip_atomic_load(gp + m * 512, RLX_AGENT) == tag32) & (__hip_atomic_load(fp + m * 224 * 16, RLX_AGENT) == tag);
                if (__all(ok)) break;
                __builtin_amdgcn_s_sleep(32);
            }
            if (lane == 0) __hip_atomic_store((GAS u64*)(p.ws + WS_EPOCH), epoch + 1, RLX_AGENT);
        }
        return;
    }

    const int gi = idx - 4, ml = gi & 3, nl = gi >> 2;
    const int mt = 4 * (xcd >> 1) + ml, nt = 7 * (xcd & 1) + nl;
    const int rot1 = ROT1;
    const int m64 = 4 * xcd + ml, n64 = nl;
    const int gpair = xcd * 28 + gi;
    const int r32 = lane & 31, hh = lane >> 5, blk = (lane >> 4) & 1, q4 = (lane & 15) >> 2, p4 = lane & 3;
    const unsigned lb = (unsigned)(size_t)sm;
    const int wq = wave & 3;
    u32x4 wvo;
    wvo[0] = (unsigned)((lane >> 4) * F + (((lane & 15) ^ ((lane >> 4) << 2)) * 8)) * 2u;
#pragma unroll
    for (int j = 1; j < 4; ++j) wvo[j] = wvo[0] + j * (8 * F - 1024);
    const u16* w1b = sptr(p.w1, wq * 16 * F + nt * 128);
    const u16* w3b = sptr(p.w3, wq * 16 * F + nt * 128);
    auto kst = [&](int si) { if (ROTM == 6 && si == 0) return 0; const int s = si + rot1; return s >= 7 ? s - ROTM : s; };
    auto group_w = [&](int si, int m) { glds16x4(sptr(m ? w3b : w1b, kst(si) * 64 * F), wvo, lb + ring(si) + 16384 + m * 16384 + wq * 4096); };
    const int kr0 = (gpair & 31) * 64, kc0 = (gpair >> 5) * 64;
    const int chn = wq >> 1, par = wq & 1, dch = 2 * gpair + chn;
    if (wave < 4) group_w(0, 0);
    {
        const bool isk = lane >= 8 && lane < 14;
        u64 mb_ = vsel(lane == 15, p.dv, vsel(lane == 14, p.hk, vsel(isk, p.k, (u64)p.sk)));
        unsigned mo = (unsigned)((kc0 + wq * 16) * 8 + (lane & 7) * 16);
        mo = isk ? (unsigned)((kr0 ? kr0 - 3 + ((lane - 8) >> 1) : 0) * D + kc0 + wq * 16 + (lane & 1) * 8) * 2u : mo;
        mo = lane == 14 ? (unsigned)dch * 4096u : mo;
        mo = lane == 15 ? (unsigned)(dch & ~7) * 2u : mo;
        const u64 ma = mb_ + mo;
        asm volatile("s_mov_b32 m0, %1\n\ts_nop 0\n\tglobal_load_lds_dwordx4 %0, off" :: "v"(ma), "s"(lb + wave * 1024));
    }
    if (wave < 4) group_w(0, 1);
    SCHED();
    const int rot2 = ROT2;
    const int w2i = L_W2 + wq * 12288;
    auto fresh_lane = [&]() { int l = lane; asm volatile("" : "+v"(l)); return l; };
    const int latei = L_CONV + wq * 8192;
    auto pair_w2 = [&](u32x4 vo, int i, int dst) {
        int t = i + rot2; t = t >= 28 ? t - 28 : t;
        glds16x4(sptr(p.w2, (wq * 448 + t * 16) * D + n64 * 64), vo, lb + dst);
    };
    auto w2_off = [&]() {
        const int l = fresh_lane();
        const unsigned o = (unsigned)((l >> 3) * D + (((l & 7) ^ (((l >> 4) & 1) << 2)) * 8)) * 2u;
        return (u32x4){o, o + (16 * D - 1024), o + 2 * (16 * D - 1024), o + 3 * (16 * D - 1024)};
    };

    if (wave < 4) {
        __builtin_amdgcn_s_setprio(MPRIO);
        const int wm = wave >> 1, wn = wave & 1;
        int xa[4], wa0, wa1;
#pragma unroll
        for (int i = 0; i < 4; ++i) xa[i] = lb + ring(0) + (((64 * wm + r32) * 128 + ((hh ^ ((r32 >> 1) & 7)) * 16)) ^ (i * 32));
        wa0 = 16384 + (8 * hh + q4) * 256 + (((8 * wn + 2 * blk + (p4 >> 1)) ^ (q4 << 2)) * 16) + 8 * (p4 & 1);
        wa1 = (wa0 ^ 64) + lb + ring(0); wa0 += lb + ring(0);
        f32x16 acc[2][2][2];
#pragma unroll
        for (int i = 0; i < 8; ++i)
#pragma unroll
            for (int r = 0; r < 16; ++r) acc[i >> 2][(i >> 1) & 1][i & 1][r] = 0.f;
        u32x4 fx[2][2], fw[2][2][2];
        auto load_frag = [&](int g, int j) {
            const int pp = g & 3, b = g & 1;
            if (j == 0) {
                if (g >= 4) {
                    const int d = ring(g >> 2) - ring((g >> 2) - 1);
                    if (pp == 0) { wa0 += d; wa1 += d; asm volatile("" : "+v"(wa0), "+v"(wa1)); }
                    xa[pp] += d; asm volatile("" : "+v"(xa[pp]));
                }
                fw[b][0][0] = ldstr2(lp(wa0) + pp * 4096, 1024);
            }
            if (j == 1) { fx[b][0] = lds128(lp(xa[pp])); fx[b][1] = lds128(lp(xa[pp]) + 4096); }
            if (j == 2) fw[b][0][1] = ldstr2(lp(wa0) + pp * 4096 + 16384, 1024);
            if (j == 3) fw[b][1][0] = ldstr2(lp(wa1) + pp * 4096, 1024);
            if (j == 4) fw[b][1][1] = ldstr2(lp(wa1) + pp * 4096 + 16384, 1024);
        };
        int ka0[2], kam[2], kwo;
#pragma unroll
        for (int rd = 0; rd < 2; ++rd) {
            const int ck = 2 * wq + rd;
            const int bq = 2 * (lane >> 4) + (p4 >> 1), bm = bq ? bq - 1 : 0;
            ka0[rd] = lb + L_KV + (8 * bq + q4) * 128 + ((ck ^ (((q4 >> 1) & 1) | ((bq & 3) << 1))) * 16) + 8 * (p4 & 1);
            kam[rd] = lb + L_KV + (8 * bm + 4 + q4) * 128 + ((ck ^ (((q4 >> 1) & 1) | ((bm & 3) << 1))) * 16) + 8 * (p4 & 1);
        }
        kwo = lb + L_KVO + wq * 2048 + (lane & 7) * 128 + (((7 - (lane >> 3)) ^ (lane & 7)) * 16);
        asm volatile("" : "+v"(ka0[0]), "+v"(ka0[1]), "+v"(kam[0]), "+v"(kam[1]), "+v"(kwo));
        WAIT_VM(4);
        u32x2 skv[2], hal[2];
#pragma unroll
        for (int rd = 0; rd < 2; ++rd) {
            LAS char* mp = sm + wave * 1024;
            skv[rd] = *(LAS u32x2*)(mp + (rd * 8 + (lane & 7)) * 8);
            const unsigned hm = kr0 ? 0xffffffffu : 0u;
            const unsigned a = *(LAS u16*)(mp + 128 + rd * 16 + (lane & 7) * 2), b = *(LAS u16*)(mp + 160 + rd * 16 + (lane & 7) * 2),
                           c = *(LAS u16*)(mp + 192 + rd * 16 + (lane & 7) * 2);
            hal[rd] = (u32x2){(a << 16) & hm, (b | (c << 16)) & hm};
            asm volatile("" : "+v"(skv[rd]), "+v"(hal[rd]));
        }
        WAIT_BAR("vmcnt(0) lgkmcnt(0)");
        STAMP(1);
        u32x2 kt[2][3], vt[2][2];
        float cf[4], kk[11], ko[8];
        u32x4 kpk;
        auto fill = [&](auto kc) {
            constexpr int k = decltype(kc)::value;
            if constexpr (k < 4) {
                constexpr int rd = k >> 1;
                if constexpr ((k & 1) == 0) { kt[rd][0] = ldstr(lp(kam[rd])); kt[rd][1] = ldstr(lp(ka0[rd])); kt[rd][2] = ldstr(lp(ka0[rd]) + 512); }
                else { vt[rd][0] = ldstr(lp(ka0[rd]) + 8192); vt[rd][1] = ldstr(lp(ka0[rd]) + 8192 + 512); }
            } else if constexpr (k < 4 + 2 * 16) {
                constexpr int rd = (k - 4) / 16, s = (k - 4) % 16;
                if constexpr (s == 0) { cf[0] = bflo1(skv[rd][0]); cf[1] = bfhi1(skv[rd][0]); cf[2] = bflo1(skv[rd][1]); cf[3] = bfhi1(skv[rd][1]); }
                else if constexpr (s == 1) {
                    const unsigned a = lane < 8 ? hal[rd][0] : kt[rd][0][0], b = lane < 8 ? hal[rd][1] : kt[rd][0][1];
                    kk[0] = bfhi1(a); kk[1] = bflo1(b); kk[2] = bfhi1(b);
                } else if constexpr (s < 4) {
                    const u32x2 t = kt[rd][s - 1];
                    kk[4 * s - 5] = bflo1(t[0]); kk[4 * s - 4] = bfhi1(t[0]); kk[4 * s - 3] = bflo1(t[1]); kk[4 * s - 2] = bfhi1(t[1]);
                } else if constexpr (s < 12) {
                    constexpr int tp = (s - 4) >> 1, o0 = ((s - 4) & 1) * 4;
#pragma unroll
                    for (int i = o0; i < o0 + 4; ++i) {
                        if (tp == 0) ko[i] = mul1(kk[i], cf[3]);
                        else fmac1(ko[i], kk[i + tp], cf[3 - tp]);
                    }
                } else {
                    constexpr int j = s - 12;
                    const unsigned v = vt[rd][j >> 1][j & 1];
                    kpk[3 - j] = pk(mul1(ko[2 * j + 1], bfhi1(v)), mul1(ko[2 * j], bflo1(v)));
                    if constexpr (j == 3) *(LAS u32x4*)(lp(kwo) + rd * 1024) = kpk;
                }
            }
        };
#pragma unroll
        for (int j = 0; j < 5; ++j) load_frag(0, j);
        f32x4 cs[2][2][4];
        unsigned tch[4], tch0 = 0;
        unsigned tc0 = (unsigned)((gpair * 4 + wq) * 14 + (lane < 14 ? lane : 13)) * 128u;
        unsigned tco = (unsigned)(gi * 4 + wq + (lane < 28 ? lane : 27) * 448) * 128u;
        asm volatile("" : "+v"(tco));
        static_for([&](auto gc) {
            constexpr int g = decltype(gc)::value, b = g & 1, pp = g & 3;
            SCHED();
            if (g == 11) STAMP(2);
            if (pp == 3 && g < 27) { STAMPG(2 + 2 * (g >> 2)); WAIT_BAR("lgkmcnt(0)"); STAMPG(3 + 2 * (g >> 2)); }
            if (g == 27) {
                WAIT_LGKM0();
                if (lane == 0) __hip_atomic_fetch_add((LAS unsigned*)(sm + L_CTL + 12), 1u, __ATOMIC_RELAXED, __HIP_MEMORY_SCOPE_WORKGROUP);
            }
            if (g == 11) STAMP(3);
            static_for([&](auto ic) {
                constexpr int i = decltype(ic)::value, ft = i >> 2, m = (i >> 1) & 1, sb = i & 1;
                acc[sb][ft][m] = mfma32(fw[b][ft][m], fx[b][sb], acc[sb][ft][m]);
                SCHED();
                if (g < 27 && i < 5) load_frag(g + 1, i);
                if constexpr (MWARM && i == 6 && g == MWARM - 1) {
                    u64 t;
                    asm volatile("s_mov_b64 %1, %3\n\tglobal_load_dword %0, %2, %1" : "=v"(tch0), "=&s"(t) : "v"(tc0), "s"(p.w2));
                }
                if constexpr (TOUCH && i == 4 && pp == 2 && g >= TOUCH0 && g < TOUCH0 + 16) {
                    u64 t;
                    asm volatile("s_mov_b64 %1, %3\n\tglobal_load_dword %0, %2, %1" : "=v"(tch[(g - TOUCH0) >> 2]), "=&s"(t)
                                 : "v"(tco), "s"((const char*)p.w2 + ((g - TOUCH0) >> 2) * (112 * 128)));
                }
                if constexpr (i >= 5 && g >= 7 + 4 * KVW) fill(std::integral_constant<int, (g - 7 - 4 * KVW) * 3 + i - 5>{});
                SCHED();
            }, std::make_integer_sequence<int, 8>{});
        }, std::make_integer_sequence<int, 28>{});
        STAMP(4);
        wait_lds_eq(sm + L_CTL + 64, 3u);
        unsigned sa[2];
#pragma unroll
        for (int i = 0; i < 8; ++i) cs[i >> 2][0][i & 3] = *(LAS f32x4*)(sm + L_CSUM + (64 * wn + 32 * (i >> 2) + 8 * (i & 3) + 4 * hh) * 4);
        sa[0] = *(LAS unsigned*)(sm + L_STAT + (64 * wm + r32) * 4);
        sa[1] = *(LAS unsigned*)(sm + L_STAT + (64 * wm + 32 + r32) * 4);
#pragma unroll
        for (int i = 0; i < 8; ++i) cs[i >> 2][1][i & 3] = *(LAS f32x4*)(sm + L_CSUM + 512 + (64 * wn + 32 * (i >> 2) + 8 * (i & 3) + 4 * hh) * 4);
        const int lw = fresh_lane();
        const int w2a0 = lb + w2i + (4 * (lw >> 5) + ((lw & 15) >> 2)) * 128 + (((2 * ((lw >> 4) & 1) + ((lw & 3) >> 1)) ^ (((lw >> 3) & 1) << 2)) * 16) + 8 * (lw & 1);
        const int w2a1 = w2a0 ^ 64;
        u32x4 w2r[12][2];
        auto park = [&](int i) {
            w2r[i][0] = ldstr2(lp(w2a0) + (i / 6) * L_SLOT + (i % 6) * 2048, 1024);
            w2r[i][1] = ldstr2(lp(w2a1) + (i / 6) * L_SLOT + (i % 6) * 2048, 1024);
        };
#pragma unroll
        for (int sb = 0; sb < 2; ++sb) {
            const float ri = __builtin_amdgcn_rcpf(bfhi(sa[sb])) * -1.44269504088896341f, nm = -bflo(sa[sb]), d2 = bfhi(sa[sb]) * bfhi(sa[sb]);
            const f32x2 ri2 = {ri, ri}, nm2 = {nm, nm}, dd2 = {d2, d2};
#pragma unroll
            for (int ft = 0; ft < 2; ++ft) {
#pragma unroll
                for (int t2 = 0; t2 < 2; ++t2) {
                    const int grp = sb * 4 + ft * 2 + t2;
                    if (EPARK && grp == 2) { SCHED(); wait_lds(sm + L_CTL + 48 + wq * 4); }
                    if (EPARK && grp >= 2) { SCHED(); park(grp - 2); }
                    f32x2 gu[4], ee[4];
#pragma unroll
                    for (int j = 0; j < 4; ++j) {
                        const int r = 8 * t2 + 2 * j;
                        const f32x2 g = __builtin_elementwise_fma(nm2, (f32x2){cs[ft][0][r >> 2][r & 3], cs[ft][0][r >> 2][(r & 3) + 1]}, (f32x2){acc[sb][ft][0][r], acc[sb][ft][0][r + 1]});
                        const f32x2 u = __builtin_elementwise_fma(nm2, (f32x2){cs[ft][1][r >> 2][r & 3], cs[ft][1][r >> 2][(r & 3) + 1]}, (f32x2){acc[sb][ft][1][r], acc[sb][ft][1][r + 1]});
                        const f32x2 t = g * ri2;
                        ee[j] = (f32x2){__builtin_amdgcn_exp2f(t[0]), __builtin_amdgcn_exp2f(t[1])};
                        gu[j] = g * u;
                    }
                    u32x4 st;
#pragma unroll
                    for (int j = 0; j < 4; ++j) {
                        const f32x2 dn = __builtin_elementwise_fma(ee[j], dd2, dd2);
                        const f32x2 hv = gu[j] * (f32x2){__builtin_amdgcn_rcpf(dn[0]), __builtin_amdgcn_rcpf(dn[1])};
                        st[j] = pk(hv[0], hv[1]);
                    }
                    const unsigned fi = (unsigned)((4 * mt + 2 * wm + sb) * 112 + 8 * nt + 4 * wn + 2 * ft + t2);
                    __builtin_amdgcn_raw_buffer_store_b128(st, wsr, WS_H + fi * 1024u + lane * 16u, 0, 16);
                }
            }
        }
        SCHED();
        STAMP(5);
        const u32x4 w2o = w2_off();
        const u64 tag = *(LAS u64*)(sm + L_CTL + 128);
        GAS u64* myflag = (GAS u64*)(p.ws + WS_FH + ((2 * mt + wm) * 28 + 2 * nt + wn) * 128) + (lw & 15);
        if (!__builtin_amdgcn_readfirstlane(__hip_atomic_load((LAS unsigned*)(sm + L_CTL + 112 + wq * 4), __ATOMIC_RELAXED, __HIP_MEMORY_SCOPE_WORKGROUP))) {
            WAIT_VM(0);
            if (lane < FLAGW) __hip_atomic_store(myflag, tag, RLX_AGENT);
            wait_lds(sm + L_CTL + 112 + wq * 4);
        }
        asm volatile("" ::: "memory");
#pragma unroll
        for (int i = 6; i < 12; ++i) park(i);
        WAIT_VM(0);
        if (TOUCH) asm volatile("" :: "v"(tch[0]), "v"(tch[1]), "v"(tch[2]), "v"(tch[3]));
        asm volatile("" :: "v"(tch0));
        if (lane < FLAGW) __hip_atomic_store(myflag, tag, RLX_AGENT);
        STAMP(6);
        if (!EPARK) {
#pragma unroll
            for (int i = 0; i < 6; ++i) park(i);
        }
        asm volatile("" ::: "memory");
        __hip_atomic_store((LAS unsigned*)(sm + L_CTL + 32 + wq * 4), 1u, __ATOMIC_RELAXED, __HIP_MEMORY_SCOPE_WORKGROUP);
        WAIT_LGKM0();
#pragma unroll
        for (int i = 0; i < 12; ++i) asm volatile("" : "+v"(w2r[i][0]), "+v"(w2r[i][1]));
#pragma unroll
        for (int i = 0; i < 3; ++i) pair_w2(w2o, 12 + 2 * i, w2i + i * 4096);
#pragma unroll
        for (int i = 0; i < 3; ++i) pair_w2(w2o, 18 + 2 * i, w2i + L_SLOT + i * 4096);
        STAMP(7);
        {
            GAS u64* fp = (GAS u64*)(p.ws + WS_FH + (m64 * 28 + 7 * wq + (lw < 7 ? lw : 0)) * 128);
#pragma unroll 1
            for (unsigned spins = 0; spins < SPIN_CAP; ++spins) {
                if (__all(__hip_atomic_load(fp, RLX_AGENT) == tag)) break;
                __builtin_amdgcn_s_sleep(POLL_SLEEP);
            }
            __builtin_amdgcn_fence(__ATOMIC_ACQUIRE, "wavefront");
        }
        STAMP(11);
        const unsigned hb = WS_H + (unsigned)(2 * m64 * 112 + 28 * wq) * 1024u + lw * 16u;
        u32x4 hf[28][2];
        auto load_h = [&](int i, int j) {
            int t = i + rot2; t = t >= 28 ? t - 28 : t;
            hf[i][j] = __builtin_amdgcn_raw_buffer_load_b128(wsr, hb + j * (112u * 1024u), t * 1024, 16);
        };
        constexpr int HD = HDEPTH, HG = HGROW;
#pragma unroll
        for (int i = 0; i < HD; ++i) { load_h(i, 0); load_h(i, 1); }
        STAMP(14);
        f32x16 ac2[2][2];
#pragma unroll
        for (int i = 0; i < 4; ++i)
#pragma unroll
            for (int r = 0; r < 16; ++r) ac2[i >> 1][i & 1][r] = 0.f;
        u32x4 a0 = w2r[0][0], a1 = w2r[0][1];
        static_for([&](auto ic) {
            constexpr int i = decltype(ic)::value, n = i + 1;
            SCHED();
            if (i == 23) wait_lds(sm + L_CTL + 80 + wq * 4);
            u32x4 b0, b1;
            if constexpr (n < 12) { b0 = w2r[n][0]; b1 = w2r[n][1]; }
            else if constexpr (n < 28) {
                constexpr int so = n < 24 ? ((n - 12) / 6) * L_SLOT + ((n - 12) % 6) * 2048 : (n - 24) * 2048;
                const int lo = n < 24 ? 0 : latei - w2i;
                b0 = ldstr2(lp(w2a0 + lo) + so, 1024);
                b1 = ldstr2(lp(w2a1 + lo) + so, 1024);
            }
            SCHED();
            ac2[0][0] = mfma32(a0, hf[i][0], ac2[0][0]);
            SCHED();
            constexpr int l0 = i < HG ? HD + 2 * i : HD + HG + i;
            if constexpr (l0 < 28) load_h(l0, 0);
            SCHED();
            ac2[1][0] = mfma32(a1, hf[i][0], ac2[1][0]);
            SCHED();
            if constexpr (l0 < 28) load_h(l0, 1);
            SCHED();
            ac2[0][1] = mfma32(a0, hf[i][1], ac2[0][1]);
            SCHED();
            if constexpr (i < HG) load_h(l0 + 1, 0);
            SCHED();
            ac2[1][1] = mfma32(a1, hf[i][1], ac2[1][1]);
            SCHED();
            if constexpr (i < HG) load_h(l0 + 1, 1);
            a0 = b0; a1 = b1;
        }, std::make_integer_sequence<int, 28>{});
        SCHED();
        STAMP(15);
        WAIT_LGKM0();
#pragma unroll
        for (int v = 0; v < 8; ++v) {
            LAS char* sl = sm + w2i + (v < 6 ? v * 2048 : L_SLOT + (v - 6) * 2048) + lw * 16;
            const f32x16& t = ac2[v >> 2][(v >> 1) & 1];
            const int r0 = 8 * (v & 1);
            *(LAS f32x4*)sl = (f32x4){t[r0], t[r0 + 1], t[r0 + 2], t[r0 + 3]};
            *(LAS f32x4*)(sl + 1024) = (f32x4){t[r0 + 4], t[r0 + 5], t[r0 + 6], t[r0 + 7]};
        }
        WAIT_BAR("lgkmcnt(0)");
        STAMP(16);
        STAMP(17);
        STAMP_OUT(18);
    } else {
        u32x4 xvo;
#pragma unroll
        for (int j = 0; j < 4; ++j) xvo[j] = (unsigned)((8 * j + (lane >> 3)) * D + ((((lane & 7) ^ (lane >> 4)) ^ (4 * (j & 1))) * 8)) * 2u - 1024u * j;
        const u16* xb = sptr(p.x, (mt * 128 + wq * 32) * D);
        auto group_x = [&](int si) { glds16x4(sptr(xb, kst(si) * 64), xvo, lb + ring(si) + wq * 4096); };
        group_x(0);
        group_x(1); group_w(1, 0); group_w(1, 1);
        const int cbi = L_CONV + chn * C_CH;
        LAS char* cb = sm + cbi;
        unsigned kvo[2], uso;
#pragma unroll
        for (int rd = 0; rd < 2; ++rd) kvo[rd] = (unsigned)((lane >> 3) * D + (((lane & 7) ^ (((lane >> 4) & 1) | (((2 * wq + rd) & 3) << 1))) * 8)) * 2u;
        uso = WS_U + (unsigned)(kc0 + wq * 16 + (lane >> 3)) * 8192u + (unsigned)(1984 - kr0 + (lane & 7) * 8) * 2u;
        int kro = lb + L_KVO + wq * 2048 + (lane >> 3) * 128 + (((lane & 7) ^ (lane >> 3)) * 16);
        const int cc = 2 * par + ((lane >> 2) & 1);
        int zo = cbi + C_U + par * C_US + (248 + (lane < 36 ? lane : 35)) * 16;
        zo = lane < 8 ? cbi + cc * C_HS + (lane & 3) * 16 : zo;
        u32x4 w2o = w2_off();
        const unsigned fo = lane * 16u;
        unsigned ao = (wq < 2 ? WS_CSUM + (unsigned)(nt * 2 + wq) * 1024u : WS_STATS + mt * 1024u) + fo;
        asm volatile("" : "+v"(zo), "+v"(w2o), "+v"(kvo[0]), "+v"(kvo[1]), "+v"(uso), "+v"(kro), "+v"(ao));
        WAIT_VM(16);
        unsigned tag32, taghi;
        u32x4 bval, ug[2];
        unsigned dbits = *(LAS u16*)(sm + wave * 1024 + 240 + (dch & 7) * 2);
        {
            const u32x4 h0 = lds128(sm + wave * 1024 + 224);
            const bool s1 = !(cc & 1), s2 = !(cc & 2);
            const unsigned a0 = s1 ? 0u : h0[0], a1 = s1 ? h0[0] : h0[1], a2 = s1 ? h0[1] : h0[2], a3 = s1 ? h0[2] : h0[3];
            const unsigned lm = (lane == 3 || lane == 7) ? 0xffffffffu : 0u;
            bval = (u32x4){(s2 ? 0u : a0) & lm, (s2 ? 0u : a1) & lm, (s2 ? a0 : a2) & lm, (s2 ? a1 : a3) & lm};
            asm volatile("" : "+v"(bval), "+v"(dbits));
        }
        auto kv_store = [&](int rd) {
            LAS unsigned* kp = (LAS unsigned*)(lp(kro) + rd * 1024);
            ug[0][1] = kp[0]; asm volatile("" ::: "memory"); ug[0][3] = kp[1]; asm volatile("" ::: "memory");
            ug[1][1] = kp[2]; asm volatile("" ::: "memory"); ug[1][3] = kp[3];
            __builtin_amdgcn_raw_buffer_store_b128(ug[0], wsr, uso, rd * 65536, 16);
            __builtin_amdgcn_raw_buffer_store_b128(ug[1], wsr, uso, rd * 65536 + 4096, 16);
        };
        WAIT_BAR("vmcnt(12) lgkmcnt(0)");
        STAMP(1);
#pragma unroll
        for (int w = 0; w < 5; ++w) {
            group_x(w + 2); group_w(w + 2, 0); group_w(w + 2, 1);
            if (w == KVW) {
#pragma unroll
                for (int j = 0; j < 2; ++j) {
                    glds16(sptr(p.k, (kr0 + (2 * wq + j) * 8) * D + kc0), kvo[j], lb + L_KV + (2 * wq + j) * 1024);
                    glds16(sptr(p.v, (kr0 + (2 * wq + j) * 8) * D + kc0), kvo[j], lb + L_KV + 8192 + (2 * wq + j) * 1024);
                }
            }
            if (w == 2) {
                u64 epoch;
                asm volatile("s_load_dwordx2 %0, %1, 0x0\n\ts_waitcnt lgkmcnt(0)" : "=s"(epoch) : "s"(p.ws) : "memory");
                tag32 = (unsigned)(epoch ^ TAG_SALT); taghi = (unsigned)((epoch ^ TAG_SALT) >> 32);
                ug[0][0] = vmov1(tag32); ug[0][2] = vmov1(tag32); ug[1][0] = vmov1(tag32); ug[1][2] = vmov1(tag32);
            }
            if (w == 3 && lane < 34) *(LAS unsigned*)(sm + L_CTL + lane * 4) = lane < 32 ? 0u : lane == 32 ? tag32 : taghi;
            if (w == 4 && KVW == 0) kv_store(0);
            if (w == 2) STAMP(2);
            STAMPG(2 + 2 * w);
            if (w == KVW) WAIT_BAR("vmcnt(16) lgkmcnt(0)"); else WAIT_BAR("vmcnt(12) lgkmcnt(0)");
            STAMPG(3 + 2 * w);
            if (w == 2) STAMP(3);
        }
        glds16c(p.ws, ao, lb + L_RAW + wq * 1024);
#pragma unroll
        for (int i = 0; i < 3; ++i) pair_w2(w2o, 2 * i, w2i + i * 4096);
        kv_store(KVW ? 0 : 1);
        STAMPG(12); WAIT_BAR("vmcnt(12) lgkmcnt(0)"); STAMPG(13);
        if (KVW) kv_store(1);
        asm volatile("" ::: "memory");
        if (lane == 0) __hip_atomic_fetch_add((LAS unsigned*)(sm + L_CTL + 12), 1u, __ATOMIC_RELAXED, __HIP_MEMORY_SCOPE_WORKGROUP);
        if (wq < 3) {
            u32x4 av = lds128(sm + L_RAW + wq * 1024 + lane * 16);
#pragma unroll 1
            for (unsigned spins = 0; spins < SPIN_CAP; ++spins) {
                if (__all(((av[0] ^ tag32) | (av[2] ^ tag32)) == 0u)) break;
                asm volatile("" ::: "memory");
                av = __builtin_amdgcn_raw_buffer_load_b128(wsr, ao, 0, 16);
                asm volatile("" : "+v"(av));
            }
            *(LAS u32x2*)(sm + L_CSUM + wq * 512 + lane * 8) = (u32x2){av[1], av[3]};
            asm volatile("" ::: "memory");
            if (lane == 0) __hip_atomic_fetch_add((LAS unsigned*)(sm + L_CTL + 64), 1u, __ATOMIC_RELAXED, __HIP_MEMORY_SCOPE_WORKGROUP);
        }
        STAMP(4);
#pragma unroll
        for (int i = 0; i < 3; ++i) pair_w2(w2o, 6 + 2 * i, w2i + L_SLOT + i * 4096);
        WAIT_VM(12);
        __hip_atomic_store((LAS unsigned*)(sm + L_CTL + 48 + wq * 4), 1u, __ATOMIC_RELAXED, __HIP_MEMORY_SCOPE_WORKGROUP);
        wait_lds_eq(sm + L_CTL + 12, 8u);
        STAMP(5);
        __builtin_amdgcn_s_setprio(2);
        const unsigned ubase = WS_U + (unsigned)dch * 8192u;
        glds16x2c(p.ws + ubase + par * 2048, fo, lb + cbi + C_U + par * 2048);
        glds16x2c(p.ws + ubase + 4096 + par * 2048, fo, lb + cbi + C_U + C_US + par * 2048);
        glds4c(p.ws + ubase + (1 - par) * 2048, (lane & 1) * 4u, lb + L_UNX + wq * 256);
        {
            const u32x4 fvo = {fo, fo, fo, lane == 63 ? 62u * 16u : fo};
#pragma unroll
            for (int c = 0; c < 2; ++c) glds16x4(sptr(p.hk, dch * S + 4 * par + 2 * c + 2), fvo, lb + cbi + (2 * par + c) * C_HS + 64);
        }
        *(LAS u32x4*)(sm + zo) = bval;
        WAIT_VM(13);
        __hip_atomic_store((LAS unsigned*)(sm + L_CTL + 112 + wq * 4), 1u, __ATOMIC_RELAXED, __HIP_MEMORY_SCOPE_WORKGROUP);
        WAIT_VM(8);
        {
            u32x4 ua[2], ub[2]; u32x2 un[2];
#pragma unroll
            for (int rd = 0; rd < 2; ++rd) {
                const int x8 = 128 * par + 64 * rd + lane;
                ua[rd] = lds128(cb + C_U + x8 * 16); ub[rd] = lds128(cb + C_U + C_US + x8 * 16);
                un[rd] = *(LAS u32x2*)((rd == 1 && lane == 63) ? sm + L_UNX + wq * 256 : cb + C_U + x8 * 16 + 16);
            }
#pragma unroll 1
            for (unsigned spins = 0; spins < SPIN_CAP; ++spins) {
                unsigned df = 0;
#pragma unroll
                for (int rd = 0; rd < 2; ++rd) df |= (ua[rd][0] ^ tag32) | (ua[rd][2] ^ tag32) | (ub[rd][0] ^ tag32) | (ub[rd][2] ^ tag32) | (un[rd][0] ^ tag32);
                if (__all(df == 0u)) break;
                __builtin_amdgcn_s_sleep(URETRY);
                asm volatile("" ::: "memory");
#pragma unroll
                for (int rd = 0; rd < 2; ++rd) {
                    const int x8 = 128 * par + 64 * rd + lane;
                    const unsigned uo = ubase + (unsigned)x8 * 16u;
                    ua[rd] = __builtin_amdgcn_raw_buffer_load_b128(wsr, uo, 0, 16);
                    ub[rd] = __builtin_amdgcn_raw_buffer_load_b128(wsr, uo + 4096u, 0, 16);
                    un[rd] = __builtin_amdgcn_raw_buffer_load_b64(wsr, x8 == 255 ? uo : uo + 16u, 0, 16);
                }
                asm volatile("" : "+v"(ua[0]), "+v"(ub[0]), "+v"(un[0]), "+v"(ua[1]), "+v"(ub[1]), "+v"(un[1]));
            }
#pragma unroll
            for (int rd = 0; rd < 2; ++rd) {
                const int x8 = 128 * par + 64 * rd + lane;
                const unsigned nx = x8 == 255 ? 0u : un[rd][1];
                *(LAS u32x4*)(cb + C_U + x8 * 16) = (u32x4){__builtin_amdgcn_alignbit(ua[rd][3], ua[rd][1], 16), __builtin_amdgcn_alignbit(ub[rd][1], ua[rd][3], 16),
                                                            __builtin_amdgcn_alignbit(ub[rd][3], ub[rd][1], 16), __builtin_amdgcn_alignbit(nx, ub[rd][3], 16)};
                *(LAS u32x4*)(cb + C_U + C_US + x8 * 16) = (u32x4){ua[rd][1], ua[rd][3], ub[rd][1], ub[rd][3]};
            }
        }
        asm volatile("s_waitcnt vmcnt(0) lgkmcnt(0)" ::: "memory");
        __hip_atomic_store((LAS unsigned*)(sm + L_CTL + 16 + wq * 4), 1u, __ATOMIC_RELAXED, __HIP_MEMORY_SCOPE_WORKGROUP);
        u32x4 lw2[8], xv[2];
        {
            const __amdgpu_buffer_rsrc_t wr2 = __builtin_amdgcn_make_buffer_rsrc((void*)p.w2, 0, F * D * 2, 0x00020000);
#pragma unroll
            for (int j = 0; j < 8; ++j) {
                int t = 24 + (j >> 2) * 2 + rot2; t = t >= 28 ? t - 28 : t;
                lw2[j] = __builtin_amdgcn_raw_buffer_load_b128(wr2, w2o[0], ((wq * 448 + t * 16 + (j & 3) * 8) * D + n64 * 64) * 2, 0);
            }
        }
        const int lf = fresh_lane();
        const int srow = 64 * m64 + 32 * (wq & 1) + (lf & 31);
        const int dd = 64 * n64 + 32 * (wq >> 1) + 16 * (lf >> 5);
        const unsigned xoff = (unsigned)(srow * D + dd) * 2u;
        {
            const __amdgpu_buffer_rsrc_t xr = __builtin_amdgcn_make_buffer_rsrc((void*)p.x, 0, S * D * 2, 0x00020000);
            xv[0] = __builtin_amdgcn_raw_buffer_load_b128(xr, xoff, 0, 0);
            xv[1] = __builtin_amdgcn_raw_buffer_load_b128(xr, xoff, 16, 0);
        }
        SCHED();
        wait_lds(sm + L_CTL + 16 + (wq ^ 1) * 4);
        STAMP(7);
        STAMP(6);
        const int n16 = lane & 15, kg = lane >> 4;
        f32x4 ca[4];
#pragma unroll
        for (int j = 0; j < 4; ++j) ca[j] = (f32x4){0.f, 0.f, 0.f, 0.f};
        int apo = lb + cbi + (n16 & 3) * C_HS + ((n16 >> 2) + kg) * 16;
        int bpo = lb + cbi + C_U + (n16 & 1) * C_US + (252 - 4 * (n16 >> 1) + kg) * 16 - 7 * 512;
        u32x4 fa[2][8], fb[2][8];
#pragma unroll
        for (int i = 0; i < 8; ++i) { fa[0][i] = lds128(lp(apo) + i * 512); fb[0][i] = lds128(lp(bpo) + (7 - i) * 512); }
        const bool odd = lf & 1;
        int ysrc = (2 * (lf >> 3) + (lf & 1) + 16 * ((lf >> 1) & 3)) * 4;
        unsigned yso = WS_Y + (unsigned)dch * 8192u + lf * 16u;
        f32x4 kd[4];
        {
            const float dval = bflo(dbits);
            const int sl0 = 32 * ((lf & 15) >> 1) + 8 * (lf >> 4) + 4 * (lf & 1);
#pragma unroll
            for (int j = 0; j < 4; ++j) {
                const int tj = (j == 0 ? 0 : j == 1 ? 3 : j == 2 ? 4 : 7) ^ par;
                const u32x2 kvr = *(LAS u32x2*)(cb + C_U + C_US + (2044 - 256 * tj - sl0) * 2);
                const unsigned ka = pk(bfhi(kvr[1]) * dval, bflo(kvr[1]) * dval), kb = pk(bfhi(kvr[0]) * dval, bflo(kvr[0]) * dval);
                kd[j] = (f32x4){bflo(ka), bfhi(ka), bflo(kb), bfhi(kb)};
                asm volatile("" : "+v"(kd[j]));
            }
            asm volatile("" : "+v"(ysrc), "+v"(yso));
        }
        wait_lds(sm + L_CTL + 32 + wq * 4);
        STAMP(8);
        auto conv_all = [&](auto pc_) {
            constexpr int P = decltype(pc_)::value;
            static_for([&](auto rc) {
                constexpr int r = decltype(rc)::value, c = r & 1, n = c ^ 1;
                static_for([&](auto kc) {
                    constexpr int k = decltype(kc)::value;
                    constexpr int T[4] = {P, 3 - P, 4 + P, 7 - P};
                    constexpr int j = k <= T[0] ? 0 : k <= T[0] + T[1] + 1 ? 1 : k <= T[0] + T[1] + T[2] + 2 ? 2 : 3;
                    constexpr int pp = k - (j == 0 ? 0 : j == 1 ? T[0] + 1 : j == 2 ? T[0] + T[1] + 2 : T[0] + T[1] + T[2] + 3);
                    ca[j] = mfma16(fa[c][pp], fb[c][T[j] - pp], ca[j]);
                    SCHED();
                    if constexpr (r < 7 && k < 2 * (T[3] + 1)) {
                        if constexpr (k & 1) fb[n][k >> 1] = lds128(lp(bpo) + (r + 1) * 64 + (7 - (k >> 1)) * 512);
                        else fa[n][k >> 1] = lds128(lp(apo) + (r + 1) * 64 + (k >> 1) * 512);
                    }
                    SCHED();
                }, std::make_integer_sequence<int, 18>{});
                asm volatile("" : "+v"(apo), "+v"(bpo));
            }, std::make_integer_sequence<int, 8>{});
        };
        if (par) conv_all(std::integral_constant<int, 1>{}); else conv_all(std::integral_constant<int, 0>{});
        SCHED();
        STAMP(9);
        {
            float r0[4], r1[4];
#pragma unroll
            for (int j = 0; j < 4; ++j) { r0[j] = dpp_xor1(odd ? ca[j][0] : ca[j][2]); r1[j] = dpp_xor1(odd ? ca[j][1] : ca[j][3]); }
            unsigned rp[4][2];
#pragma unroll
            for (int j = 0; j < 4; ++j) {
                const unsigned ya = pk(odd ? r0[j] : ca[j][0], odd ? ca[j][2] : r0[j]), yb = pk(odd ? r1[j] : ca[j][1], odd ? ca[j][3] : r1[j]);
                rp[j][0] = __builtin_amdgcn_ds_bpermute(ysrc, pk(add1(bflo1(ya), kd[j][0]), add1(bfhi1(ya), kd[j][1])));
                rp[j][1] = __builtin_amdgcn_ds_bpermute(ysrc, pk(add1(bflo1(yb), kd[j][2]), add1(bfhi1(yb), kd[j][3])));
            }
#pragma unroll
            for (int j = 0; j < 4; ++j)
                __builtin_amdgcn_raw_buffer_store_b128((u32x4){vmov1(tag32), rp[j][0], vmov1(tag32), rp[j][1]}, wsr, yso,
                                                       ((j == 0 ? 0 : j == 1 ? 3 : j == 2 ? 4 : 7) ^ par) * 1024, 16);
        }
        STAMP(10);
        WAIT_LGKM0();
        if (lane == 0) __hip_atomic_fetch_add((LAS unsigned*)(sm + L_CTL + 8), 1u, __ATOMIC_RELAXED, __HIP_MEMORY_SCOPE_WORKGROUP);
#pragma unroll 1
        for (unsigned spins = 0; spins < (1u << 22); ++spins) {
            if (__builtin_amdgcn_readfirstlane(__hip_atomic_load((LAS unsigned*)(sm + L_CTL + 8), __ATOMIC_RELAXED, __HIP_MEMORY_SCOPE_WORKGROUP)) == 4u) break;
            __builtin_amdgcn_s_sleep(1);
        }
        asm volatile("" ::: "memory");
#pragma unroll
        for (int j = 0; j < 8; ++j) *(LAS u32x4*)(sm + latei + j * 1024 + lf * 16) = lw2[j];
        asm volatile("" ::: "memory");
        __hip_atomic_store((LAS unsigned*)(sm + L_CTL + 80 + wq * 4), 1u, __ATOMIC_RELAXED, __HIP_MEMORY_SCOPE_WORKGROUP);
        STAMP(11);
        __builtin_amdgcn_s_sleep(YSLEEP);
        u32x2 yv[16], stv;
        const int yl = L_CONV + 32768 + wq * 4096;
        const unsigned yg = (unsigned)(lf >> 3) * 8192u + (unsigned)(lf & 7) * 16u;
        const u32x4 yvo = {yg, yg + (65536u - 1024u), yg + 2 * (65536u - 1024u), yg + 3 * (65536u - 1024u)};
        const char* ybase = p.ws + WS_Y + (unsigned)(64 * n64 + 32 * (wq >> 1)) * 8192u + (unsigned)(64 * m64 + 32 * (wq & 1)) * 4u;
#pragma unroll 1
        for (unsigned spins = 0; spins < SPIN_CAP; ++spins) {
            glds16x4c(ybase, yvo, lb + yl);
            stv = __builtin_amdgcn_raw_buffer_load_b64(wsr, WS_STATS + (unsigned)srow * 8u, 0, 16);
            WAIT_VM(0);
#pragma unroll
            for (int i = 0; i < 16; ++i) yv[i] = *(LAS u32x2*)(sm + yl + ((lf >> 5) * 16 + i) * 128 + ((lf & 31) >> 1) * 8);
            unsigned df = stv[0] ^ tag32;
#pragma unroll
            for (int i = 0; i < 16; ++i) df |= yv[i][0] ^ tag32;
            if (__all(df == 0u)) break;
            __builtin_amdgcn_s_sleep(8);
        }
        STAMP(14);
        __builtin_amdgcn_s_setprio(0);
        f32x2 sf[8], xf[8];
        {
            const float nmb = -bflo(stv[1]);
            float ri = __builtin_amdgcn_rcpf(bfhi(stv[1]));
            asm volatile("s_nop 1" : "+v"(ri));
            const unsigned ysh = (srow & 1) ? 0u : 16u;
#pragma unroll
            for (int i = 0; i < 8; ++i) {
                const unsigned xd = xv[i >> 2][i & 3];
                xf[i] = (f32x2){bflo1(xd), bfhi1(xd)};
                const unsigned tp = pk(add1(xf[i][0], nmb), add1(xf[i][1], nmb));
                const unsigned np = pk(mul1(bflo1(tp), ri), mul1(bfhi1(tp), ri));
                sf[i] = (f32x2){mul1(bflo1(np), bfhi1(yv[2 * i][1] << ysh)), mul1(bfhi1(np), bfhi1(yv[2 * i + 1][1] << ysh))};
                asm volatile("" : "+v"(sf[i]), "+v"(xf[i]));
            }
        }
        const int rm = lb + L_W2 + (wq < 3 ? wq * 4096 : L_SLOT) + (lf >> 5) * 2048 + (lf & 31) * 16;
        STAMP(15);
        WAIT_BAR("lgkmcnt(0)");
        STAMP(16);
        f32x4 o[4];
#pragma unroll
        for (int j = 0; j < 4; ++j) {
            LAS char* rp = lp(rm) + j * 512;
            o[j] = (*(LAS f32x4*)rp + *(LAS f32x4*)(rp + 12288)) + (*(LAS f32x4*)(rp + 24576) + *(LAS f32x4*)(rp + 36864));
        }
        f32x2 so[8], ev[8];
#pragma unroll
        for (int i = 0; i < 8; ++i) {
            const f32x2 ov = {o[i >> 1][(i & 1) * 2], o[i >> 1][(i & 1) * 2 + 1]};
            const f32x2 t = ov * (f32x2){-1.44269504088896341f, -1.44269504088896341f};
            ev[i] = (f32x2){__builtin_amdgcn_exp2f(t[0]), __builtin_amdgcn_exp2f(t[1])};
            so[i] = sf[i] * ov;
        }
        SCHED();
        u32x4 st[2];
#pragma unroll
        for (int i = 0; i < 8; ++i) {
            const f32x2 dn = ev[i] + (f32x2){1.f, 1.f};
            const f32x2 r = __builtin_elementwise_fma(so[i], (f32x2){__builtin_amdgcn_rcpf(dn[0]), __builtin_amdgcn_rcpf(dn[1])}, xf[i]);
            st[i >> 2][i & 3] = pk(r[0], r[1]);
        }
        const __amdgpu_buffer_rsrc_t orr = __builtin_amdgcn_make_buffer_rsrc((void*)p.out, 0, S * D * 2, 0x00020000);
        __builtin_amdgcn_raw_buffer_store_b128(st[0], orr, xoff, 0, 0);
        __builtin_amdgcn_raw_buffer_store_b128(st[1], orr, xoff, 16, 0);
        STAMP(17);
        STAMP_OUT(18);
    }
}

extern "C" void kernel_launch(void* const* d_in, const int* in_sizes, int n_in,
                              void* d_out, int out_size, void* d_ws, size_t ws_size,
                              hipStream_t stream) {
    Params p;
    p.x = (const u16*)d_in[0]; p.k = (const u16*)d_in[1]; p.v = (const u16*)d_in[2];
    p.sk = (const u16*)d_in[3]; p.hk = (const u16*)d_in[4]; p.dv = (const u16*)d_in[5];
    p.w1 = (const u16*)d_in[7]; p.w3 = (const u16*)d_in[8]; p.w2 = (const u16*)d_in[9];
    p.out = (u16*)d_out; p.ws = (char*)d_ws;
    (void)hipFuncSetAttribute((const void*)fused_kernel, hipFuncAttributeMaxDynamicSharedMemorySize, L_TOTAL);
    fused_kernel<<<256, 512, L_TOTAL, stream>>>(p);
}
```

```cpp
#include <hip/hip_runtime.h>
#include <stdint.h>
#include <utility>

typedef unsigned short u16;
typedef unsigned long long u64;
typedef short s16x4 __attribute__((ext_vector_type(4)));
typedef short s16x8 __attribute__((ext_vector_type(8)));
typedef __bf16 bf16x2 __attribute__((ext_vector_type(2)));
typedef __bf16 bf16x8 __attribute__((ext_vector_type(8)));
typedef float f32x2 __attribute__((ext_vector_type(2)));
typedef float f32x4 __attribute__((ext_vector_type(4)));
typedef float f32x16 __attribute__((ext_vector_type(16)));
typedef unsigned u32x2 __attribute__((ext_vector_type(2)));
typedef unsigned u32x4 __attribute__((ext_vector_type(4)));
typedef unsigned u32x16 __attribute__((ext_vector_type(16)));
#define LAS __attribute__((address_space(3)))
#define GAS __attribute__((address_space(1)))
#define RLX_AGENT __ATOMIC_RELAXED, __HIP_MEMORY_SCOPE_AGENT

constexpr int S = 2048, D = 448, F = 1792;

constexpr unsigned WS_EPOCH = 0;
constexpr unsigned WS_FH = 65536;
constexpr unsigned WS_STATS = 8192;
constexpr unsigned WS_CSUM = 24576;
constexpr unsigned WS_U = 262144;
constexpr unsigned WS_Y = WS_U + D * S * 4;
constexpr unsigned WS_H = WS_Y + D * S * 4;
constexpr unsigned WS_END = WS_H + 64 * 112 * 1024;
constexpr unsigned WS_DBG = WS_END;

constexpr int L_KV = 0;
constexpr int L_CTL = 0;
constexpr int L_CSUM = 256;
constexpr int L_STAT = 1280;
constexpr int L_RAW = 2048;
constexpr int L_KVO = 8192;
constexpr int L_UNX = 6144;
constexpr int L_SLOT = 49152;
constexpr int L_CONV = 12288;
constexpr int C_CH = 26112, C_HS = 4160, C_U = 16640, C_US = 4640;
constexpr int L_W2 = 16384 + L_SLOT;
constexpr int L_TOTAL = 163840;
__host__ __device__ constexpr int ring(int stage) { return 16384 + (stage % 3) * L_SLOT; }
constexpr unsigned CODE_PREFETCH = 36864;
#define POLL_SLEEP 4
#define HDEPTH 6
#define ROT2 (8 * ml)
#define MPRIO 1
#define EPARK 0
#define YSLEEP 16
#define TOUCH 1
#define TOUCH0 8
#define HGROW 4
#define ROTM 6
#define KVW 0
#define MWARM 0
#define URETRY 4
#define KVNT " nt"
#define FLAGW 1
#define AUXW2 1
#define NTOUCH 2
#define ROT1 ((2 * ml + nl) % ROTM)

constexpr u64 TAG_SALT = 0x9E3779B97F4A7C15ull;
constexpr unsigned SPIN_CAP = 1u << 18;

struct Params {
    const u16* x; const u16* k; const u16* v; const u16* sk; const u16* hk; const u16* dv;
    const u16* w1; const u16* w3; const u16* w2; u16* out; char* ws;
};

__device__ __forceinline__ float bflo(unsigned p) { return __uint_as_float(p << 16); }
__device__ __forceinline__ float bfhi(unsigned p) { return __uint_as_float(p & 0xffff0000u); }
__device__ __forceinline__ float bflo1(unsigned p) { float r; asm("v_lshlrev_b32 %0, 16, %1" : "=v"(r) : "v"(p)); return r; }
__device__ __forceinline__ float bfhi1(unsigned p) { float r; asm("v_and_b32 %0, 0xffff0000, %1" : "=v"(r) : "v"(p)); return r; }
__device__ __forceinline__ float mul1(float a, float b) { float r; asm("v_mul_f32 %0, %1, %2" : "=v"(r) : "v"(a), "v"(b)); return r; }
__device__ __forceinline__ void fmac1(float& d, float a, float b) { asm("v_fmac_f32 %0, %1, %2" : "+v"(d) : "v"(a), "v"(b)); }
__device__ __forceinline__ float dpp_xor1(float v) {
    return __int_as_float(__builtin_amdgcn_update_dpp(0, __float_as_int(v), 0xB1, 0xf, 0xf, true));
}
__device__ __forceinline__ float add1(float a, float b) { float r; asm("v_add_f32 %0, %1, %2" : "=v"(r) : "v"(a), "v"(b)); return r; }
__device__ __forceinline__ unsigned vmov1(unsigned s) { unsigned r; asm volatile("v_mov_b32 %0, %1" : "=v"(r) : "s"(s)); return r; }
__device__ __forceinline__ unsigned pk(float a, float b) {
    f32x2 f = {a, b};
    return __builtin_bit_cast(unsigned, __builtin_convertvector(f, bf16x2));
}
__device__ __forceinline__ float rbf(float a) { return bflo(pk(a, a)); }
__device__ __forceinline__ float dot2(unsigned a, unsigned b, float c) {
    return __builtin_amdgcn_fdot2_f32_bf16(__builtin_bit_cast(bf16x2, a), __builtin_bit_cast(bf16x2, b), c, false);
}
__device__ __forceinline__ void glds16(const void* base, unsigned voff, unsigned ldst) {
    u64 t;
    asm volatile("s_mov_b32 m0, %3\n\ts_mov_b64 %0, %2\n\tglobal_load_lds_dwordx4 %1, %0" KVNT : "=&s"(t) : "v"(voff), "s"(base), "s"(ldst));
}
__device__ __forceinline__ void glds16x4(const void* base, u32x4 vo, unsigned ldst) {
    u64 t;
    asm volatile("s_mov_b32 m0, %6\n\ts_mov_b64 %0, %5\n\tglobal_load_lds_dwordx4 %1, %0\n\tglobal_load_lds_dwordx4 %2, %0 offset:1024\n\t"
                 "global_load_lds_dwordx4 %3, %0 offset:2048\n\tglobal_load_lds_dwordx4 %4, %0 offset:3072"
                 : "=&s"(t) : "v"(vo[0]), "v"(vo[1]), "v"(vo[2]), "v"(vo[3]), "s"(base), "s"(ldst));
}
__device__ __forceinline__ void glds16x4c(const void* base, u32x4 vo, unsigned ldst) {
    u64 t;
    asm volatile("s_mov_b32 m0, %6\n\ts_mov_b64 %0, %5\n\tglobal_load_lds_dwordx4 %1, %0 sc1\n\tglobal_load_lds_dwordx4 %2, %0 offset:1024 sc1\n\t"
                 "global_load_lds_dwordx4 %3, %0 offset:2048 sc1\n\tglobal_load_lds_dwordx4 %4, %0 offset:3072 sc1"
                 : "=&s"(t) : "v"(vo[0]), "v"(vo[1]), "v"(vo[2]), "v"(vo[3]), "s"(base), "s"(ldst) : "memory");
}
__device__ __forceinline__ void glds16x2c(const void* base, unsigned voff, unsigned ldst) {
    u64 t;
    asm volatile("s_mov_b32 m0, %3\n\ts_mov_b64 %0, %2\n\tglobal_load_lds_dwordx4 %1, %0 sc1\n\tglobal_load_lds_dwordx4 %1, %0 offset:1024 sc1"
                 : "=&s"(t) : "v"(voff), "s"(base), "s"(ldst));
}
__device__ __forceinline__ void glds16c(const void* base, unsigned voff, unsigned ldst) {
    u64 t;
    asm volatile("s_mov_b32 m0, %3\n\ts_mov_b64 %0, %2\n\tglobal_load_lds_dwordx4 %1, %0 sc1" : "=&s"(t) : "v"(voff), "s"(base), "s"(ldst));
}
__device__ __forceinline__ void glds4c(const void* base, unsigned voff, unsigned ldst) {
    u64 t;
    asm volatile("s_mov_b32 m0, %3\n\ts_mov_b64 %0, %2\n\tglobal_load_lds_dword %1, %0 sc1" : "=&s"(t) : "v"(voff), "s"(base), "s"(ldst));
}
__device__ __forceinline__ const u16* sptr(const u16* base, int off) {
    const u16* r = base + __builtin_amdgcn_readfirstlane(off);
    asm("" : "+s"(r));
    return r;
}
__device__ __forceinline__ u64 vsel(bool c, const void* a, u64 b) {
    u64 va = (u64)a;
    asm("" : "+v"(va), "+v"(b));
    return c ? va : b;
}
__device__ __forceinline__ LAS char* lp(int a) { return (LAS char*)(size_t)(unsigned)a; }
__device__ __forceinline__ u32x4 lds128(LAS char* p) { return *(LAS u32x4*)p; }
__device__ __forceinline__ u32x2 ldstr(LAS char* p) {
    return __builtin_bit_cast(u32x2, __builtin_amdgcn_ds_read_tr16_b64_v4i16((LAS s16x4*)p));
}
__device__ __forceinline__ u32x4 ldstr2(LAS char* p, int off) {
    u32x2 a = ldstr(p), b = ldstr(p + off);
    return (u32x4){a.x, a.y, b.x, b.y};
}
__device__ __forceinline__ f32x16 mfma32(u32x4 a, u32x4 b, f32x16 c) {
    return __builtin_amdgcn_mfma_f32_32x32x16_bf16(__builtin_bit_cast(bf16x8, a), __builtin_bit_cast(bf16x8, b), c, 0, 0, 0);
}
__device__ __forceinline__ f32x4 mfma16(u32x4 a, u32x4 b, f32x4 c) {
    return __builtin_amdgcn_mfma_f32_16x16x32_bf16(__builtin_bit_cast(bf16x8, a), __builtin_bit_cast(bf16x8, b), c, 0, 0, 0);
}
__device__ __forceinline__ float silu_f(float g) {
    return g * __builtin_amdgcn_rcpf(1.f + __builtin_amdgcn_exp2f(g * -1.44269504088896341f));
}
template <class Fn, int... I>
__device__ __forceinline__ void static_for(Fn&& fn, std::integer_sequence<int, I...>) { (fn(std::integral_constant<int, I>{}), ...); }
#define WAIT_VM(n) asm volatile("s_waitcnt vmcnt(" #n ")" ::: "memory")
#define WAIT_LGKM0() asm volatile("s_waitcnt lgkmcnt(0)" ::: "memory")
#define WAIT_BAR(w) asm volatile("s_waitcnt " w "\n\ts_barrier" ::: "memory")
#define SCHED() __builtin_amdgcn_sched_barrier(0)
#define CAS __attribute__((address_space(4)))
#define NSTAMP 18

__device__ __forceinline__ void wait_lds(LAS char* word) {
#pragma unroll 1
    for (unsigned spins = 0; spins < (SPIN_CAP << 4); ++spins) {
        if (__builtin_amdgcn_readfirstlane(__hip_atomic_load((LAS unsigned*)word, __ATOMIC_RELAXED, __HIP_MEMORY_SCOPE_WORKGROUP))) break;
        __builtin_amdgcn_s_sleep(1);
    }
    asm volatile("" ::: "memory");
}
__device__ __forceinline__ void wait_lds_eq(LAS char* word, unsigned want) {
#pragma unroll 1
    for (unsigned spins = 0; spins < (1u << 22); ++spins) {
        if ((unsigned)__builtin_amdgcn_readfirstlane(__hip_atomic_load((LAS unsigned*)word, __ATOMIC_RELAXED, __HIP_MEMORY_SCOPE_WORKGROUP)) == want) break;
        __builtin_amdgcn_s_sleep(1);
    }
    asm volatile("" ::: "memory");
}
#define STAMP(i)
#define STAMPG(i)
#define STAMPA(i)
#define STAMP_DECL()
#define STAMP_OUT(n)

__global__ __launch_bounds__(512, 2) void fused_kernel(Params p) {
    extern __shared__ __attribute__((aligned(16))) char smem_raw[];
    u64 pc;
    asm volatile("s_getpc_b64 %0" : "=s"(pc));
    LAS char* sm = (LAS char*)smem_raw;
    const int tid = threadIdx.x, lane = tid & 63;
    const int wave = __builtin_amdgcn_readfirstlane(tid >> 6);
    const int xcd = blockIdx.x & 7, idx = blockIdx.x >> 3;
    if (idx == 0 && wave < 5) {
        const unsigned o = (unsigned)tid * 128u;
        asm volatile("s_mov_b32 m0, %2\n\ts_nop 0\n\tglobal_load_lds_dword %0, %1" :: "v"(o < CODE_PREFETCH ? o : 0u), "s"(pc & ~(u64)255),
                     "s"((unsigned)(size_t)sm + 32768u));
    }
    SCHED();
    const __amdgpu_buffer_rsrc_t wsr = __builtin_amdgcn_make_buffer_rsrc(p.ws, 0, WS_END, 0x00020000);
    asm volatile("" :: "s"(p.x), "s"(p.k), "s"(p.v), "s"(p.sk), "s"(p.hk), "s"(p.dv), "s"(p.w1), "s"(p.w3), "s"(p.w2), "s"(p.out), "s"(p.ws));
    STAMP_DECL();
    STAMP(0);

    if (idx < 4) {
        const int ca = (xcd >> 1) * 4 + idx, cmat = ca >= 7, cnt = 7 * (xcd & 1) + (ca >= 7 ? ca - 7 : ca);
        const bool job = ca < 14;
        u32x4 xr[7], wr[14];
        const int row = (8 * (xcd >> 1) + 4 * (xcd & 1) + idx) * 64 + wave * 8 + (lane >> 3);
#pragma unroll
        for (int i = 0; i < 7; ++i) xr[i] = *(const u32x4*)(p.x + (size_t)row * D + i * 64 + (lane & 7) * 8);
        const u64 epoch = __hip_atomic_load((GAS u64*)(p.ws + WS_EPOCH), RLX_AGENT);
        const u64 tag = epoch ^ TAG_SALT;
        const unsigned tag32 = (unsigned)tag;
        {
            float s4[4] = {0.f, 0.f, 0.f, 0.f};
#pragma unroll
            for (int i = 0; i < 7; ++i)
#pragma unroll
                for (int j = 0; j < 4; ++j) s4[j] = dot2(xr[i][j], 0x3f803f80u, s4[j]);
            float sum = (s4[0] + s4[1]) + (s4[2] + s4[3]);
            sum += __shfl_xor(sum, 1); sum += __shfl_xor(sum, 2); sum += __shfl_xor(sum, 4);
            const float mean = sum * (1.f / D);
            f32x2 q4[4] = {{0.f, 0.f}, {0.f, 0.f}, {0.f, 0.f}, {0.f, 0.f}};
#pragma unroll
            for (int i = 0; i < 7; ++i)
#pragma unroll
                for (int j = 0; j < 4; ++j) {
                    const f32x2 d = (f32x2){bflo(xr[i][j]), bfhi(xr[i][j])} - (f32x2){mean, mean};
                    q4[j] = __builtin_elementwise_fma(d, d, q4[j]);
                }
            const f32x2 q2 = (q4[0] + q4[1]) + (q4[2] + q4[3]);
            float sq = q2[0] + q2[1];
            sq += __shfl_xor(sq, 1); sq += __shfl_xor(sq, 2); sq += __shfl_xor(sq, 4);
            const u32x2 st = {tag32, pk(mean, __builtin_amdgcn_sqrtf(rbf(rbf(sq * (1.f / D)) + 1e-5f)))};
            __builtin_amdgcn_raw_buffer_store_b64(st, wsr, (lane & 7) == 0 ? WS_STATS + (unsigned)row * 8u : 0xfffffff0u, 0, 16);
        }
        STAMPA(2);
        if (job) {
            SCHED();
            const u16* wp = (cmat ? p.w3 : p.w1) + (size_t)(wave * 4 + (lane >> 4)) * F + cnt * 128 + (lane & 15) * 8;
#pragma unroll
            for (int i = 0; i < 14; ++i) wr[i] = *(const u32x4*)(wp + (size_t)(32 * i) * F);
        }
        if (job) {
            float c[8];
#pragma unroll
            for (int j = 0; j < 8; ++j) c[j] = 0.f;
#pragma unroll
            for (int i = 0; i < 14; ++i)
#pragma unroll
                for (int j = 0; j < 4; ++j) { c[2 * j] += bflo(wr[i][j]); c[2 * j + 1] += bfhi(wr[i][j]); }
            LAS char* cp = sm + ((wave * 4 + (lane >> 4)) * 136 + (lane & 15) * 8) * 4;
            *(LAS f32x4*)cp = (f32x4){c[0], c[1], c[2], c[3]};
            *(LAS f32x4*)(cp + 16) = (f32x4){c[4], c[5], c[6], c[7]};
        }
        STAMPA(3);
        if (job) {
            WAIT_BAR("lgkmcnt(0)");
            float t = 0.f;
#pragma unroll
            for (int i = 0; i < 8; ++i) t += *(LAS float*)(sm + ((i * 4 + (tid & 3)) * 136 + (tid >> 2)) * 4);
            t += __shfl_xor(t, 1); t += __shfl_xor(t, 2);
            __builtin_amdgcn_raw_buffer_store_b64((u32x2){tag32, __float_as_uint(t)}, wsr,
                (tid & 3) == 0 ? WS_CSUM + (unsigned)((cnt * 2 + cmat) * 128 + (tid >> 2)) * 8u : 0xfffffff0u, 0, 16);
        }
        STAMP(1);
        SCHED();
        if (!job) __builtin_amdgcn_s_sleep(AUXW2 ? 40 : 80);
        const unsigned pf2 = *(const unsigned*)((const char*)p.hk + (size_t)((xcd * 4 + idx) * 448 + wave * 56 + (lane < 56 ? lane : 0)) * 128);
        if (AUXW2) {
            const unsigned fo = lane * 16u;
            constexpr int PER = ((4 - NTOUCH) * 14336 + 4095) / 4096;
#pragma unroll 1
            for (int i = wave; i < 7 * PER; i += 8) {
                const int g = i / PER, j = i - g * PER;
                int o = NTOUCH * 14336 + j * 4096; o = o > 57344 - 4096 ? 57344 - 4096 : o;
                glds16x4((const char*)p.w2 + (size_t)idx * 401408 + g * 57344 + o, (u32x4){fo, fo, fo, fo}, (unsigned)(size_t)sm + 65536u + wave * 4096);
            }
        }
        asm volatile("" :: "v"(pf2));
        STAMP_OUT(4);
        if (blockIdx.x == 0 && wave == 7) {
            __builtin_amdgcn_s_sleep(127);
            __builtin_amdgcn_s_sleep(100);
            GAS u64* gp = (GAS u64*)(p.ws + WS_STATS) + lane * 8;
            const int fl = lane < 56 ? lane : 0;
            GAS u64* fp = (GAS u64*)(p.ws + WS_FH) + ((fl / 14) * 56 + (fl % 14) * 2) * 16;
#pragma unroll 1
            for (unsigned spins = 0; spins < SPIN_CAP; ++spins) {
                bool ok = true;
#pragma unroll
                for (int m = 0; m < 4; ++m)
                    ok &= ((unsigned)__hip_atomic_load(gp + m * 512, RLX_AGENT) == tag32) & (__hip_atomic_load(fp + m * 224 * 16, RLX_AGENT) == tag);
                if (__all(ok)) break;
                __builtin_amdgcn_s_sleep(32);
            }
            if (lane == 0) __hip_atomic_store((GAS u64*)(p.ws + WS_EPOCH), epoch + 1, RLX_AGENT);
        }
        return;
    }

    const int gi = idx - 4, ml = gi & 3, nl = gi >> 2;
    const int mt = 4 * (xcd >> 1) + ml, nt = 7 * (xcd & 1) + nl;
    const int rot1 = ROT1;
    const int m64 = 4 * xcd + ml, n64 = nl;
    const int gpair = xcd * 28 + gi;
    const int r32 = lane & 31, hh = lane >> 5, blk = (lane >> 4) & 1, q4 = (lane & 15) >> 2, p4 = lane & 3;
    const unsigned lb = (unsigned)(size_t)sm;
    const int wq = wave & 3;
    u32x4 wvo;
    wvo[0] = (unsigned)((lane >> 4) * F + (((lane & 15) ^ ((lane >> 4) << 2)) * 8)) * 2u;
#pragma unroll
    for (int j = 1; j < 4; ++j) wvo[j] = wvo[0] + j * (8 * F - 1024);
    const u16* w1b = sptr(p.w1, wq * 16 * F + nt * 128);
    const u16* w3b = sptr(p.w3, wq * 16 * F + nt * 128);
    auto kst = [&](int si) { if (ROTM == 6 && si == 0) return 0; const int s = si + rot1; return s >= 7 ? s - ROTM : s; };
    auto group_w = [&](int si, int m) { glds16x4(sptr(m ? w3b : w1b, kst(si) * 64 * F), wvo, lb + ring(si) + 16384 + m * 16384 + wq * 4096); };
    const int kr0 = (gpair & 31) * 64, kc0 = (gpair >> 5) * 64;
    const int chn = wq >> 1, par = wq & 1, dch = 2 * gpair + chn;
    if (wave < 4) group_w(0, 0);
    {
        const bool isk = lane >= 8 && lane < 14;
        u64 mb_ = vsel(lane == 15, p.dv, vsel(lane == 14, p.hk, vsel(isk, p.k, (u64)p.sk)));
        unsigned mo = (unsigned)((kc0 + wq * 16) * 8 + (lane & 7) * 16);
        mo = isk ? (unsigned)((kr0 ? kr0 - 3 + ((lane - 8) >> 1) : 0) * D + kc0 + wq * 16 + (lane & 1) * 8) * 2u : mo;
        mo = lane == 14 ? (unsigned)dch * 4096u : mo;
        mo = lane == 15 ? (unsigned)(dch & ~7) * 2u : mo;
        const u64 ma = mb_ + mo;
        asm volatile("s_mov_b32 m0, %1\n\ts_nop 0\n\tglobal_load_lds_dwordx4 %0, off" :: "v"(ma), "s"(lb + wave * 1024));
    }
    if (wave < 4) group_w(0, 1);
    SCHED();
    const int rot2 = ROT2;
    const int w2i = L_W2 + wq * 12288;
    auto fresh_lane = [&]() { int l = lane; asm volatile("" : "+v"(l)); return l; };
    const int latei = L_CONV + wq * 8192;
    auto pair_w2 = [&](u32x4 vo, int i, int dst) {
        int t = i + rot2; t = t >= 28 ? t - 28 : t;
        glds16x4(sptr(p.w2, (wq * 448 + t * 16) * D + n64 * 64), vo, lb + dst);
    };
    auto w2_off = [&]() {
        const int l = fresh_lane();
        const unsigned o = (unsigned)((l >> 3) * D + (((l & 7) ^ (((l >> 4) & 1) << 2)) * 8)) * 2u;
        return (u32x4){o, o + (16 * D - 1024), o + 2 * (16 * D - 1024), o + 3 * (16 * D - 1024)};
    };

    if (wave < 4) {
        __builtin_amdgcn_s_setprio(MPRIO);
        const int wm = wave >> 1, wn = wave & 1;
        int xa[4], wa0, wa1;
#pragma unroll
        for (int i = 0; i < 4; ++i) xa[i] = lb + ring(0) + (((64 * wm + r32) * 128 + ((hh ^ ((r32 >> 1) & 7)) * 16)) ^ (i * 32));
        wa0 = 16384 + (8 * hh + q4) * 256 + (((8 * wn + 2 * blk + (p4 >> 1)) ^ (q4 << 2)) * 16) + 8 * (p4 & 1);
        wa1 = (wa0 ^ 64) + lb + ring(0); wa0 += lb + ring(0);
        f32x16 acc[2][2][2];
#pragma unroll
        for (int i = 0; i < 8; ++i)
#pragma unroll
            for (int r = 0; r < 16; ++r) acc[i >> 2][(i >> 1) & 1][i & 1][r] = 0.f;
        u32x4 fx[2][2], fw[2][2][2];
        auto load_frag = [&](int g, int j) {
            const int pp = g & 3, b = g & 1;
            if (j == 0) {
                if (g >= 4) {
                    const int d = ring(g >> 2) - ring((g >> 2) - 1);
                    if (pp == 0) { wa0 += d; wa1 += d; asm volatile("" : "+v"(wa0), "+v"(wa1)); }
                    xa[pp] += d; asm volatile("" : "+v"(xa[pp]));
                }
                fw[b][0][0] = ldstr2(lp(wa0) + pp * 4096, 1024);
            }
            if (j == 1) { fx[b][0] = lds128(lp(xa[pp])); fx[b][1] = lds128(lp(xa[pp]) + 4096); }
            if (j == 2) fw[b][0][1] = ldstr2(lp(wa0) + pp * 4096 + 16384, 1024);
            if (j == 3) fw[b][1][0] = ldstr2(lp(wa1) + pp * 4096, 1024);
            if (j == 4) fw[b][1][1] = ldstr2(lp(wa1) + pp * 4096 + 16384, 1024);
        };
        int ka0[2], kam[2], kwo;
#pragma unroll
        for (int rd = 0; rd < 2; ++rd) {
            const int ck = 2 * wq + rd;
            const int bq = 2 * (lane >> 4) + (p4 >> 1), bm = bq ? bq - 1 : 0;
            ka0[rd] = lb + L_KV + (8 * bq + q4) * 128 + ((ck ^ (((q4 >> 1) & 1) | ((bq & 3) << 1))) * 16) + 8 * (p4 & 1);
            kam[rd] = lb + L_KV + (8 * bm + 4 + q4) * 128 + ((ck ^ (((q4 >> 1) & 1) | ((bm & 3) << 1))) * 16) + 8 * (p4 & 1);
        }
        kwo = lb + L_KVO + wq * 2048 + (lane & 7) * 128 + (((7 - (lane >> 3)) ^ (lane & 7)) * 16);
        asm volatile("" : "+v"(ka0[0]), "+v"(ka0[1]), "+v"(kam[0]), "+v"(kam[1]), "+v"(kwo));
        WAIT_VM(4);
        u32x2 skv[2], hal[2];
#pragma unroll
        for (int rd = 0; rd < 2; ++rd) {
            LAS char* mp = sm + wave * 1024;
            skv[rd] = *(LAS u32x2*)(mp + (rd * 8 + (lane & 7)) * 8);
            const unsigned hm = kr0 ? 0xffffffffu : 0u;
            const unsigned a = *(LAS u16*)(mp + 128 + rd * 16 + (lane & 7) * 2), b = *(LAS u16*)(mp + 160 + rd * 16 + (lane & 7) * 2),
                           c = *(LAS u16*)(mp + 192 + rd * 16 + (lane & 7) * 2);
            hal[rd] = (u32x2){(a << 16) & hm, (b | (c << 16)) & hm};
            asm volatile("" : "+v"(skv[rd]), "+v"(hal[rd]));
        }
        WAIT_BAR("vmcnt(0) lgkmcnt(0)");
        STAMP(1);
        u32x2 kt[2][3], vt[2][2];
        float cf[4], kk[11], ko[8];
        u32x4 kpk;
        auto fill = [&](auto kc) {
            constexpr int k = decltype(kc)::value;
            if constexpr (k < 4) {
                constexpr int rd = k >> 1;
                if constexpr ((k & 1) == 0) { kt[rd][0] = ldstr(lp(kam[rd])); kt[rd][1] = ldstr(lp(ka0[rd])); kt[rd][2] = ldstr(lp(ka0[rd]) + 512); }
                else { vt[rd][0] = ldstr(lp(ka0[rd]) + 8192); vt[rd][1] = ldstr(lp(ka0[rd]) + 8192 + 512); }
            } else if constexpr (k < 4 + 2 * 16) {
                constexpr int rd = (k - 4) / 16, s = (k - 4) % 16;
                if constexpr (s == 0) { cf[0] = bflo1(skv[rd][0]); cf[1] = bfhi1(skv[rd][0]); cf[2] = bflo1(skv[rd][1]); cf[3] = bfhi1(skv[rd][1]); }
                else if constexpr (s == 1) {
                    const unsigned a = lane < 8 ? hal[rd][0] : kt[rd][0][0], b = lane < 8 ? hal[rd][1] : kt[rd][0][1];
                    kk[0] = bfhi1(a); kk[1] = bflo1(b); kk[2] = bfhi1(b);
                } else if constexpr (s < 4) {
                    const u32x2 t = kt[rd][s - 1];
                    kk[4 * s - 5] = bflo1(t[0]); kk[4 * s - 4] = bfhi1(t[0]); kk[4 * s - 3] = bflo1(t[1]); kk[4 * s - 2] = bfhi1(t[1]);
                } else if constexpr (s < 12) {
                    constexpr int tp = (s - 4) >> 1, o0 = ((s - 4) & 1) * 4;
#pragma unroll
                    for (int i = o0; i < o0 + 4; ++i) {
                        if (tp == 0) ko[i] = mul1(kk[i], cf[3]);
                        else fmac1(ko[i], kk[i + tp], cf[3 - tp]);
                    }
                } else {
                    constexpr int j = s - 12;
                    const unsigned v = vt[rd][j >> 1][j & 1];
                    kpk[3 - j] = pk(mul1(ko[2 * j + 1], bfhi1(v)), mul1(ko[2 * j], bflo1(v)));
                    if constexpr (j == 3) *(LAS u32x4*)(lp(kwo) + rd * 1024) = kpk;
                }
            }
        };
#pragma unroll
        for (int j = 0; j < 5; ++j) load_frag(0, j);
        f32x4 cs[2][2][4];
        unsigned tch[4] = {0u, 0u, 0u, 0u}, tch0 = 0;
        unsigned tc0 = (unsigned)((gpair * 4 + wq) * 14 + (lane < 14 ? lane : 13)) * 128u;
        unsigned tco = (unsigned)(gi * 4 + wq + (lane < 28 ? lane : 27) * 448) * 128u;
        asm volatile("" : "+v"(tco));
        static_for([&](auto gc) {
            constexpr int g = decltype(gc)::value, b = g & 1, pp = g & 3;
            SCHED();
            if (g == 11) STAMP(2);
            if (pp == 3 && g < 27) { STAMPG(2 + 2 * (g >> 2)); WAIT_BAR("lgkmcnt(0)"); STAMPG(3 + 2 * (g >> 2)); }
            if (g == 27) {
                WAIT_LGKM0();
                if (lane == 0) __hip_atomic_fetch_add((LAS unsigned*)(sm + L_CTL + 12), 1u, __ATOMIC_RELAXED, __HIP_MEMORY_SCOPE_WORKGROUP);
            }
            if (g == 11) STAMP(3);
            static_for([&](auto ic) {
                constexpr int i = decltype(ic)::value, ft = i >> 2, m = (i >> 1) & 1, sb = i & 1;
                acc[sb][ft][m] = mfma32(fw[b][ft][m], fx[b][sb], acc[sb][ft][m]);
                SCHED();
                if (g < 27 && i < 5) load_frag(g + 1, i);
                if constexpr (MWARM && i == 6 && g == MWARM - 1) {
                    u64 t;
                    asm volatile("s_mov_b64 %1, %3\n\tglobal_load_dword %0, %2, %1" : "=v"(tch0), "=&s"(t) : "v"(tc0), "s"(p.w2));
                }
                if constexpr (TOUCH && i == 4 && pp == 2 && g >= TOUCH0 && g < TOUCH0 + 4 * NTOUCH) {
                    u64 t;
                    asm volatile("s_mov_b64 %1, %3\n\tglobal_load_dword %0, %2, %1" : "=v"(tch[(g - TOUCH0) >> 2]), "=&s"(t)
                                 : "v"(tco), "s"((const char*)p.w2 + ((g - TOUCH0) >> 2) * (112 * 128)));
                }
                if constexpr (i >= 5 && g >= 7 + 4 * KVW) fill(std::integral_constant<int, (g - 7 - 4 * KVW) * 3 + i - 5>{});
                SCHED();
            }, std::make_integer_sequence<int, 8>{});
        }, std::make_integer_sequence<int, 28>{});
        STAMP(4);
        wait_lds_eq(sm + L_CTL + 64, 3u);
        unsigned sa[2];
#pragma unroll
        for (int i = 0; i < 8; ++i) cs[i >> 2][0][i & 3] = *(LAS f32x4*)(sm + L_CSUM + (64 * wn + 32 * (i >> 2) + 8 * (i & 3) + 4 * hh) * 4);
        sa[0] = *(LAS unsigned*)(sm + L_STAT + (64 * wm + r32) * 4);
        sa[1] = *(LAS unsigned*)(sm + L_STAT + (64 * wm + 32 + r32) * 4);
#pragma unroll
        for (int i = 0; i < 8; ++i) cs[i >> 2][1][i & 3] = *(LAS f32x4*)(sm + L_CSUM + 512 + (64 * wn + 32 * (i >> 2) + 8 * (i & 3) + 4 * hh) * 4);
        const int lw = fresh_lane();
        const int w2a0 = lb + w2i + (4 * (lw >> 5) + ((lw & 15) >> 2)) * 128 + (((2 * ((lw >> 4) & 1) + ((lw & 3) >> 1)) ^ (((lw >> 3) & 1) << 2)) * 16) + 8 * (lw & 1);
        const int w2a1 = w2a0 ^ 64;
        u32x4 w2r[12][2];
        auto park = [&](int i) {
            w2r[i][0] = ldstr2(lp(w2a0) + (i / 6) * L_SLOT + (i % 6) * 2048, 1024);
            w2r[i][1] = ldstr2(lp(w2a1) + (i / 6) * L_SLOT + (i % 6) * 2048, 1024);
        };
#pragma unroll
        for (int sb = 0; sb < 2; ++sb) {
            const float ri = __builtin_amdgcn_rcpf(bfhi(sa[sb])) * -1.44269504088896341f, nm = -bflo(sa[sb]), d2 = bfhi(sa[sb]) * bfhi(sa[sb]);
            const f32x2 ri2 = {ri, ri}, nm2 = {nm, nm}, dd2 = {d2, d2};
#pragma unroll
            for (int ft = 0; ft < 2; ++ft) {
#pragma unroll
                for (int t2 = 0; t2 < 2; ++t2) {
                    const int grp = sb * 4 + ft * 2 + t2;
                    if (EPARK && grp == 2) { SCHED(); wait_lds(sm + L_CTL + 48 + wq * 4); }
                    if (EPARK && grp >= 2) { SCHED(); park(grp - 2); }
                    f32x2 gu[4], ee[4];
#pragma unroll
                    for (int j = 0; j < 4; ++j) {
                        const int r = 8 * t2 + 2 * j;
                        const f32x2 g = __builtin_elementwise_fma(nm2, (f32x2){cs[ft][0][r >> 2][r & 3], cs[ft][0][r >> 2][(r & 3) + 1]}, (f32x2){acc[sb][ft][0][r], acc[sb][ft][0][r + 1]});
                        const f32x2 u = __builtin_elementwise_fma(nm2, (f32x2){cs[ft][1][r >> 2][r & 3], cs[ft][1][r >> 2][(r & 3) + 1]}, (f32x2){acc[sb][ft][1][r], acc[sb][ft][1][r + 1]});
                        const f32x2 t = g * ri2;
                        ee[j] = (f32x2){__builtin_amdgcn_exp2f(t[0]), __builtin_amdgcn_exp2f(t[1])};
                        gu[j] = g * u;
                    }
                    u32x4 st;
#pragma unroll
                    for (int j = 0; j < 4; ++j) {
                        const f32x2 dn = __builtin_elementwise_fma(ee[j], dd2, dd2);
                        const f32x2 hv = gu[j] * (f32x2){__builtin_amdgcn_rcpf(dn[0]), __builtin_amdgcn_rcpf(dn[1])};
                        st[j] = pk(hv[0], hv[1]);
                    }
                    const unsigned fi = (unsigned)((4 * mt + 2 * wm + sb) * 112 + 8 * nt + 4 * wn + 2 * ft + t2);
                    __builtin_amdgcn_raw_buffer_store_b128(st, wsr, WS_H + fi * 1024u + lane * 16u, 0, 16);
                }
            }
        }
        SCHED();
        STAMP(5);
        const u32x4 w2o = w2_off();
        const u64 tag = *(LAS u64*)(sm + L_CTL + 128);
        GAS u64* myflag = (GAS u64*)(p.ws + WS_FH + ((2 * mt + wm) * 28 + 2 * nt + wn) * 128) + (lw & 15);
        if (!__builtin_amdgcn_readfirstlane(__hip_atomic_load((LAS unsigned*)(sm + L_CTL + 112 + wq * 4), __ATOMIC_RELAXED, __HIP_MEMORY_SCOPE_WORKGROUP))) {
            WAIT_VM(0);
            if (lane < FLAGW) __hip_atomic_store(myflag, tag, RLX_AGENT);
            wait_lds(sm + L_CTL + 112 + wq * 4);
        }
        asm volatile("" ::: "memory");
#pragma unroll
        for (int i = 6; i < 12; ++i) park(i);
        WAIT_VM(0);
        if (TOUCH) asm volatile("" :: "v"(tch[0]), "v"(tch[1]), "v"(tch[2]), "v"(tch[3]));
        asm volatile("" :: "v"(tch0));
        if (lane < FLAGW) __hip_atomic_store(myflag, tag, RLX_AGENT);
        STAMP(6);
        if (!EPARK) {
#pragma unroll
            for (int i = 0; i < 6; ++i) park(i);
        }
        asm volatile("" ::: "memory");
        __hip_atomic_store((LAS unsigned*)(sm + L_CTL + 32 + wq * 4), 1u, __ATOMIC_RELAXED, __HIP_MEMORY_SCOPE_WORKGROUP);
        WAIT_LGKM0();
#pragma unroll
        for (int i = 0; i < 12; ++i) asm volatile("" : "+v"(w2r[i][0]), "+v"(w2r[i][1]));
#pragma unroll
        for (int i = 0; i < 3; ++i) pair_w2(w2o, 12 + 2 * i, w2i + i * 4096);
#pragma unroll
        for (int i = 0; i < 3; ++i) pair_w2(w2o, 18 + 2 * i, w2i + L_SLOT + i * 4096);
        STAMP(7);
        {
            GAS u64* fp = (GAS u64*)(p.ws + WS_FH + (m64 * 28 + 7 * wq + (lw < 7 ? lw : 0)) * 128);
#pragma unroll 1
            for (unsigned spins = 0; spins < SPIN_CAP; ++spins) {
                if (__all(__hip_atomic_load(fp, RLX_AGENT) == tag)) break;
                __builtin_amdgcn_s_sleep(POLL_SLEEP);
            }
            __builtin_amdgcn_fence(__ATOMIC_ACQUIRE, "wavefront");
        }
        STAMP(11);
        const unsigned hb = WS_H + (unsigned)(2 * m64 * 112 + 28 * wq) * 1024u + lw * 16u;
        u32x4 hf[28][2];
        auto load_h = [&](int i, int j) {
            int t = i + rot2; t = t >= 28 ? t - 28 : t;
            hf[i][j] = __builtin_amdgcn_raw_buffer_load_b128(wsr, hb + j * (112u * 1024u), t * 1024, 16);
        };
        constexpr int HD = HDEPTH, HG = HGROW;
#pragma unroll
        for (int i = 0; i < HD; ++i) { load_h(i, 0); load_h(i, 1); }
        STAMP(14);
        f32x16 ac2[2][2];
#pragma unroll
        for (int i = 0; i < 4; ++i)
#pragma unroll
            for (int r = 0; r < 16; ++r) ac2[i >> 1][i & 1][r] = 0.f;
        u32x4 a0 = w2r[0][0], a1 = w2r[0][1];
        static_for([&](auto ic) {
            constexpr int i = decltype(ic)::value, n = i + 1;
            SCHED();
            if (i == 23) wait_lds(sm + L_CTL + 80 + wq * 4);
            u32x4 b0, b1;
            if constexpr (n < 12) { b0 = w2r[n][0]; b1 = w2r[n][1]; }
            else if constexpr (n < 28) {
                constexpr int so = n < 24 ? ((n - 12) / 6) * L_SLOT + ((n - 12) % 6) * 2048 : (n - 24) * 2048;
                const int lo = n < 24 ? 0 : latei - w2i;
                b0 = ldstr2(lp(w2a0 + lo) + so, 1024);
                b1 = ldstr2(lp(w2a1 + lo) + so, 1024);
            }
            SCHED();
            ac2[0][0] = mfma32(a0, hf[i][0], ac2[0][0]);
            SCHED();
            constexpr int l0 = i < HG ? HD + 2 * i : HD + HG + i;
            if constexpr (l0 < 28) load_h(l0, 0);
            SCHED();
            ac2[1][0] = mfma32(a1, hf[i][0], ac2[1][0]);
            SCHED();
            if constexpr (l0 < 28) load_h(l0, 1);
            SCHED();
            ac2[0][1] = mfma32(a0, hf[i][1], ac2[0][1]);
            SCHED();
            if constexpr (i < HG) load_h(l0 + 1, 0);
            SCHED();
            ac2[1][1] = mfma32(a1, hf[i][1], ac2[1][1]);
            SCHED();
            if constexpr (i < HG) load_h(l0 + 1, 1);
            a0 = b0; a1 = b1;
        }, std::make_integer_sequence<int, 28>{});
        SCHED();
        STAMP(15);
        WAIT_LGKM0();
#pragma unroll
        for (int v = 0; v < 8; ++v) {
            LAS char* sl = sm + w2i + (v < 6 ? v * 2048 : L_SLOT + (v - 6) * 2048) + lw * 16;
            const f32x16& t = ac2[v >> 2][(v >> 1) & 1];
            const int r0 = 8 * (v & 1);
            *(LAS f32x4*)sl = (f32x4){t[r0], t[r0 + 1], t[r0 + 2], t[r0 + 3]};
            *(LAS f32x4*)(sl + 1024) = (f32x4){t[r0 + 4], t[r0 + 5], t[r0 + 6], t[r0 + 7]};
        }
        WAIT_BAR("lgkmcnt(0)");
        STAMP(16);
        STAMP(17);
        STAMP_OUT(18);
    } else {
        u32x4 xvo;
#pragma unroll
        for (int j = 0; j < 4; ++j) xvo[j] = (unsigned)((8 * j + (lane >> 3)) * D + ((((lane & 7) ^ (lane >> 4)) ^ (4 * (j & 1))) * 8)) * 2u - 1024u * j;
        const u16* xb = sptr(p.x, (mt * 128 + wq * 32) * D);
        auto group_x = [&](int si) { glds16x4(sptr(xb, kst(si) * 64), xvo, lb + ring(si) + wq * 4096); };
        group_x(0);
        group_x(1); group_w(1, 0); group_w(1, 1);
        const int cbi = L_CONV + chn * C_CH;
        LAS char* cb = sm + cbi;
        unsigned kvo[2], uso;
#pragma unroll
        for (int rd = 0; rd < 2; ++rd) kvo[rd] = (unsigned)((lane >> 3) * D + (((lane & 7) ^ (((lane >> 4) & 1) | (((2 * wq + rd) & 3) << 1))) * 8)) * 2u;
        uso = WS_U + (unsigned)(kc0 + wq * 16 + (lane >> 3)) * 8192u + (unsigned)(1984 - kr0 + (lane & 7) * 8) * 2u;
        int kro = lb + L_KVO + wq * 2048 + (lane >> 3) * 128 + (((lane & 7) ^ (lane >> 3)) * 16);
        const int cc = 2 * par + ((lane >> 2) & 1);
        int zo = cbi + C_U + par * C_US + (248 + (lane < 36 ? lane : 35)) * 16;
        zo = lane < 8 ? cbi + cc * C_HS + (lane & 3) * 16 : zo;
        u32x4 w2o = w2_off();
        const unsigned fo = lane * 16u;
        unsigned ao = (wq < 2 ? WS_CSUM + (unsigned)(nt * 2 + wq) * 1024u : WS_STATS + mt * 1024u) + fo;
        asm volatile("" : "+v"(zo), "+v"(w2o), "+v"(kvo[0]), "+v"(kvo[1]), "+v"(uso), "+v"(kro), "+v"(ao));
        unsigned eo = (lane & 1) * 4u;
        asm volatile("" : "+v"(eo));
        WAIT_VM(16);
        unsigned tag32, taghi;
        u32x4 bval, ug[2];
        unsigned dbits = *(LAS u16*)(sm + wave * 1024 + 240 + (dch & 7) * 2);
        {
            const u32x4 h0 = lds128(sm + wave * 1024 + 224);
            const bool s1 = !(cc & 1), s2 = !(cc & 2);
            const unsigned a0 = s1 ? 0u : h0[0], a1 = s1 ? h0[0] : h0[1], a2 = s1 ? h0[1] : h0[2], a3 = s1 ? h0[2] : h0[3];
            const unsigned lm = (lane == 3 || lane == 7) ? 0xffffffffu : 0u;
            bval = (u32x4){(s2 ? 0u : a0) & lm, (s2 ? 0u : a1) & lm, (s2 ? a0 : a2) & lm, (s2 ? a1 : a3) & lm};
            asm volatile("" : "+v"(bval), "+v"(dbits));
        }
        auto kv_store = [&](int rd) {
            LAS unsigned* kp = (LAS unsigned*)(lp(kro) + rd * 1024);
            ug[0][1] = kp[0]; asm volatile("" ::: "memory"); ug[0][3] = kp[1]; asm volatile("" ::: "memory");
            ug[1][1] = kp[2]; asm volatile("" ::: "memory"); ug[1][3] = kp[3];
            __builtin_amdgcn_raw_buffer_store_b128(ug[0], wsr, uso, rd * 65536, 16);
            __builtin_amdgcn_raw_buffer_store_b128(ug[1], wsr, uso, rd * 65536 + 4096, 16);
        };
        WAIT_BAR("vmcnt(12) lgkmcnt(0)");
        STAMP(1);
#pragma unroll
        for (int w = 0; w < 5; ++w) {
            if (w == 3) glds4c(p.ws + WS_EPOCH, eo, lb + L_RAW + wq * 1024);
            group_x(w + 2); group_w(w + 2, 0); group_w(w + 2, 1);
            if (w == KVW) {
#pragma unroll
                for (int j = 0; j < 2; ++j) {
                    glds16(sptr(p.k, (kr0 + (2 * wq + j) * 8) * D + kc0), kvo[j], lb + L_KV + (2 * wq + j) * 1024);
                    glds16(sptr(p.v, (kr0 + (2 * wq + j) * 8) * D + kc0), kvo[j], lb + L_KV + 8192 + (2 * wq + j) * 1024);
                }
            }
            if (w == 4) {
                const u32x2 ev = *(LAS u32x2*)(sm + L_RAW + wq * 1024);
                tag32 = __builtin_amdgcn_readfirstlane(ev[0]) ^ (unsigned)TAG_SALT;
                taghi = __builtin_amdgcn_readfirstlane(ev[1]) ^ (unsigned)(TAG_SALT >> 32);
                asm volatile("s_nop 1" : "+s"(tag32), "+s"(taghi));
                ug[0][0] = vmov1(tag32); ug[0][2] = vmov1(tag32); ug[1][0] = vmov1(tag32); ug[1][2] = vmov1(tag32);
                if (lane < 34) *(LAS unsigned*)(sm + L_CTL + lane * 4) = lane < 32 ? 0u : lane == 32 ? tag32 : taghi;
            }
            if (w == 4 && KVW == 0) kv_store(0);
            if (w == 2) STAMP(2);
            STAMPG(2 + 2 * w);
            if (w == KVW) WAIT_BAR("vmcnt(16) lgkmcnt(0)"); else WAIT_BAR("vmcnt(12) lgkmcnt(0)");
            STAMPG(3 + 2 * w);
            if (w == 2) STAMP(3);
        }
        glds16c(p.ws, ao, lb + L_RAW + wq * 1024);
#pragma unroll
        for (int i = 0; i < 3; ++i) pair_w2(w2o, 2 * i, w2i + i * 4096);
        kv_store(KVW ? 0 : 1);
        STAMPG(12); WAIT_BAR("vmcnt(12) lgkmcnt(0)"); STAMPG(13);
        if (KVW) kv_store(1);
        asm volatile("" ::: "memory");
        if (lane == 0) __hip_atomic_fetch_add((LAS unsigned*)(sm + L_CTL + 12), 1u, __ATOMIC_RELAXED, __HIP_MEMORY_SCOPE_WORKGROUP);
        if (wq < 3) {
            u32x4 av = lds128(sm + L_RAW + wq * 1024 + lane * 16);
#pragma unroll 1
            for (unsigned spins = 0; spins < SPIN_CAP; ++spins) {
                if (__all(((av[0] ^ tag32) | (av[2] ^ tag32)) == 0u)) break;
                asm volatile("" ::: "memory");
                av = __builtin_amdgcn_raw_buffer_load_b128(wsr, ao, 0, 16);
                asm volatile("" : "+v"(av));
            }
            *(LAS u32x2*)(sm + L_CSUM + wq * 512 + lane * 8) = (u32x2){av[1], av[3]};
            asm volatile("" ::: "memory");
            if (lane == 0) __hip_atomic_fetch_add((LAS unsigned*)(sm + L_CTL + 64), 1u, __ATOMIC_RELAXED, __HIP_MEMORY_SCOPE_WORKGROUP);
        }
        STAMP(4);
#pragma unroll
        for (int i = 0; i < 3; ++i) pair_w2(w2o, 6 + 2 * i, w2i + L_SLOT + i * 4096);
        WAIT_VM(12);
        __hip_atomic_store((LAS unsigned*)(sm + L_CTL + 48 + wq * 4), 1u, __ATOMIC_RELAXED, __HIP_MEMORY_SCOPE_WORKGROUP);
        wait_lds_eq(sm + L_CTL + 12, 8u);
        STAMP(5);
        __builtin_amdgcn_s_setprio(2);
        const unsigned ubase = WS_U + (unsigned)dch * 8192u;
        glds16x2c(p.ws + ubase + par * 2048, fo, lb + cbi + C_U + par * 2048);
        glds16x2c(p.ws + ubase + 4096 + par * 2048, fo, lb + cbi + C_U + C_US + par * 2048);
        glds4c(p.ws + ubase + (1 - par) * 2048, eo, lb + L_UNX + wq * 256);
        {
            const u32x4 fvo = {fo, fo, fo, lane == 63 ? 62u * 16u : fo};
#pragma unroll
            for (int c = 0; c < 2; ++c) glds16x4(sptr(p.hk, dch * S + 4 * par + 2 * c + 2), fvo, lb + cbi + (2 * par + c) * C_HS + 64);
        }
        *(LAS u32x4*)(sm + zo) = bval;
        WAIT_VM(13);
        __hip_atomic_store((LAS unsigned*)(sm + L_CTL + 112 + wq * 4), 1u, __ATOMIC_RELAXED, __HIP_MEMORY_SCOPE_WORKGROUP);
        WAIT_VM(8);
        {
            u32x4 ua[2], ub[2]; u32x2 un[2];
#pragma unroll
            for (int rd = 0; rd < 2; ++rd) {
                const int x8 = 128 * par + 64 * rd + lane;
                ua[rd] = lds128(cb + C_U + x8 * 16); ub[rd] = lds128(cb + C_U + C_US + x8 * 16);
                un[rd] = *(LAS u32x2*)((rd == 1 && lane == 63) ? sm + L_UNX + wq * 256 : cb + C_U + x8 * 16 + 16);
            }
#pragma unroll 1
            for (unsigned spins = 0; spins < SPIN_CAP; ++spins) {
                unsigned df = 0;
#pragma unroll
                for (int rd = 0; rd < 2; ++rd) df |= (ua[rd][0] ^ tag32) | (ua[rd][2] ^ tag32) | (ub[rd][0] ^ tag32) | (ub[rd][2] ^ tag32) | (un[rd][0] ^ tag32);
                if (__all(df == 0u)) break;
                __builtin_amdgcn_s_sleep(URETRY);
                asm volatile("" ::: "memory");
#pragma unroll
                for (int rd = 0; rd < 2; ++rd) {
                    const int x8 = 128 * par + 64 * rd + lane;
                    const unsigned uo = ubase + (unsigned)x8 * 16u;
                    ua[rd] = __builtin_amdgcn_raw_buffer_load_b128(wsr, uo, 0, 16);
                    ub[rd] = __builtin_amdgcn_raw_buffer_load_b128(wsr, uo + 4096u, 0, 16);
                    un[rd] = __builtin_amdgcn_raw_buffer_load_b64(wsr, x8 == 255 ? uo : uo + 16u, 0, 16);
                }
                asm volatile("" : "+v"(ua[0]), "+v"(ub[0]), "+v"(un[0]), "+v"(ua[1]), "+v"(ub[1]), "+v"(un[1]));
            }
#pragma unroll
            for (int rd = 0; rd < 2; ++rd) {
                const int x8 = 128 * par + 64 * rd + lane;
                const unsigned nx = x8 == 255 ? 0u : un[rd][1];
                *(LAS u32x4*)(cb + C_U + x8 * 16) = (u32x4){__builtin_amdgcn_alignbit(ua[rd][3], ua[rd][1], 16), __builtin_amdgcn_alignbit(ub[rd][1], ua[rd][3], 16),
                                                            __builtin_amdgcn_alignbit(ub[rd][3], ub[rd][1], 16), __builtin_amdgcn_alignbit(nx, ub[rd][3], 16)};
                *(LAS u32x4*)(cb + C_U + C_US + x8 * 16) = (u32x4){ua[rd][1], ua[rd][3], ub[rd][1], ub[rd][3]};
            }
        }
        asm volatile("s_waitcnt vmcnt(0) lgkmcnt(0)" ::: "memory");
        __hip_atomic_store((LAS unsigned*)(sm + L_CTL + 16 + wq * 4), 1u, __ATOMIC_RELAXED, __HIP_MEMORY_SCOPE_WORKGROUP);
        u32x4 lw2[8], xv[2];
        {
            const __amdgpu_buffer_rsrc_t wr2 = __builtin_amdgcn_make_buffer_rsrc((void*)p.w2, 0, F * D * 2, 0x00020000);
#pragma unroll
            for (int j = 0; j < 8; ++j) {
                int t = 24 + (j >> 2) * 2 + rot2; t = t >= 28 ? t - 28 : t;
                lw2[j] = __builtin_amdgcn_raw_buffer_load_b128(wr2, w2o[0], ((wq * 448 + t * 16 + (j & 3) * 8) * D + n64 * 64) * 2, 0);
            }
        }
        const int lf = fresh_lane();
        const int srow = 64 * m64 + 32 * (wq & 1) + (lf & 31);
        const int dd = 64 * n64 + 32 * (wq >> 1) + 16 * (lf >> 5);
        const unsigned xoff = (unsigned)(srow * D + dd) * 2u;
        {
            const __amdgpu_buffer_rsrc_t xr = __builtin_amdgcn_make_buffer_rsrc((void*)p.x, 0, S * D * 2, 0x00020000);
            xv[0] = __builtin_amdgcn_raw_buffer_load_b128(xr, xoff, 0, 0);
            xv[1] = __builtin_amdgcn_raw_buffer_load_b128(xr, xoff, 16, 0);
        }
        SCHED();
        wait_lds(sm + L_CTL + 16 + (wq ^ 1) * 4);
        STAMP(7);
        STAMP(6);
        const int n16 = lane & 15, kg = lane >> 4;
        f32x4 ca[4];
#pragma unroll
        for (int j = 0; j < 4; ++j) ca[j] = (f32x4){0.f, 0.f, 0.f, 0.f};
        int apo = lb + cbi + (n16 & 3) * C_HS + ((n16 >> 2) + kg) * 16;
        int bpo = lb + cbi + C_U + (n16 & 1) * C_US + (252 - 4 * (n16 >> 1) + kg) * 16 - 7 * 512;
        u32x4 fa[2][8], fb[2][8];
#pragma unroll
        for (int i = 0; i < 8; ++i) { fa[0][i] = lds128(lp(apo) + i * 512); fb[0][i] = lds128(lp(bpo) + (7 - i) * 512); }
        const bool odd = lf & 1;
        int ysrc = (2 * (lf >> 3) + (lf & 1) + 16 * ((lf >> 1) & 3)) * 4;
        unsigned yso = WS_Y + (unsigned)dch * 8192u + lf * 16u;
        f32x4 kd[4];
        {
            const float dval = bflo(dbits);
            const int sl0 = 32 * ((lf & 15) >> 1) + 8 * (lf >> 4) + 4 * (lf & 1);
#pragma unroll
            for (int j = 0; j < 4; ++j) {
                const int tj = (j == 0 ? 0 : j == 1 ? 3 : j == 2 ? 4 : 7) ^ par;
                const u32x2 kvr = *(LAS u32x2*)(cb + C_U + C_US + (2044 - 256 * tj - sl0) * 2);
                const unsigned ka = pk(bfhi(kvr[1]) * dval, bflo(kvr[1]) * dval), kb = pk(bfhi(kvr[0]) * dval, bflo(kvr[0]) * dval);
                kd[j] = (f32x4){bflo(ka), bfhi(ka), bflo(kb), bfhi(kb)};
                asm volatile("" : "+v"(kd[j]));
            }
            asm volatile("" : "+v"(ysrc), "+v"(yso));
        }
        wait_lds(sm + L_CTL + 32 + wq * 4);
        STAMP(8);
        auto conv_all = [&](auto pc_) {
            constexpr int P = decltype(pc_)::value;
            static_for([&](auto rc) {
                constexpr int r = decltype(rc)::value, c = r & 1, n = c ^ 1;
                static_for([&](auto kc) {
                    constexpr int k = decltype(kc)::value;
                    constexpr int T[4] = {P, 3 - P, 4 + P, 7 - P};
                    constexpr int j = k <= T[0] ? 0 : k <= T[0] + T[1] + 1 ? 1 : k <= T[0] + T[1] + T[2] + 2 ? 2 : 3;
                    constexpr int pp = k - (j == 0 ? 0 : j == 1 ? T[0] + 1 : j == 2 ? T[0] + T[1] + 2 : T[0] + T[1] + T[2] + 3);
                    ca[j] = mfma16(fa[c][pp], fb[c][T[j] - pp], ca[j]);
                    SCHED();
                    if constexpr (r < 7 && k < 2 * (T[3] + 1)) {
                        if constexpr (k & 1) fb[n][k >> 1] = lds128(lp(bpo) + (r + 1) * 64 + (7 - (k >> 1)) * 512);
                        else fa[n][k >> 1] = lds128(lp(apo) + (r + 1) * 64 + (k >> 1) * 512);
                    }
                    SCHED();
                }, std::make_integer_sequence<int, 18>{});
                asm volatile("" : "+v"(apo), "+v"(bpo));
            }, std::make_integer_sequence<int, 8>{});
        };
        if (par) conv_all(std::integral_constant<int, 1>{}); else conv_all(std::integral_constant<int, 0>{});
        SCHED();
        STAMP(9);
        {
            float r0[4], r1[4];
#pragma unroll
            for (int j = 0; j < 4; ++j) { r0[j] = dpp_xor1(odd ? ca[j][0] : ca[j][2]); r1[j] = dpp_xor1(odd ? ca[j][1] : ca[j][3]); }
            unsigned rp[4][2];
#pragma unroll
            for (int j = 0; j < 4; ++j) {
                const unsigned ya = pk(odd ? r0[j] : ca[j][0], odd ? ca[j][2] : r0[j]), yb = pk(odd ? r1[j] : ca[j][1], odd ? ca[j][3] : r1[j]);
                rp[j][0] = __builtin_amdgcn_ds_bpermute(ysrc, pk(add1(bflo1(ya), kd[j][0]), add1(bfhi1(ya), kd[j][1])));
                rp[j][1] = __builtin_amdgcn_ds_bpermute(ysrc, pk(add1(bflo1(yb), kd[j][2]), add1(bfhi1(yb), kd[j][3])));
            }
#pragma unroll
            for (int j = 0; j < 4; ++j)
                __builtin_amdgcn_raw_buffer_store_b128((u32x4){vmov1(tag32), rp[j][0], vmov1(tag32), rp[j][1]}, wsr, yso,
                                                       ((j == 0 ? 0 : j == 1 ? 3 : j == 2 ? 4 : 7) ^ par) * 1024, 16);
        }
        STAMP(10);
        WAIT_LGKM0();
        if (lane == 0) __hip_atomic_fetch_add((LAS unsigned*)(sm + L_CTL + 8), 1u, __ATOMIC_RELAXED, __HIP_MEMORY_SCOPE_WORKGROUP);
#pragma unroll 1
        for (unsigned spins = 0; spins < (1u << 22); ++spins) {
            if (__builtin_amdgcn_readfirstlane(__hip_atomic_load((LAS unsigned*)(sm + L_CTL + 8), __ATOMIC_RELAXED, __HIP_MEMORY_SCOPE_WORKGROUP)) == 4u) break;
            __builtin_amdgcn_s_sleep(1);
        }
        asm volatile("" ::: "memory");
#pragma unroll
        for (int j = 0; j < 8; ++j) *(LAS u32x4*)(sm + latei + j * 1024 + lf * 16) = lw2[j];
        asm volatile("" ::: "memory");
        __hip_atomic_store((LAS unsigned*)(sm + L_CTL + 80 + wq * 4), 1u, __ATOMIC_RELAXED, __HIP_MEMORY_SCOPE_WORKGROUP);
        STAMP(11);
        __builtin_amdgcn_s_sleep(YSLEEP);
        u32x2 yv[16], stv;
        const int yl = L_CONV + 32768 + wq * 4096;
        const unsigned yg = (unsigned)(lf >> 3) * 8192u + (unsigned)(lf & 7) * 16u;
        const u32x4 yvo = {yg, yg + (65536u - 1024u), yg + 2 * (65536u - 1024u), yg + 3 * (65536u - 1024u)};
        const char* ybase = p.ws + WS_Y + (unsigned)(64 * n64 + 32 * (wq >> 1)) * 8192u + (unsigned)(64 * m64 + 32 * (wq & 1)) * 4u;
#pragma unroll 1
        for (unsigned spins = 0; spins < SPIN_CAP; ++spins) {
            glds16x4c(ybase, yvo, lb + yl);
            stv = __builtin_amdgcn_raw_buffer_load_b64(wsr, WS_STATS + (unsigned)srow * 8u, 0, 16);
            WAIT_VM(0);
#pragma unroll
            for (int i = 0; i < 16; ++i) yv[i] = *(LAS u32x2*)(sm + yl + ((lf >> 5) * 16 + i) * 128 + ((lf & 31) >> 1) * 8);
            unsigned df = stv[0] ^ tag32;
#pragma unroll
            for (int i = 0; i < 16; ++i) df |= yv[i][0] ^ tag32;
            if (__all(df == 0u)) break;
            __builtin_amdgcn_s_sleep(8);
        }
        STAMP(14);
        __builtin_amdgcn_s_setprio(0);
        f32x2 sf[8], xf[8];
        {
            const float nmb = -bflo(stv[1]);
            float ri = __builtin_amdgcn_rcpf(bfhi(stv[1]));
            asm volatile("s_nop 1" : "+v"(ri));
            const unsigned ysh = (srow & 1) ? 0u : 16u;
#pragma unroll
            for (int i = 0; i < 8; ++i) {
                const unsigned xd = xv[i >> 2][i & 3];
                xf[i] = (f32x2){bflo1(xd), bfhi1(xd)};
                const unsigned tp = pk(add1(xf[i][0], nmb), add1(xf[i][1], nmb));
                const unsigned np = pk(mul1(bflo1(tp), ri), mul1(bfhi1(tp), ri));
                sf[i] = (f32x2){mul1(bflo1(np), bfhi1(yv[2 * i][1] << ysh)), mul1(bfhi1(np), bfhi1(yv[2 * i + 1][1] << ysh))};
                asm volatile("" : "+v"(sf[i]), "+v"(xf[i]));
            }
        }
        const int rm = lb + L_W2 + (wq < 3 ? wq * 4096 : L_SLOT) + (lf >> 5) * 2048 + (lf & 31) * 16;
        STAMP(15);
        WAIT_BAR("lgkmcnt(0)");
        STAMP(16);
        f32x4 o[4];
#pragma unroll
        for (int j = 0; j < 4; ++j) {
            LAS char* rp = lp(rm) + j * 512;
            o[j] = (*(LAS f32x4*)rp + *(LAS f32x4*)(rp + 12288)) + (*(LAS f32x4*)(rp + 24576) + *(LAS f32x4*)(rp + 36864));
        }
        f32x2 so[8], ev[8];
#pragma unroll
        for (int i = 0; i < 8; ++i) {
            const f32x2 ov = {o[i >> 1][(i & 1) * 2], o[i >> 1][(i & 1) * 2 + 1]};
            const f32x2 t = ov * (f32x2){-1.44269504088896341f, -1.44269504088896341f};
            ev[i] = (f32x2){__builtin_amdgcn_exp2f(t[0]), __builtin_amdgcn_exp2f(t[1])};
            so[i] = sf[i] * ov;
        }
        SCHED();
        u32x4 st[2];
#pragma unroll
        for (int i = 0; i < 8; ++i) {
            const f32x2 dn = ev[i] + (f32x2){1.f, 1.f};
            const f32x2 r = __builtin_elementwise_fma(so[i], (f32x2){__builtin_amdgcn_rcpf(dn[0]), __builtin_amdgcn_rcpf(dn[1])}, xf[i]);
            st[i >> 2][i & 3] = pk(r[0], r[1]);
        }
        const __amdgpu_buffer_rsrc_t orr = __builtin_amdgcn_make_buffer_rsrc((void*)p.out, 0, S * D * 2, 0x00020000);
        __builtin_amdgcn_raw_buffer_store_b128(st[0], orr, xoff, 0, 0);
        __builtin_amdgcn_raw_buffer_store_b128(st[1], orr, xoff, 16, 0);
        STAMP(17);
        STAMP_OUT(18);
    }
}

extern "C" void kernel_launch(void* const* d_in, const int* in_sizes, int n_in,
                              void* d_out, int out_size, void* d_ws, size_t ws_size,
                              hipStream_t stream) {
    Params p;
    p.x = (const u16*)d_in[0]; p.k = (const u16*)d_in[1]; p.v = (const u16*)d_in[2];
    p.sk = (const u16*)d_in[3]; p.hk = (const u16*)d_in[4]; p.dv = (const u16*)d_in[5];
    p.w1 = (const u16*)d_in[7]; p.w3 = (const u16*)d_in[8]; p.w2 = (const u16*)d_in[9];
    p.out = (u16*)d_out; p.ws = (char*)d_ws;
    (void)hipFuncSetAttribute((const void*)fused_kernel, hipFuncAttributeMaxDynamicSharedMemorySize, L_TOTAL);
    fused_kernel<<<256, 512, L_TOTAL, stream>>>(p);
}
```

```cpp
#include <hip/hip_runtime.h>
#include <stdint.h>
#include <utility>

typedef unsigned short u16;
typedef unsigned long long u64;
typedef short s16x4 __attribute__((ext_vector_type(4)));
typedef short s16x8 __attribute__((ext_vector_type(8)));
typedef __bf16 bf16x2 __attribute__((ext_vector_type(2)));
typedef __bf16 bf16x8 __attribute__((ext_vector_type(8)));
typedef float f32x2 __attribute__((ext_vector_type(2)));
typedef float f32x4 __attribute__((ext_vector_type(4)));
typedef float f32x16 __attribute__((ext_vector_type(16)));
typedef unsigned u32x2 __attribute__((ext_vector_type(2)));
typedef unsigned u32x4 __attribute__((ext_vector_type(4)));
typedef unsigned u32x16 __attribute__((ext_vector_type(16)));
#define LAS __attribute__((address_space(3)))
#define GAS __attribute__((address_space(1)))
#define RLX_AGENT __ATOMIC_RELAXED, __HIP_MEMORY_SCOPE_AGENT

constexpr int S = 2048, D = 448, F = 1792;

constexpr unsigned WS_EPOCH = 0;
constexpr unsigned WS_FH = 65536;
constexpr unsigned WS_STATS = 8192;
constexpr unsigned WS_CSUM = 24576;
constexpr unsigned WS_U = 262144;
constexpr unsigned WS_Y = WS_U;
constexpr unsigned WS_H = WS_Y + D * S * 4;
constexpr unsigned WS_END = WS_H + 64 * 112 * 1024;
constexpr unsigned WS_DBG = WS_END;

constexpr int L_KV = 0;
constexpr int L_CTL = 0;
constexpr int L_CSUM = 256;
constexpr int L_STAT = 1280;
constexpr int L_RAW = 2048;
constexpr int L_KVO = 8192;
constexpr int L_UNX = 6144;
constexpr int L_SLOT = 49152;
constexpr int L_CONV = 12288;
constexpr int C_CH = 26112, C_HS = 4160, C_U = 16640, C_US = 4640;
constexpr int L_W2 = 16384 + L_SLOT;
constexpr int L_TOTAL = 163840;
__host__ __device__ constexpr int ring(int stage) { return 16384 + (stage % 3) * L_SLOT; }
constexpr unsigned CODE_PREFETCH = 36864;
#define POLL_SLEEP 4
#define HDEPTH 6
#define ROT2 (8 * ml)
#define MPRIO 1
#define EPARK 0
#define YSLEEP 16
#define TOUCH 1
#define TOUCH0 4
#define HGROW 4
#define ROTM 6
#define KVW 0
#define MWARM 0
#define URETRY 4
#define KVNT " nt"
#define FLAGW 1
#define AUXW2 1
#define NTOUCH 2
#define ROT1 ((2 * ml + nl) % ROTM)

constexpr u64 TAG_SALT = 0x9E3779B97F4A7C15ull;
constexpr unsigned Y_TAG = 0x80000000u;
constexpr unsigned SPIN_CAP = 1u << 18;

struct Params {
    const u16* x; const u16* k; const u16* v; const u16* sk; const u16* hk; const u16* dv;
    const u16* w1; const u16* w3; const u16* w2; u16* out; char* ws;
};

__device__ __forceinline__ float bflo(unsigned p) { return __uint_as_float(p << 16); }
__device__ __forceinline__ float bfhi(unsigned p) { return __uint_as_float(p & 0xffff0000u); }
__device__ __forceinline__ float bflo1(unsigned p) { float r; asm("v_lshlrev_b32 %0, 16, %1" : "=v"(r) : "v"(p)); return r; }
__device__ __forceinline__ float bfhi1(unsigned p) { float r; asm("v_and_b32 %0, 0xffff0000, %1" : "=v"(r) : "v"(p)); return r; }
__device__ __forceinline__ float mul1(float a, float b) { float r; asm("v_mul_f32 %0, %1, %2" : "=v"(r) : "v"(a), "v"(b)); return r; }
__device__ __forceinline__ void fmac1(float& d, float a, float b) { asm("v_fmac_f32 %0, %1, %2" : "+v"(d) : "v"(a), "v"(b)); }
__device__ __forceinline__ float dpp_xor1(float v) {
    return __int_as_float(__builtin_amdgcn_update_dpp(0, __float_as_int(v), 0xB1, 0xf, 0xf, true));
}
__device__ __forceinline__ float add1(float a, float b) { float r; asm("v_add_f32 %0, %1, %2" : "=v"(r) : "v"(a), "v"(b)); return r; }
__device__ __forceinline__ unsigned vmov1(unsigned s) { unsigned r; asm volatile("v_mov_b32 %0, %1" : "=v"(r) : "s"(s)); return r; }
__device__ __forceinline__ unsigned pk(float a, float b) {
    f32x2 f = {a, b};
    return __builtin_bit_cast(unsigned, __builtin_convertvector(f, bf16x2));
}
__device__ __forceinline__ float rbf(float a) { return bflo(pk(a, a)); }
__device__ __forceinline__ float dot2(unsigned a, unsigned b, float c) {
    return __builtin_amdgcn_fdot2_f32_bf16(__builtin_bit_cast(bf16x2, a), __builtin_bit_cast(bf16x2, b), c, false);
}
__device__ __forceinline__ void glds16(const void* base, unsigned voff, unsigned ldst) {
    u64 t;
    asm volatile("s_mov_b32 m0, %3\n\ts_mov_b64 %0, %2\n\tglobal_load_lds_dwordx4 %1, %0" KVNT : "=&s"(t) : "v"(voff), "s"(base), "s"(ldst));
}
__device__ __forceinline__ void glds16x4(const void* base, u32x4 vo, unsigned ldst) {
    u64 t;
    asm volatile("s_mov_b32 m0, %6\n\ts_mov_b64 %0, %5\n\tglobal_load_lds_dwordx4 %1, %0\n\tglobal_load_lds_dwordx4 %2, %0 offset:1024\n\t"
                 "global_load_lds_dwordx4 %3, %0 offset:2048\n\tglobal_load_lds_dwordx4 %4, %0 offset:3072"
                 : "=&s"(t) : "v"(vo[0]), "v"(vo[1]), "v"(vo[2]), "v"(vo[3]), "s"(base), "s"(ldst));
}
__device__ __forceinline__ void glds16x4c(const void* base, u32x4 vo, unsigned ldst) {
    u64 t;
    asm volatile("s_mov_b32 m0, %6\n\ts_mov_b64 %0, %5\n\tglobal_load_lds_dwordx4 %1, %0 sc1\n\tglobal_load_lds_dwordx4 %2, %0 offset:1024 sc1\n\t"
                 "global_load_lds_dwordx4 %3, %0 offset:2048 sc1\n\tglobal_load_lds_dwordx4 %4, %0 offset:3072 sc1"
                 : "=&s"(t) : "v"(vo[0]), "v"(vo[1]), "v"(vo[2]), "v"(vo[3]), "s"(base), "s"(ldst) : "memory");
}
__device__ __forceinline__ void glds16x2c(const void* base, unsigned voff, unsigned ldst) {
    u64 t;
    asm volatile("s_mov_b32 m0, %3\n\ts_mov_b64 %0, %2\n\tglobal_load_lds_dwordx4 %1, %0 sc1\n\tglobal_load_lds_dwordx4 %1, %0 offset:1024 sc1"
                 : "=&s"(t) : "v"(voff), "s"(base), "s"(ldst));
}
__device__ __forceinline__ void glds16c(const void* base, unsigned voff, unsigned ldst) {
    u64 t;
    asm volatile("s_mov_b32 m0, %3\n\ts_mov_b64 %0, %2\n\tglobal_load_lds_dwordx4 %1, %0 sc1" : "=&s"(t) : "v"(voff), "s"(base), "s"(ldst));
}
__device__ __forceinline__ void glds4c(const void* base, unsigned voff, unsigned ldst) {
    u64 t;
    asm volatile("s_mov_b32 m0, %3\n\ts_mov_b64 %0, %2\n\tglobal_load_lds_dword %1, %0 sc1" : "=&s"(t) : "v"(voff), "s"(base), "s"(ldst));
}
__device__ __forceinline__ const u16* sptr(const u16* base, int off) {
    const u16* r = base + __builtin_amdgcn_readfirstlane(off);
    asm("" : "+s"(r));
    return r;
}
__device__ __forceinline__ u64 vsel(bool c, const void* a, u64 b) {
    u64 va = (u64)a;
    asm("" : "+v"(va), "+v"(b));
    return c ? va : b;
}
__device__ __forceinline__ LAS char* lp(int a) { return (LAS char*)(size_t)(unsigned)a; }
__device__ __forceinline__ u32x4 lds128(LAS char* p) { return *(LAS u32x4*)p; }
__device__ __forceinline__ u32x2 ldstr(LAS char* p) {
    return __builtin_bit_cast(u32x2, __builtin_amdgcn_ds_read_tr16_b64_v4i16((LAS s16x4*)p));
}
__device__ __forceinline__ u32x4 ldstr2(LAS char* p, int off) {
    u32x2 a = ldstr(p), b = ldstr(p + off);
    return (u32x4){a.x, a.y, b.x, b.y};
}
__device__ __forceinline__ f32x16 mfma32(u32x4 a, u32x4 b, f32x16 c) {
    return __builtin_amdgcn_mfma_f32_32x32x16_bf16(__builtin_bit_cast(bf16x8, a), __builtin_bit_cast(bf16x8, b), c, 0, 0, 0);
}
__device__ __forceinline__ f32x4 mfma16(u32x4 a, u32x4 b, f32x4 c) {
    return __builtin_amdgcn_mfma_f32_16x16x32_bf16(__builtin_bit_cast(bf16x8, a), __builtin_bit_cast(bf16x8, b), c, 0, 0, 0);
}
__device__ __forceinline__ float silu_f(float g) {
    return g * __builtin_amdgcn_rcpf(1.f + __builtin_amdgcn_exp2f(g * -1.44269504088896341f));
}
template <class Fn, int... I>
__device__ __forceinline__ void static_for(Fn&& fn, std::integer_sequence<int, I...>) { (fn(std::integral_constant<int, I>{}), ...); }
#define WAIT_VM(n) asm volatile("s_waitcnt vmcnt(" #n ")" ::: "memory")
#define WAIT_LGKM0() asm volatile("s_waitcnt lgkmcnt(0)" ::: "memory")
#define WAIT_BAR(w) asm volatile("s_waitcnt " w "\n\ts_barrier" ::: "memory")
#define SCHED() __builtin_amdgcn_sched_barrier(0)
#define CAS __attribute__((address_space(4)))
#define NSTAMP 18

__device__ __forceinline__ void wait_lds(LAS char* word) {
#pragma unroll 1
    for (unsigned spins = 0; spins < (SPIN_CAP << 4); ++spins) {
        if (__builtin_amdgcn_readfirstlane(__hip_atomic_load((LAS unsigned*)word, __ATOMIC_RELAXED, __HIP_MEMORY_SCOPE_WORKGROUP))) break;
        __builtin_amdgcn_s_sleep(1);
    }
    asm volatile("" ::: "memory");
}
__device__ __forceinline__ void wait_lds_eq(LAS char* word, unsigned want) {
#pragma unroll 1
    for (unsigned spins = 0; spins < (1u << 22); ++spins) {
        if ((unsigned)__builtin_amdgcn_readfirstlane(__hip_atomic_load((LAS unsigned*)word, __ATOMIC_RELAXED, __HIP_MEMORY_SCOPE_WORKGROUP)) == want) break;
        __builtin_amdgcn_s_sleep(1);
    }
    asm volatile("" ::: "memory");
}
#define STAMP(i)
#define STAMPG(i)
#define STAMPA(i)
#define STAMP_DECL()
#define STAMP_OUT(n)

__global__ __launch_bounds__(512, 2) void fused_kernel(Params p) {
    extern __shared__ __attribute__((aligned(16))) char smem_raw[];
    u64 pc;
    asm volatile("s_getpc_b64 %0" : "=s"(pc));
    LAS char* sm = (LAS char*)smem_raw;
    const int tid = threadIdx.x, lane = tid & 63;
    const int wave = __builtin_amdgcn_readfirstlane(tid >> 6);
    const int xcd = blockIdx.x & 7, idx = blockIdx.x >> 3;
    if (idx == 0 && wave < 5) {
        const unsigned o = (unsigned)tid * 128u;
        asm volatile("s_mov_b32 m0, %2\n\ts_nop 0\n\tglobal_load_lds_dword %0, %1" :: "v"(o < CODE_PREFETCH ? o : 0u), "s"(pc & ~(u64)255),
                     "s"((unsigned)(size_t)sm + 32768u));
    }
    SCHED();
    const __amdgpu_buffer_rsrc_t wsr = __builtin_amdgcn_make_buffer_rsrc(p.ws, 0, WS_END, 0x00020000);
    asm volatile("" :: "s"(p.x), "s"(p.k), "s"(p.v), "s"(p.sk), "s"(p.hk), "s"(p.dv), "s"(p.w1), "s"(p.w3), "s"(p.w2), "s"(p.out), "s"(p.ws));
    STAMP_DECL();
    STAMP(0);

    if (idx < 4) {
        const int ca = (xcd >> 1) * 4 + idx, cmat = ca >= 7, cnt = 7 * (xcd & 1) + (ca >= 7 ? ca - 7 : ca);
        const bool job = ca < 14;
        u32x4 xr[7], wr[14];
        const int row = (8 * (xcd >> 1) + 4 * (xcd & 1) + idx) * 64 + wave * 8 + (lane >> 3);
#pragma unroll
        for (int i = 0; i < 7; ++i) xr[i] = *(const u32x4*)(p.x + (size_t)row * D + i * 64 + (lane & 7) * 8);
        const u64 epoch = __hip_atomic_load((GAS u64*)(p.ws + WS_EPOCH), RLX_AGENT);
        const u64 tag = epoch ^ TAG_SALT;
        const unsigned tag32 = (unsigned)tag;
        {
            float s4[4] = {0.f, 0.f, 0.f, 0.f};
#pragma unroll
            for (int i = 0; i < 7; ++i)
#pragma unroll
                for (int j = 0; j < 4; ++j) s4[j] = dot2(xr[i][j], 0x3f803f80u, s4[j]);
            float sum = (s4[0] + s4[1]) + (s4[2] + s4[3]);
            sum += __shfl_xor(sum, 1); sum += __shfl_xor(sum, 2); sum += __shfl_xor(sum, 4);
            const float mean = sum * (1.f / D);
            f32x2 q4[4] = {{0.f, 0.f}, {0.f, 0.f}, {0.f, 0.f}, {0.f, 0.f}};
#pragma unroll
            for (int i = 0; i < 7; ++i)
#pragma unroll
                for (int j = 0; j < 4; ++j) {
                    const f32x2 d = (f32x2){bflo(xr[i][j]), bfhi(xr[i][j])} - (f32x2){mean, mean};
                    q4[j] = __builtin_elementwise_fma(d, d, q4[j]);
                }
            const f32x2 q2 = (q4[0] + q4[1]) + (q4[2] + q4[3]);
            float sq = q2[0] + q2[1];
            sq += __shfl_xor(sq, 1); sq += __shfl_xor(sq, 2); sq += __shfl_xor(sq, 4);
            const u32x2 st = {tag32, pk(mean, __builtin_amdgcn_sqrtf(rbf(rbf(sq * (1.f / D)) + 1e-5f)))};
            __builtin_amdgcn_raw_buffer_store_b64(st, wsr, (lane & 7) == 0 ? WS_STATS + (unsigned)row * 8u : 0xfffffff0u, 0, 16);
        }
        STAMPA(2);
        if (job) {
            SCHED();
            const u16* wp = (cmat ? p.w3 : p.w1) + (size_t)(wave * 4 + (lane >> 4)) * F + cnt * 128 + (lane & 15) * 8;
#pragma unroll
            for (int i = 0; i < 14; ++i) wr[i] = *(const u32x4*)(wp + (size_t)(32 * i) * F);
        }
        if (job) {
            float c[8];
#pragma unroll
            for (int j = 0; j < 8; ++j) c[j] = 0.f;
#pragma unroll
            for (int i = 0; i < 14; ++i)
#pragma unroll
                for (int j = 0; j < 4; ++j) { c[2 * j] += bflo(wr[i][j]); c[2 * j + 1] += bfhi(wr[i][j]); }
            LAS char* cp = sm + ((wave * 4 + (lane >> 4)) * 136 + (lane & 15) * 8) * 4;
            *(LAS f32x4*)cp = (f32x4){c[0], c[1], c[2], c[3]};
            *(LAS f32x4*)(cp + 16) = (f32x4){c[4], c[5], c[6], c[7]};
        }
        STAMPA(3);
        if (job) {
            WAIT_BAR("lgkmcnt(0)");
            float t = 0.f;
#pragma unroll
            for (int i = 0; i < 8; ++i) t += *(LAS float*)(sm + ((i * 4 + (tid & 3)) * 136 + (tid >> 2)) * 4);
            t += __shfl_xor(t, 1); t += __shfl_xor(t, 2);
            __builtin_amdgcn_raw_buffer_store_b64((u32x2){tag32, __float_as_uint(t)}, wsr,
                (tid & 3) == 0 ? WS_CSUM + (unsigned)((cnt * 2 + cmat) * 128 + (tid >> 2)) * 8u : 0xfffffff0u, 0, 16);
        }
        STAMP(1);
        SCHED();
        if (!job) __builtin_amdgcn_s_sleep(AUXW2 ? 40 : 80);
        const unsigned pf2 = *(const unsigned*)((const char*)p.hk + (size_t)((xcd * 4 + idx) * 448 + wave * 56 + (lane < 56 ? lane : 0)) * 128);
        if (AUXW2) {
            const unsigned fo = lane * 16u;
            constexpr int PER = ((4 - NTOUCH) * 14336 + 4095) / 4096;
#pragma unroll 1
            for (int i = wave; i < 7 * PER; i += 8) {
                const int g = i / PER, j = i - g * PER;
                int o = NTOUCH * 14336 + j * 4096; o = o > 57344 - 4096 ? 57344 - 4096 : o;
                glds16x4((const char*)p.w2 + (size_t)idx * 401408 + g * 57344 + o, (u32x4){fo, fo, fo, fo}, (unsigned)(size_t)sm + 65536u + wave * 4096);
            }
        }
        asm volatile("" :: "v"(pf2));
        STAMP_OUT(4);
        if (blockIdx.x == 0 && wave == 7) {
            __builtin_amdgcn_s_sleep(127);
            __builtin_amdgcn_s_sleep(100);
            GAS u64* gp = (GAS u64*)(p.ws + WS_STATS) + lane * 8;
            const int fl = lane < 56 ? lane : 0;
            GAS u64* fp = (GAS u64*)(p.ws + WS_FH) + ((fl / 14) * 56 + (fl % 14) * 2) * 16;
#pragma unroll 1
            for (unsigned spins = 0; spins < SPIN_CAP; ++spins) {
                bool ok = true;
#pragma unroll
                for (int m = 0; m < 4; ++m)
                    ok &= ((unsigned)__hip_atomic_load(gp + m * 512, RLX_AGENT) == tag32) & (__hip_atomic_load(fp + m * 224 * 16, RLX_AGENT) == tag);
                if (__all(ok)) break;
                __builtin_amdgcn_s_sleep(32);
            }
            if (lane == 0) __hip_atomic_store((GAS u64*)(p.ws + WS_EPOCH), epoch + 1, RLX_AGENT);
        }
        return;
    }

    const int gi = idx - 4, ml = gi & 3, nl = gi >> 2;
    const int mt = 4 * (xcd >> 1) + ml, nt = 7 * (xcd & 1) + nl;
    const int rot1 = ROT1;
    const int m64 = 4 * xcd + ml, n64 = nl;
    const int gpair = xcd * 28 + gi;
    const int r32 = lane & 31, hh = lane >> 5, blk = (lane >> 4) & 1, q4 = (lane & 15) >> 2, p4 = lane & 3;
    const unsigned lb = (unsigned)(size_t)sm;
    const int wq = wave & 3;
    u32x4 wvo;
    wvo[0] = (unsigned)((lane >> 4) * F + (((lane & 15) ^ ((lane >> 4) << 2)) * 8)) * 2u;
#pragma unroll
    for (int j = 1; j < 4; ++j) wvo[j] = wvo[0] + j * (8 * F - 1024);
    const u16* w1b = sptr(p.w1, wq * 16 * F + nt * 128);
    const u16* w3b = sptr(p.w3, wq * 16 * F + nt * 128);
    auto kst = [&](int si) { if (ROTM == 6 && si == 0) return 0; const int s = si + rot1; return s >= 7 ? s - ROTM : s; };
    auto group_w = [&](int si, int m) { glds16x4(sptr(m ? w3b : w1b, kst(si) * 64 * F), wvo, lb + ring(si) + 16384 + m * 16384 + wq * 4096); };
    const int kr0 = (gpair & 31) * 64, kc0 = (gpair >> 5) * 64;
    const int chn = wq >> 1, par = wq & 1, dch = 2 * gpair + chn;
    if (wave < 4) group_w(0, 0);
    {
        const bool isk = lane >= 8 && lane < 14;
        u64 mb_ = vsel(lane == 15, p.dv, vsel(lane == 14, p.hk, vsel(isk, p.k, (u64)p.sk)));
        unsigned mo = (unsigned)((kc0 + wq * 16) * 8 + (lane & 7) * 16);
        mo = isk ? (unsigned)((kr0 ? kr0 - 3 + ((lane - 8) >> 1) : 0) * D + kc0 + wq * 16 + (lane & 1) * 8) * 2u : mo;
        mo = lane == 14 ? (unsigned)dch * 4096u : mo;
        mo = lane == 15 ? (unsigned)(dch & ~7) * 2u : mo;
        const u64 ma = mb_ + mo;
        asm volatile("s_mov_b32 m0, %1\n\ts_nop 0\n\tglobal_load_lds_dwordx4 %0, off" :: "v"(ma), "s"(lb + wave * 1024));
    }
    if (wave < 4) group_w(0, 1);
    SCHED();
    const int rot2 = ROT2;
    const int w2i = L_W2 + wq * 12288;
    auto fresh_lane = [&]() { int l = lane; asm volatile("" : "+v"(l)); return l; };
    const int latei = L_CONV + wq * 8192;
    auto pair_w2 = [&](u32x4 vo, int i, int dst) {
        int t = i + rot2; t = t >= 28 ? t - 28 : t;
        glds16x4(sptr(p.w2, (wq * 448 + t * 16) * D + n64 * 64), vo, lb + dst);
    };
    auto w2_off = [&]() {
        const int l = fresh_lane();
        const unsigned o = (unsigned)((l >> 3) * D + (((l & 7) ^ (((l >> 4) & 1) << 2)) * 8)) * 2u;
        return (u32x4){o, o + (16 * D - 1024), o + 2 * (16 * D - 1024), o + 3 * (16 * D - 1024)};
    };

    if (wave < 4) {
        __builtin_amdgcn_s_setprio(MPRIO);
        const int wm = wave >> 1, wn = wave & 1;
        int xa[4], wa0, wa1;
#pragma unroll
        for (int i = 0; i < 4; ++i) xa[i] = lb + ring(0) + (((64 * wm + r32) * 128 + ((hh ^ ((r32 >> 1) & 7)) * 16)) ^ (i * 32));
        wa0 = 16384 + (8 * hh + q4) * 256 + (((8 * wn + 2 * blk + (p4 >> 1)) ^ (q4 << 2)) * 16) + 8 * (p4 & 1);
        wa1 = (wa0 ^ 64) + lb + ring(0); wa0 += lb + ring(0);
        f32x16 acc[2][2][2];
#pragma unroll
        for (int i = 0; i < 8; ++i)
#pragma unroll
            for (int r = 0; r < 16; ++r) acc[i >> 2][(i >> 1) & 1][i & 1][r] = 0.f;
        u32x4 fx[2][2], fw[2][2][2];
        auto load_frag = [&](int g, int j) {
            const int pp = g & 3, b = g & 1;
            if (j == 0) {
                if (g >= 4) {
                    const int d = ring(g >> 2) - ring((g >> 2) - 1);
                    if (pp == 0) { wa0 += d; wa1 += d; asm volatile("" : "+v"(wa0), "+v"(wa1)); }
                    xa[pp] += d; asm volatile("" : "+v"(xa[pp]));
                }
                fw[b][0][0] = ldstr2(lp(wa0) + pp * 4096, 1024);
            }
            if (j == 1) { fx[b][0] = lds128(lp(xa[pp])); fx[b][1] = lds128(lp(xa[pp]) + 4096); }
            if (j == 2) fw[b][0][1] = ldstr2(lp(wa0) + pp * 4096 + 16384, 1024);
            if (j == 3) fw[b][1][0] = ldstr2(lp(wa1) + pp * 4096, 1024);
            if (j == 4) fw[b][1][1] = ldstr2(lp(wa1) + pp * 4096 + 16384, 1024);
        };
        int ka0[2], kam[2], kwo;
#pragma unroll
        for (int rd = 0; rd < 2; ++rd) {
            const int ck = 2 * wq + rd;
            const int bq = 2 * (lane >> 4) + (p4 >> 1), bm = bq ? bq - 1 : 0;
            ka0[rd] = lb + L_KV + (8 * bq + q4) * 128 + ((ck ^ (((q4 >> 1) & 1) | ((bq & 3) << 1))) * 16) + 8 * (p4 & 1);
            kam[rd] = lb + L_KV + (8 * bm + 4 + q4) * 128 + ((ck ^ (((q4 >> 1) & 1) | ((bm & 3) << 1))) * 16) + 8 * (p4 & 1);
        }
        kwo = lb + L_KVO + wq * 2048 + (lane & 7) * 128 + (((7 - (lane >> 3)) ^ (lane & 7)) * 16);
        asm volatile("" : "+v"(ka0[0]), "+v"(ka0[1]), "+v"(kam[0]), "+v"(kam[1]), "+v"(kwo));
        WAIT_VM(4);
        u32x2 skv[2], hal[2];
#pragma unroll
        for (int rd = 0; rd < 2; ++rd) {
            LAS char* mp = sm + wave * 1024;
            skv[rd] = *(LAS u32x2*)(mp + (rd * 8 + (lane & 7)) * 8);
            const unsigned hm = kr0 ? 0xffffffffu : 0u;
            const unsigned a = *(LAS u16*)(mp + 128 + rd * 16 + (lane & 7) * 2), b = *(LAS u16*)(mp + 160 + rd * 16 + (lane & 7) * 2),
                           c = *(LAS u16*)(mp + 192 + rd * 16 + (lane & 7) * 2);
            hal[rd] = (u32x2){(a << 16) & hm, (b | (c << 16)) & hm};
            asm volatile("" : "+v"(skv[rd]), "+v"(hal[rd]));
        }
        WAIT_BAR("vmcnt(0) lgkmcnt(0)");
        STAMP(1);
        u32x2 kt[2][3], vt[2][2];
        float cf[4], kk[11], ko[8];
        u32x4 kpk;
        auto fill = [&](auto kc) {
            constexpr int k = decltype(kc)::value;
            if constexpr (k < 4) {
                constexpr int rd = k >> 1;
                if constexpr ((k & 1) == 0) { kt[rd][0] = ldstr(lp(kam[rd])); kt[rd][1] = ldstr(lp(ka0[rd])); kt[rd][2] = ldstr(lp(ka0[rd]) + 512); }
                else { vt[rd][0] = ldstr(lp(ka0[rd]) + 8192); vt[rd][1] = ldstr(lp(ka0[rd]) + 8192 + 512); }
            } else if constexpr (k < 4 + 2 * 16) {
                constexpr int rd = (k - 4) / 16, s = (k - 4) % 16;
                if constexpr (s == 0) { cf[0] = bflo1(skv[rd][0]); cf[1] = bfhi1(skv[rd][0]); cf[2] = bflo1(skv[rd][1]); cf[3] = bfhi1(skv[rd][1]); }
                else if constexpr (s == 1) {
                    const unsigned a = lane < 8 ? hal[rd][0] : kt[rd][0][0], b = lane < 8 ? hal[rd][1] : kt[rd][0][1];
                    kk[0] = bfhi1(a); kk[1] = bflo1(b); kk[2] = bfhi1(b);
                } else if constexpr (s < 4) {
                    const u32x2 t = kt[rd][s - 1];
                    kk[4 * s - 5] = bflo1(t[0]); kk[4 * s - 4] = bfhi1(t[0]); kk[4 * s - 3] = bflo1(t[1]); kk[4 * s - 2] = bfhi1(t[1]);
                } else if constexpr (s < 12) {
                    constexpr int tp = (s - 4) >> 1, o0 = ((s - 4) & 1) * 4;
#pragma unroll
                    for (int i = o0; i < o0 + 4; ++i) {
                        if (tp == 0) ko[i] = mul1(kk[i], cf[3]);
                        else fmac1(ko[i], kk[i + tp], cf[3 - tp]);
                    }
                } else {
                    constexpr int j = s - 12;
                    const unsigned v = vt[rd][j >> 1][j & 1];
                    kpk[3 - j] = pk(mul1(ko[2 * j + 1], bfhi1(v)), mul1(ko[2 * j], bflo1(v)));
                    if constexpr (j == 3) *(LAS u32x4*)(lp(kwo) + rd * 1024) = kpk;
                }
            }
        };
#pragma unroll
        for (int j = 0; j < 5; ++j) load_frag(0, j);
        f32x4 cs[2][2][4];
        unsigned tch[4] = {0u, 0u, 0u, 0u}, tch0 = 0;
        unsigned tc0 = (unsigned)((gpair * 4 + wq) * 14 + (lane < 14 ? lane : 13)) * 128u;
        unsigned tco = (unsigned)(gi * 4 + wq + (lane < 28 ? lane : 27) * 448) * 128u;
        asm volatile("" : "+v"(tco));
        static_for([&](auto gc) {
            constexpr int g = decltype(gc)::value, b = g & 1, pp = g & 3;
            SCHED();
            if (g == 11) STAMP(2);
            if (pp == 3 && g < 27) { STAMPG(2 + 2 * (g >> 2)); WAIT_BAR("lgkmcnt(0)"); STAMPG(3 + 2 * (g >> 2)); }
            if (g == 27) {
                WAIT_LGKM0();
                if (lane == 0) __hip_atomic_fetch_add((LAS unsigned*)(sm + L_CTL + 12), 1u, __ATOMIC_RELAXED, __HIP_MEMORY_SCOPE_WORKGROUP);
            }
            if (g == 11) STAMP(3);
            static_for([&](auto ic) {
                constexpr int i = decltype(ic)::value, ft = i >> 2, m = (i >> 1) & 1, sb = i & 1;
                acc[sb][ft][m] = mfma32(fw[b][ft][m], fx[b][sb], acc[sb][ft][m]);
                SCHED();
                if (g < 27 && i < 5) load_frag(g + 1, i);
                if constexpr (MWARM && i == 6 && g == MWARM - 1) {
                    u64 t;
                    asm volatile("s_mov_b64 %1, %3\n\tglobal_load_dword %0, %2, %1" : "=v"(tch0), "=&s"(t) : "v"(tc0), "s"(p.w2));
                }
                if constexpr (TOUCH && i == 4 && pp == 2 && g >= TOUCH0 && g < TOUCH0 + 4 * NTOUCH) {
                    u64 t;
                    asm volatile("s_mov_b64 %1, %3\n\tglobal_load_dword %0, %2, %1" : "=v"(tch[(g - TOUCH0) >> 2]), "=&s"(t)
                                 : "v"(tco), "s"((const char*)p.w2 + ((g - TOUCH0) >> 2) * (112 * 128)));
                }
                if constexpr (i >= 5 && g >= 7 + 4 * KVW) fill(std::integral_constant<int, (g - 7 - 4 * KVW) * 3 + i - 5>{});
                SCHED();
            }, std::make_integer_sequence<int, 8>{});
        }, std::make_integer_sequence<int, 28>{});
        STAMP(4);
        wait_lds_eq(sm + L_CTL + 64, 3u);
        unsigned sa[2];
#pragma unroll
        for (int i = 0; i < 8; ++i) cs[i >> 2][0][i & 3] = *(LAS f32x4*)(sm + L_CSUM + (64 * wn + 32 * (i >> 2) + 8 * (i & 3) + 4 * hh) * 4);
        sa[0] = *(LAS unsigned*)(sm + L_STAT + (64 * wm + r32) * 4);
        sa[1] = *(LAS unsigned*)(sm + L_STAT + (64 * wm + 32 + r32) * 4);
#pragma unroll
        for (int i = 0; i < 8; ++i) cs[i >> 2][1][i & 3] = *(LAS f32x4*)(sm + L_CSUM + 512 + (64 * wn + 32 * (i >> 2) + 8 * (i & 3) + 4 * hh) * 4);
        const int lw = fresh_lane();
        const int w2a0 = lb + w2i + (4 * (lw >> 5) + ((lw & 15) >> 2)) * 128 + (((2 * ((lw >> 4) & 1) + ((lw & 3) >> 1)) ^ (((lw >> 3) & 1) << 2)) * 16) + 8 * (lw & 1);
        const int w2a1 = w2a0 ^ 64;
        u32x4 w2r[12][2];
        auto park = [&](int i) {
            w2r[i][0] = ldstr2(lp(w2a0) + (i / 6) * L_SLOT + (i % 6) * 2048, 1024);
            w2r[i][1] = ldstr2(lp(w2a1) + (i / 6) * L_SLOT + (i % 6) * 2048, 1024);
        };
#pragma unroll
        for (int sb = 0; sb < 2; ++sb) {
            const float ri = __builtin_amdgcn_rcpf(bfhi(sa[sb])) * -1.44269504088896341f, nm = -bflo(sa[sb]), d2 = bfhi(sa[sb]) * bfhi(sa[sb]);
            const f32x2 ri2 = {ri, ri}, nm2 = {nm, nm}, dd2 = {d2, d2};
#pragma unroll
            for (int ft = 0; ft < 2; ++ft) {
#pragma unroll
                for (int t2 = 0; t2 < 2; ++t2) {
                    const int grp = sb * 4 + ft * 2 + t2;
                    if (EPARK && grp == 2) { SCHED(); wait_lds(sm + L_CTL + 48 + wq * 4); }
                    if (EPARK && grp >= 2) { SCHED(); park(grp - 2); }
                    f32x2 gu[4], ee[4];
#pragma unroll
                    for (int j = 0; j < 4; ++j) {
                        const int r = 8 * t2 + 2 * j;
                        const f32x2 g = __builtin_elementwise_fma(nm2, (f32x2){cs[ft][0][r >> 2][r & 3], cs[ft][0][r >> 2][(r & 3) + 1]}, (f32x2){acc[sb][ft][0][r], acc[sb][ft][0][r + 1]});
                        const f32x2 u = __builtin_elementwise_fma(nm2, (f32x2){cs[ft][1][r >> 2][r & 3], cs[ft][1][r >> 2][(r & 3) + 1]}, (f32x2){acc[sb][ft][1][r], acc[sb][ft][1][r + 1]});
                        const f32x2 t = g * ri2;
                        ee[j] = (f32x2){__builtin_amdgcn_exp2f(t[0]), __builtin_amdgcn_exp2f(t[1])};
                        gu[j] = g * u;
                    }
                    u32x4 st;
#pragma unroll
                    for (int j = 0; j < 4; ++j) {
                        const f32x2 dn = __builtin_elementwise_fma(ee[j], dd2, dd2);
                        const f32x2 hv = gu[j] * (f32x2){__builtin_amdgcn_rcpf(dn[0]), __builtin_amdgcn_rcpf(dn[1])};
                        st[j] = pk(hv[0], hv[1]);
                    }
                    const unsigned fi = (unsigned)((4 * mt + 2 * wm + sb) * 112 + 8 * nt + 4 * wn + 2 * ft + t2);
                    __builtin_amdgcn_raw_buffer_store_b128(st, wsr, WS_H + fi * 1024u + lane * 16u, 0, 16);
                }
            }
        }
        SCHED();
        STAMP(5);
        const u32x4 w2o = w2_off();
        const u64 tag = *(LAS u64*)(sm + L_CTL + 128);
        GAS u64* myflag = (GAS u64*)(p.ws + WS_FH + ((2 * mt + wm) * 28 + 2 * nt + wn) * 128) + (lw & 15);
        if (!__builtin_amdgcn_readfirstlane(__hip_atomic_load((LAS unsigned*)(sm + L_CTL + 112 + wq * 4), __ATOMIC_RELAXED, __HIP_MEMORY_SCOPE_WORKGROUP))) {
            WAIT_VM(0);
            if (lane < FLAGW) __hip_atomic_store(myflag, tag, RLX_AGENT);
            wait_lds(sm + L_CTL + 112 + wq * 4);
        }
        asm volatile("" ::: "memory");
#pragma unroll
        for (int i = 6; i < 12; ++i) park(i);
        WAIT_VM(0);
        if (TOUCH) asm volatile("" :: "v"(tch[0]), "v"(tch[1]), "v"(tch[2]), "v"(tch[3]));
        asm volatile("" :: "v"(tch0));
        if (lane < FLAGW) __hip_atomic_store(myflag, tag, RLX_AGENT);
        STAMP(6);
        if (!EPARK) {
#pragma unroll
            for (int i = 0; i < 6; ++i) park(i);
        }
        asm volatile("" ::: "memory");
        __hip_atomic_store((LAS unsigned*)(sm + L_CTL + 32 + wq * 4), 1u, __ATOMIC_RELAXED, __HIP_MEMORY_SCOPE_WORKGROUP);
        WAIT_LGKM0();
#pragma unroll
        for (int i = 0; i < 12; ++i) asm volatile("" : "+v"(w2r[i][0]), "+v"(w2r[i][1]));
#pragma unroll
        for (int i = 0; i < 3; ++i) pair_w2(w2o, 12 + 2 * i, w2i + i * 4096);
#pragma unroll
        for (int i = 0; i < 3; ++i) pair_w2(w2o, 18 + 2 * i, w2i + L_SLOT + i * 4096);
        STAMP(7);
        {
            GAS u64* fp = (GAS u64*)(p.ws + WS_FH + (m64 * 28 + 7 * wq + (lw < 7 ? lw : 0)) * 128);
#pragma unroll 1
            for (unsigned spins = 0; spins < SPIN_CAP; ++spins) {
                if (__all(__hip_atomic_load(fp, RLX_AGENT) == tag)) break;
                __builtin_amdgcn_s_sleep(POLL_SLEEP);
            }
            __builtin_amdgcn_fence(__ATOMIC_ACQUIRE, "wavefront");
        }
        STAMP(11);
        const unsigned hb = WS_H + (unsigned)(2 * m64 * 112 + 28 * wq) * 1024u + lw * 16u;
        u32x4 hf[28][2];
        auto load_h = [&](int i, int j) {
            int t = i + rot2; t = t >= 28 ? t - 28 : t;
            hf[i][j] = __builtin_amdgcn_raw_buffer_load_b128(wsr, hb + j * (112u * 1024u), t * 1024, 16);
        };
        constexpr int HD = HDEPTH, HG = HGROW;
#pragma unroll
        for (int i = 0; i < HD; ++i) { load_h(i, 0); load_h(i, 1); }
        STAMP(14);
        f32x16 ac2[2][2];
#pragma unroll
        for (int i = 0; i < 4; ++i)
#pragma unroll
            for (int r = 0; r < 16; ++r) ac2[i >> 1][i & 1][r] = 0.f;
        u32x4 a0 = w2r[0][0], a1 = w2r[0][1];
        static_for([&](auto ic) {
            constexpr int i = decltype(ic)::value, n = i + 1;
            SCHED();
            if (i == 23) wait_lds(sm + L_CTL + 80 + wq * 4);
            u32x4 b0, b1;
            if constexpr (n < 12) { b0 = w2r[n][0]; b1 = w2r[n][1]; }
            else if constexpr (n < 28) {
                constexpr int so = n < 24 ? ((n - 12) / 6) * L_SLOT + ((n - 12) % 6) * 2048 : (n - 24) * 2048;
                const int lo = n < 24 ? 0 : latei - w2i;
                b0 = ldstr2(lp(w2a0 + lo) + so, 1024);
                b1 = ldstr2(lp(w2a1 + lo) + so, 1024);
            }
            SCHED();
            ac2[0][0] = mfma32(a0, hf[i][0], ac2[0][0]);
            SCHED();
            constexpr int l0 = i < HG ? HD + 2 * i : HD + HG + i;
            if constexpr (l0 < 28) load_h(l0, 0);
            SCHED();
            ac2[1][0] = mfma32(a1, hf[i][0], ac2[1][0]);
            SCHED();
            if constexpr (l0 < 28) load_h(l0, 1);
            SCHED();
            ac2[0][1] = mfma32(a0, hf[i][1], ac2[0][1]);
            SCHED();
            if constexpr (i < HG) load_h(l0 + 1, 0);
            SCHED();
            ac2[1][1] = mfma32(a1, hf[i][1], ac2[1][1]);
            SCHED();
            if constexpr (i < HG) load_h(l0 + 1, 1);
            a0 = b0; a1 = b1;
        }, std::make_integer_sequence<int, 28>{});
        SCHED();
        STAMP(15);
        WAIT_LGKM0();
#pragma unroll
        for (int v = 0; v < 8; ++v) {
            LAS char* sl = sm + w2i + (v < 6 ? v * 2048 : L_SLOT + (v - 6) * 2048) + lw * 16;
            const f32x16& t = ac2[v >> 2][(v >> 1) & 1];
            const int r0 = 8 * (v & 1);
            *(LAS f32x4*)sl = (f32x4){t[r0], t[r0 + 1], t[r0 + 2], t[r0 + 3]};
            *(LAS f32x4*)(sl + 1024) = (f32x4){t[r0 + 4], t[r0 + 5], t[r0 + 6], t[r0 + 7]};
        }
        WAIT_BAR("lgkmcnt(0)");
        STAMP(16);
        STAMP(17);
        STAMP_OUT(18);
    } else {
        u32x4 xvo;
#pragma unroll
        for (int j = 0; j < 4; ++j) xvo[j] = (unsigned)((8 * j + (lane >> 3)) * D + ((((lane & 7) ^ (lane >> 4)) ^ (4 * (j & 1))) * 8)) * 2u - 1024u * j;
        const u16* xb = sptr(p.x, (mt * 128 + wq * 32) * D);
        auto group_x = [&](int si) { glds16x4(sptr(xb, kst(si) * 64), xvo, lb + ring(si) + wq * 4096); };
        group_x(0);
        group_x(1); group_w(1, 0); group_w(1, 1);
        const int cbi = L_CONV + chn * C_CH;
        LAS char* cb = sm + cbi;
        unsigned kvo[2], uso;
#pragma unroll
        for (int rd = 0; rd < 2; ++rd) kvo[rd] = (unsigned)((lane >> 3) * D + (((lane & 7) ^ (((lane >> 4) & 1) | (((2 * wq + rd) & 3) << 1))) * 8)) * 2u;
        uso = WS_U + (unsigned)(kc0 + wq * 16 + (lane >> 3)) * 8192u + (unsigned)(1984 - kr0 + (lane & 7) * 8) * 2u;
        int kro = lb + L_KVO + wq * 2048 + (lane >> 3) * 128 + (((lane & 7) ^ (lane >> 3)) * 16);
        const int cc = 2 * par + ((lane >> 2) & 1);
        int zo = cbi + C_U + par * C_US + (248 + (lane < 36 ? lane : 35)) * 16;
        zo = lane < 8 ? cbi + cc * C_HS + (lane & 3) * 16 : zo;
        u32x4 w2o = w2_off();
        const unsigned fo = lane * 16u;
        unsigned ao = (wq < 2 ? WS_CSUM + (unsigned)(nt * 2 + wq) * 1024u : WS_STATS + mt * 1024u) + fo;
        asm volatile("" : "+v"(zo), "+v"(w2o), "+v"(kvo[0]), "+v"(kvo[1]), "+v"(uso), "+v"(kro), "+v"(ao));
        unsigned eo = (lane & 1) * 4u;
        asm volatile("" : "+v"(eo));
        WAIT_VM(16);
        unsigned tag32, taghi;
        u32x4 bval, ug[2];
        unsigned dbits = *(LAS u16*)(sm + wave * 1024 + 240 + (dch & 7) * 2);
        {
            const u32x4 h0 = lds128(sm + wave * 1024 + 224);
            const bool s1 = !(cc & 1), s2 = !(cc & 2);
            const unsigned a0 = s1 ? 0u : h0[0], a1 = s1 ? h0[0] : h0[1], a2 = s1 ? h0[1] : h0[2], a3 = s1 ? h0[2] : h0[3];
            const unsigned lm = (lane == 3 || lane == 7) ? 0xffffffffu : 0u;
            bval = (u32x4){(s2 ? 0u : a0) & lm, (s2 ? 0u : a1) & lm, (s2 ? a0 : a2) & lm, (s2 ? a1 : a3) & lm};
            asm volatile("" : "+v"(bval), "+v"(dbits));
        }
        auto kv_store = [&](int rd) {
            LAS unsigned* kp = (LAS unsigned*)(lp(kro) + rd * 1024);
            ug[0][1] = kp[0]; asm volatile("" ::: "memory"); ug[0][3] = kp[1]; asm volatile("" ::: "memory");
            ug[1][1] = kp[2]; asm volatile("" ::: "memory"); ug[1][3] = kp[3];
            __builtin_amdgcn_raw_buffer_store_b128(ug[0], wsr, uso, rd * 65536, 16);
            __builtin_amdgcn_raw_buffer_store_b128(ug[1], wsr, uso, rd * 65536 + 4096, 16);
        };
        WAIT_BAR("vmcnt(12) lgkmcnt(0)");
        STAMP(1);
#pragma unroll
        for (int w = 0; w < 5; ++w) {
            if (w == 3) glds4c(p.ws + WS_EPOCH, eo, lb + L_RAW + wq * 1024);
            group_x(w + 2); group_w(w + 2, 0); group_w(w + 2, 1);
            if (w == KVW) {
#pragma unroll
                for (int j = 0; j < 2; ++j) {
                    glds16(sptr(p.k, (kr0 + (2 * wq + j) * 8) * D + kc0), kvo[j], lb + L_KV + (2 * wq + j) * 1024);
                    glds16(sptr(p.v, (kr0 + (2 * wq + j) * 8) * D + kc0), kvo[j], lb + L_KV + 8192 + (2 * wq + j) * 1024);
                }
            }
            if (w == 4) {
                const u32x2 ev = *(LAS u32x2*)(sm + L_RAW + wq * 1024);
                tag32 = __builtin_amdgcn_readfirstlane(ev[0]) ^ (unsigned)TAG_SALT;
                taghi = __builtin_amdgcn_readfirstlane(ev[1]) ^ (unsigned)(TAG_SALT >> 32);
                asm volatile("s_nop 1" : "+s"(tag32), "+s"(taghi));
                ug[0][0] = vmov1(tag32); ug[0][2] = vmov1(tag32); ug[1][0] = vmov1(tag32); ug[1][2] = vmov1(tag32);
                if (lane < 34) *(LAS unsigned*)(sm + L_CTL + lane * 4) = lane < 32 ? 0u : lane == 32 ? tag32 : taghi;
            }
            if (w == 4 && KVW == 0) kv_store(0);
            if (w == 2) STAMP(2);
            STAMPG(2 + 2 * w);
            if (w == KVW) WAIT_BAR("vmcnt(16) lgkmcnt(0)"); else WAIT_BAR("vmcnt(12) lgkmcnt(0)");
            STAMPG(3 + 2 * w);
            if (w == 2) STAMP(3);
        }
        glds16c(p.ws, ao, lb + L_RAW + wq * 1024);
#pragma unroll
        for (int i = 0; i < 3; ++i) pair_w2(w2o, 2 * i, w2i + i * 4096);
        kv_store(KVW ? 0 : 1);
        STAMPG(12); WAIT_BAR("vmcnt(12) lgkmcnt(0)"); STAMPG(13);
        if (KVW) kv_store(1);
        asm volatile("" ::: "memory");
        if (lane == 0) __hip_atomic_fetch_add((LAS unsigned*)(sm + L_CTL + 12), 1u, __ATOMIC_RELAXED, __HIP_MEMORY_SCOPE_WORKGROUP);
        if (wq < 3) {
            u32x4 av = lds128(sm + L_RAW + wq * 1024 + lane * 16);
#pragma unroll 1
            for (unsigned spins = 0; spins < SPIN_CAP; ++spins) {
                if (__all(((av[0] ^ tag32) | (av[2] ^ tag32)) == 0u)) break;
                asm volatile("" ::: "memory");
                av = __builtin_amdgcn_raw_buffer_load_b128(wsr, ao, 0, 16);
                asm volatile("" : "+v"(av));
            }
            *(LAS u32x2*)(sm + L_CSUM + wq * 512 + lane * 8) = (u32x2){av[1], av[3]};
            asm volatile("" ::: "memory");
            if (lane == 0) __hip_atomic_fetch_add((LAS unsigned*)(sm + L_CTL + 64), 1u, __ATOMIC_RELAXED, __HIP_MEMORY_SCOPE_WORKGROUP);
        }
        STAMP(4);
#pragma unroll
        for (int i = 0; i < 3; ++i) pair_w2(w2o, 6 + 2 * i, w2i + L_SLOT + i * 4096);
        WAIT_VM(12);
        __hip_atomic_store((LAS unsigned*)(sm + L_CTL + 48 + wq * 4), 1u, __ATOMIC_RELAXED, __HIP_MEMORY_SCOPE_WORKGROUP);
        wait_lds_eq(sm + L_CTL + 12, 8u);
        STAMP(5);
        __builtin_amdgcn_s_setprio(2);
        const unsigned ubase = WS_U + (unsigned)dch * 8192u;
        glds16x2c(p.ws + ubase + par * 2048, fo, lb + cbi + C_U + par * 2048);
        glds16x2c(p.ws + ubase + 4096 + par * 2048, fo, lb + cbi + C_U + C_US + par * 2048);
        glds4c(p.ws + ubase + (1 - par) * 2048, eo, lb + L_UNX + wq * 256);
        {
            const u32x4 fvo = {fo, fo, fo, lane == 63 ? 62u * 16u : fo};
#pragma unroll
            for (int c = 0; c < 2; ++c) glds16x4(sptr(p.hk, dch * S + 4 * par + 2 * c + 2), fvo, lb + cbi + (2 * par + c) * C_HS + 64);
        }
        *(LAS u32x4*)(sm + zo) = bval;
        WAIT_VM(13);
        __hip_atomic_store((LAS unsigned*)(sm + L_CTL + 112 + wq * 4), 1u, __ATOMIC_RELAXED, __HIP_MEMORY_SCOPE_WORKGROUP);
        WAIT_VM(8);
        {
            u32x4 ua[2], ub[2]; u32x2 un[2];
#pragma unroll
            for (int rd = 0; rd < 2; ++rd) {
                const int x8 = 128 * par + 64 * rd + lane;
                ua[rd] = lds128(cb + C_U + x8 * 16); ub[rd] = lds128(cb + C_U + C_US + x8 * 16);
                un[rd] = *(LAS u32x2*)((rd == 1 && lane == 63) ? sm + L_UNX + wq * 256 : cb + C_U + x8 * 16 + 16);
            }
#pragma unroll 1
            for (unsigned spins = 0; spins < SPIN_CAP; ++spins) {
                unsigned df = 0;
#pragma unroll
                for (int rd = 0; rd < 2; ++rd) df |= (ua[rd][0] ^ tag32) | (ua[rd][2] ^ tag32) | (ub[rd][0] ^ tag32) | (ub[rd][2] ^ tag32) | (un[rd][0] ^ tag32);
                if (__all(df == 0u)) break;
                __builtin_amdgcn_s_sleep(URETRY);
                asm volatile("" ::: "memory");
#pragma unroll
                for (int rd = 0; rd < 2; ++rd) {
                    const int x8 = 128 * par + 64 * rd + lane;
                    const unsigned uo = ubase + (unsigned)x8 * 16u;
                    ua[rd] = __builtin_amdgcn_raw_buffer_load_b128(wsr, uo, 0, 16);
                    ub[rd] = __builtin_amdgcn_raw_buffer_load_b128(wsr, uo + 4096u, 0, 16);
                    un[rd] = __builtin_amdgcn_raw_buffer_load_b64(wsr, x8 == 255 ? uo : uo + 16u, 0, 16);
                }
                asm volatile("" : "+v"(ua[0]), "+v"(ub[0]), "+v"(un[0]), "+v"(ua[1]), "+v"(ub[1]), "+v"(un[1]));
            }
#pragma unroll
            for (int rd = 0; rd < 2; ++rd) {
                const int x8 = 128 * par + 64 * rd + lane;
                const unsigned nx = x8 == 255 ? 0u : un[rd][1];
                *(LAS u32x4*)(cb + C_U + x8 * 16) = (u32x4){__builtin_amdgcn_alignbit(ua[rd][3], ua[rd][1], 16), __builtin_amdgcn_alignbit(ub[rd][1], ua[rd][3], 16),
                                                            __builtin_amdgcn_alignbit(ub[rd][3], ub[rd][1], 16), __builtin_amdgcn_alignbit(nx, ub[rd][3], 16)};
                *(LAS u32x4*)(cb + C_U + C_US + x8 * 16) = (u32x4){ua[rd][1], ua[rd][3], ub[rd][1], ub[rd][3]};
            }
        }
        asm volatile("s_waitcnt vmcnt(0) lgkmcnt(0)" ::: "memory");
        __hip_atomic_store((LAS unsigned*)(sm + L_CTL + 16 + wq * 4), 1u, __ATOMIC_RELAXED, __HIP_MEMORY_SCOPE_WORKGROUP);
        u32x4 lw2[8], xv[2];
        {
            const __amdgpu_buffer_rsrc_t wr2 = __builtin_amdgcn_make_buffer_rsrc((void*)p.w2, 0, F * D * 2, 0x00020000);
#pragma unroll
            for (int j = 0; j < 8; ++j) {
                int t = 24 + (j >> 2) * 2 + rot2; t = t >= 28 ? t - 28 : t;
                lw2[j] = __builtin_amdgcn_raw_buffer_load_b128(wr2, w2o[0], ((wq * 448 + t * 16 + (j & 3) * 8) * D + n64 * 64) * 2, 0);
            }
        }
        const int lf = fresh_lane();
        const int srow = 64 * m64 + 32 * (wq & 1) + (lf & 31);
        const int dd = 64 * n64 + 32 * (wq >> 1) + 16 * (lf >> 5);
        const unsigned xoff = (unsigned)(srow * D + dd) * 2u;
        {
            const __amdgpu_buffer_rsrc_t xr = __builtin_amdgcn_make_buffer_rsrc((void*)p.x, 0, S * D * 2, 0x00020000);
            xv[0] = __builtin_amdgcn_raw_buffer_load_b128(xr, xoff, 0, 0);
            xv[1] = __builtin_amdgcn_raw_buffer_load_b128(xr, xoff, 16, 0);
        }
        SCHED();
        wait_lds(sm + L_CTL + 16 + (wq ^ 1) * 4);
        STAMP(7);
        STAMP(6);
        const int n16 = lane & 15, kg = lane >> 4;
        f32x4 ca[4];
#pragma unroll
        for (int j = 0; j < 4; ++j) ca[j] = (f32x4){0.f, 0.f, 0.f, 0.f};
        int apo = lb + cbi + (n16 & 3) * C_HS + ((n16 >> 2) + kg) * 16;
        int bpo = lb + cbi + C_U + (n16 & 1) * C_US + (252 - 4 * (n16 >> 1) + kg) * 16 - 7 * 512;
        u32x4 fa[2][8], fb[2][8];
#pragma unroll
        for (int i = 0; i < 8; ++i) { fa[0][i] = lds128(lp(apo) + i * 512); fb[0][i] = lds128(lp(bpo) + (7 - i) * 512); }
        const bool odd = lf & 1;
        int ysrc = (2 * (lf >> 3) + (lf & 1) + 16 * ((lf >> 1) & 3)) * 4;
        unsigned yso = WS_Y + (unsigned)dch * 8192u + lf * 16u;
        f32x4 kd[4];
        {
            const float dval = bflo(dbits);
            const int sl0 = 32 * ((lf & 15) >> 1) + 8 * (lf >> 4) + 4 * (lf & 1);
#pragma unroll
            for (int j = 0; j < 4; ++j) {
                const int tj = (j == 0 ? 0 : j == 1 ? 3 : j == 2 ? 4 : 7) ^ par;
                const u32x2 kvr = *(LAS u32x2*)(cb + C_U + C_US + (2044 - 256 * tj - sl0) * 2);
                const unsigned ka = pk(bfhi(kvr[1]) * dval, bflo(kvr[1]) * dval), kb = pk(bfhi(kvr[0]) * dval, bflo(kvr[0]) * dval);
                kd[j] = (f32x4){bflo(ka), bfhi(ka), bflo(kb), bfhi(kb)};
                asm volatile("" : "+v"(kd[j]));
            }
            asm volatile("" : "+v"(ysrc), "+v"(yso));
        }
        wait_lds(sm + L_CTL + 32 + wq * 4);
        STAMP(8);
        auto conv_all = [&](auto pc_) {
            constexpr int P = decltype(pc_)::value;
            static_for([&](auto rc) {
                constexpr int r = decltype(rc)::value, c = r & 1, n = c ^ 1;
                static_for([&](auto kc) {
                    constexpr int k = decltype(kc)::value;
                    constexpr int T[4] = {P, 3 - P, 4 + P, 7 - P};
                    constexpr int j = k <= T[0] ? 0 : k <= T[0] + T[1] + 1 ? 1 : k <= T[0] + T[1] + T[2] + 2 ? 2 : 3;
                    constexpr int pp = k - (j == 0 ? 0 : j == 1 ? T[0] + 1 : j == 2 ? T[0] + T[1] + 2 : T[0] + T[1] + T[2] + 3);
                    ca[j] = mfma16(fa[c][pp], fb[c][T[j] - pp], ca[j]);
                    SCHED();
                    if constexpr (r < 7 && k < 2 * (T[3] + 1)) {
                        if constexpr (k & 1) fb[n][k >> 1] = lds128(lp(bpo) + (r + 1) * 64 + (7 - (k >> 1)) * 512);
                        else fa[n][k >> 1] = lds128(lp(apo) + (r + 1) * 64 + (k >> 1) * 512);
                    }
                    SCHED();
                }, std::make_integer_sequence<int, 18>{});
                asm volatile("" : "+v"(apo), "+v"(bpo));
            }, std::make_integer_sequence<int, 8>{});
        };
        if (par) conv_all(std::integral_constant<int, 1>{}); else conv_all(std::integral_constant<int, 0>{});
        SCHED();
        STAMP(9);
        {
            float r0[4], r1[4];
#pragma unroll
            for (int j = 0; j < 4; ++j) { r0[j] = dpp_xor1(odd ? ca[j][0] : ca[j][2]); r1[j] = dpp_xor1(odd ? ca[j][1] : ca[j][3]); }
            unsigned rp[4][2];
#pragma unroll
            for (int j = 0; j < 4; ++j) {
                const unsigned ya = pk(odd ? r0[j] : ca[j][0], odd ? ca[j][2] : r0[j]), yb = pk(odd ? r1[j] : ca[j][1], odd ? ca[j][3] : r1[j]);
                rp[j][0] = __builtin_amdgcn_ds_bpermute(ysrc, pk(add1(bflo1(ya), kd[j][0]), add1(bfhi1(ya), kd[j][1])));
                rp[j][1] = __builtin_amdgcn_ds_bpermute(ysrc, pk(add1(bflo1(yb), kd[j][2]), add1(bfhi1(yb), kd[j][3])));
            }
#pragma unroll
            for (int j = 0; j < 4; ++j)
                __builtin_amdgcn_raw_buffer_store_b128((u32x4){vmov1(tag32 ^ Y_TAG), rp[j][0], vmov1(tag32 ^ Y_TAG), rp[j][1]}, wsr, yso,
                                                       ((j == 0 ? 0 : j == 1 ? 3 : j == 2 ? 4 : 7) ^ par) * 1024, 16);
        }
        STAMP(10);
        WAIT_LGKM0();
        if (lane == 0) __hip_atomic_fetch_add((LAS unsigned*)(sm + L_CTL + 8), 1u, __ATOMIC_RELAXED, __HIP_MEMORY_SCOPE_WORKGROUP);
#pragma unroll 1
        for (unsigned spins = 0; spins < (1u << 22); ++spins) {
            if (__builtin_amdgcn_readfirstlane(__hip_atomic_load((LAS unsigned*)(sm + L_CTL + 8), __ATOMIC_RELAXED, __HIP_MEMORY_SCOPE_WORKGROUP)) == 4u) break;
            __builtin_amdgcn_s_sleep(1);
        }
        asm volatile("" ::: "memory");
#pragma unroll
        for (int j = 0; j < 8; ++j) *(LAS u32x4*)(sm + latei + j * 1024 + lf * 16) = lw2[j];
        asm volatile("" ::: "memory");
        __hip_atomic_store((LAS unsigned*)(sm + L_CTL + 80 + wq * 4), 1u, __ATOMIC_RELAXED, __HIP_MEMORY_SCOPE_WORKGROUP);
        STAMP(11);
        __builtin_amdgcn_s_sleep(YSLEEP);
        u32x2 yv[16], stv;
        const int yl = L_CONV + 32768 + wq * 4096;
        const unsigned yg = (unsigned)(lf >> 3) * 8192u + (unsigned)(lf & 7) * 16u;
        const u32x4 yvo = {yg, yg + (65536u - 1024u), yg + 2 * (65536u - 1024u), yg + 3 * (65536u - 1024u)};
        const char* ybase = p.ws + WS_Y + (unsigned)(64 * n64 + 32 * (wq >> 1)) * 8192u + (unsigned)(64 * m64 + 32 * (wq & 1)) * 4u;
#pragma unroll 1
        for (unsigned spins = 0; spins < SPIN_CAP; ++spins) {
            glds16x4c(ybase, yvo, lb + yl);
            stv = __builtin_amdgcn_raw_buffer_load_b64(wsr, WS_STATS + (unsigned)srow * 8u, 0, 16);
            WAIT_VM(0);
#pragma unroll
            for (int i = 0; i < 16; ++i) yv[i] = *(LAS u32x2*)(sm + yl + ((lf >> 5) * 16 + i) * 128 + ((lf & 31) >> 1) * 8);
            unsigned df = stv[0] ^ tag32;
#pragma unroll
            for (int i = 0; i < 16; ++i) df |= yv[i][0] ^ (tag32 ^ Y_TAG);
            if (__all(df == 0u)) break;
            __builtin_amdgcn_s_sleep(8);
        }
        STAMP(14);
        __builtin_amdgcn_s_setprio(0);
        f32x2 sf[8], xf[8];
        {
            const float nmb = -bflo(stv[1]);
            float ri = __builtin_amdgcn_rcpf(bfhi(stv[1]));
            asm volatile("s_nop 1" : "+v"(ri));
            const unsigned ysh = (srow & 1) ? 0u : 16u;
#pragma unroll
            for (int i = 0; i < 8; ++i) {
                const unsigned xd = xv[i >> 2][i & 3];
                xf[i] = (f32x2){bflo1(xd), bfhi1(xd)};
                const unsigned tp = pk(add1(xf[i][0], nmb), add1(xf[i][1], nmb));
                const unsigned np = pk(mul1(bflo1(tp), ri), mul1(bfhi1(tp), ri));
                sf[i] = (f32x2){mul1(bflo1(np), bfhi1(yv[2 * i][1] << ysh)), mul1(bfhi1(np), bfhi1(yv[2 * i + 1][1] << ysh))};
                asm volatile("" : "+v"(sf[i]), "+v"(xf[i]));
            }
        }
        const int rm = lb + L_W2 + (wq < 3 ? wq * 4096 : L_SLOT) + (lf >> 5) * 2048 + (lf & 31) * 16;
        STAMP(15);
        WAIT_BAR("lgkmcnt(0)");
        STAMP(16);
        f32x4 o[4];
#pragma unroll
        for (int j = 0; j < 4; ++j) {
            LAS char* rp = lp(rm) + j * 512;
            o[j] = (*(LAS f32x4*)rp + *(LAS f32x4*)(rp + 12288)) + (*(LAS f32x4*)(rp + 24576) + *(LAS f32x4*)(rp + 36864));
        }
        f32x2 so[8], ev[8];
#pragma unroll
        for (int i = 0; i < 8; ++i) {
            const f32x2 ov = {o[i >> 1][(i & 1) * 2], o[i >> 1][(i & 1) * 2 + 1]};
            const f32x2 t = ov * (f32x2){-1.44269504088896341f, -1.44269504088896341f};
            ev[i] = (f32x2){__builtin_amdgcn_exp2f(t[0]), __builtin_amdgcn_exp2f(t[1])};
            so[i] = sf[i] * ov;
        }
        SCHED();
        u32x4 st[2];
#pragma unroll
        for (int i = 0; i < 8; ++i) {
            const f32x2 dn = ev[i] + (f32x2){1.f, 1.f};
            const f32x2 r = __builtin_elementwise_fma(so[i], (f32x2){__builtin_amdgcn_rcpf(dn[0]), __builtin_amdgcn_rcpf(dn[1])}, xf[i]);
            st[i >> 2][i & 3] = pk(r[0], r[1]);
        }
        const __amdgpu_buffer_rsrc_t orr = __builtin_amdgcn_make_buffer_rsrc((void*)p.out, 0, S * D * 2, 0x00020000);
        __builtin_amdgcn_raw_buffer_store_b128(st[0], orr, xoff, 0, 0);
        __builtin_amdgcn_raw_buffer_store_b128(st[1], orr, xoff, 16, 0);
        STAMP(17);
        STAMP_OUT(18);
    }
}

extern "C" void kernel_launch(void* const* d_in, const int* in_sizes, int n_in,
                              void* d_out, int out_size, void* d_ws, size_t ws_size,
                              hipStream_t stream) {
    Params p;
    p.x = (const u16*)d_in[0]; p.k = (const u16*)d_in[1]; p.v = (const u16*)d_in[2];
    p.sk = (const u16*)d_in[3]; p.hk = (const u16*)d_in[4]; p.dv = (const u16*)d_in[5];
    p.w1 = (const u16*)d_in[7]; p.w3 = (const u16*)d_in[8]; p.w2 = (const u16*)d_in[9];
    p.out = (u16*)d_out; p.ws = (char*)d_ws;
    (void)hipFuncSetAttribute((const void*)fused_kernel, hipFuncAttributeMaxDynamicSharedMemorySize, L_TOTAL);
    fused_kernel<<<256, 512, L_TOTAL, stream>>>(p);
}
```

```cpp
#include <hip/hip_runtime.h>
#include <stdint.h>
#include <utility>

typedef unsigned short u16;
typedef unsigned long long u64;
typedef short s16x4 __attribute__((ext_vector_type(4)));
typedef short s16x8 __attribute__((ext_vector_type(8)));
typedef __bf16 bf16x2 __attribute__((ext_vector_type(2)));
typedef __bf16 bf16x8 __attribute__((ext_vector_type(8)));
typedef float f32x2 __attribute__((ext_vector_type(2)));
typedef float f32x4 __attribute__((ext_vector_type(4)));
typedef float f32x16 __attribute__((ext_vector_type(16)));
typedef unsigned u32x2 __attribute__((ext_vector_type(2)));
typedef unsigned u32x4 __attribute__((ext_vector_type(4)));
typedef unsigned u32x16 __attribute__((ext_vector_type(16)));
#define LAS __attribute__((address_space(3)))
#define GAS __attribute__((address_space(1)))
#define RLX_AGENT __ATOMIC_RELAXED, __HIP_MEMORY_SCOPE_AGENT

constexpr int S = 2048, D = 448, F = 1792;

constexpr unsigned WS_EPOCH = 0;
constexpr unsigned WS_FH = 65536;
constexpr unsigned WS_STATS = 8192;
constexpr unsigned WS_CSUM = 24576;
constexpr unsigned WS_U = 262144;
constexpr unsigned WS_Y = WS_U;
constexpr unsigned WS_H = WS_Y + D * S * 4;
constexpr unsigned WS_END = WS_H + 64 * 112 * 1024;
constexpr unsigned WS_DBG = WS_END;

constexpr int L_KV = 0;
constexpr int L_CTL = 0;
constexpr int L_CSUM = 256;
constexpr int L_STAT = 1280;
constexpr int L_RAW = 2048;
constexpr int L_KVO = 8192;
constexpr int L_UNX = 6144;
constexpr int L_SLOT = 49152;
constexpr int L_CONV = 12288;
constexpr int C_CH = 26112, C_HS = 4160, C_U = 16640, C_US = 4640;
constexpr int L_W2 = 16384 + L_SLOT;
constexpr int L_TOTAL = 163840;
__host__ __device__ constexpr int part(int db, int sb, int a) { return db * L_SLOT + sb * 4352 + a * 1024 + (a & 1) * 16 + (a >> 1) * 128; }
__host__ __device__ constexpr int ring(int stage) { return 16384 + (stage % 3) * L_SLOT; }
constexpr unsigned CODE_PREFETCH = 36864;
#define POLL_SLEEP 4
#define HDEPTH 6
#define ROT2 (8 * ml)
#define MPRIO 1
#define EPARK 0
#define YSLEEP 16
#define TOUCH 1
#define TOUCH0 4
#define HGROW 4
#define ROTM 6
#define KVW 0
#define MWARM 0
#define URETRY 4
#define KVNT " nt"
#define FLAGW 1
#define AUXW2 1
#define NTOUCH 2
#define ROT1 ((2 * ml + nl) % ROTM)

constexpr u64 TAG_SALT = 0x9E3779B97F4A7C15ull;
constexpr unsigned Y_TAG = 0x80000000u;
constexpr unsigned SPIN_CAP = 1u << 18;

struct Params {
    const u16* x; const u16* k; const u16* v; const u16* sk; const u16* hk; const u16* dv;
    const u16* w1; const u16* w3; const u16* w2; u16* out; char* ws;
};

__device__ __forceinline__ float bflo(unsigned p) { return __uint_as_float(p << 16); }
__device__ __forceinline__ float bfhi(unsigned p) { return __uint_as_float(p & 0xffff0000u); }
__device__ __forceinline__ float bflo1(unsigned p) { float r; asm("v_lshlrev_b32 %0, 16, %1" : "=v"(r) : "v"(p)); return r; }
__device__ __forceinline__ float bfhi1(unsigned p) { float r; asm("v_and_b32 %0, 0xffff0000, %1" : "=v"(r) : "v"(p)); return r; }
__device__ __forceinline__ float mul1(float a, float b) { float r; asm("v_mul_f32 %0, %1, %2" : "=v"(r) : "v"(a), "v"(b)); return r; }
__device__ __forceinline__ void fmac1(float& d, float a, float b) { asm("v_fmac_f32 %0, %1, %2" : "+v"(d) : "v"(a), "v"(b)); }
__device__ __forceinline__ float dpp_xor1(float v) {
    return __int_as_float(__builtin_amdgcn_update_dpp(0, __float_as_int(v), 0xB1, 0xf, 0xf, true));
}
__device__ __forceinline__ float add1(float a, float b) { float r; asm("v_add_f32 %0, %1, %2" : "=v"(r) : "v"(a), "v"(b)); return r; }
__device__ __forceinline__ unsigned vmov1(unsigned s) { unsigned r; asm volatile("v_mov_b32 %0, %1" : "=v"(r) : "s"(s)); return r; }
__device__ __forceinline__ unsigned pk(float a, float b) {
    f32x2 f = {a, b};
    return __builtin_bit_cast(unsigned, __builtin_convertvector(f, bf16x2));
}
__device__ __forceinline__ float rbf(float a) { return bflo(pk(a, a)); }
__device__ __forceinline__ float dot2(unsigned a, unsigned b, float c) {
    return __builtin_amdgcn_fdot2_f32_bf16(__builtin_bit_cast(bf16x2, a), __builtin_bit_cast(bf16x2, b), c, false);
}
__device__ __forceinline__ void glds16(const void* base, unsigned voff, unsigned ldst) {
    u64 t;
    asm volatile("s_mov_b32 m0, %3\n\ts_mov_b64 %0, %2\n\tglobal_load_lds_dwordx4 %1, %0" KVNT : "=&s"(t) : "v"(voff), "s"(base), "s"(ldst));
}
__device__ __forceinline__ void glds16x4(const void* base, u32x4 vo, unsigned ldst) {
    u64 t;
    asm volatile("s_mov_b32 m0, %6\n\ts_mov_b64 %0, %5\n\tglobal_load_lds_dwordx4 %1, %0\n\tglobal_load_lds_dwordx4 %2, %0 offset:1024\n\t"
                 "global_load_lds_dwordx4 %3, %0 offset:2048\n\tglobal_load_lds_dwordx4 %4, %0 offset:3072"
                 : "=&s"(t) : "v"(vo[0]), "v"(vo[1]), "v"(vo[2]), "v"(vo[3]), "s"(base), "s"(ldst));
}
__device__ __forceinline__ void glds16x4c(const void* base, u32x4 vo, unsigned ldst) {
    u64 t;
    asm volatile("s_mov_b32 m0, %6\n\ts_mov_b64 %0, %5\n\tglobal_load_lds_dwordx4 %1, %0 sc1\n\tglobal_load_lds_dwordx4 %2, %0 offset:1024 sc1\n\t"
                 "global_load_lds_dwordx4 %3, %0 offset:2048 sc1\n\tglobal_load_lds_dwordx4 %4, %0 offset:3072 sc1"
                 : "=&s"(t) : "v"(vo[0]), "v"(vo[1]), "v"(vo[2]), "v"(vo[3]), "s"(base), "s"(ldst) : "memory");
}
__device__ __forceinline__ void glds16x2c(const void* base, unsigned voff, unsigned ldst) {
    u64 t;
    asm volatile("s_mov_b32 m0, %3\n\ts_mov_b64 %0, %2\n\tglobal_load_lds_dwordx4 %1, %0 sc1\n\tglobal_load_lds_dwordx4 %1, %0 offset:1024 sc1"
                 : "=&s"(t) : "v"(voff), "s"(base), "s"(ldst));
}
__device__ __forceinline__ void glds16c(const void* base, unsigned voff, unsigned ldst) {
    u64 t;
    asm volatile("s_mov_b32 m0, %3\n\ts_mov_b64 %0, %2\n\tglobal_load_lds_dwordx4 %1, %0 sc1" : "=&s"(t) : "v"(voff), "s"(base), "s"(ldst));
}
__device__ __forceinline__ void glds4c(const void* base, unsigned voff, unsigned ldst) {
    u64 t;
    asm volatile("s_mov_b32 m0, %3\n\ts_mov_b64 %0, %2\n\tglobal_load_lds_dword %1, %0 sc1" : "=&s"(t) : "v"(voff), "s"(base), "s"(ldst));
}
__device__ __forceinline__ const u16* sptr(const u16* base, int off) {
    const u16* r = base + __builtin_amdgcn_readfirstlane(off);
    asm("" : "+s"(r));
    return r;
}
__device__ __forceinline__ u64 vsel(bool c, const void* a, u64 b) {
    u64 va = (u64)a;
    asm("" : "+v"(va), "+v"(b));
    return c ? va : b;
}
__device__ __forceinline__ LAS char* lp(int a) { return (LAS char*)(size_t)(unsigned)a; }
__device__ __forceinline__ u32x4 lds128(LAS char* p) { return *(LAS u32x4*)p; }
__device__ __forceinline__ u32x2 ldstr(LAS char* p) {
    return __builtin_bit_cast(u32x2, __builtin_amdgcn_ds_read_tr16_b64_v4i16((LAS s16x4*)p));
}
__device__ __forceinline__ u32x4 ldstr2(LAS char* p, int off) {
    u32x2 a = ldstr(p), b = ldstr(p + off);
    return (u32x4){a.x, a.y, b.x, b.y};
}
__device__ __forceinline__ f32x16 mfma32(u32x4 a, u32x4 b, f32x16 c) {
    return __builtin_amdgcn_mfma_f32_32x32x16_bf16(__builtin_bit_cast(bf16x8, a), __builtin_bit_cast(bf16x8, b), c, 0, 0, 0);
}
__device__ __forceinline__ f32x4 mfma16(u32x4 a, u32x4 b, f32x4 c) {
    return __builtin_amdgcn_mfma_f32_16x16x32_bf16(__builtin_bit_cast(bf16x8, a), __builtin_bit_cast(bf16x8, b), c, 0, 0, 0);
}
__device__ __forceinline__ float silu_f(float g) {
    return g * __builtin_amdgcn_rcpf(1.f + __builtin_amdgcn_exp2f(g * -1.44269504088896341f));
}
template <class Fn, int... I>
__device__ __forceinline__ void static_for(Fn&& fn, std::integer_sequence<int, I...>) { (fn(std::integral_constant<int, I>{}), ...); }
#define WAIT_VM(n) asm volatile("s_waitcnt vmcnt(" #n ")" ::: "memory")
#define WAIT_LGKM0() asm volatile("s_waitcnt lgkmcnt(0)" ::: "memory")
#define WAIT_BAR(w) asm volatile("s_waitcnt " w "\n\ts_barrier" ::: "memory")
#define SCHED() __builtin_amdgcn_sched_barrier(0)
#define CAS __attribute__((address_space(4)))
#define NSTAMP 18

__device__ __forceinline__ void wait_lds(LAS char* word) {
#pragma unroll 1
    for (unsigned spins = 0; spins < (SPIN_CAP << 4); ++spins) {
        if (__builtin_amdgcn_readfirstlane(__hip_atomic_load((LAS unsigned*)word, __ATOMIC_RELAXED, __HIP_MEMORY_SCOPE_WORKGROUP))) break;
        __builtin_amdgcn_s_sleep(1);
    }
    asm volatile("" ::: "memory");
}
__device__ __forceinline__ void wait_lds_eq(LAS char* word, unsigned want) {
#pragma unroll 1
    for (unsigned spins = 0; spins < (1u << 22); ++spins) {
        if ((unsigned)__builtin_amdgcn_readfirstlane(__hip_atomic_load((LAS unsigned*)word, __ATOMIC_RELAXED, __HIP_MEMORY_SCOPE_WORKGROUP)) == want) break;
        __builtin_amdgcn_s_sleep(1);
    }
    asm volatile("" ::: "memory");
}
#define STAMP(i)
#define STAMPG(i)
#define STAMPA(i)
#define STAMP_DECL()
#define STAMP_OUT(n)

__global__ __launch_bounds__(512, 2) void fused_kernel(Params p) {
    extern __shared__ __attribute__((aligned(16))) char smem_raw[];
    u64 pc;
    asm volatile("s_getpc_b64 %0" : "=s"(pc));
    LAS char* sm = (LAS char*)smem_raw;
    const int tid = threadIdx.x, lane = tid & 63;
    const int wave = __builtin_amdgcn_readfirstlane(tid >> 6);
    const int xcd = blockIdx.x & 7, idx = blockIdx.x >> 3;
    if (idx == 0 && wave < 5) {
        const unsigned o = (unsigned)tid * 128u;
        asm volatile("s_mov_b32 m0, %2\n\ts_nop 0\n\tglobal_load_lds_dword %0, %1" :: "v"(o < CODE_PREFETCH ? o : 0u), "s"(pc & ~(u64)255),
                     "s"((unsigned)(size_t)sm + 32768u));
    }
    SCHED();
    const __amdgpu_buffer_rsrc_t wsr = __builtin_amdgcn_make_buffer_rsrc(p.ws, 0, WS_END, 0x00020000);
    asm volatile("" :: "s"(p.x), "s"(p.k), "s"(p.v), "s"(p.sk), "s"(p.hk), "s"(p.dv), "s"(p.w1), "s"(p.w3), "s"(p.w2), "s"(p.out), "s"(p.ws));
    STAMP_DECL();
    STAMP(0);

    if (idx < 4) {
        const int ca = (xcd >> 1) * 4 + idx, cmat = ca >= 7, cnt = 7 * (xcd & 1) + (ca >= 7 ? ca - 7 : ca);
        const bool job = ca < 14;
        u32x4 xr[7], wr[14];
        const int row = (8 * (xcd >> 1) + 4 * (xcd & 1) + idx) * 64 + wave * 8 + (lane >> 3);
#pragma unroll
        for (int i = 0; i < 7; ++i) xr[i] = *(const u32x4*)(p.x + (size_t)row * D + i * 64 + (lane & 7) * 8);
        const u64 epoch = __hip_atomic_load((GAS u64*)(p.ws + WS_EPOCH), RLX_AGENT);
        const u64 tag = epoch ^ TAG_SALT;
        const unsigned tag32 = (unsigned)tag;
        {
            float s4[4] = {0.f, 0.f, 0.f, 0.f};
#pragma unroll
            for (int i = 0; i < 7; ++i)
#pragma unroll
                for (int j = 0; j < 4; ++j) s4[j] = dot2(xr[i][j], 0x3f803f80u, s4[j]);
            float sum = (s4[0] + s4[1]) + (s4[2] + s4[3]);
            sum += __shfl_xor(sum, 1); sum += __shfl_xor(sum, 2); sum += __shfl_xor(sum, 4);
            const float mean = sum * (1.f / D);
            f32x2 q4[4] = {{0.f, 0.f}, {0.f, 0.f}, {0.f, 0.f}, {0.f, 0.f}};
#pragma unroll
            for (int i = 0; i < 7; ++i)
#pragma unroll
                for (int j = 0; j < 4; ++j) {
                    const f32x2 d = (f32x2){bflo(xr[i][j]), bfhi(xr[i][j])} - (f32x2){mean, mean};
                    q4[j] = __builtin_elementwise_fma(d, d, q4[j]);
                }
            const f32x2 q2 = (q4[0] + q4[1]) + (q4[2] + q4[3]);
            float sq = q2[0] + q2[1];
            sq += __shfl_xor(sq, 1); sq += __shfl_xor(sq, 2); sq += __shfl_xor(sq, 4);
            const u32x2 st = {tag32, pk(mean, __builtin_amdgcn_sqrtf(rbf(rbf(sq * (1.f / D)) + 1e-5f)))};
            __builtin_amdgcn_raw_buffer_store_b64(st, wsr, (lane & 7) == 0 ? WS_STATS + (unsigned)row * 8u : 0xfffffff0u, 0, 16);
        }
        STAMPA(2);
        if (job) {
            SCHED();
            const u16* wp = (cmat ? p.w3 : p.w1) + (size_t)(wave * 4 + (lane >> 4)) * F + cnt * 128 + (lane & 15) * 8;
#pragma unroll
            for (int i = 0; i < 14; ++i) wr[i] = *(const u32x4*)(wp + (size_t)(32 * i) * F);
        }
        if (job) {
            float c[8];
#pragma unroll
            for (int j = 0; j < 8; ++j) c[j] = 0.f;
#pragma unroll
            for (int i = 0; i < 14; ++i)
#pragma unroll
                for (int j = 0; j < 4; ++j) { c[2 * j] += bflo(wr[i][j]); c[2 * j + 1] += bfhi(wr[i][j]); }
            LAS char* cp = sm + ((wave * 4 + (lane >> 4)) * 136 + (lane & 15) * 8) * 4;
            *(LAS f32x4*)cp = (f32x4){c[0], c[1], c[2], c[3]};
            *(LAS f32x4*)(cp + 16) = (f32x4){c[4], c[5], c[6], c[7]};
        }
        STAMPA(3);
        if (job) {
            WAIT_BAR("lgkmcnt(0)");
            float t = 0.f;
#pragma unroll
            for (int i = 0; i < 8; ++i) t += *(LAS float*)(sm + ((i * 4 + (tid & 3)) * 136 + (tid >> 2)) * 4);
            t += __shfl_xor(t, 1); t += __shfl_xor(t, 2);
            __builtin_amdgcn_raw_buffer_store_b64((u32x2){tag32, __float_as_uint(t)}, wsr,
                (tid & 3) == 0 ? WS_CSUM + (unsigned)((cnt * 2 + cmat) * 128 + (tid >> 2)) * 8u : 0xfffffff0u, 0, 16);
        }
        STAMP(1);
        SCHED();
        if (!job) __builtin_amdgcn_s_sleep(AUXW2 ? 40 : 80);
        const unsigned pf2 = *(const unsigned*)((const char*)p.hk + (size_t)((xcd * 4 + idx) * 448 + wave * 56 + (lane < 56 ? lane : 0)) * 128);
        if (AUXW2) {
            const unsigned fo = lane * 16u;
            constexpr int PER = ((4 - NTOUCH) * 14336 + 4095) / 4096;
#pragma unroll 1
            for (int i = wave; i < 7 * PER; i += 8) {
                const int g = i / PER, j = i - g * PER;
                int o = NTOUCH * 14336 + j * 4096; o = o > 57344 - 4096 ? 57344 - 4096 : o;
                glds16x4((const char*)p.w2 + (size_t)idx * 401408 + g * 57344 + o, (u32x4){fo, fo, fo, fo}, (unsigned)(size_t)sm + 65536u + wave * 4096);
            }
        }
        asm volatile("" :: "v"(pf2));
        STAMP_OUT(4);
        if (blockIdx.x == 0 && wave == 7) {
            __builtin_amdgcn_s_sleep(127);
            __builtin_amdgcn_s_sleep(100);
            GAS u64* gp = (GAS u64*)(p.ws + WS_STATS) + lane * 8;
            const int fl = lane < 56 ? lane : 0;
            GAS u64* fp = (GAS u64*)(p.ws + WS_FH) + ((fl / 14) * 56 + (fl % 14) * 2) * 16;
#pragma unroll 1
            for (unsigned spins = 0; spins < SPIN_CAP; ++spins) {
                bool ok = true;
#pragma unroll
                for (int m = 0; m < 4; ++m)
                    ok &= ((unsigned)__hip_atomic_load(gp + m * 512, RLX_AGENT) == tag32) & (__hip_atomic_load(fp + m * 224 * 16, RLX_AGENT) == tag);
                if (__all(ok)) break;
                __builtin_amdgcn_s_sleep(32);
            }
            if (lane == 0) __hip_atomic_store((GAS u64*)(p.ws + WS_EPOCH), epoch + 1, RLX_AGENT);
        }
        return;
    }

    const int gi = idx - 4, ml = gi & 3, nl = gi >> 2;
    const int mt = 4 * (xcd >> 1) + ml, nt = 7 * (xcd & 1) + nl;
    const int rot1 = ROT1;
    const int m64 = 4 * xcd + ml, n64 = nl;
    const int gpair = xcd * 28 + gi;
    const int r32 = lane & 31, hh = lane >> 5, blk = (lane >> 4) & 1, q4 = (lane & 15) >> 2, p4 = lane & 3;
    const unsigned lb = (unsigned)(size_t)sm;
    const int wq = wave & 3;
    u32x4 wvo;
    wvo[0] = (unsigned)((lane >> 4) * F + (((lane & 15) ^ ((lane >> 4) << 2)) * 8)) * 2u;
#pragma unroll
    for (int j = 1; j < 4; ++j) wvo[j] = wvo[0] + j * (8 * F - 1024);
    const u16* w1b = sptr(p.w1, wq * 16 * F + nt * 128);
    const u16* w3b = sptr(p.w3, wq * 16 * F + nt * 128);
    auto kst = [&](int si) { if (ROTM == 6 && si == 0) return 0; const int s = si + rot1; return s >= 7 ? s - ROTM : s; };
    auto group_w = [&](int si, int m) { glds16x4(sptr(m ? w3b : w1b, kst(si) * 64 * F), wvo, lb + ring(si) + 16384 + m * 16384 + wq * 4096); };
    const int kr0 = (gpair & 31) * 64, kc0 = (gpair >> 5) * 64;
    const int chn = wq >> 1, par = wq & 1, dch = 2 * gpair + chn;
    if (wave < 4) group_w(0, 0);
    {
        const bool isk = lane >= 8 && lane < 14;
        u64 mb_ = vsel(lane == 15, p.dv, vsel(lane == 14, p.hk, vsel(isk, p.k, (u64)p.sk)));
        unsigned mo = (unsigned)((kc0 + wq * 16) * 8 + (lane & 7) * 16);
        mo = isk ? (unsigned)((kr0 ? kr0 - 3 + ((lane - 8) >> 1) : 0) * D + kc0 + wq * 16 + (lane & 1) * 8) * 2u : mo;
        mo = lane == 14 ? (unsigned)dch * 4096u : mo;
        mo = lane == 15 ? (unsigned)(dch & ~7) * 2u : mo;
        const u64 ma = mb_ + mo;
        asm volatile("s_mov_b32 m0, %1\n\ts_nop 0\n\tglobal_load_lds_dwordx4 %0, off" :: "v"(ma), "s"(lb + wave * 1024));
    }
    if (wave < 4) group_w(0, 1);
    SCHED();
    const int rot2 = ROT2;
    const int w2i = L_W2 + wq * 12288;
    auto fresh_lane = [&]() { int l = lane; asm volatile("" : "+v"(l)); return l; };
    const int latei = L_CONV + wq * 8192;
    auto pair_w2 = [&](u32x4 vo, int i, int dst) {
        int t = i + rot2; t = t >= 28 ? t - 28 : t;
        glds16x4(sptr(p.w2, (wq * 448 + t * 16) * D + n64 * 64), vo, lb + dst);
    };
    auto w2_off = [&]() {
        const int l = fresh_lane();
        const unsigned o = (unsigned)((l >> 3) * D + (((l & 7) ^ (((l >> 4) & 1) << 2)) * 8)) * 2u;
        return (u32x4){o, o + (16 * D - 1024), o + 2 * (16 * D - 1024), o + 3 * (16 * D - 1024)};
    };

    if (wave < 4) {
        __builtin_amdgcn_s_setprio(MPRIO);
        const int wm = wave >> 1, wn = wave & 1;
        int xa[4], wa0, wa1;
#pragma unroll
        for (int i = 0; i < 4; ++i) xa[i] = lb + ring(0) + (((64 * wm + r32) * 128 + ((hh ^ ((r32 >> 1) & 7)) * 16)) ^ (i * 32));
        wa0 = 16384 + (8 * hh + q4) * 256 + (((8 * wn + 2 * blk + (p4 >> 1)) ^ (q4 << 2)) * 16) + 8 * (p4 & 1);
        wa1 = (wa0 ^ 64) + lb + ring(0); wa0 += lb + ring(0);
        f32x16 acc[2][2][2];
#pragma unroll
        for (int i = 0; i < 8; ++i)
#pragma unroll
            for (int r = 0; r < 16; ++r) acc[i >> 2][(i >> 1) & 1][i & 1][r] = 0.f;
        u32x4 fx[2][2], fw[2][2][2];
        auto load_frag = [&](int g, int j) {
            const int pp = g & 3, b = g & 1;
            if (j == 0) {
                if (g >= 4) {
                    const int d = ring(g >> 2) - ring((g >> 2) - 1);
                    if (pp == 0) { wa0 += d; wa1 += d; asm volatile("" : "+v"(wa0), "+v"(wa1)); }
                    xa[pp] += d; asm volatile("" : "+v"(xa[pp]));
                }
                fw[b][0][0] = ldstr2(lp(wa0) + pp * 4096, 1024);
            }
            if (j == 1) { fx[b][0] = lds128(lp(xa[pp])); fx[b][1] = lds128(lp(xa[pp]) + 4096); }
            if (j == 2) fw[b][0][1] = ldstr2(lp(wa0) + pp * 4096 + 16384, 1024);
            if (j == 3) fw[b][1][0] = ldstr2(lp(wa1) + pp * 4096, 1024);
            if (j == 4) fw[b][1][1] = ldstr2(lp(wa1) + pp * 4096 + 16384, 1024);
        };
        int ka0[2], kam[2], kwo;
#pragma unroll
        for (int rd = 0; rd < 2; ++rd) {
            const int ck = 2 * wq + rd;
            const int bq = 2 * (lane >> 4) + (p4 >> 1), bm = bq ? bq - 1 : 0;
            ka0[rd] = lb + L_KV + (8 * bq + q4) * 128 + ((ck ^ (((q4 >> 1) & 1) | ((bq & 3) << 1))) * 16) + 8 * (p4 & 1);
            kam[rd] = lb + L_KV + (8 * bm + 4 + q4) * 128 + ((ck ^ (((q4 >> 1) & 1) | ((bm & 3) << 1))) * 16) + 8 * (p4 & 1);
        }
        kwo = lb + L_KVO + wq * 2048 + (lane & 7) * 128 + (((7 - (lane >> 3)) ^ (lane & 7)) * 16);
        asm volatile("" : "+v"(ka0[0]), "+v"(ka0[1]), "+v"(kam[0]), "+v"(kam[1]), "+v"(kwo));
        WAIT_VM(4);
        u32x2 skv[2], hal[2];
#pragma unroll
        for (int rd = 0; rd < 2; ++rd) {
            LAS char* mp = sm + wave * 1024;
            skv[rd] = *(LAS u32x2*)(mp + (rd * 8 + (lane & 7)) * 8);
            const unsigned hm = kr0 ? 0xffffffffu : 0u;
            const unsigned a = *(LAS u16*)(mp + 128 + rd * 16 + (lane & 7) * 2), b = *(LAS u16*)(mp + 160 + rd * 16 + (lane & 7) * 2),
                           c = *(LAS u16*)(mp + 192 + rd * 16 + (lane & 7) * 2);
            hal[rd] = (u32x2){(a << 16) & hm, (b | (c << 16)) & hm};
            asm volatile("" : "+v"(skv[rd]), "+v"(hal[rd]));
        }
        WAIT_BAR("vmcnt(0) lgkmcnt(0)");
        STAMP(1);
        u32x2 kt[2][3], vt[2][2];
        float cf[4], kk[11], ko[8];
        u32x4 kpk;
        auto fill = [&](auto kc) {
            constexpr int k = decltype(kc)::value;
            if constexpr (k < 4) {
                constexpr int rd = k >> 1;
                if constexpr ((k & 1) == 0) { kt[rd][0] = ldstr(lp(kam[rd])); kt[rd][1] = ldstr(lp(ka0[rd])); kt[rd][2] = ldstr(lp(ka0[rd]) + 512); }
                else { vt[rd][0] = ldstr(lp(ka0[rd]) + 8192); vt[rd][1] = ldstr(lp(ka0[rd]) + 8192 + 512); }
            } else if constexpr (k < 4 + 2 * 16) {
                constexpr int rd = (k - 4) / 16, s = (k - 4) % 16;
                if constexpr (s == 0) { cf[0] = bflo1(skv[rd][0]); cf[1] = bfhi1(skv[rd][0]); cf[2] = bflo1(skv[rd][1]); cf[3] = bfhi1(skv[rd][1]); }
                else if constexpr (s == 1) {
                    const unsigned a = lane < 8 ? hal[rd][0] : kt[rd][0][0], b = lane < 8 ? hal[rd][1] : kt[rd][0][1];
                    kk[0] = bfhi1(a); kk[1] = bflo1(b); kk[2] = bfhi1(b);
                } else if constexpr (s < 4) {
                    const u32x2 t = kt[rd][s - 1];
                    kk[4 * s - 5] = bflo1(t[0]); kk[4 * s - 4] = bfhi1(t[0]); kk[4 * s - 3] = bflo1(t[1]); kk[4 * s - 2] = bfhi1(t[1]);
                } else if constexpr (s < 12) {
                    constexpr int tp = (s - 4) >> 1, o0 = ((s - 4) & 1) * 4;
#pragma unroll
                    for (int i = o0; i < o0 + 4; ++i) {
                        if (tp == 0) ko[i] = mul1(kk[i], cf[3]);
                        else fmac1(ko[i], kk[i + tp], cf[3 - tp]);
                    }
                } else {
                    constexpr int j = s - 12;
                    const unsigned v = vt[rd][j >> 1][j & 1];
                    kpk[3 - j] = pk(mul1(ko[2 * j + 1], bfhi1(v)), mul1(ko[2 * j], bflo1(v)));
                    if constexpr (j == 3) *(LAS u32x4*)(lp(kwo) + rd * 1024) = kpk;
                }
            }
        };
#pragma unroll
        for (int j = 0; j < 5; ++j) load_frag(0, j);
        f32x4 cs[2][2][4];
        unsigned tch[4] = {0u, 0u, 0u, 0u}, tch0 = 0;
        unsigned tc0 = (unsigned)((gpair * 4 + wq) * 14 + (lane < 14 ? lane : 13)) * 128u;
        unsigned tco = (unsigned)(gi * 4 + wq + (lane < 28 ? lane : 27) * 448) * 128u;
        asm volatile("" : "+v"(tco));
        static_for([&](auto gc) {
            constexpr int g = decltype(gc)::value, b = g & 1, pp = g & 3;
            SCHED();
            if (g == 11) STAMP(2);
            if (pp == 3 && g < 27) { STAMPG(2 + 2 * (g >> 2)); WAIT_BAR("lgkmcnt(0)"); STAMPG(3 + 2 * (g >> 2)); }
            if (g == 27) {
                WAIT_LGKM0();
                if (lane == 0) __hip_atomic_fetch_add((LAS unsigned*)(sm + L_CTL + 12), 1u, __ATOMIC_RELAXED, __HIP_MEMORY_SCOPE_WORKGROUP);
            }
            if (g == 11) STAMP(3);
            static_for([&](auto ic) {
                constexpr int i = decltype(ic)::value, ft = i >> 2, m = (i >> 1) & 1, sb = i & 1;
                acc[sb][ft][m] = mfma32(fw[b][ft][m], fx[b][sb], acc[sb][ft][m]);
                SCHED();
                if (g < 27 && i < 5) load_frag(g + 1, i);
                if constexpr (MWARM && i == 6 && g == MWARM - 1) {
                    u64 t;
                    asm volatile("s_mov_b64 %1, %3\n\tglobal_load_dword %0, %2, %1" : "=v"(tch0), "=&s"(t) : "v"(tc0), "s"(p.w2));
                }
                if constexpr (TOUCH && i == 4 && pp == 2 && g >= TOUCH0 && g < TOUCH0 + 4 * NTOUCH) {
                    u64 t;
                    asm volatile("s_mov_b64 %1, %3\n\tglobal_load_dword %0, %2, %1" : "=v"(tch[(g - TOUCH0) >> 2]), "=&s"(t)
                                 : "v"(tco), "s"((const char*)p.w2 + ((g - TOUCH0) >> 2) * (112 * 128)));
                }
                if constexpr (i >= 5 && g >= 7 + 4 * KVW) fill(std::integral_constant<int, (g - 7 - 4 * KVW) * 3 + i - 5>{});
                SCHED();
            }, std::make_integer_sequence<int, 8>{});
        }, std::make_integer_sequence<int, 28>{});
        STAMP(4);
        wait_lds_eq(sm + L_CTL + 64, 3u);
        unsigned sa[2];
#pragma unroll
        for (int i = 0; i < 8; ++i) cs[i >> 2][0][i & 3] = *(LAS f32x4*)(sm + L_CSUM + (64 * wn + 32 * (i >> 2) + 8 * (i & 3) + 4 * hh) * 4);
        sa[0] = *(LAS unsigned*)(sm + L_STAT + (64 * wm + r32) * 4);
        sa[1] = *(LAS unsigned*)(sm + L_STAT + (64 * wm + 32 + r32) * 4);
#pragma unroll
        for (int i = 0; i < 8; ++i) cs[i >> 2][1][i & 3] = *(LAS f32x4*)(sm + L_CSUM + 512 + (64 * wn + 32 * (i >> 2) + 8 * (i & 3) + 4 * hh) * 4);
        const int lw = fresh_lane();
        const int w2a0 = lb + w2i + (4 * (lw >> 5) + ((lw & 15) >> 2)) * 128 + (((2 * ((lw >> 4) & 1) + ((lw & 3) >> 1)) ^ (((lw >> 3) & 1) << 2)) * 16) + 8 * (lw & 1);
        const int w2a1 = w2a0 ^ 64;
        u32x4 w2r[12][2];
        auto park = [&](int i) {
            w2r[i][0] = ldstr2(lp(w2a0) + (i / 6) * L_SLOT + (i % 6) * 2048, 1024);
            w2r[i][1] = ldstr2(lp(w2a1) + (i / 6) * L_SLOT + (i % 6) * 2048, 1024);
        };
#pragma unroll
        for (int sb = 0; sb < 2; ++sb) {
            const float ri = __builtin_amdgcn_rcpf(bfhi(sa[sb])) * -1.44269504088896341f, nm = -bflo(sa[sb]), d2 = bfhi(sa[sb]) * bfhi(sa[sb]);
            const f32x2 ri2 = {ri, ri}, nm2 = {nm, nm}, dd2 = {d2, d2};
#pragma unroll
            for (int ft = 0; ft < 2; ++ft) {
#pragma unroll
                for (int t2 = 0; t2 < 2; ++t2) {
                    const int grp = sb * 4 + ft * 2 + t2;
                    if (EPARK && grp == 2) { SCHED(); wait_lds(sm + L_CTL + 48 + wq * 4); }
                    if (EPARK && grp >= 2) { SCHED(); park(grp - 2); }
                    f32x2 gu[4], ee[4];
#pragma unroll
                    for (int j = 0; j < 4; ++j) {
                        const int r = 8 * t2 + 2 * j;
                        const f32x2 g = __builtin_elementwise_fma(nm2, (f32x2){cs[ft][0][r >> 2][r & 3], cs[ft][0][r >> 2][(r & 3) + 1]}, (f32x2){acc[sb][ft][0][r], acc[sb][ft][0][r + 1]});
                        const f32x2 u = __builtin_elementwise_fma(nm2, (f32x2){cs[ft][1][r >> 2][r & 3], cs[ft][1][r >> 2][(r & 3) + 1]}, (f32x2){acc[sb][ft][1][r], acc[sb][ft][1][r + 1]});
                        const f32x2 t = g * ri2;
                        ee[j] = (f32x2){__builtin_amdgcn_exp2f(t[0]), __builtin_amdgcn_exp2f(t[1])};
                        gu[j] = g * u;
                    }
                    u32x4 st;
#pragma unroll
                    for (int j = 0; j < 4; ++j) {
                        const f32x2 dn = __builtin_elementwise_fma(ee[j], dd2, dd2);
                        const f32x2 hv = gu[j] * (f32x2){__builtin_amdgcn_rcpf(dn[0]), __builtin_amdgcn_rcpf(dn[1])};
                        st[j] = pk(hv[0], hv[1]);
                    }
                    const unsigned fi = (unsigned)((4 * mt + 2 * wm + sb) * 112 + 8 * nt + 4 * wn + 2 * ft + t2);
                    __builtin_amdgcn_raw_buffer_store_b128(st, wsr, WS_H + fi * 1024u + lane * 16u, 0, 16);
                }
            }
        }
        SCHED();
        STAMP(5);
        const u32x4 w2o = w2_off();
        const u64 tag = *(LAS u64*)(sm + L_CTL + 128);
        GAS u64* myflag = (GAS u64*)(p.ws + WS_FH + ((2 * mt + wm) * 28 + 2 * nt + wn) * 128) + (lw & 15);
        if (!__builtin_amdgcn_readfirstlane(__hip_atomic_load((LAS unsigned*)(sm + L_CTL + 112 + wq * 4), __ATOMIC_RELAXED, __HIP_MEMORY_SCOPE_WORKGROUP))) {
            WAIT_VM(0);
            if (lane < FLAGW) __hip_atomic_store(myflag, tag, RLX_AGENT);
            wait_lds(sm + L_CTL + 112 + wq * 4);
        }
        asm volatile("" ::: "memory");
#pragma unroll
        for (int i = 6; i < 12; ++i) park(i);
        WAIT_VM(0);
        if (TOUCH) asm volatile("" :: "v"(tch[0]), "v"(tch[1]), "v"(tch[2]), "v"(tch[3]));
        asm volatile("" :: "v"(tch0));
        if (lane < FLAGW) __hip_atomic_store(myflag, tag, RLX_AGENT);
        STAMP(6);
        if (!EPARK) {
#pragma unroll
            for (int i = 0; i < 6; ++i) park(i);
        }
        asm volatile("" ::: "memory");
        __hip_atomic_store((LAS unsigned*)(sm + L_CTL + 32 + wq * 4), 1u, __ATOMIC_RELAXED, __HIP_MEMORY_SCOPE_WORKGROUP);
        WAIT_LGKM0();
#pragma unroll
        for (int i = 0; i < 12; ++i) asm volatile("" : "+v"(w2r[i][0]), "+v"(w2r[i][1]));
#pragma unroll
        for (int i = 0; i < 3; ++i) pair_w2(w2o, 12 + 2 * i, w2i + i * 4096);
#pragma unroll
        for (int i = 0; i < 3; ++i) pair_w2(w2o, 18 + 2 * i, w2i + L_SLOT + i * 4096);
        STAMP(7);
        {
            GAS u64* fp = (GAS u64*)(p.ws + WS_FH + (m64 * 28 + 7 * wq + (lw < 7 ? lw : 0)) * 128);
#pragma unroll 1
            for (unsigned spins = 0; spins < SPIN_CAP; ++spins) {
                if (__all(__hip_atomic_load(fp, RLX_AGENT) == tag)) break;
                __builtin_amdgcn_s_sleep(POLL_SLEEP);
            }
            __builtin_amdgcn_fence(__ATOMIC_ACQUIRE, "wavefront");
        }
        STAMP(11);
        const unsigned hb = WS_H + (unsigned)(2 * m64 * 112 + 28 * wq) * 1024u + lw * 16u;
        u32x4 hf[28][2];
        auto load_h = [&](int i, int j) {
            int t = i + rot2; t = t >= 28 ? t - 28 : t;
            hf[i][j] = __builtin_amdgcn_raw_buffer_load_b128(wsr, hb + j * (112u * 1024u), t * 1024, 16);
        };
        constexpr int HD = HDEPTH, HG = HGROW;
#pragma unroll
        for (int i = 0; i < HD; ++i) { load_h(i, 0); load_h(i, 1); }
        STAMP(14);
        f32x16 ac2[2][2];
#pragma unroll
        for (int i = 0; i < 4; ++i)
#pragma unroll
            for (int r = 0; r < 16; ++r) ac2[i >> 1][i & 1][r] = 0.f;
        u32x4 a0 = w2r[0][0], a1 = w2r[0][1];
        static_for([&](auto ic) {
            constexpr int i = decltype(ic)::value, n = i + 1;
            SCHED();
            if (i == 23) wait_lds(sm + L_CTL + 80 + wq * 4);
            u32x4 b0, b1;
            if constexpr (n < 12) { b0 = w2r[n][0]; b1 = w2r[n][1]; }
            else if constexpr (n < 28) {
                constexpr int so = n < 24 ? ((n - 12) / 6) * L_SLOT + ((n - 12) % 6) * 2048 : (n - 24) * 2048;
                const int lo = n < 24 ? 0 : latei - w2i;
                b0 = ldstr2(lp(w2a0 + lo) + so, 1024);
                b1 = ldstr2(lp(w2a1 + lo) + so, 1024);
            }
            SCHED();
            ac2[0][0] = mfma32(a0, hf[i][0], ac2[0][0]);
            SCHED();
            constexpr int l0 = i < HG ? HD + 2 * i : HD + HG + i;
            if constexpr (l0 < 28) load_h(l0, 0);
            SCHED();
            ac2[1][0] = mfma32(a1, hf[i][0], ac2[1][0]);
            SCHED();
            if constexpr (l0 < 28) load_h(l0, 1);
            SCHED();
            ac2[0][1] = mfma32(a0, hf[i][1], ac2[0][1]);
            SCHED();
            if constexpr (i < HG) load_h(l0 + 1, 0);
            SCHED();
            ac2[1][1] = mfma32(a1, hf[i][1], ac2[1][1]);
            SCHED();
            if constexpr (i < HG) load_h(l0 + 1, 1);
            a0 = b0; a1 = b1;
        }, std::make_integer_sequence<int, 28>{});
        SCHED();
        STAMP(15);
        WAIT_LGKM0();
#pragma unroll
        for (int v = 0; v < 16; ++v) {
            const f32x16& t = ac2[v >> 3][(v >> 2) & 1];
            const int r0 = 4 * (v & 3);
            *(LAS f32x4*)(sm + w2i + part(v >> 3, (v >> 2) & 1, v & 3) + lw * 16) = (f32x4){t[r0], t[r0 + 1], t[r0 + 2], t[r0 + 3]};
        }
        WAIT_BAR("lgkmcnt(0)");
        STAMP(16);
        STAMP(17);
        STAMP_OUT(18);
    } else {
        u32x4 xvo;
#pragma unroll
        for (int j = 0; j < 4; ++j) xvo[j] = (unsigned)((8 * j + (lane >> 3)) * D + ((((lane & 7) ^ (lane >> 4)) ^ (4 * (j & 1))) * 8)) * 2u - 1024u * j;
        const u16* xb = sptr(p.x, (mt * 128 + wq * 32) * D);
        auto group_x = [&](int si) { glds16x4(sptr(xb, kst(si) * 64), xvo, lb + ring(si) + wq * 4096); };
        group_x(0);
        group_x(1); group_w(1, 0); group_w(1, 1);
        const int cbi = L_CONV + chn * C_CH;
        LAS char* cb = sm + cbi;
        unsigned kvo[2], uso;
#pragma unroll
        for (int rd = 0; rd < 2; ++rd) kvo[rd] = (unsigned)((lane >> 3) * D + (((lane & 7) ^ (((lane >> 4) & 1) | (((2 * wq + rd) & 3) << 1))) * 8)) * 2u;
        uso = WS_U + (unsigned)(kc0 + wq * 16 + (lane >> 3)) * 8192u + (unsigned)(1984 - kr0 + (lane & 7) * 8) * 2u;
        int kro = lb + L_KVO + wq * 2048 + (lane >> 3) * 128 + (((lane & 7) ^ (lane >> 3)) * 16);
        const int cc = 2 * par + ((lane >> 2) & 1);
        int zo = cbi + C_U + par * C_US + (248 + (lane < 36 ? lane : 35)) * 16;
        zo = lane < 8 ? cbi + cc * C_HS + (lane & 3) * 16 : zo;
        u32x4 w2o = w2_off();
        const unsigned fo = lane * 16u;
        unsigned ao = (wq < 2 ? WS_CSUM + (unsigned)(nt * 2 + wq) * 1024u : WS_STATS + mt * 1024u) + fo;
        asm volatile("" : "+v"(zo), "+v"(w2o), "+v"(kvo[0]), "+v"(kvo[1]), "+v"(uso), "+v"(kro), "+v"(ao));
        unsigned eo = (lane & 1) * 4u;
        asm volatile("" : "+v"(eo));
        WAIT_VM(16);
        unsigned tag32, taghi;
        u32x4 bval, ug[2];
        unsigned dbits = *(LAS u16*)(sm + wave * 1024 + 240 + (dch & 7) * 2);
        {
            const u32x4 h0 = lds128(sm + wave * 1024 + 224);
            const bool s1 = !(cc & 1), s2 = !(cc & 2);
            const unsigned a0 = s1 ? 0u : h0[0], a1 = s1 ? h0[0] : h0[1], a2 = s1 ? h0[1] : h0[2], a3 = s1 ? h0[2] : h0[3];
            const unsigned lm = (lane == 3 || lane == 7) ? 0xffffffffu : 0u;
            bval = (u32x4){(s2 ? 0u : a0) & lm, (s2 ? 0u : a1) & lm, (s2 ? a0 : a2) & lm, (s2 ? a1 : a3) & lm};
            asm volatile("" : "+v"(bval), "+v"(dbits));
        }
        auto kv_store = [&](int rd) {
            LAS unsigned* kp = (LAS unsigned*)(lp(kro) + rd * 1024);
            ug[0][1] = kp[0]; asm volatile("" ::: "memory"); ug[0][3] = kp[1]; asm volatile("" ::: "memory");
            ug[1][1] = kp[2]; asm volatile("" ::: "memory"); ug[1][3] = kp[3];
            __builtin_amdgcn_raw_buffer_store_b128(ug[0], wsr, uso, rd * 65536, 16);
            __builtin_amdgcn_raw_buffer_store_b128(ug[1], wsr, uso, rd * 65536 + 4096, 16);
        };
        WAIT_BAR("vmcnt(12) lgkmcnt(0)");
        STAMP(1);
#pragma unroll
        for (int w = 0; w < 5; ++w) {
            if (w == 3) glds4c(p.ws + WS_EPOCH, eo, lb + L_RAW + wq * 1024);
            group_x(w + 2); group_w(w + 2, 0); group_w(w + 2, 1);
            if (w == KVW) {
#pragma unroll
                for (int j = 0; j < 2; ++j) {
                    glds16(sptr(p.k, (kr0 + (2 * wq + j) * 8) * D + kc0), kvo[j], lb + L_KV + (2 * wq + j) * 1024);
                    glds16(sptr(p.v, (kr0 + (2 * wq + j) * 8) * D + kc0), kvo[j], lb + L_KV + 8192 + (2 * wq + j) * 1024);
                }
            }
            if (w == 4) {
                const u32x2 ev = *(LAS u32x2*)(sm + L_RAW + wq * 1024);
                tag32 = __builtin_amdgcn_readfirstlane(ev[0]) ^ (unsigned)TAG_SALT;
                taghi = __builtin_amdgcn_readfirstlane(ev[1]) ^ (unsigned)(TAG_SALT >> 32);
                asm volatile("s_nop 1" : "+s"(tag32), "+s"(taghi));
                ug[0][0] = vmov1(tag32); ug[0][2] = vmov1(tag32); ug[1][0] = vmov1(tag32); ug[1][2] = vmov1(tag32);
                if (lane < 34) *(LAS unsigned*)(sm + L_CTL + lane * 4) = lane < 32 ? 0u : lane == 32 ? tag32 : taghi;
            }
            if (w == 4 && KVW == 0) kv_store(0);
            if (w == 2) STAMP(2);
            STAMPG(2 + 2 * w);
            if (w == KVW) WAIT_BAR("vmcnt(16) lgkmcnt(0)"); else WAIT_BAR("vmcnt(12) lgkmcnt(0)");
            STAMPG(3 + 2 * w);
            if (w == 2) STAMP(3);
        }
        glds16c(p.ws, ao, lb + L_RAW + wq * 1024);
#pragma unroll
        for (int i = 0; i < 3; ++i) pair_w2(w2o, 2 * i, w2i + i * 4096);
        kv_store(KVW ? 0 : 1);
        STAMPG(12); WAIT_BAR("vmcnt(12) lgkmcnt(0)"); STAMPG(13);
        if (KVW) kv_store(1);
        asm volatile("" ::: "memory");
        if (lane == 0) __hip_atomic_fetch_add((LAS unsigned*)(sm + L_CTL + 12), 1u, __ATOMIC_RELAXED, __HIP_MEMORY_SCOPE_WORKGROUP);
        if (wq < 3) {
            u32x4 av = lds128(sm + L_RAW + wq * 1024 + lane * 16);
#pragma unroll 1
            for (unsigned spins = 0; spins < SPIN_CAP; ++spins) {
                if (__all(((av[0] ^ tag32) | (av[2] ^ tag32)) == 0u)) break;
                asm volatile("" ::: "memory");
                av = __builtin_amdgcn_raw_buffer_load_b128(wsr, ao, 0, 16);
                asm volatile("" : "+v"(av));
            }
            *(LAS u32x2*)(sm + L_CSUM + wq * 512 + lane * 8) = (u32x2){av[1], av[3]};
            asm volatile("" ::: "memory");
            if (lane == 0) __hip_atomic_fetch_add((LAS unsigned*)(sm + L_CTL + 64), 1u, __ATOMIC_RELAXED, __HIP_MEMORY_SCOPE_WORKGROUP);
        }
        STAMP(4);
#pragma unroll
        for (int i = 0; i < 3; ++i) pair_w2(w2o, 6 + 2 * i, w2i + L_SLOT + i * 4096);
        WAIT_VM(12);
        __hip_atomic_store((LAS unsigned*)(sm + L_CTL + 48 + wq * 4), 1u, __ATOMIC_RELAXED, __HIP_MEMORY_SCOPE_WORKGROUP);
        wait_lds_eq(sm + L_CTL + 12, 8u);
        STAMP(5);
        __builtin_amdgcn_s_setprio(2);
        const unsigned ubase = WS_U + (unsigned)dch * 8192u;
        glds16x2c(p.ws + ubase + par * 2048, fo, lb + cbi + C_U + par * 2048);
        glds16x2c(p.ws + ubase + 4096 + par * 2048, fo, lb + cbi + C_U + C_US + par * 2048);
        glds4c(p.ws + ubase + (1 - par) * 2048, eo, lb + L_UNX + wq * 256);
        {
            const u32x4 fvo = {fo, fo, fo, lane == 63 ? 62u * 16u : fo};
#pragma unroll
            for (int c = 0; c < 2; ++c) glds16x4(sptr(p.hk, dch * S + 4 * par + 2 * c + 2), fvo, lb + cbi + (2 * par + c) * C_HS + 64);
        }
        *(LAS u32x4*)(sm + zo) = bval;
        WAIT_VM(13);
        __hip_atomic_store((LAS unsigned*)(sm + L_CTL + 112 + wq * 4), 1u, __ATOMIC_RELAXED, __HIP_MEMORY_SCOPE_WORKGROUP);
        WAIT_VM(8);
        {
            u32x4 ua[2], ub[2]; u32x2 un[2];
#pragma unroll
            for (int rd = 0; rd < 2; ++rd) {
                const int x8 = 128 * par + 64 * rd + lane;
                ua[rd] = lds128(cb + C_U + x8 * 16); ub[rd] = lds128(cb + C_U + C_US + x8 * 16);
                un[rd] = *(LAS u32x2*)((rd == 1 && lane == 63) ? sm + L_UNX + wq * 256 : cb + C_U + x8 * 16 + 16);
            }
#pragma unroll 1
            for (unsigned spins = 0; spins < SPIN_CAP; ++spins) {
                unsigned df = 0;
#pragma unroll
                for (int rd = 0; rd < 2; ++rd) df |= (ua[rd][0] ^ tag32) | (ua[rd][2] ^ tag32) | (ub[rd][0] ^ tag32) | (ub[rd][2] ^ tag32) | (un[rd][0] ^ tag32);
                if (__all(df == 0u)) break;
                __builtin_amdgcn_s_sleep(URETRY);
                asm volatile("" ::: "memory");
#pragma unroll
                for (int rd = 0; rd < 2; ++rd) {
                    const int x8 = 128 * par + 64 * rd + lane;
                    const unsigned uo = ubase + (unsigned)x8 * 16u;
                    ua[rd] = __builtin_amdgcn_raw_buffer_load_b128(wsr, uo, 0, 16);
                    ub[rd] = __builtin_amdgcn_raw_buffer_load_b128(wsr, uo + 4096u, 0, 16);
                    un[rd] = __builtin_amdgcn_raw_buffer_load_b64(wsr, x8 == 255 ? uo : uo + 16u, 0, 16);
                }
                asm volatile("" : "+v"(ua[0]), "+v"(ub[0]), "+v"(un[0]), "+v"(ua[1]), "+v"(ub[1]), "+v"(un[1]));
            }
#pragma unroll
            for (int rd = 0; rd < 2; ++rd) {
                const int x8 = 128 * par + 64 * rd + lane;
                const unsigned nx = x8 == 255 ? 0u : un[rd][1];
                *(LAS u32x4*)(cb + C_U + x8 * 16) = (u32x4){__builtin_amdgcn_alignbit(ua[rd][3], ua[rd][1], 16), __builtin_amdgcn_alignbit(ub[rd][1], ua[rd][3], 16),
                                                            __builtin_amdgcn_alignbit(ub[rd][3], ub[rd][1], 16), __builtin_amdgcn_alignbit(nx, ub[rd][3], 16)};
                *(LAS u32x4*)(cb + C_U + C_US + x8 * 16) = (u32x4){ua[rd][1], ua[rd][3], ub[rd][1], ub[rd][3]};
            }
        }
        asm volatile("s_waitcnt vmcnt(0) lgkmcnt(0)" ::: "memory");
        __hip_atomic_store((LAS unsigned*)(sm + L_CTL + 16 + wq * 4), 1u, __ATOMIC_RELAXED, __HIP_MEMORY_SCOPE_WORKGROUP);
        u32x4 lw2[8], xv[2];
        {
            const __amdgpu_buffer_rsrc_t wr2 = __builtin_amdgcn_make_buffer_rsrc((void*)p.w2, 0, F * D * 2, 0x00020000);
#pragma unroll
            for (int j = 0; j < 8; ++j) {
                int t = 24 + (j >> 2) * 2 + rot2; t = t >= 28 ? t - 28 : t;
                lw2[j] = __builtin_amdgcn_raw_buffer_load_b128(wr2, w2o[0], ((wq * 448 + t * 16 + (j & 3) * 8) * D + n64 * 64) * 2, 0);
            }
        }
        const int lf = fresh_lane();
        const int srow = 64 * m64 + 16 * wq + 2 * (lf >> 3);
        const unsigned xoff = (unsigned)(srow * D + 64 * n64 + 8 * (lf & 7)) * 2u;
        {
            const __amdgpu_buffer_rsrc_t xr = __builtin_amdgcn_make_buffer_rsrc((void*)p.x, 0, S * D * 2, 0x00020000);
            xv[0] = __builtin_amdgcn_raw_buffer_load_b128(xr, xoff, 0, 0);
            xv[1] = __builtin_amdgcn_raw_buffer_load_b128(xr, xoff, D * 2, 0);
        }
        SCHED();
        wait_lds(sm + L_CTL + 16 + (wq ^ 1) * 4);
        STAMP(7);
        STAMP(6);
        const int n16 = lane & 15, kg = lane >> 4;
        f32x4 ca[4];
#pragma unroll
        for (int j = 0; j < 4; ++j) ca[j] = (f32x4){0.f, 0.f, 0.f, 0.f};
        int apo = lb + cbi + (n16 & 3) * C_HS + ((n16 >> 2) + kg) * 16;
        int bpo = lb + cbi + C_U + (n16 & 1) * C_US + (252 - 4 * (n16 >> 1) + kg) * 16 - 7 * 512;
        u32x4 fa[2][8], fb[2][8];
#pragma unroll
        for (int i = 0; i < 8; ++i) { fa[0][i] = lds128(lp(apo) + i * 512); fb[0][i] = lds128(lp(bpo) + (7 - i) * 512); }
        const bool odd = lf & 1;
        int ysrc = (2 * (lf >> 3) + (lf & 1) + 16 * ((lf >> 1) & 3)) * 4;
        unsigned yso = WS_Y + (unsigned)dch * 8192u + lf * 16u;
        f32x4 kd[4];
        {
            const float dval = bflo(dbits);
            const int sl0 = 32 * ((lf & 15) >> 1) + 8 * (lf >> 4) + 4 * (lf & 1);
#pragma unroll
            for (int j = 0; j < 4; ++j) {
                const int tj = (j == 0 ? 0 : j == 1 ? 3 : j == 2 ? 4 : 7) ^ par;
                const u32x2 kvr = *(LAS u32x2*)(cb + C_U + C_US + (2044 - 256 * tj - sl0) * 2);
                const unsigned ka = pk(bfhi(kvr[1]) * dval, bflo(kvr[1]) * dval), kb = pk(bfhi(kvr[0]) * dval, bflo(kvr[0]) * dval);
                kd[j] = (f32x4){bflo(ka), bfhi(ka), bflo(kb), bfhi(kb)};
                asm volatile("" : "+v"(kd[j]));
            }
            asm volatile("" : "+v"(ysrc), "+v"(yso));
        }
        wait_lds(sm + L_CTL + 32 + wq * 4);
        STAMP(8);
        auto conv_all = [&](auto pc_) {
            constexpr int P = decltype(pc_)::value;
            static_for([&](auto rc) {
                constexpr int r = decltype(rc)::value, c = r & 1, n = c ^ 1;
                static_for([&](auto kc) {
                    constexpr int k = decltype(kc)::value;
                    constexpr int T[4] = {P, 3 - P, 4 + P, 7 - P};
                    constexpr int j = k <= T[0] ? 0 : k <= T[0] + T[1] + 1 ? 1 : k <= T[0] + T[1] + T[2] + 2 ? 2 : 3;
                    constexpr int pp = k - (j == 0 ? 0 : j == 1 ? T[0] + 1 : j == 2 ? T[0] + T[1] + 2 : T[0] + T[1] + T[2] + 3);
                    ca[j] = mfma16(fa[c][pp], fb[c][T[j] - pp], ca[j]);
                    SCHED();
                    if constexpr (r < 7 && k < 2 * (T[3] + 1)) {
                        if constexpr (k & 1) fb[n][k >> 1] = lds128(lp(bpo) + (r + 1) * 64 + (7 - (k >> 1)) * 512);
                        else fa[n][k >> 1] = lds128(lp(apo) + (r + 1) * 64 + (k >> 1) * 512);
                    }
                    SCHED();
                }, std::make_integer_sequence<int, 18>{});
                asm volatile("" : "+v"(apo), "+v"(bpo));
            }, std::make_integer_sequence<int, 8>{});
        };
        if (par) conv_all(std::integral_constant<int, 1>{}); else conv_all(std::integral_constant<int, 0>{});
        SCHED();
        STAMP(9);
        {
            float r0[4], r1[4];
#pragma unroll
            for (int j = 0; j < 4; ++j) { r0[j] = dpp_xor1(odd ? ca[j][0] : ca[j][2]); r1[j] = dpp_xor1(odd ? ca[j][1] : ca[j][3]); }
            unsigned rp[4][2];
#pragma unroll
            for (int j = 0; j < 4; ++j) {
                const unsigned ya = pk(odd ? r0[j] : ca[j][0], odd ? ca[j][2] : r0[j]), yb = pk(odd ? r1[j] : ca[j][1], odd ? ca[j][3] : r1[j]);
                rp[j][0] = __builtin_amdgcn_ds_bpermute(ysrc, pk(add1(bflo1(ya), kd[j][0]), add1(bfhi1(ya), kd[j][1])));
                rp[j][1] = __builtin_amdgcn_ds_bpermute(ysrc, pk(add1(bflo1(yb), kd[j][2]), add1(bfhi1(yb), kd[j][3])));
            }
#pragma unroll
            for (int j = 0; j < 4; ++j)
                __builtin_amdgcn_raw_buffer_store_b128((u32x4){vmov1(tag32 ^ Y_TAG), rp[j][0], vmov1(tag32 ^ Y_TAG), rp[j][1]}, wsr, yso,
                                                       ((j == 0 ? 0 : j == 1 ? 3 : j == 2 ? 4 : 7) ^ par) * 1024, 16);
        }
        STAMP(10);
        WAIT_LGKM0();
        if (lane == 0) __hip_atomic_fetch_add((LAS unsigned*)(sm + L_CTL + 8), 1u, __ATOMIC_RELAXED, __HIP_MEMORY_SCOPE_WORKGROUP);
#pragma unroll 1
        for (unsigned spins = 0; spins < (1u << 22); ++spins) {
            if (__builtin_amdgcn_readfirstlane(__hip_atomic_load((LAS unsigned*)(sm + L_CTL + 8), __ATOMIC_RELAXED, __HIP_MEMORY_SCOPE_WORKGROUP)) == 4u) break;
            __builtin_amdgcn_s_sleep(1);
        }
        asm volatile("" ::: "memory");
#pragma unroll
        for (int j = 0; j < 8; ++j) *(LAS u32x4*)(sm + latei + j * 1024 + lf * 16) = lw2[j];
        asm volatile("" ::: "memory");
        __hip_atomic_store((LAS unsigned*)(sm + L_CTL + 80 + wq * 4), 1u, __ATOMIC_RELAXED, __HIP_MEMORY_SCOPE_WORKGROUP);
        STAMP(11);
        __builtin_amdgcn_s_sleep(YSLEEP);
        u32x2 yv[8];
        u32x4 stv;
        const int yl = L_CONV + 32768 + wq * 4096;
        const unsigned yg = (unsigned)(((lf >> 2) & 7) * 8 + (lf >> 5)) * 8192u + (unsigned)((lf & 3) ^ (((lf >> 4) & 1) << 1)) * 16u;
        const u32x4 yvo = {yg, yg + (16384u - 1024u), yg + 2 * (16384u - 1024u), yg + 3 * (16384u - 1024u)};
        const char* ybase = p.ws + WS_Y + (unsigned)(64 * n64) * 8192u + (unsigned)(64 * m64 + 16 * wq) * 4u;
        const int yr = lb + yl + (lf & 7) * 64 + (((lf >> 3) * 8) ^ (((lf >> 2) & 1) << 5));
#pragma unroll 1
        for (unsigned spins = 0; spins < SPIN_CAP; ++spins) {
            glds16x4c(ybase, yvo, lb + yl);
            stv = __builtin_amdgcn_raw_buffer_load_b128(wsr, WS_STATS + (unsigned)srow * 8u, 0, 16);
            WAIT_VM(0);
#pragma unroll
            for (int i = 0; i < 8; ++i) yv[i] = *(LAS u32x2*)(lp(yr) + i * 512);
            unsigned df = (stv[0] ^ tag32) | (stv[2] ^ tag32);
#pragma unroll
            for (int i = 0; i < 8; ++i) df |= yv[i][0] ^ (tag32 ^ Y_TAG);
            if (__all(df == 0u)) break;
            __builtin_amdgcn_s_sleep(8);
        }
        STAMP(14);
        __builtin_amdgcn_s_setprio(0);
        f32x2 sf[8], xf[8];
#pragma unroll
        for (int e = 0; e < 2; ++e) {
            const float nmb = -bflo(stv[1 + 2 * e]);
            float ri = __builtin_amdgcn_rcpf(bfhi(stv[1 + 2 * e]));
            asm volatile("s_nop 1" : "+v"(ri));
#pragma unroll
            for (int j = 0; j < 4; ++j) {
                const int i = 4 * e + j;
                const unsigned xd = xv[e][j];
                xf[i] = (f32x2){bflo1(xd), bfhi1(xd)};
                const unsigned tp = pk(add1(xf[i][0], nmb), add1(xf[i][1], nmb));
                const unsigned np = pk(mul1(bflo1(tp), ri), mul1(bfhi1(tp), ri));
                sf[i] = (f32x2){mul1(bflo1(np), e ? bfhi1(yv[2 * j][1]) : bflo1(yv[2 * j][1])), mul1(bfhi1(np), e ? bfhi1(yv[2 * j + 1][1]) : bflo1(yv[2 * j + 1][1]))};
                asm volatile("" : "+v"(sf[i]), "+v"(xf[i]));
            }
        }
        const int rm = lb + L_W2 + part((lf >> 2) & 1, wq >> 1, lf & 3) + (16 * (wq & 1) + 2 * (lf >> 3)) * 16;
        STAMP(15);
        WAIT_BAR("lgkmcnt(0)");
        STAMP(16);
        f32x4 o[4];
#pragma unroll
        for (int j = 0; j < 4; ++j) {
            LAS char* rp = lp(rm) + (j >> 1) * 16 + (j & 1) * 512;
            o[j] = (*(LAS f32x4*)rp + *(LAS f32x4*)(rp + 12288)) + (*(LAS f32x4*)(rp + 24576) + *(LAS f32x4*)(rp + 36864));
        }
        f32x2 so[8], ev[8];
#pragma unroll
        for (int i = 0; i < 8; ++i) {
            const f32x2 ov = {o[i >> 1][(i & 1) * 2], o[i >> 1][(i & 1) * 2 + 1]};
            const f32x2 t = ov * (f32x2){-1.44269504088896341f, -1.44269504088896341f};
            ev[i] = (f32x2){__builtin_amdgcn_exp2f(t[0]), __builtin_amdgcn_exp2f(t[1])};
            so[i] = sf[i] * ov;
        }
        SCHED();
        u32x4 st[2];
#pragma unroll
        for (int i = 0; i < 8; ++i) {
            const f32x2 dn = ev[i] + (f32x2){1.f, 1.f};
            const f32x2 r = __builtin_elementwise_fma(so[i], (f32x2){__builtin_amdgcn_rcpf(dn[0]), __builtin_amdgcn_rcpf(dn[1])}, xf[i]);
            st[i >> 2][i & 3] = pk(r[0], r[1]);
        }
        const __amdgpu_buffer_rsrc_t orr = __builtin_amdgcn_make_buffer_rsrc((void*)p.out, 0, S * D * 2, 0x00020000);
        __builtin_amdgcn_raw_buffer_store_b128(st[0], orr, xoff, 0, 16);
        __builtin_amdgcn_raw_buffer_store_b128(st[1], orr, xoff, D * 2, 16);
        STAMP(17);
        STAMP_OUT(18);
    }
}

extern "C" void kernel_launch(void* const* d_in, const int* in_sizes, int n_in,
                              void* d_out, int out_size, void* d_ws, size_t ws_size,
                              hipStream_t stream) {
    Params p;
    p.x = (const u16*)d_in[0]; p.k = (const u16*)d_in[1]; p.v = (const u16*)d_in[2];
    p.sk = (const u16*)d_in[3]; p.hk = (const u16*)d_in[4]; p.dv = (const u16*)d_in[5];
    p.w1 = (const u16*)d_in[7]; p.w3 = (const u16*)d_in[8]; p.w2 = (const u16*)d_in[9];
    p.out = (u16*)d_out; p.ws = (char*)d_ws;
    (void)hipFuncSetAttribute((const void*)fused_kernel, hipFuncAttributeMaxDynamicSharedMemorySize, L_TOTAL);
    fused_kernel<<<256, 512, L_TOTAL, stream>>>(p);
}
```

```cpp
#include <hip/hip_runtime.h>
#include <stdint.h>
#include <utility>

typedef unsigned short u16;
typedef unsigned long long u64;
typedef short s16x4 __attribute__((ext_vector_type(4)));
typedef __bf16 bf16x2 __attribute__((ext_vector_type(2)));
typedef __bf16 bf16x8 __attribute__((ext_vector_type(8)));
typedef float f32x2 __attribute__((ext_vector_type(2)));
typedef float f32x4 __attribute__((ext_vector_type(4)));
typedef float f32x16 __attribute__((ext_vector_type(16)));
typedef unsigned u32x2 __attribute__((ext_vector_type(2)));
typedef unsigned u32x4 __attribute__((ext_vector_type(4)));
#define LAS __attribute__((address_space(3)))
#define GAS __attribute__((address_space(1)))
#define RLX_AGENT __ATOMIC_RELAXED, __HIP_MEMORY_SCOPE_AGENT

constexpr int S = 2048, D = 448, F = 1792;

constexpr unsigned WS_EPOCH = 0;
constexpr unsigned WS_FH = 65536;
constexpr unsigned WS_STATS = 8192;
constexpr unsigned WS_CSUM = 24576;
constexpr unsigned WS_U = 262144;
constexpr unsigned WS_Y = WS_U;
constexpr unsigned WS_H = WS_Y + D * S * 4;
constexpr unsigned WS_END = WS_H + 64 * 112 * 1024;

constexpr int L_KV = 0;
constexpr int L_CTL = 0;
constexpr int L_CSUM = 256;
constexpr int L_STAT = 1280;
constexpr int L_RAW = 2048;
constexpr int L_KVO = 8192;
constexpr int L_UNX = 6144;
constexpr int L_SLOT = 49152;
constexpr int L_CONV = 12288;
constexpr int C_CH = 26112, C_HS = 4160, C_U = 16640, C_US = 4640;
constexpr int L_W2 = 16384 + L_SLOT;
constexpr int L_TOTAL = 163840;
__host__ __device__ constexpr int part(int db, int sb, int a) { return db * L_SLOT + sb * 4352 + a * 1024 + (a & 1) * 16 + (a >> 1) * 128; }
__host__ __device__ constexpr int ring(int stage) { return 16384 + (stage % 3) * L_SLOT; }
constexpr unsigned CODE_PREFETCH = 36864;
constexpr int HD = 6, HG = 4;
constexpr int NTOUCH = 2;
constexpr int YSTEP = 18;

constexpr u64 TAG_SALT = 0x9E3779B97F4A7C15ull;
constexpr unsigned Y_TAG = 0x80000000u;
constexpr unsigned SPIN_CAP = 1u << 18;

struct Params {
    const u16* x; const u16* k; const u16* v; const u16* sk; const u16* hk; const u16* dv;
    const u16* w1; const u16* w3; const u16* w2; u16* out; char* ws;
};

__device__ __forceinline__ float bflo(unsigned p) { return __uint_as_float(p << 16); }
__device__ __forceinline__ float bfhi(unsigned p) { return __uint_as_float(p & 0xffff0000u); }
__device__ __forceinline__ float bflo1(unsigned p) { float r; asm("v_lshlrev_b32 %0, 16, %1" : "=v"(r) : "v"(p)); return r; }
__device__ __forceinline__ float bfhi1(unsigned p) { float r; asm("v_and_b32 %0, 0xffff0000, %1" : "=v"(r) : "v"(p)); return r; }
__device__ __forceinline__ float mul1(float a, float b) { float r; asm("v_mul_f32 %0, %1, %2" : "=v"(r) : "v"(a), "v"(b)); return r; }
__device__ __forceinline__ void fmac1(float& d, float a, float b) { asm("v_fmac_f32 %0, %1, %2" : "+v"(d) : "v"(a), "v"(b)); }
__device__ __forceinline__ float dpp_xor1(float v) {
    return __int_as_float(__builtin_amdgcn_update_dpp(0, __float_as_int(v), 0xB1, 0xf, 0xf, true));
}
__device__ __forceinline__ float add1(float a, float b) { float r; asm("v_add_f32 %0, %1, %2" : "=v"(r) : "v"(a), "v"(b)); return r; }
__device__ __forceinline__ unsigned vmov1(unsigned s) { unsigned r; asm volatile("v_mov_b32 %0, %1" : "=v"(r) : "s"(s)); return r; }
__device__ __forceinline__ unsigned pk(float a, float b) {
    f32x2 f = {a, b};
    return __builtin_bit_cast(unsigned, __builtin_convertvector(f, bf16x2));
}
__device__ __forceinline__ float rbf(float a) { return bflo(pk(a, a)); }
__device__ __forceinline__ float dot2(unsigned a, unsigned b, float c) {
    return __builtin_amdgcn_fdot2_f32_bf16(__builtin_bit_cast(bf16x2, a), __builtin_bit_cast(bf16x2, b), c, false);
}
__device__ __forceinline__ void glds16(const void* base, unsigned voff, unsigned ldst) {
    u64 t;
    asm volatile("s_mov_b32 m0, %3\n\ts_mov_b64 %0, %2\n\tglobal_load_lds_dwordx4 %1, %0 nt" : "=&s"(t) : "v"(voff), "s"(base), "s"(ldst));
}
__device__ __forceinline__ void glds16x4(const void* base, u32x4 vo, unsigned ldst) {
    u64 t;
    asm volatile("s_mov_b32 m0, %6\n\ts_mov_b64 %0, %5\n\tglobal_load_lds_dwordx4 %1, %0\n\tglobal_load_lds_dwordx4 %2, %0 offset:1024\n\t"
                 "global_load_lds_dwordx4 %3, %0 offset:2048\n\tglobal_load_lds_dwordx4 %4, %0 offset:3072"
                 : "=&s"(t) : "v"(vo[0]), "v"(vo[1]), "v"(vo[2]), "v"(vo[3]), "s"(base), "s"(ldst));
}
__device__ __forceinline__ void glds16x4c(const void* base, u32x4 vo, unsigned ldst) {
    u64 t;
    asm volatile("s_mov_b32 m0, %6\n\ts_mov_b64 %0, %5\n\tglobal_load_lds_dwordx4 %1, %0 sc1\n\tglobal_load_lds_dwordx4 %2, %0 offset:1024 sc1\n\t"
                 "global_load_lds_dwordx4 %3, %0 offset:2048 sc1\n\tglobal_load_lds_dwordx4 %4, %0 offset:3072 sc1"
                 : "=&s"(t) : "v"(vo[0]), "v"(vo[1]), "v"(vo[2]), "v"(vo[3]), "s"(base), "s"(ldst) : "memory");
}
__device__ __forceinline__ void glds16x2c(const void* base, unsigned voff, unsigned ldst) {
    u64 t;
    asm volatile("s_mov_b32 m0, %3\n\ts_mov_b64 %0, %2\n\tglobal_load_lds_dwordx4 %1, %0 sc1\n\tglobal_load_lds_dwordx4 %1, %0 offset:1024 sc1"
                 : "=&s"(t) : "v"(voff), "s"(base), "s"(ldst));
}
__device__ __forceinline__ void glds16c(const void* base, unsigned voff, unsigned ldst) {
    u64 t;
    asm volatile("s_mov_b32 m0, %3\n\ts_mov_b64 %0, %2\n\tglobal_load_lds_dwordx4 %1, %0 sc1" : "=&s"(t) : "v"(voff), "s"(base), "s"(ldst));
}
__device__ __forceinline__ void glds4c(const void* base, unsigned voff, unsigned ldst) {
    u64 t;
    asm volatile("s_mov_b32 m0, %3\n\ts_mov_b64 %0, %2\n\tglobal_load_lds_dword %1, %0 sc1" : "=&s"(t) : "v"(voff), "s"(base), "s"(ldst));
}
__device__ __forceinline__ const u16* sptr(const u16* base, int off) {
    const u16* r = base + __builtin_amdgcn_readfirstlane(off);
    asm("" : "+s"(r));
    return r;
}
__device__ __forceinline__ u64 vsel(bool c, const void* a, u64 b) {
    u64 va = (u64)a;
    asm("" : "+v"(va), "+v"(b));
    return c ? va : b;
}
__device__ __forceinline__ LAS char* lp(int a) { return (LAS char*)(size_t)(unsigned)a; }
__device__ __forceinline__ u32x4 lds128(LAS char* p) { return *(LAS u32x4*)p; }
__device__ __forceinline__ u32x2 ldstr(LAS char* p) {
    return __builtin_bit_cast(u32x2, __builtin_amdgcn_ds_read_tr16_b64_v4i16((LAS s16x4*)p));
}
__device__ __forceinline__ u32x4 ldstr2(LAS char* p, int off) {
    u32x2 a = ldstr(p), b = ldstr(p + off);
    return (u32x4){a.x, a.y, b.x, b.y};
}
__device__ __forceinline__ f32x16 mfma32(u32x4 a, u32x4 b, f32x16 c) {
    return __builtin_amdgcn_mfma_f32_32x32x16_bf16(__builtin_bit_cast(bf16x8, a), __builtin_bit_cast(bf16x8, b), c, 0, 0, 0);
}
__device__ __forceinline__ f32x4 mfma16(u32x4 a, u32x4 b, f32x4 c) {
    return __builtin_amdgcn_mfma_f32_16x16x32_bf16(__builtin_bit_cast(bf16x8, a), __builtin_bit_cast(bf16x8, b), c, 0, 0, 0);
}
template <class Fn, int... I>
__device__ __forceinline__ void static_for(Fn&& fn, std::integer_sequence<int, I...>) { (fn(std::integral_constant<int, I>{}), ...); }
#define WAIT_VM(n) asm volatile("s_waitcnt vmcnt(" #n ")" ::: "memory")
#define WAIT_LGKM0() asm volatile("s_waitcnt lgkmcnt(0)" ::: "memory")
#define WAIT_BAR(w) asm volatile("s_waitcnt " w "\n\ts_barrier" ::: "memory")
#define SCHED() __builtin_amdgcn_sched_barrier(0)

__device__ __forceinline__ void wait_lds(LAS char* word) {
#pragma unroll 1
    for (unsigned spins = 0; spins < (SPIN_CAP << 4); ++spins) {
        if (__builtin_amdgcn_readfirstlane(__hip_atomic_load((LAS unsigned*)word, __ATOMIC_RELAXED, __HIP_MEMORY_SCOPE_WORKGROUP))) break;
        __builtin_amdgcn_s_sleep(1);
    }
    asm volatile("" ::: "memory");
}
__device__ __forceinline__ void wait_lds_eq(LAS char* word, unsigned want) {
#pragma unroll 1
    for (unsigned spins = 0; spins < (1u << 22); ++spins) {
        if ((unsigned)__builtin_amdgcn_readfirstlane(__hip_atomic_load((LAS unsigned*)word, __ATOMIC_RELAXED, __HIP_MEMORY_SCOPE_WORKGROUP)) == want) break;
        __builtin_amdgcn_s_sleep(1);
    }
    asm volatile("" ::: "memory");
}
__global__ __launch_bounds__(512, 2) void fused_kernel(Params p) {
    extern __shared__ __attribute__((aligned(16))) char smem_raw[];
    u64 pc;
    asm volatile("s_getpc_b64 %0" : "=s"(pc));
    LAS char* sm = (LAS char*)smem_raw;
    const int tid = threadIdx.x, lane = tid & 63;
    const int wave = __builtin_amdgcn_readfirstlane(tid >> 6);
    const int xcd = blockIdx.x & 7, idx = blockIdx.x >> 3;
    if (idx == 0 && wave < 5) {
        const unsigned o = (unsigned)tid * 128u;
        asm volatile("s_mov_b32 m0, %2\n\ts_nop 0\n\tglobal_load_lds_dword %0, %1" :: "v"(o < CODE_PREFETCH ? o : 0u), "s"(pc & ~(u64)255),
                     "s"((unsigned)(size_t)sm + 32768u));
    }
    SCHED();
    const __amdgpu_buffer_rsrc_t wsr = __builtin_amdgcn_make_buffer_rsrc(p.ws, 0, WS_END, 0x00020000);
    asm volatile("" :: "s"(p.x), "s"(p.k), "s"(p.v), "s"(p.sk), "s"(p.hk), "s"(p.dv), "s"(p.w1), "s"(p.w3), "s"(p.w2), "s"(p.out), "s"(p.ws));

    if (idx < 4) {
        const int ca = (xcd >> 1) * 4 + idx, cmat = ca >= 7, cnt = 7 * (xcd & 1) + (ca >= 7 ? ca - 7 : ca);
        const bool job = ca < 14;
        u32x4 xr[7], wr[14];
        const int row = (8 * (xcd >> 1) + 4 * (xcd & 1) + idx) * 64 + wave * 8 + (lane >> 3);
#pragma unroll
        for (int i = 0; i < 7; ++i) xr[i] = *(const u32x4*)(p.x + (size_t)row * D + i * 64 + (lane & 7) * 8);
        const u64 epoch = __hip_atomic_load((GAS u64*)(p.ws + WS_EPOCH), RLX_AGENT);
        const u64 tag = epoch ^ TAG_SALT;
        const unsigned tag32 = (unsigned)tag;
        unsigned stat;
        {
            float s4[4] = {0.f, 0.f, 0.f, 0.f};
#pragma unroll
            for (int i = 0; i < 7; ++i)
#pragma unroll
                for (int j = 0; j < 4; ++j) s4[j] = dot2(xr[i][j], 0x3f803f80u, s4[j]);
            float sum = (s4[0] + s4[1]) + (s4[2] + s4[3]);
            sum += __shfl_xor(sum, 1); sum += __shfl_xor(sum, 2); sum += __shfl_xor(sum, 4);
            const float mean = sum * (1.f / D);
            f32x2 q4[4] = {{0.f, 0.f}, {0.f, 0.f}, {0.f, 0.f}, {0.f, 0.f}};
#pragma unroll
            for (int i = 0; i < 7; ++i)
#pragma unroll
                for (int j = 0; j < 4; ++j) {
                    const f32x2 d = (f32x2){bflo(xr[i][j]), bfhi(xr[i][j])} - (f32x2){mean, mean};
                    q4[j] = __builtin_elementwise_fma(d, d, q4[j]);
                }
            const f32x2 q2 = (q4[0] + q4[1]) + (q4[2] + q4[3]);
            float sq = q2[0] + q2[1];
            sq += __shfl_xor(sq, 1); sq += __shfl_xor(sq, 2); sq += __shfl_xor(sq, 4);
            stat = pk(mean, __builtin_amdgcn_sqrtf(rbf(rbf(sq * (1.f / D)) + 1e-5f)));
            asm volatile("" : "+v"(stat));
        }
        if (job) {
            SCHED();
            const u16* wp = (cmat ? p.w3 : p.w1) + (size_t)(wave * 4 + (lane >> 4)) * F + cnt * 128 + (lane & 15) * 8;
#pragma unroll
            for (int i = 0; i < 14; ++i) wr[i] = *(const u32x4*)(wp + (size_t)(32 * i) * F);
            SCHED();
        }
        __builtin_amdgcn_raw_buffer_store_b64((u32x2){tag32, stat}, wsr, (lane & 7) == 0 ? WS_STATS + (unsigned)row * 8u : 0xfffffff0u, 0, 16);
        if (job) {
            float c[8];
#pragma unroll
            for (int j = 0; j < 8; ++j) c[j] = 0.f;
#pragma unroll
            for (int i = 0; i < 14; ++i)
#pragma unroll
                for (int j = 0; j < 4; ++j) { c[2 * j] += bflo(wr[i][j]); c[2 * j + 1] += bfhi(wr[i][j]); }
            LAS char* cp = sm + ((wave * 4 + (lane >> 4)) * 136 + (lane & 15) * 8) * 4;
            *(LAS f32x4*)cp = (f32x4){c[0], c[1], c[2], c[3]};
            *(LAS f32x4*)(cp + 16) = (f32x4){c[4], c[5], c[6], c[7]};
        }
        if (job) {
            WAIT_BAR("lgkmcnt(0)");
            float t = 0.f;
#pragma unroll
            for (int i = 0; i < 8; ++i) t += *(LAS float*)(sm + ((i * 4 + (tid & 3)) * 136 + (tid >> 2)) * 4);
            t += __shfl_xor(t, 1); t += __shfl_xor(t, 2);
            __builtin_amdgcn_raw_buffer_store_b64((u32x2){tag32, __float_as_uint(t)}, wsr,
                (tid & 3) == 0 ? WS_CSUM + (unsigned)((cnt * 2 + cmat) * 128 + (tid >> 2)) * 8u : 0xfffffff0u, 0, 16);
        }
        SCHED();
        if (!job) __builtin_amdgcn_s_sleep(40);
        const unsigned pf2 = *(const unsigned*)((const char*)p.hk + (size_t)((xcd * 4 + idx) * 448 + wave * 56 + (lane < 56 ? lane : 0)) * 128);
        {
            const unsigned fo = lane * 16u;
            constexpr int PER = ((4 - NTOUCH) * 14336 + 4095) / 4096;
#pragma unroll 1
            for (int i = wave; i < 7 * PER; i += 8) {
                const int g = i / PER, j = i - g * PER;
                int o = NTOUCH * 14336 + j * 4096; o = o > 57344 - 4096 ? 57344 - 4096 : o;
                glds16x4((const char*)p.w2 + (size_t)idx * 401408 + g * 57344 + o, (u32x4){fo, fo, fo, fo}, (unsigned)(size_t)sm + 65536u + wave * 4096);
            }
        }
        asm volatile("" :: "v"(pf2));
        if (blockIdx.x == 0 && wave == 7) {
            __builtin_amdgcn_s_sleep(127);
            GAS u64* gp = (GAS u64*)(p.ws + WS_STATS) + lane * 8;
            const int fl = lane < 56 ? lane : 0;
            GAS u64* fp = (GAS u64*)(p.ws + WS_FH) + ((fl / 14) * 56 + (fl % 14) * 2) * 16;
#pragma unroll 1
            for (unsigned spins = 0; spins < SPIN_CAP; ++spins) {
                bool ok = true;
#pragma unroll
                for (int m = 0; m < 4; ++m)
                    ok &= ((unsigned)__hip_atomic_load(gp + m * 512, RLX_AGENT) == tag32) & (__hip_atomic_load(fp + m * 224 * 16, RLX_AGENT) == tag);
                if (__all(ok)) break;
                __builtin_amdgcn_s_sleep(16);
            }
            if (lane == 0) __hip_atomic_store((GAS u64*)(p.ws + WS_EPOCH), epoch + 1, RLX_AGENT);
        }
        return;
    }

    const int gi = idx - 4, ml = gi & 3, nl = gi >> 2;
    const int mt = 4 * (xcd >> 1) + ml, nt = 7 * (xcd & 1) + nl;
    const int rot1 = (2 * ml + nl) % 6;
    const int m64 = 4 * xcd + ml, n64 = nl;
    const int gpair = xcd * 28 + gi;
    const int r32 = lane & 31, hh = lane >> 5, blk = (lane >> 4) & 1, q4 = (lane & 15) >> 2, p4 = lane & 3;
    const unsigned lb = (unsigned)(size_t)sm;
    const int wq = wave & 3;
    u32x4 wvo;
    wvo[0] = (unsigned)((lane >> 4) * F + (((lane & 15) ^ ((lane >> 4) << 2)) * 8)) * 2u;
#pragma unroll
    for (int j = 1; j < 4; ++j) wvo[j] = wvo[0] + j * (8 * F - 1024);
    const u16* w1b = sptr(p.w1, wq * 16 * F + nt * 128);
    const u16* w3b = sptr(p.w3, wq * 16 * F + nt * 128);
    auto kst = [&](int si) { if (si == 0) return 0; const int s = si + rot1; return s >= 7 ? s - 6 : s; };
    auto group_w = [&](int si, int m) { glds16x4(sptr(m ? w3b : w1b, kst(si) * 64 * F), wvo, lb + ring(si) + 16384 + m * 16384 + wq * 4096); };
    const int kr0 = (gpair & 31) * 64, kc0 = (gpair >> 5) * 64;
    const int chn = wq >> 1, par = wq & 1, dch = 2 * gpair + chn;
    if (wave < 4) group_w(0, 0);
    {
        const bool isk = lane >= 8 && lane < 14;
        u64 mb_ = vsel(lane == 15, p.dv, vsel(lane == 14, p.hk, vsel(isk, p.k, (u64)p.sk)));
        unsigned mo = (unsigned)((kc0 + wq * 16) * 8 + (lane & 7) * 16);
        mo = isk ? (unsigned)((kr0 ? kr0 - 3 + ((lane - 8) >> 1) : 0) * D + kc0 + wq * 16 + (lane & 1) * 8) * 2u : mo;
        mo = lane == 14 ? (unsigned)dch * 4096u : mo;
        mo = lane == 15 ? (unsigned)(dch & ~7) * 2u : mo;
        const u64 ma = mb_ + mo;
        asm volatile("s_mov_b32 m0, %1\n\ts_nop 0\n\tglobal_load_lds_dwordx4 %0, off" :: "v"(ma), "s"(lb + wave * 1024));
    }
    if (wave < 4) group_w(0, 1);
    SCHED();
    const int rot2 = 8 * ml;
    const int w2i = L_W2 + wq * 12288;
    auto fresh_lane = [&]() { int l = lane; asm volatile("" : "+v"(l)); return l; };
    const int latei = L_CONV + wq * 8192;
    auto pair_w2 = [&](u32x4 vo, int i, int dst) {
        int t = i + rot2; t = t >= 28 ? t - 28 : t;
        glds16x4(sptr(p.w2, (wq * 448 + t * 16) * D + n64 * 64), vo, lb + dst);
    };
    auto w2_off = [&]() {
        const int l = fresh_lane();
        const unsigned o = (unsigned)((l >> 3) * D + (((l & 7) ^ (((l >> 4) & 1) << 2)) * 8)) * 2u;
        return (u32x4){o, o + (16 * D - 1024), o + 2 * (16 * D - 1024), o + 3 * (16 * D - 1024)};
    };

    if (wave < 4) {
        __builtin_amdgcn_s_setprio(1);
        const int wm = wave >> 1, wn = wave & 1;
        int xa[4], wa0, wa1;
#pragma unroll
        for (int i = 0; i < 4; ++i) xa[i] = lb + ring(0) + (((64 * wm + r32) * 128 + ((hh ^ ((r32 >> 1) & 7)) * 16)) ^ (i * 32));
        wa0 = 16384 + (8 * hh + q4) * 256 + (((8 * wn + 2 * blk + (p4 >> 1)) ^ (q4 << 2)) * 16) + 8 * (p4 & 1);
        wa1 = (wa0 ^ 64) + lb + ring(0); wa0 += lb + ring(0);
        f32x16 acc[2][2][2];
#pragma unroll
        for (int i = 0; i < 8; ++i)
#pragma unroll
            for (int r = 0; r < 16; ++r) acc[i >> 2][(i >> 1) & 1][i & 1][r] = 0.f;
        u32x4 fx[2][2], fw[2][2][2];
        auto load_frag = [&](int g, int j) {
            const int pp = g & 3, b = g & 1;
            if (j == 0) {
                if (g >= 4) {
                    const int d = ring(g >> 2) - ring((g >> 2) - 1);
                    if (pp == 0) { wa0 += d; wa1 += d; asm volatile("" : "+v"(wa0), "+v"(wa1)); }
                    xa[pp] += d; asm volatile("" : "+v"(xa[pp]));
                }
                fw[b][0][0] = ldstr2(lp(wa0) + pp * 4096, 1024);
            }
            if (j == 1) { fx[b][0] = lds128(lp(xa[pp])); fx[b][1] = lds128(lp(xa[pp]) + 4096); }
            if (j == 2) fw[b][0][1] = ldstr2(lp(wa0) + pp * 4096 + 16384, 1024);
            if (j == 3) fw[b][1][0] = ldstr2(lp(wa1) + pp * 4096, 1024);
            if (j == 4) fw[b][1][1] = ldstr2(lp(wa1) + pp * 4096 + 16384, 1024);
        };
        int ka0[2], kam[2], kwo;
#pragma unroll
        for (int rd = 0; rd < 2; ++rd) {
            const int ck = 2 * wq + rd;
            const int bq = 2 * (lane >> 4) + (p4 >> 1), bm = bq ? bq - 1 : 0;
            ka0[rd] = lb + L_KV + (8 * bq + q4) * 128 + ((ck ^ (((q4 >> 1) & 1) | ((bq & 3) << 1))) * 16) + 8 * (p4 & 1);
            kam[rd] = lb + L_KV + (8 * bm + 4 + q4) * 128 + ((ck ^ (((q4 >> 1) & 1) | ((bm & 3) << 1))) * 16) + 8 * (p4 & 1);
        }
        kwo = lb + L_KVO + wq * 2048 + (lane & 7) * 128 + (((7 - (lane >> 3)) ^ (lane & 7)) * 16);
        asm volatile("" : "+v"(ka0[0]), "+v"(ka0[1]), "+v"(kam[0]), "+v"(kam[1]), "+v"(kwo));
        WAIT_VM(4);
        u32x2 skv[2], hal[2];
#pragma unroll
        for (int rd = 0; rd < 2; ++rd) {
            LAS char* mp = sm + wave * 1024;
            skv[rd] = *(LAS u32x2*)(mp + (rd * 8 + (lane & 7)) * 8);
            const unsigned hm = kr0 ? 0xffffffffu : 0u;
            const unsigned a = *(LAS u16*)(mp + 128 + rd * 16 + (lane & 7) * 2), b = *(LAS u16*)(mp + 160 + rd * 16 + (lane & 7) * 2),
                           c = *(LAS u16*)(mp + 192 + rd * 16 + (lane & 7) * 2);
            hal[rd] = (u32x2){(a << 16) & hm, (b | (c << 16)) & hm};
            asm volatile("" : "+v"(skv[rd]), "+v"(hal[rd]));
        }
        WAIT_BAR("vmcnt(0) lgkmcnt(0)");
        u32x2 kt[2][3], vt[2][2];
        float cf[4], kk[11], ko[8];
        u32x4 kpk;
        auto fill = [&](auto kc) {
            constexpr int k = decltype(kc)::value;
            if constexpr (k < 4) {
                constexpr int rd = k >> 1;
                if constexpr ((k & 1) == 0) { kt[rd][0] = ldstr(lp(kam[rd])); kt[rd][1] = ldstr(lp(ka0[rd])); kt[rd][2] = ldstr(lp(ka0[rd]) + 512); }
                else { vt[rd][0] = ldstr(lp(ka0[rd]) + 8192); vt[rd][1] = ldstr(lp(ka0[rd]) + 8192 + 512); }
            } else if constexpr (k < 4 + 2 * 16) {
                constexpr int rd = (k - 4) / 16, s = (k - 4) % 16;
                if constexpr (s == 0) { cf[0] = bflo1(skv[rd][0]); cf[1] = bfhi1(skv[rd][0]); cf[2] = bflo1(skv[rd][1]); cf[3] = bfhi1(skv[rd][1]); }
                else if constexpr (s == 1) {
                    const unsigned a = lane < 8 ? hal[rd][0] : kt[rd][0][0], b = lane < 8 ? hal[rd][1] : kt[rd][0][1];
                    kk[0] = bfhi1(a); kk[1] = bflo1(b); kk[2] = bfhi1(b);
                } else if constexpr (s < 4) {
                    const u32x2 t = kt[rd][s - 1];
                    kk[4 * s - 5] = bflo1(t[0]); kk[4 * s - 4] = bfhi1(t[0]); kk[4 * s - 3] = bflo1(t[1]); kk[4 * s - 2] = bfhi1(t[1]);
                } else if constexpr (s < 12) {
                    constexpr int tp = (s - 4) >> 1, o0 = ((s - 4) & 1) * 4;
#pragma unroll
                    for (int i = o0; i < o0 + 4; ++i) {
                        if (tp == 0) ko[i] = mul1(kk[i], cf[3]);
                        else fmac1(ko[i], kk[i + tp], cf[3 - tp]);
                    }
                } else {
                    constexpr int j = s - 12;
                    const unsigned v = vt[rd][j >> 1][j & 1];
                    kpk[3 - j] = pk(mul1(ko[2 * j + 1], bfhi1(v)), mul1(ko[2 * j], bflo1(v)));
                    if constexpr (j == 3) *(LAS u32x4*)(lp(kwo) + rd * 1024) = kpk;
                }
            }
        };
#pragma unroll
        for (int j = 0; j < 5; ++j) load_frag(0, j);
        f32x4 cs[2][2][4];
        unsigned tch[NTOUCH];
        unsigned tco = (unsigned)(gi * 4 + wq + (lane < 28 ? lane : 27) * 448) * 128u;
        asm volatile("" : "+v"(tco));
        static_for([&](auto gc) {
            constexpr int g = decltype(gc)::value, b = g & 1, pp = g & 3;
            SCHED();
            if (pp == 3 && g < 27) { WAIT_BAR("lgkmcnt(0)"); }
            if (g == 27) {
                WAIT_LGKM0();
                if (lane == 0) __hip_atomic_fetch_add((LAS unsigned*)(sm + L_CTL + 12), 1u, __ATOMIC_RELAXED, __HIP_MEMORY_SCOPE_WORKGROUP);
            }
            static_for([&](auto ic) {
                constexpr int i = decltype(ic)::value, ft = i >> 2, m = (i >> 1) & 1, sb = i & 1;
                acc[sb][ft][m] = mfma32(fw[b][ft][m], fx[b][sb], acc[sb][ft][m]);
                SCHED();
                if (g < 27 && i < 5) load_frag(g + 1, i);
                if constexpr (i == 4 && pp == 2 && g >= 4 && g < 4 + 4 * NTOUCH) {
                    u64 t;
                    asm volatile("s_mov_b64 %1, %3\n\tglobal_load_dword %0, %2, %1" : "=v"(tch[(g - 4) >> 2]), "=&s"(t)
                                 : "v"(tco), "s"((const char*)p.w2 + ((g - 4) >> 2) * (112 * 128)));
                }
                if constexpr (i >= 5 && g >= 7) fill(std::integral_constant<int, (g - 7) * 3 + i - 5>{});
                SCHED();
            }, std::make_integer_sequence<int, 8>{});
        }, std::make_integer_sequence<int, 28>{});
        wait_lds_eq(sm + L_CTL + 64, 3u);
        unsigned sa[2];
#pragma unroll
        for (int i = 0; i < 8; ++i) cs[i >> 2][0][i & 3] = *(LAS f32x4*)(sm + L_CSUM + (64 * wn + 32 * (i >> 2) + 8 * (i & 3) + 4 * hh) * 4);
        sa[0] = *(LAS unsigned*)(sm + L_STAT + (64 * wm + r32) * 4);
        sa[1] = *(LAS unsigned*)(sm + L_STAT + (64 * wm + 32 + r32) * 4);
#pragma unroll
        for (int i = 0; i < 8; ++i) cs[i >> 2][1][i & 3] = *(LAS f32x4*)(sm + L_CSUM + 512 + (64 * wn + 32 * (i >> 2) + 8 * (i & 3) + 4 * hh) * 4);
        const int lw = fresh_lane();
        const int w2a0 = lb + w2i + (4 * (lw >> 5) + ((lw & 15) >> 2)) * 128 + (((2 * ((lw >> 4) & 1) + ((lw & 3) >> 1)) ^ (((lw >> 3) & 1) << 2)) * 16) + 8 * (lw & 1);
        const int w2a1 = w2a0 ^ 64;
        u32x4 w2r[12][2];
        auto park = [&](int i) {
            w2r[i][0] = ldstr2(lp(w2a0) + (i / 6) * L_SLOT + (i % 6) * 2048, 1024);
            w2r[i][1] = ldstr2(lp(w2a1) + (i / 6) * L_SLOT + (i % 6) * 2048, 1024);
        };
#pragma unroll
        for (int sb = 0; sb < 2; ++sb) {
            const float ri = __builtin_amdgcn_rcpf(bfhi(sa[sb])) * -1.44269504088896341f, nm = -bflo(sa[sb]), d2 = bfhi(sa[sb]) * bfhi(sa[sb]);
            const f32x2 ri2 = {ri, ri}, nm2 = {nm, nm}, dd2 = {d2, d2};
#pragma unroll
            for (int ft = 0; ft < 2; ++ft) {
#pragma unroll
                for (int t2 = 0; t2 < 2; ++t2) {
                    f32x2 gu[4], ee[4];
#pragma unroll
                    for (int j = 0; j < 4; ++j) {
                        const int r = 8 * t2 + 2 * j;
                        const f32x2 g = __builtin_elementwise_fma(nm2, (f32x2){cs[ft][0][r >> 2][r & 3], cs[ft][0][r >> 2][(r & 3) + 1]}, (f32x2){acc[sb][ft][0][r], acc[sb][ft][0][r + 1]});
                        const f32x2 u = __builtin_elementwise_fma(nm2, (f32x2){cs[ft][1][r >> 2][r & 3], cs[ft][1][r >> 2][(r & 3) + 1]}, (f32x2){acc[sb][ft][1][r], acc[sb][ft][1][r + 1]});
                        const f32x2 t = g * ri2;
                        ee[j] = (f32x2){__builtin_amdgcn_exp2f(t[0]), __builtin_amdgcn_exp2f(t[1])};
                        gu[j] = g * u;
                    }
                    u32x4 st;
#pragma unroll
                    for (int j = 0; j < 4; ++j) {
                        const f32x2 dn = __builtin_elementwise_fma(ee[j], dd2, dd2);
                        const f32x2 hv = gu[j] * (f32x2){__builtin_amdgcn_rcpf(dn[0]), __builtin_amdgcn_rcpf(dn[1])};
                        st[j] = pk(hv[0], hv[1]);
                    }
                    const unsigned fi = (unsigned)((4 * mt + 2 * wm + sb) * 112 + 8 * nt + 4 * wn + 2 * ft + t2);
                    __builtin_amdgcn_raw_buffer_store_b128(st, wsr, WS_H + fi * 1024u + lane * 16u, 0, 16);
                }
            }
        }
        SCHED();
        const u32x4 w2o = w2_off();
        const u64 tag = *(LAS u64*)(sm + L_CTL + 128);
        GAS u64* myflag = (GAS u64*)(p.ws + WS_FH + ((2 * mt + wm) * 28 + 2 * nt + wn) * 128);
        if (!__builtin_amdgcn_readfirstlane(__hip_atomic_load((LAS unsigned*)(sm + L_CTL + 112 + wq * 4), __ATOMIC_RELAXED, __HIP_MEMORY_SCOPE_WORKGROUP))) {
            WAIT_VM(0);
            if (lane == 0) __hip_atomic_store(myflag, tag, RLX_AGENT);
            wait_lds(sm + L_CTL + 112 + wq * 4);
        }
        asm volatile("" ::: "memory");
#pragma unroll
        for (int i = 6; i < 12; ++i) park(i);
        WAIT_VM(0);
        asm volatile("" :: "v"(tch[0]), "v"(tch[1]));
        if (lane == 0) __hip_atomic_store(myflag, tag, RLX_AGENT);
#pragma unroll
        for (int i = 0; i < 6; ++i) park(i);
        asm volatile("" ::: "memory");
        __hip_atomic_store((LAS unsigned*)(sm + L_CTL + 32 + wq * 4), 1u, __ATOMIC_RELAXED, __HIP_MEMORY_SCOPE_WORKGROUP);
        WAIT_LGKM0();
#pragma unroll
        for (int i = 0; i < 12; ++i) asm volatile("" : "+v"(w2r[i][0]), "+v"(w2r[i][1]));
#pragma unroll
        for (int i = 0; i < 3; ++i) pair_w2(w2o, 12 + 2 * i, w2i + i * 4096);
#pragma unroll
        for (int i = 0; i < 3; ++i) pair_w2(w2o, 18 + 2 * i, w2i + L_SLOT + i * 4096);
        {
            GAS u64* fp = (GAS u64*)(p.ws + WS_FH + (m64 * 28 + 7 * wq + (lw < 7 ? lw : 0)) * 128);
#pragma unroll 1
            for (unsigned spins = 0; spins < SPIN_CAP; ++spins) {
                if (__all(__hip_atomic_load(fp, RLX_AGENT) == tag)) break;
                __builtin_amdgcn_s_sleep(4);
            }
            __builtin_amdgcn_fence(__ATOMIC_ACQUIRE, "wavefront");
        }
        const unsigned hb = WS_H + (unsigned)(2 * m64 * 112 + 28 * wq) * 1024u + lw * 16u;
        u32x4 hf[28][2];
        auto load_h = [&](int i, int j) {
            int t = i + rot2; t = t >= 28 ? t - 28 : t;
            hf[i][j] = __builtin_amdgcn_raw_buffer_load_b128(wsr, hb + j * (112u * 1024u), t * 1024, 16);
        };
#pragma unroll
        for (int i = 0; i < HD; ++i) { load_h(i, 0); load_h(i, 1); }
        f32x16 ac2[2][2];
#pragma unroll
        for (int i = 0; i < 4; ++i)
#pragma unroll
            for (int r = 0; r < 16; ++r) ac2[i >> 1][i & 1][r] = 0.f;
        u32x4 a0 = w2r[0][0], a1 = w2r[0][1];
        static_for([&](auto ic) {
            constexpr int i = decltype(ic)::value, n = i + 1;
            SCHED();
            if (i == YSTEP) __hip_atomic_store((LAS unsigned*)(sm + L_CTL + 96 + wq * 4), 1u, __ATOMIC_RELAXED, __HIP_MEMORY_SCOPE_WORKGROUP);
            if (i == 23) wait_lds(sm + L_CTL + 80 + wq * 4);
            u32x4 b0, b1;
            if constexpr (n < 12) { b0 = w2r[n][0]; b1 = w2r[n][1]; }
            else if constexpr (n < 28) {
                constexpr int so = n < 24 ? ((n - 12) / 6) * L_SLOT + ((n - 12) % 6) * 2048 : (n - 24) * 2048;
                const int lo = n < 24 ? 0 : latei - w2i;
                b0 = ldstr2(lp(w2a0 + lo) + so, 1024);
                b1 = ldstr2(lp(w2a1 + lo) + so, 1024);
            }
            SCHED();
            ac2[0][0] = mfma32(a0, hf[i][0], ac2[0][0]);
            SCHED();
            constexpr int l0 = i < HG ? HD + 2 * i : HD + HG + i;
            if constexpr (l0 < 28) load_h(l0, 0);
            SCHED();
            ac2[1][0] = mfma32(a1, hf[i][0], ac2[1][0]);
            SCHED();
            if constexpr (l0 < 28) load_h(l0, 1);
            SCHED();
            ac2[0][1] = mfma32(a0, hf[i][1], ac2[0][1]);
            SCHED();
            if constexpr (i < HG) load_h(l0 + 1, 0);
            SCHED();
            ac2[1][1] = mfma32(a1, hf[i][1], ac2[1][1]);
            SCHED();
            if constexpr (i < HG) load_h(l0 + 1, 1);
            a0 = b0; a1 = b1;
        }, std::make_integer_sequence<int, 28>{});
        SCHED();
        WAIT_LGKM0();
#pragma unroll
        for (int v = 0; v < 16; ++v) {
            const f32x16& t = ac2[v >> 3][(v >> 2) & 1];
            const int r0 = 4 * (v & 3);
            *(LAS f32x4*)(sm + w2i + part(v >> 3, (v >> 2) & 1, v & 3) + lw * 16) = (f32x4){t[r0], t[r0 + 1], t[r0 + 2], t[r0 + 3]};
        }
        WAIT_BAR("lgkmcnt(0)");
    } else {
        u32x4 xvo;
#pragma unroll
        for (int j = 0; j < 4; ++j) xvo[j] = (unsigned)((8 * j + (lane >> 3)) * D + ((((lane & 7) ^ (lane >> 4)) ^ (4 * (j & 1))) * 8)) * 2u - 1024u * j;
        const u16* xb = sptr(p.x, (mt * 128 + wq * 32) * D);
        auto group_x = [&](int si) { glds16x4(sptr(xb, kst(si) * 64), xvo, lb + ring(si) + wq * 4096); };
        group_x(0);
        group_x(1); group_w(1, 0); group_w(1, 1);
        const int cbi = L_CONV + chn * C_CH;
        LAS char* cb = sm + cbi;
        unsigned kvo[2], uso;
#pragma unroll
        for (int rd = 0; rd < 2; ++rd) kvo[rd] = (unsigned)((lane >> 3) * D + (((lane & 7) ^ (((lane >> 4) & 1) | (((2 * wq + rd) & 3) << 1))) * 8)) * 2u;
        uso = WS_U + (unsigned)(kc0 + wq * 16 + (lane >> 3)) * 8192u + (unsigned)(1984 - kr0 + (lane & 7) * 8) * 2u;
        int kro = lb + L_KVO + wq * 2048 + (lane >> 3) * 128 + (((lane & 7) ^ (lane >> 3)) * 16);
        const int cc = 2 * par + ((lane >> 2) & 1);
        int zo = cbi + C_U + par * C_US + (248 + (lane < 36 ? lane : 35)) * 16;
        zo = lane < 8 ? cbi + cc * C_HS + (lane & 3) * 16 : zo;
        u32x4 w2o = w2_off();
        const unsigned fo = lane * 16u;
        unsigned ao = (wq < 2 ? WS_CSUM + (unsigned)(nt * 2 + wq) * 1024u : WS_STATS + mt * 1024u) + fo;
        asm volatile("" : "+v"(zo), "+v"(w2o), "+v"(kvo[0]), "+v"(kvo[1]), "+v"(uso), "+v"(kro), "+v"(ao));
        unsigned eo = (lane & 1) * 4u;
        asm volatile("" : "+v"(eo));
        WAIT_VM(16);
        unsigned tag32, taghi;
        u32x4 bval, ug[2];
        unsigned dbits = *(LAS u16*)(sm + wave * 1024 + 240 + (dch & 7) * 2);
        {
            const u32x4 h0 = lds128(sm + wave * 1024 + 224);
            const bool s1 = !(cc & 1), s2 = !(cc & 2);
            const unsigned a0 = s1 ? 0u : h0[0], a1 = s1 ? h0[0] : h0[1], a2 = s1 ? h0[1] : h0[2], a3 = s1 ? h0[2] : h0[3];
            const unsigned lm = (lane == 3 || lane == 7) ? 0xffffffffu : 0u;
            bval = (u32x4){(s2 ? 0u : a0) & lm, (s2 ? 0u : a1) & lm, (s2 ? a0 : a2) & lm, (s2 ? a1 : a3) & lm};
            asm volatile("" : "+v"(bval), "+v"(dbits));
        }
        auto kv_store = [&](int rd) {
            LAS unsigned* kp = (LAS unsigned*)(lp(kro) + rd * 1024);
            ug[0][1] = kp[0]; asm volatile("" ::: "memory"); ug[0][3] = kp[1]; asm volatile("" ::: "memory");
            ug[1][1] = kp[2]; asm volatile("" ::: "memory"); ug[1][3] = kp[3];
            __builtin_amdgcn_raw_buffer_store_b128(ug[0], wsr, uso, rd * 65536, 16);
            __builtin_amdgcn_raw_buffer_store_b128(ug[1], wsr, uso, rd * 65536 + 4096, 16);
        };
        WAIT_BAR("vmcnt(12) lgkmcnt(0)");
#pragma unroll
        for (int w = 0; w < 5; ++w) {
            if (w == 3) glds4c(p.ws + WS_EPOCH, eo, lb + L_RAW + wq * 1024);
            group_x(w + 2); group_w(w + 2, 0); group_w(w + 2, 1);
            if (w == 0) {
#pragma unroll
                for (int j = 0; j < 2; ++j) {
                    glds16(sptr(p.k, (kr0 + (2 * wq + j) * 8) * D + kc0), kvo[j], lb + L_KV + (2 * wq + j) * 1024);
                    glds16(sptr(p.v, (kr0 + (2 * wq + j) * 8) * D + kc0), kvo[j], lb + L_KV + 8192 + (2 * wq + j) * 1024);
                }
            }
            if (w == 4) {
                const u32x2 ev = *(LAS u32x2*)(sm + L_RAW + wq * 1024);
                tag32 = __builtin_amdgcn_readfirstlane(ev[0]) ^ (unsigned)TAG_SALT;
                taghi = __builtin_amdgcn_readfirstlane(ev[1]) ^ (unsigned)(TAG_SALT >> 32);
                asm volatile("s_nop 1" : "+s"(tag32), "+s"(taghi));
                ug[0][0] = vmov1(tag32); ug[0][2] = vmov1(tag32); ug[1][0] = vmov1(tag32); ug[1][2] = vmov1(tag32);
                if (lane < 34) *(LAS unsigned*)(sm + L_CTL + lane * 4) = lane < 32 ? 0u : lane == 32 ? tag32 : taghi;
            }
            if (w == 4) kv_store(0);
            if (w == 0) WAIT_BAR("vmcnt(16) lgkmcnt(0)"); else WAIT_BAR("vmcnt(12) lgkmcnt(0)");
        }
        glds16c(p.ws, ao, lb + L_RAW + wq * 1024);
#pragma unroll
        for (int i = 0; i < 3; ++i) pair_w2(w2o, 2 * i, w2i + i * 4096);
        kv_store(1);
        WAIT_BAR("vmcnt(12) lgkmcnt(0)");
        asm volatile("" ::: "memory");
        if (lane == 0) __hip_atomic_fetch_add((LAS unsigned*)(sm + L_CTL + 12), 1u, __ATOMIC_RELAXED, __HIP_MEMORY_SCOPE_WORKGROUP);
        if (wq < 3) {
            u32x4 av = lds128(sm + L_RAW + wq * 1024 + lane * 16);
#pragma unroll 1
            for (unsigned spins = 0; spins < SPIN_CAP; ++spins) {
                if (__all(((av[0] ^ tag32) | (av[2] ^ tag32)) == 0u)) break;
                asm volatile("" ::: "memory");
                av = __builtin_amdgcn_raw_buffer_load_b128(wsr, ao, 0, 16);
                asm volatile("" : "+v"(av));
            }
            *(LAS u32x2*)(sm + L_CSUM + wq * 512 + lane * 8) = (u32x2){av[1], av[3]};
            asm volatile("" ::: "memory");
            if (lane == 0) __hip_atomic_fetch_add((LAS unsigned*)(sm + L_CTL + 64), 1u, __ATOMIC_RELAXED, __HIP_MEMORY_SCOPE_WORKGROUP);
        }
#pragma unroll
        for (int i = 0; i < 3; ++i) pair_w2(w2o, 6 + 2 * i, w2i + L_SLOT + i * 4096);
        WAIT_VM(12);
        __hip_atomic_store((LAS unsigned*)(sm + L_CTL + 48 + wq * 4), 1u, __ATOMIC_RELAXED, __HIP_MEMORY_SCOPE_WORKGROUP);
        wait_lds_eq(sm + L_CTL + 12, 8u);
        __builtin_amdgcn_s_setprio(2);
        const unsigned ubase = WS_U + (unsigned)dch * 8192u;
        glds16x2c(p.ws + ubase + par * 2048, fo, lb + cbi + C_U + par * 2048);
        glds16x2c(p.ws + ubase + 4096 + par * 2048, fo, lb + cbi + C_U + C_US + par * 2048);
        glds4c(p.ws + ubase + (1 - par) * 2048, eo, lb + L_UNX + wq * 256);
        {
            const u32x4 fvo = {fo, fo, fo, lane == 63 ? 62u * 16u : fo};
#pragma unroll
            for (int c = 0; c < 2; ++c) glds16x4(sptr(p.hk, dch * S + 4 * par + 2 * c + 2), fvo, lb + cbi + (2 * par + c) * C_HS + 64);
        }
        *(LAS u32x4*)(sm + zo) = bval;
        WAIT_VM(13);
        __hip_atomic_store((LAS unsigned*)(sm + L_CTL + 112 + wq * 4), 1u, __ATOMIC_RELAXED, __HIP_MEMORY_SCOPE_WORKGROUP);
        WAIT_VM(8);
        {
            u32x4 ua[2], ub[2]; u32x2 un[2];
#pragma unroll
            for (int rd = 0; rd < 2; ++rd) {
                const int x8 = 128 * par + 64 * rd + lane;
                ua[rd] = lds128(cb + C_U + x8 * 16); ub[rd] = lds128(cb + C_U + C_US + x8 * 16);
                un[rd] = *(LAS u32x2*)((rd == 1 && lane == 63) ? sm + L_UNX + wq * 256 : cb + C_U + x8 * 16 + 16);
            }
#pragma unroll 1
            for (unsigned spins = 0; spins < SPIN_CAP; ++spins) {
                unsigned df = 0;
#pragma unroll
                for (int rd = 0; rd < 2; ++rd) df |= (ua[rd][0] ^ tag32) | (ua[rd][2] ^ tag32) | (ub[rd][0] ^ tag32) | (ub[rd][2] ^ tag32) | (un[rd][0] ^ tag32);
                if (__all(df == 0u)) break;
                __builtin_amdgcn_s_sleep(4);
                asm volatile("" ::: "memory");
#pragma unroll
                for (int rd = 0; rd < 2; ++rd) {
                    const int x8 = 128 * par + 64 * rd + lane;
                    const unsigned uo = ubase + (unsigned)x8 * 16u;
                    ua[rd] = __builtin_amdgcn_raw_buffer_load_b128(wsr, uo, 0, 16);
                    ub[rd] = __builtin_amdgcn_raw_buffer_load_b128(wsr, uo + 4096u, 0, 16);
                    un[rd] = __builtin_amdgcn_raw_buffer_load_b64(wsr, x8 == 255 ? uo : uo + 16u, 0, 16);
                }
                asm volatile("" : "+v"(ua[0]), "+v"(ub[0]), "+v"(un[0]), "+v"(ua[1]), "+v"(ub[1]), "+v"(un[1]));
            }
#pragma unroll
            for (int rd = 0; rd < 2; ++rd) {
                const int x8 = 128 * par + 64 * rd + lane;
                const unsigned nx = x8 == 255 ? 0u : un[rd][1];
                *(LAS u32x4*)(cb + C_U + x8 * 16) = (u32x4){__builtin_amdgcn_alignbit(ua[rd][3], ua[rd][1], 16), __builtin_amdgcn_alignbit(ub[rd][1], ua[rd][3], 16),
                                                            __builtin_amdgcn_alignbit(ub[rd][3], ub[rd][1], 16), __builtin_amdgcn_alignbit(nx, ub[rd][3], 16)};
                *(LAS u32x4*)(cb + C_U + C_US + x8 * 16) = (u32x4){ua[rd][1], ua[rd][3], ub[rd][1], ub[rd][3]};
            }
        }
        asm volatile("s_waitcnt vmcnt(0) lgkmcnt(0)" ::: "memory");
        __hip_atomic_store((LAS unsigned*)(sm + L_CTL + 16 + wq * 4), 1u, __ATOMIC_RELAXED, __HIP_MEMORY_SCOPE_WORKGROUP);
        u32x4 lw2[8], xv[2];
        {
            const __amdgpu_buffer_rsrc_t wr2 = __builtin_amdgcn_make_buffer_rsrc((void*)p.w2, 0, F * D * 2, 0x00020000);
#pragma unroll
            for (int j = 0; j < 8; ++j) {
                int t = 24 + (j >> 2) * 2 + rot2; t = t >= 28 ? t - 28 : t;
                lw2[j] = __builtin_amdgcn_raw_buffer_load_b128(wr2, w2o[0], ((wq * 448 + t * 16 + (j & 3) * 8) * D + n64 * 64) * 2, 0);
            }
        }
        const int lf = fresh_lane();
        const int srow = 64 * m64 + 16 * wq + 2 * (lf >> 3);
        const unsigned xoff = (unsigned)(srow * D + 64 * n64 + 8 * (lf & 7)) * 2u;
        {
            const __amdgpu_buffer_rsrc_t xr = __builtin_amdgcn_make_buffer_rsrc((void*)p.x, 0, S * D * 2, 0x00020000);
            xv[0] = __builtin_amdgcn_raw_buffer_load_b128(xr, xoff, 0, 0);
            xv[1] = __builtin_amdgcn_raw_buffer_load_b128(xr, xoff, D * 2, 0);
        }
        SCHED();
        wait_lds(sm + L_CTL + 16 + (wq ^ 1) * 4);
        const int n16 = lane & 15, kg = lane >> 4;
        f32x4 ca[4];
#pragma unroll
        for (int j = 0; j < 4; ++j) ca[j] = (f32x4){0.f, 0.f, 0.f, 0.f};
        int apo = lb + cbi + (n16 & 3) * C_HS + ((n16 >> 2) + kg) * 16;
        int bpo = lb + cbi + C_U + (n16 & 1) * C_US + (252 - 4 * (n16 >> 1) + kg) * 16 - 7 * 512;
        u32x4 fa[2][8], fb[2][8];
#pragma unroll
        for (int i = 0; i < 8; ++i) { fa[0][i] = lds128(lp(apo) + i * 512); fb[0][i] = lds128(lp(bpo) + (7 - i) * 512); }
        const bool odd = lf & 1;
        int ysrc = (2 * (lf >> 3) + (lf & 1) + 16 * ((lf >> 1) & 3)) * 4;
        unsigned yso = WS_Y + (unsigned)dch * 8192u + lf * 16u;
        f32x4 kd[4];
        {
            const float dval = bflo(dbits);
            const int sl0 = 32 * ((lf & 15) >> 1) + 8 * (lf >> 4) + 4 * (lf & 1);
#pragma unroll
            for (int j = 0; j < 4; ++j) {
                const int tj = (j == 0 ? 0 : j == 1 ? 3 : j == 2 ? 4 : 7) ^ par;
                const u32x2 kvr = *(LAS u32x2*)(cb + C_U + C_US + (2044 - 256 * tj - sl0) * 2);
                const unsigned ka = pk(bfhi(kvr[1]) * dval, bflo(kvr[1]) * dval), kb = pk(bfhi(kvr[0]) * dval, bflo(kvr[0]) * dval);
                kd[j] = (f32x4){bflo(ka), bfhi(ka), bflo(kb), bfhi(kb)};
                asm volatile("" : "+v"(kd[j]));
            }
            asm volatile("" : "+v"(ysrc), "+v"(yso));
        }
        wait_lds(sm + L_CTL + 32 + wq * 4);
        auto conv_all = [&](auto pc_) {
            constexpr int P = decltype(pc_)::value;
            static_for([&](auto rc) {
                constexpr int r = decltype(rc)::value, c = r & 1, n = c ^ 1;
                static_for([&](auto kc) {
                    constexpr int k = decltype(kc)::value;
                    constexpr int T[4] = {P, 3 - P, 4 + P, 7 - P};
                    constexpr int j = k <= T[0] ? 0 : k <= T[0] + T[1] + 1 ? 1 : k <= T[0] + T[1] + T[2] + 2 ? 2 : 3;
                    constexpr int pp = k - (j == 0 ? 0 : j == 1 ? T[0] + 1 : j == 2 ? T[0] + T[1] + 2 : T[0] + T[1] + T[2] + 3);
                    ca[j] = mfma16(fa[c][pp], fb[c][T[j] - pp], ca[j]);
                    SCHED();
                    if constexpr (r < 7 && k < 2 * (T[3] + 1)) {
                        if constexpr (k & 1) fb[n][k >> 1] = lds128(lp(bpo) + (r + 1) * 64 + (7 - (k >> 1)) * 512);
                        else fa[n][k >> 1] = lds128(lp(apo) + (r + 1) * 64 + (k >> 1) * 512);
                    }
                    SCHED();
                }, std::make_integer_sequence<int, 18>{});
                asm volatile("" : "+v"(apo), "+v"(bpo));
            }, std::make_integer_sequence<int, 8>{});
        };
        if (par) conv_all(std::integral_constant<int, 1>{}); else conv_all(std::integral_constant<int, 0>{});
        SCHED();
        {
            float r0[4], r1[4];
#pragma unroll
            for (int j = 0; j < 4; ++j) { r0[j] = dpp_xor1(odd ? ca[j][0] : ca[j][2]); r1[j] = dpp_xor1(odd ? ca[j][1] : ca[j][3]); }
            unsigned rp[4][2];
#pragma unroll
            for (int j = 0; j < 4; ++j) {
                const unsigned ya = pk(odd ? r0[j] : ca[j][0], odd ? ca[j][2] : r0[j]), yb = pk(odd ? r1[j] : ca[j][1], odd ? ca[j][3] : r1[j]);
                rp[j][0] = __builtin_amdgcn_ds_bpermute(ysrc, pk(add1(bflo1(ya), kd[j][0]), add1(bfhi1(ya), kd[j][1])));
                rp[j][1] = __builtin_amdgcn_ds_bpermute(ysrc, pk(add1(bflo1(yb), kd[j][2]), add1(bfhi1(yb), kd[j][3])));
            }
#pragma unroll
            for (int j = 0; j < 4; ++j)
                __builtin_amdgcn_raw_buffer_store_b128((u32x4){vmov1(tag32 ^ Y_TAG), rp[j][0], vmov1(tag32 ^ Y_TAG), rp[j][1]}, wsr, yso,
                                                       ((j == 0 ? 0 : j == 1 ? 3 : j == 2 ? 4 : 7) ^ par) * 1024, 16);
        }
        WAIT_LGKM0();
        if (lane == 0) __hip_atomic_fetch_add((LAS unsigned*)(sm + L_CTL + 8), 1u, __ATOMIC_RELAXED, __HIP_MEMORY_SCOPE_WORKGROUP);
#pragma unroll 1
        for (unsigned spins = 0; spins < (1u << 22); ++spins) {
            if (__builtin_amdgcn_readfirstlane(__hip_atomic_load((LAS unsigned*)(sm + L_CTL + 8), __ATOMIC_RELAXED, __HIP_MEMORY_SCOPE_WORKGROUP)) == 4u) break;
            __builtin_amdgcn_s_sleep(1);
        }
        asm volatile("" ::: "memory");
#pragma unroll
        for (int j = 0; j < 8; ++j) *(LAS u32x4*)(sm + latei + j * 1024 + lf * 16) = lw2[j];
        asm volatile("" ::: "memory");
        __hip_atomic_store((LAS unsigned*)(sm + L_CTL + 80 + wq * 4), 1u, __ATOMIC_RELAXED, __HIP_MEMORY_SCOPE_WORKGROUP);
        wait_lds(sm + L_CTL + 96 + wq * 4);
        u32x2 yv[8];
        u32x4 stv;
        const int yl = L_CONV + 32768 + wq * 4096;
        const unsigned yg = (unsigned)(((lf >> 2) & 7) * 8 + (lf >> 5)) * 8192u + (unsigned)((lf & 3) ^ (((lf >> 4) & 1) << 1)) * 16u;
        const u32x4 yvo = {yg, yg + (16384u - 1024u), yg + 2 * (16384u - 1024u), yg + 3 * (16384u - 1024u)};
        const char* ybase = p.ws + WS_Y + (unsigned)(64 * n64) * 8192u + (unsigned)(64 * m64 + 16 * wq) * 4u;
        const int yr = lb + yl + (lf & 7) * 64 + (((lf >> 3) * 8) ^ (((lf >> 2) & 1) << 5));
#pragma unroll 1
        for (unsigned spins = 0; spins < SPIN_CAP; ++spins) {
            glds16x4c(ybase, yvo, lb + yl);
            stv = __builtin_amdgcn_raw_buffer_load_b128(wsr, WS_STATS + (unsigned)srow * 8u, 0, 16);
            WAIT_VM(0);
#pragma unroll
            for (int i = 0; i < 8; ++i) yv[i] = *(LAS u32x2*)(lp(yr) + i * 512);
            unsigned df = (stv[0] ^ tag32) | (stv[2] ^ tag32);
#pragma unroll
            for (int i = 0; i < 8; ++i) df |= yv[i][0] ^ (tag32 ^ Y_TAG);
            if (__all(df == 0u)) break;
            __builtin_amdgcn_s_sleep(8);
        }
        __builtin_amdgcn_s_setprio(0);
        f32x2 sf[8], xf[8];
#pragma unroll
        for (int e = 0; e < 2; ++e) {
            const float nmb = -bflo(stv[1 + 2 * e]);
            float ri = __builtin_amdgcn_rcpf(bfhi(stv[1 + 2 * e]));
            asm volatile("s_nop 1" : "+v"(ri));
#pragma unroll
            for (int j = 0; j < 4; ++j) {
                const int i = 4 * e + j;
                const unsigned xd = xv[e][j];
                xf[i] = (f32x2){bflo1(xd), bfhi1(xd)};
                const unsigned tp = pk(add1(xf[i][0], nmb), add1(xf[i][1], nmb));
                const unsigned np = pk(mul1(bflo1(tp), ri), mul1(bfhi1(tp), ri));
                sf[i] = (f32x2){mul1(bflo1(np), e ? bfhi1(yv[2 * j][1]) : bflo1(yv[2 * j][1])), mul1(bfhi1(np), e ? bfhi1(yv[2 * j + 1][1]) : bflo1(yv[2 * j + 1][1]))};
                asm volatile("" : "+v"(sf[i]), "+v"(xf[i]));
            }
        }
        const int rm = lb + L_W2 + part((lf >> 2) & 1, wq >> 1, lf & 3) + (16 * (wq & 1) + 2 * (lf >> 3)) * 16;
        WAIT_BAR("lgkmcnt(0)");
        f32x4 o[4];
#pragma unroll
        for (int j = 0; j < 4; ++j) {
            LAS char* rp = lp(rm) + (j >> 1) * 16 + (j & 1) * 512;
            o[j] = (*(LAS f32x4*)rp + *(LAS f32x4*)(rp + 12288)) + (*(LAS f32x4*)(rp + 24576) + *(LAS f32x4*)(rp + 36864));
        }
        f32x2 so[8], ev[8];
#pragma unroll
        for (int i = 0; i < 8; ++i) {
            const f32x2 ov = {o[i >> 1][(i & 1) * 2], o[i >> 1][(i & 1) * 2 + 1]};
            const f32x2 t = ov * (f32x2){-1.44269504088896341f, -1.44269504088896341f};
            ev[i] = (f32x2){__builtin_amdgcn_exp2f(t[0]), __builtin_amdgcn_exp2f(t[1])};
            so[i] = sf[i] * ov;
        }
        SCHED();
        u32x4 st[2];
#pragma unroll
        for (int i = 0; i < 8; ++i) {
            const f32x2 dn = ev[i] + (f32x2){1.f, 1.f};
            const f32x2 r = __builtin_elementwise_fma(so[i], (f32x2){__builtin_amdgcn_rcpf(dn[0]), __builtin_amdgcn_rcpf(dn[1])}, xf[i]);
            st[i >> 2][i & 3] = pk(r[0], r[1]);
        }
        const __amdgpu_buffer_rsrc_t orr = __builtin_amdgcn_make_buffer_rsrc((void*)p.out, 0, S * D * 2, 0x00020000);
        __builtin_amdgcn_raw_buffer_store_b128(st[0], orr, xoff, 0, 16);
        __builtin_amdgcn_raw_buffer_store_b128(st[1], orr, xoff, D * 2, 16);
    }
}

extern "C" void kernel_launch(void* const* d_in, const int* in_sizes, int n_in,
                              void* d_out, int out_size, void* d_ws, size_t ws_size,
                              hipStream_t stream) {
    Params p;
    p.x = (const u16*)d_in[0]; p.k = (const u16*)d_in[1]; p.v = (const u16*)d_in[2];
    p.sk = (const u16*)d_in[3]; p.hk = (const u16*)d_in[4]; p.dv = (const u16*)d_in[5];
    p.w1 = (const u16*)d_in[7]; p.w3 = (const u16*)d_in[8]; p.w2 = (const u16*)d_in[9];
    p.out = (u16*)d_out; p.ws = (char*)d_ws;
    (void)hipFuncSetAttribute((const void*)fused_kernel, hipFuncAttributeMaxDynamicSharedMemorySize, L_TOTAL);
    fused_kernel<<<256, 512, L_TOTAL, stream>>>(p);
}
```

```cpp
#include <hip/hip_runtime.h>
#include <stdint.h>
#include <utility>

typedef unsigned short u16;
typedef unsigned long long u64;
typedef short s16x4 __attribute__((ext_vector_type(4)));
typedef __bf16 bf16x2 __attribute__((ext_vector_type(2)));
typedef __bf16 bf16x8 __attribute__((ext_vector_type(8)));
typedef float f32x2 __attribute__((ext_vector_type(2)));
typedef float f32x4 __attribute__((ext_vector_type(4)));
typedef float f32x16 __attribute__((ext_vector_type(16)));
typedef unsigned u32x2 __attribute__((ext_vector_type(2)));
typedef unsigned u32x4 __attribute__((ext_vector_type(4)));
#define LAS __attribute__((address_space(3)))
#define GAS __attribute__((address_space(1)))
#define RLX_AGENT __ATOMIC_RELAXED, __HIP_MEMORY_SCOPE_AGENT

constexpr int S = 2048, D = 448, F = 1792;

constexpr unsigned WS_EPOCH = 0;
constexpr unsigned WS_FH = 65536;
constexpr unsigned WS_STATS = 8192;
constexpr unsigned WS_CSUM = 24576;
constexpr unsigned WS_U = 262144;
constexpr unsigned WS_Y = WS_U;
constexpr unsigned WS_H = WS_Y + D * S * 4;
constexpr unsigned WS_END = WS_H + 64 * 112 * 1024;

constexpr int L_KV = 0;
constexpr int L_CTL = 0;
constexpr int L_CSUM = 256;
constexpr int L_STAT = 1280;
constexpr int L_RAW = 2048;
constexpr int L_KVO = 8192;
constexpr int L_UNX = 6144;
constexpr int L_SLOT = 49152;
constexpr int L_CONV = 12288;
constexpr int C_CH = 26112, C_HS = 4160, C_U = 16640, C_US = 4640;
constexpr int L_W2 = 16384 + L_SLOT;
constexpr int L_TOTAL = 163840;
__host__ __device__ constexpr int part(int db, int sb, int a) { return db * L_SLOT + sb * 4352 + a * 1024 + (a & 1) * 16 + (a >> 1) * 128; }
__host__ __device__ constexpr int ring(int stage) { return 16384 + (stage % 3) * L_SLOT; }
constexpr unsigned CODE_PREFETCH = 36864;
constexpr int HD = 6, HG = 4;
constexpr int NTOUCH = 2;
constexpr int YSTEP = 18;

constexpr u64 TAG_SALT = 0x9E3779B97F4A7C15ull;
constexpr unsigned Y_TAG = 0x80000000u;
constexpr unsigned SPIN_CAP = 1u << 18;

struct Params {
    const u16* x; const u16* k; const u16* v; const u16* sk; const u16* hk; const u16* dv;
    const u16* w1; const u16* w3; const u16* w2; u16* out; char* ws;
};

__device__ __forceinline__ float bflo(unsigned p) { return __uint_as_float(p << 16); }
__device__ __forceinline__ float bfhi(unsigned p) { return __uint_as_float(p & 0xffff0000u); }
__device__ __forceinline__ float bflo1(unsigned p) { float r; asm("v_lshlrev_b32 %0, 16, %1" : "=v"(r) : "v"(p)); return r; }
__device__ __forceinline__ float bfhi1(unsigned p) { float r; asm("v_and_b32 %0, 0xffff0000, %1" : "=v"(r) : "v"(p)); return r; }
__device__ __forceinline__ float mul1(float a, float b) { float r; asm("v_mul_f32 %0, %1, %2" : "=v"(r) : "v"(a), "v"(b)); return r; }
__device__ __forceinline__ void fmac1(float& d, float a, float b) { asm("v_fmac_f32 %0, %1, %2" : "+v"(d) : "v"(a), "v"(b)); }
__device__ __forceinline__ float dpp_xor1(float v) {
    return __int_as_float(__builtin_amdgcn_update_dpp(0, __float_as_int(v), 0xB1, 0xf, 0xf, true));
}
__device__ __forceinline__ float add1(float a, float b) { float r; asm("v_add_f32 %0, %1, %2" : "=v"(r) : "v"(a), "v"(b)); return r; }
__device__ __forceinline__ unsigned vmov1(unsigned s) { unsigned r; asm volatile("v_mov_b32 %0, %1" : "=v"(r) : "s"(s)); return r; }
__device__ __forceinline__ unsigned pk(float a, float b) {
    f32x2 f = {a, b};
    return __builtin_bit_cast(unsigned, __builtin_convertvector(f, bf16x2));
}
__device__ __forceinline__ float rbf(float a) { return bflo(pk(a, a)); }
__device__ __forceinline__ float dot2(unsigned a, unsigned b, float c) {
    return __builtin_amdgcn_fdot2_f32_bf16(__builtin_bit_cast(bf16x2, a), __builtin_bit_cast(bf16x2, b), c, false);
}
__device__ __forceinline__ void glds16(const void* base, unsigned voff, unsigned ldst) {
    u64 t;
    asm volatile("s_mov_b32 m0, %3\n\ts_mov_b64 %0, %2\n\tglobal_load_lds_dwordx4 %1, %0 nt" : "=&s"(t) : "v"(voff), "s"(base), "s"(ldst));
}
__device__ __forceinline__ void glds16x4(const void* base, u32x4 vo, unsigned ldst) {
    u64 t;
    asm volatile("s_mov_b32 m0, %6\n\ts_mov_b64 %0, %5\n\tglobal_load_lds_dwordx4 %1, %0\n\tglobal_load_lds_dwordx4 %2, %0 offset:1024\n\t"
                 "global_load_lds_dwordx4 %3, %0 offset:2048\n\tglobal_load_lds_dwordx4 %4, %0 offset:3072"
                 : "=&s"(t) : "v"(vo[0]), "v"(vo[1]), "v"(vo[2]), "v"(vo[3]), "s"(base), "s"(ldst));
}
__device__ __forceinline__ void glds16x4c(const void* base, u32x4 vo, unsigned ldst) {
    u64 t;
    asm volatile("s_mov_b32 m0, %6\n\ts_mov_b64 %0, %5\n\tglobal_load_lds_dwordx4 %1, %0 sc1\n\tglobal_load_lds_dwordx4 %2, %0 offset:1024 sc1\n\t"
                 "global_load_lds_dwordx4 %3, %0 offset:2048 sc1\n\tglobal_load_lds_dwordx4 %4, %0 offset:3072 sc1"
                 : "=&s"(t) : "v"(vo[0]), "v"(vo[1]), "v"(vo[2]), "v"(vo[3]), "s"(base), "s"(ldst) : "memory");
}
__device__ __forceinline__ void glds16x2c(const void* base, unsigned voff, unsigned ldst) {
    u64 t;
    asm volatile("s_mov_b32 m0, %3\n\ts_mov_b64 %0, %2\n\tglobal_load_lds_dwordx4 %1, %0 sc1\n\tglobal_load_lds_dwordx4 %1, %0 offset:1024 sc1"
                 : "=&s"(t) : "v"(voff), "s"(base), "s"(ldst));
}
__device__ __forceinline__ void glds16c(const void* base, unsigned voff, unsigned ldst) {
    u64 t;
    asm volatile("s_mov_b32 m0, %3\n\ts_mov_b64 %0, %2\n\tglobal_load_lds_dwordx4 %1, %0 sc1" : "=&s"(t) : "v"(voff), "s"(base), "s"(ldst));
}
__device__ __forceinline__ void glds4c(const void* base, unsigned voff, unsigned ldst) {
    u64 t;
    asm volatile("s_mov_b32 m0, %3\n\ts_mov_b64 %0, %2\n\tglobal_load_lds_dword %1, %0 sc1" : "=&s"(t) : "v"(voff), "s"(base), "s"(ldst));
}
__device__ __forceinline__ const u16* sptr(const u16* base, int off) {
    const u16* r = base + __builtin_amdgcn_readfirstlane(off);
    asm("" : "+s"(r));
    return r;
}
__device__ __forceinline__ u64 vsel(bool c, const void* a, u64 b) {
    u64 va = (u64)a;
    asm("" : "+v"(va), "+v"(b));
    return c ? va : b;
}
__device__ __forceinline__ LAS char* lp(int a) { return (LAS char*)(size_t)(unsigned)a; }
__device__ __forceinline__ u32x4 lds128(LAS char* p) { return *(LAS u32x4*)p; }
__device__ __forceinline__ u32x2 ldstr(LAS char* p) {
    return __builtin_bit_cast(u32x2, __builtin_amdgcn_ds_read_tr16_b64_v4i16((LAS s16x4*)p));
}
__device__ __forceinline__ u32x4 ldstr2(LAS char* p, int off) {
    u32x2 a = ldstr(p), b = ldstr(p + off);
    return (u32x4){a.x, a.y, b.x, b.y};
}
__device__ __forceinline__ f32x16 mfma32(u32x4 a, u32x4 b, f32x16 c) {
    return __builtin_amdgcn_mfma_f32_32x32x16_bf16(__builtin_bit_cast(bf16x8, a), __builtin_bit_cast(bf16x8, b), c, 0, 0, 0);
}
__device__ __forceinline__ f32x4 mfma16(u32x4 a, u32x4 b, f32x4 c) {
    return __builtin_amdgcn_mfma_f32_16x16x32_bf16(__builtin_bit_cast(bf16x8, a), __builtin_bit_cast(bf16x8, b), c, 0, 0, 0);
}
template <class Fn, int... I>
__device__ __forceinline__ void static_for(Fn&& fn, std::integer_sequence<int, I...>) { (fn(std::integral_constant<int, I>{}), ...); }
#define WAIT_VM(n) asm volatile("s_waitcnt vmcnt(" #n ")" ::: "memory")
#define WAIT_LGKM0() asm volatile("s_waitcnt lgkmcnt(0)" ::: "memory")
#define WAIT_BAR(w) asm volatile("s_waitcnt " w "\n\ts_barrier" ::: "memory")
#define SCHED() __builtin_amdgcn_sched_barrier(0)

__device__ __forceinline__ void wait_lds(LAS char* word) {
#pragma unroll 1
    for (unsigned spins = 0; spins < (SPIN_CAP << 4); ++spins) {
        if (__builtin_amdgcn_readfirstlane(__hip_atomic_load((LAS unsigned*)word, __ATOMIC_RELAXED, __HIP_MEMORY_SCOPE_WORKGROUP))) break;
        __builtin_amdgcn_s_sleep(1);
    }
    asm volatile("" ::: "memory");
}
__device__ __forceinline__ void wait_lds_eq(LAS char* word, unsigned want) {
#pragma unroll 1
    for (unsigned spins = 0; spins < (1u << 22); ++spins) {
        if ((unsigned)__builtin_amdgcn_readfirstlane(__hip_atomic_load((LAS unsigned*)word, __ATOMIC_RELAXED, __HIP_MEMORY_SCOPE_WORKGROUP)) == want) break;
        __builtin_amdgcn_s_sleep(1);
    }
    asm volatile("" ::: "memory");
}
__global__ __launch_bounds__(512, 2) void fused_kernel(Params p) {
    extern __shared__ __attribute__((aligned(16))) char smem_raw[];
    u64 pc;
    asm volatile("s_getpc_b64 %0" : "=s"(pc));
    LAS char* sm = (LAS char*)smem_raw;
    const int tid = threadIdx.x, lane = tid & 63;
    const int wave = __builtin_amdgcn_readfirstlane(tid >> 6);
    const int xcd = blockIdx.x & 7, idx = blockIdx.x >> 3;
    if (idx == 0 && wave < 5) {
        const unsigned o = (unsigned)tid * 128u;
        asm volatile("s_mov_b32 m0, %2\n\ts_nop 0\n\tglobal_load_lds_dword %0, %1" :: "v"(o < CODE_PREFETCH ? o : 0u), "s"(pc & ~(u64)255),
                     "s"((unsigned)(size_t)sm + 32768u));
    }
    SCHED();
    const __amdgpu_buffer_rsrc_t wsr = __builtin_amdgcn_make_buffer_rsrc(p.ws, 0, WS_END, 0x00020000);
    asm volatile("" :: "s"(p.x), "s"(p.k), "s"(p.v), "s"(p.sk), "s"(p.hk), "s"(p.dv), "s"(p.w1), "s"(p.w3), "s"(p.w2), "s"(p.out), "s"(p.ws));

    if (idx < 4) {
        const int ca = (xcd >> 1) * 4 + idx, cmat = ca >= 7, cnt = 7 * (xcd & 1) + (ca >= 7 ? ca - 7 : ca);
        const bool job = ca < 14;
        u32x4 xr[7], wr[14];
        const int row = (8 * (xcd >> 1) + 4 * (xcd & 1) + idx) * 64 + wave * 8 + (lane >> 3);
#pragma unroll
        for (int i = 0; i < 7; ++i) xr[i] = *(const u32x4*)(p.x + (size_t)row * D + i * 64 + (lane & 7) * 8);
        const u64 epoch = __hip_atomic_load((GAS u64*)(p.ws + WS_EPOCH), RLX_AGENT);
        const u64 tag = epoch ^ TAG_SALT;
        const unsigned tag32 = (unsigned)tag;
        unsigned stat;
        {
            float s4[4] = {0.f, 0.f, 0.f, 0.f};
#pragma unroll
            for (int i = 0; i < 7; ++i)
#pragma unroll
                for (int j = 0; j < 4; ++j) s4[j] = dot2(xr[i][j], 0x3f803f80u, s4[j]);
            float sum = (s4[0] + s4[1]) + (s4[2] + s4[3]);
            sum += __shfl_xor(sum, 1); sum += __shfl_xor(sum, 2); sum += __shfl_xor(sum, 4);
            const float mean = sum * (1.f / D);
            f32x2 q4[4] = {{0.f, 0.f}, {0.f, 0.f}, {0.f, 0.f}, {0.f, 0.f}};
#pragma unroll
            for (int i = 0; i < 7; ++i)
#pragma unroll
                for (int j = 0; j < 4; ++j) {
                    const f32x2 d = (f32x2){bflo(xr[i][j]), bfhi(xr[i][j])} - (f32x2){mean, mean};
                    q4[j] = __builtin_elementwise_fma(d, d, q4[j]);
                }
            const f32x2 q2 = (q4[0] + q4[1]) + (q4[2] + q4[3]);
            float sq = q2[0] + q2[1];
            sq += __shfl_xor(sq, 1); sq += __shfl_xor(sq, 2); sq += __shfl_xor(sq, 4);
            stat = pk(mean, __builtin_amdgcn_sqrtf(rbf(rbf(sq * (1.f / D)) + 1e-5f)));
            asm volatile("" : "+v"(stat));
        }
        if (job) {
            SCHED();
            const u16* wp = (cmat ? p.w3 : p.w1) + (size_t)(wave * 4 + (lane >> 4)) * F + cnt * 128 + (lane & 15) * 8;
#pragma unroll
            for (int i = 0; i < 14; ++i) wr[i] = *(const u32x4*)(wp + (size_t)(32 * i) * F);
            SCHED();
        }
        __builtin_amdgcn_raw_buffer_store_b64((u32x2){tag32, stat}, wsr, (lane & 7) == 0 ? WS_STATS + (unsigned)row * 8u : 0xfffffff0u, 0, 16);
        if (job) {
            float c[8];
#pragma unroll
            for (int j = 0; j < 8; ++j) c[j] = 0.f;
#pragma unroll
            for (int i = 0; i < 14; ++i)
#pragma unroll
                for (int j = 0; j < 4; ++j) { c[2 * j] += bflo(wr[i][j]); c[2 * j + 1] += bfhi(wr[i][j]); }
            LAS char* cp = sm + ((wave * 4 + (lane >> 4)) * 136 + (lane & 15) * 8) * 4;
            *(LAS f32x4*)cp = (f32x4){c[0], c[1], c[2], c[3]};
            *(LAS f32x4*)(cp + 16) = (f32x4){c[4], c[5], c[6], c[7]};
        }
        if (job) {
            WAIT_BAR("lgkmcnt(0)");
            float t = 0.f;
#pragma unroll
            for (int i = 0; i < 8; ++i) t += *(LAS float*)(sm + ((i * 4 + (tid & 3)) * 136 + (tid >> 2)) * 4);
            t += __shfl_xor(t, 1); t += __shfl_xor(t, 2);
            __builtin_amdgcn_raw_buffer_store_b64((u32x2){tag32, __float_as_uint(t)}, wsr,
                (tid & 3) == 0 ? WS_CSUM + (unsigned)((cnt * 2 + cmat) * 128 + (tid >> 2)) * 8u : 0xfffffff0u, 0, 16);
        }
        SCHED();
        if (!job) __builtin_amdgcn_s_sleep(40);
        const unsigned pf2 = *(const unsigned*)((const char*)p.hk + (size_t)((xcd * 4 + idx) * 448 + wave * 56 + (lane < 56 ? lane : 0)) * 128);
        {
            const unsigned fo = lane * 16u;
            constexpr int PER = ((4 - NTOUCH) * 14336 + 4095) / 4096;
#pragma unroll 1
            for (int i = wave; i < 7 * PER; i += 8) {
                const int g = i / PER, j = i - g * PER;
                int o = NTOUCH * 14336 + j * 4096; o = o > 57344 - 4096 ? 57344 - 4096 : o;
                glds16x4((const char*)p.w2 + (size_t)idx * 401408 + g * 57344 + o, (u32x4){fo, fo, fo, fo}, (unsigned)(size_t)sm + 65536u + wave * 4096);
            }
        }
        asm volatile("" :: "v"(pf2));
        if (blockIdx.x == 0 && wave == 7) {
            GAS u64* gp = (GAS u64*)(p.ws + WS_STATS) + lane * 8;
            GAS u64* fp[4];
#pragma unroll
            for (int m = 0; m < 4; ++m) {
                const int g = m * 56 + (lane < 56 ? lane : 0);
                fp[m] = (GAS u64*)(p.ws + WS_U + (unsigned)((g >> 5) * 64 + 8) * 8192u + (unsigned)(1984 - (g & 31) * 64) * 2u);
            }
#pragma unroll 1
            for (unsigned spins = 0; spins < SPIN_CAP; ++spins) {
                __builtin_amdgcn_s_sleep(32);
                bool ok = true;
#pragma unroll
                for (int m = 0; m < 4; ++m)
                    ok &= ((unsigned)__hip_atomic_load(gp + m * 512, RLX_AGENT) == tag32) &
                          ((((unsigned)__hip_atomic_load(fp[m], RLX_AGENT) ^ tag32) & ~Y_TAG) == 0u);
                if (__all(ok)) break;
            }
            if (lane == 0) __hip_atomic_store((GAS u64*)(p.ws + WS_EPOCH), epoch + 1, RLX_AGENT);
        }
        return;
    }

    const int gi = idx - 4, ml = gi & 3, nl = gi >> 2;
    const int mt = 4 * (xcd >> 1) + ml, nt = 7 * (xcd & 1) + nl;
    const int rot1 = (2 * ml + nl) % 6;
    const int m64 = 4 * xcd + ml, n64 = nl;
    const int gpair = xcd * 28 + gi;
    const int r32 = lane & 31, hh = lane >> 5, blk = (lane >> 4) & 1, q4 = (lane & 15) >> 2, p4 = lane & 3;
    const unsigned lb = (unsigned)(size_t)sm;
    const int wq = wave & 3;
    u32x4 wvo;
    wvo[0] = (unsigned)((lane >> 4) * F + (((lane & 15) ^ ((lane >> 4) << 2)) * 8)) * 2u;
#pragma unroll
    for (int j = 1; j < 4; ++j) wvo[j] = wvo[0] + j * (8 * F - 1024);
    const u16* w1b = sptr(p.w1, wq * 16 * F + nt * 128);
    const u16* w3b = sptr(p.w3, wq * 16 * F + nt * 128);
    auto kst = [&](int si) { if (si == 0) return 0; const int s = si + rot1; return s >= 7 ? s - 6 : s; };
    auto group_w = [&](int si, int m) { glds16x4(sptr(m ? w3b : w1b, kst(si) * 64 * F), wvo, lb + ring(si) + 16384 + m * 16384 + wq * 4096); };
    const int kr0 = (gpair & 31) * 64, kc0 = (gpair >> 5) * 64;
    const int chn = wq >> 1, par = wq & 1, dch = 2 * gpair + chn;
    if (wave < 4) group_w(0, 0);
    {
        const bool isk = lane >= 8 && lane < 14;
        u64 mb_ = vsel(lane == 15, p.dv, vsel(lane == 14, p.hk, vsel(isk, p.k, (u64)p.sk)));
        unsigned mo = (unsigned)((kc0 + wq * 16) * 8 + (lane & 7) * 16);
        mo = isk ? (unsigned)((kr0 ? kr0 - 3 + ((lane - 8) >> 1) : 0) * D + kc0 + wq * 16 + (lane & 1) * 8) * 2u : mo;
        mo = lane == 14 ? (unsigned)dch * 4096u : mo;
        mo = lane == 15 ? (unsigned)(dch & ~7) * 2u : mo;
        const u64 ma = mb_ + mo;
        asm volatile("s_mov_b32 m0, %1\n\ts_nop 0\n\tglobal_load_lds_dwordx4 %0, off" :: "v"(ma), "s"(lb + wave * 1024));
    }
    if (wave < 4) group_w(0, 1);
    SCHED();
    const int rot2 = 8 * ml;
    const int w2i = L_W2 + wq * 12288;
    auto fresh_lane = [&]() { int l = lane; asm volatile("" : "+v"(l)); return l; };
    const int latei = L_CONV + wq * 8192;
    auto pair_w2 = [&](u32x4 vo, int i, int dst) {
        int t = i + rot2; t = t >= 28 ? t - 28 : t;
        glds16x4(sptr(p.w2, (wq * 448 + t * 16) * D + n64 * 64), vo, lb + dst);
    };
    auto w2_off = [&]() {
        const int l = fresh_lane();
        const unsigned o = (unsigned)((l >> 3) * D + (((l & 7) ^ (((l >> 4) & 1) << 2)) * 8)) * 2u;
        return (u32x4){o, o + (16 * D - 1024), o + 2 * (16 * D - 1024), o + 3 * (16 * D - 1024)};
    };

    if (wave < 4) {
        __builtin_amdgcn_s_setprio(1);
        const int wm = wave >> 1, wn = wave & 1;
        int xa[4], wa0, wa1;
#pragma unroll
        for (int i = 0; i < 4; ++i) xa[i] = lb + ring(0) + (((64 * wm + r32) * 128 + ((hh ^ ((r32 >> 1) & 7)) * 16)) ^ (i * 32));
        wa0 = 16384 + (8 * hh + q4) * 256 + (((8 * wn + 2 * blk + (p4 >> 1)) ^ (q4 << 2)) * 16) + 8 * (p4 & 1);
        wa1 = (wa0 ^ 64) + lb + ring(0); wa0 += lb + ring(0);
        f32x16 acc[2][2][2];
#pragma unroll
        for (int i = 0; i < 8; ++i)
#pragma unroll
            for (int r = 0; r < 16; ++r) acc[i >> 2][(i >> 1) & 1][i & 1][r] = 0.f;
        u32x4 fx[2][2], fw[2][2][2];
        auto load_frag = [&](int g, int j) {
            const int pp = g & 3, b = g & 1;
            if (j == 0) {
                if (g >= 4) {
                    const int d = ring(g >> 2) - ring((g >> 2) - 1);
                    if (pp == 0) { wa0 += d; wa1 += d; asm volatile("" : "+v"(wa0), "+v"(wa1)); }
                    xa[pp] += d; asm volatile("" : "+v"(xa[pp]));
                }
                fw[b][0][0] = ldstr2(lp(wa0) + pp * 4096, 1024);
            }
            if (j == 1) { fx[b][0] = lds128(lp(xa[pp])); fx[b][1] = lds128(lp(xa[pp]) + 4096); }
            if (j == 2) fw[b][0][1] = ldstr2(lp(wa0) + pp * 4096 + 16384, 1024);
            if (j == 3) fw[b][1][0] = ldstr2(lp(wa1) + pp * 4096, 1024);
            if (j == 4) fw[b][1][1] = ldstr2(lp(wa1) + pp * 4096 + 16384, 1024);
        };
        int ka0[2], kam[2], kwo;
#pragma unroll
        for (int rd = 0; rd < 2; ++rd) {
            const int ck = 2 * wq + rd;
            const int bq = 2 * (lane >> 4) + (p4 >> 1), bm = bq ? bq - 1 : 0;
            ka0[rd] = lb + L_KV + (8 * bq + q4) * 128 + ((ck ^ (((q4 >> 1) & 1) | ((bq & 3) << 1))) * 16) + 8 * (p4 & 1);
            kam[rd] = lb + L_KV + (8 * bm + 4 + q4) * 128 + ((ck ^ (((q4 >> 1) & 1) | ((bm & 3) << 1))) * 16) + 8 * (p4 & 1);
        }
        kwo = lb + L_KVO + wq * 2048 + (lane & 7) * 128 + (((7 - (lane >> 3)) ^ (lane & 7)) * 16);
        asm volatile("" : "+v"(ka0[0]), "+v"(ka0[1]), "+v"(kam[0]), "+v"(kam[1]), "+v"(kwo));
        WAIT_VM(4);
        u32x2 skv[2], hal[2];
#pragma unroll
        for (int rd = 0; rd < 2; ++rd) {
            LAS char* mp = sm + wave * 1024;
            skv[rd] = *(LAS u32x2*)(mp + (rd * 8 + (lane & 7)) * 8);
            const unsigned hm = kr0 ? 0xffffffffu : 0u;
            const unsigned a = *(LAS u16*)(mp + 128 + rd * 16 + (lane & 7) * 2), b = *(LAS u16*)(mp + 160 + rd * 16 + (lane & 7) * 2),
                           c = *(LAS u16*)(mp + 192 + rd * 16 + (lane & 7) * 2);
            hal[rd] = (u32x2){(a << 16) & hm, (b | (c << 16)) & hm};
            asm volatile("" : "+v"(skv[rd]), "+v"(hal[rd]));
        }
        WAIT_BAR("vmcnt(0) lgkmcnt(0)");
        u32x2 kt[2][3], vt[2][2];
        float cf[4], kk[11], ko[8];
        u32x4 kpk;
        auto fill = [&](auto kc) {
            constexpr int k = decltype(kc)::value;
            if constexpr (k < 4) {
                constexpr int rd = k >> 1;
                if constexpr ((k & 1) == 0) { kt[rd][0] = ldstr(lp(kam[rd])); kt[rd][1] = ldstr(lp(ka0[rd])); kt[rd][2] = ldstr(lp(ka0[rd]) + 512); }
                else { vt[rd][0] = ldstr(lp(ka0[rd]) + 8192); vt[rd][1] = ldstr(lp(ka0[rd]) + 8192 + 512); }
            } else if constexpr (k < 4 + 2 * 16) {
                constexpr int rd = (k - 4) / 16, s = (k - 4) % 16;
                if constexpr (s == 0) { cf[0] = bflo1(skv[rd][0]); cf[1] = bfhi1(skv[rd][0]); cf[2] = bflo1(skv[rd][1]); cf[3] = bfhi1(skv[rd][1]); }
                else if constexpr (s == 1) {
                    const unsigned a = lane < 8 ? hal[rd][0] : kt[rd][0][0], b = lane < 8 ? hal[rd][1] : kt[rd][0][1];
                    kk[0] = bfhi1(a); kk[1] = bflo1(b); kk[2] = bfhi1(b);
                } else if constexpr (s < 4) {
                    const u32x2 t = kt[rd][s - 1];
                    kk[4 * s - 5] = bflo1(t[0]); kk[4 * s - 4] = bfhi1(t[0]); kk[4 * s - 3] = bflo1(t[1]); kk[4 * s - 2] = bfhi1(t[1]);
                } else if constexpr (s < 12) {
                    constexpr int tp = (s - 4) >> 1, o0 = ((s - 4) & 1) * 4;
#pragma unroll
                    for (int i = o0; i < o0 + 4; ++i) {
                        if (tp == 0) ko[i] = mul1(kk[i], cf[3]);
                        else fmac1(ko[i], kk[i + tp], cf[3 - tp]);
                    }
                } else {
                    constexpr int j = s - 12;
                    const unsigned v = vt[rd][j >> 1][j & 1];
                    kpk[3 - j] = pk(mul1(ko[2 * j + 1], bfhi1(v)), mul1(ko[2 * j], bflo1(v)));
                    if constexpr (j == 3) *(LAS u32x4*)(lp(kwo) + rd * 1024) = kpk;
                }
            }
        };
#pragma unroll
        for (int j = 0; j < 5; ++j) load_frag(0, j);
        f32x4 cs[2][2][4];
        unsigned tch[NTOUCH];
        unsigned tco = (unsigned)(gi * 4 + wq + (lane < 28 ? lane : 27) * 448) * 128u;
        asm volatile("" : "+v"(tco));
        static_for([&](auto gc) {
            constexpr int g = decltype(gc)::value, b = g & 1, pp = g & 3;
            SCHED();
            if (pp == 3 && g < 27) { WAIT_BAR("lgkmcnt(0)"); }
            if (g == 27) {
                WAIT_LGKM0();
                if (lane == 0) __hip_atomic_fetch_add((LAS unsigned*)(sm + L_CTL + 12), 1u, __ATOMIC_RELAXED, __HIP_MEMORY_SCOPE_WORKGROUP);
            }
            static_for([&](auto ic) {
                constexpr int i = decltype(ic)::value, ft = i >> 2, m = (i >> 1) & 1, sb = i & 1;
                acc[sb][ft][m] = mfma32(fw[b][ft][m], fx[b][sb], acc[sb][ft][m]);
                SCHED();
                if (g < 27 && i < 5) load_frag(g + 1, i);
                if constexpr (i == 4 && pp == 2 && g >= 4 && g < 4 + 4 * NTOUCH) {
                    u64 t;
                    asm volatile("s_mov_b64 %1, %3\n\tglobal_load_dword %0, %2, %1" : "=v"(tch[(g - 4) >> 2]), "=&s"(t)
                                 : "v"(tco), "s"((const char*)p.w2 + ((g - 4) >> 2) * (112 * 128)));
                }
                if constexpr (i >= 5 && g >= 7) fill(std::integral_constant<int, (g - 7) * 3 + i - 5>{});
                SCHED();
            }, std::make_integer_sequence<int, 8>{});
        }, std::make_integer_sequence<int, 28>{});
        wait_lds_eq(sm + L_CTL + 64, 3u);
        unsigned sa[2];
#pragma unroll
        for (int i = 0; i < 8; ++i) cs[i >> 2][0][i & 3] = *(LAS f32x4*)(sm + L_CSUM + (64 * wn + 32 * (i >> 2) + 8 * (i & 3) + 4 * hh) * 4);
        sa[0] = *(LAS unsigned*)(sm + L_STAT + (64 * wm + r32) * 4);
        sa[1] = *(LAS unsigned*)(sm + L_STAT + (64 * wm + 32 + r32) * 4);
#pragma unroll
        for (int i = 0; i < 8; ++i) cs[i >> 2][1][i & 3] = *(LAS f32x4*)(sm + L_CSUM + 512 + (64 * wn + 32 * (i >> 2) + 8 * (i & 3) + 4 * hh) * 4);
        const int lw = fresh_lane();
        const int w2a0 = lb + w2i + (4 * (lw >> 5) + ((lw & 15) >> 2)) * 128 + (((2 * ((lw >> 4) & 1) + ((lw & 3) >> 1)) ^ (((lw >> 3) & 1) << 2)) * 16) + 8 * (lw & 1);
        const int w2a1 = w2a0 ^ 64;
        u32x4 w2r[12][2];
        auto park = [&](int i) {
            w2r[i][0] = ldstr2(lp(w2a0) + (i / 6) * L_SLOT + (i % 6) * 2048, 1024);
            w2r[i][1] = ldstr2(lp(w2a1) + (i / 6) * L_SLOT + (i % 6) * 2048, 1024);
        };
#pragma unroll
        for (int sb = 0; sb < 2; ++sb) {
            const float ri = __builtin_amdgcn_rcpf(bfhi(sa[sb])) * -1.44269504088896341f, nm = -bflo(sa[sb]), d2 = bfhi(sa[sb]) * bfhi(sa[sb]);
            const f32x2 ri2 = {ri, ri}, nm2 = {nm, nm}, dd2 = {d2, d2};
#pragma unroll
            for (int ft = 0; ft < 2; ++ft) {
#pragma unroll
                for (int t2 = 0; t2 < 2; ++t2) {
                    f32x2 gu[4], ee[4];
#pragma unroll
                    for (int j = 0; j < 4; ++j) {
                        const int r = 8 * t2 + 2 * j;
                        const f32x2 g = __builtin_elementwise_fma(nm2, (f32x2){cs[ft][0][r >> 2][r & 3], cs[ft][0][r >> 2][(r & 3) + 1]}, (f32x2){acc[sb][ft][0][r], acc[sb][ft][0][r + 1]});
                        const f32x2 u = __builtin_elementwise_fma(nm2, (f32x2){cs[ft][1][r >> 2][r & 3], cs[ft][1][r >> 2][(r & 3) + 1]}, (f32x2){acc[sb][ft][1][r], acc[sb][ft][1][r + 1]});
                        const f32x2 t = g * ri2;
                        ee[j] = (f32x2){__builtin_amdgcn_exp2f(t[0]), __builtin_amdgcn_exp2f(t[1])};
                        gu[j] = g * u;
                    }
                    u32x4 st;
#pragma unroll
                    for (int j = 0; j < 4; ++j) {
                        const f32x2 dn = __builtin_elementwise_fma(ee[j], dd2, dd2);
                        const f32x2 hv = gu[j] * (f32x2){__builtin_amdgcn_rcpf(dn[0]), __builtin_amdgcn_rcpf(dn[1])};
                        st[j] = pk(hv[0], hv[1]);
                    }
                    const unsigned fi = (unsigned)((4 * mt + 2 * wm + sb) * 112 + 8 * nt + 4 * wn + 2 * ft + t2);
                    __builtin_amdgcn_raw_buffer_store_b128(st, wsr, WS_H + fi * 1024u + lane * 16u, 0, 16);
                }
            }
        }
        SCHED();
        const u32x4 w2o = w2_off();
        const u64 tag = *(LAS u64*)(sm + L_CTL + 128);
        GAS u64* myflag = (GAS u64*)(p.ws + WS_FH + ((2 * mt + wm) * 28 + 2 * nt + wn) * 128);
        if (!__builtin_amdgcn_readfirstlane(__hip_atomic_load((LAS unsigned*)(sm + L_CTL + 112 + wq * 4), __ATOMIC_RELAXED, __HIP_MEMORY_SCOPE_WORKGROUP))) {
            WAIT_VM(0);
            if (lane == 0) __hip_atomic_store(myflag, tag, RLX_AGENT);
            wait_lds(sm + L_CTL + 112 + wq * 4);
        }
        asm volatile("" ::: "memory");
#pragma unroll
        for (int i = 6; i < 12; ++i) park(i);
        WAIT_VM(0);
        asm volatile("" :: "v"(tch[0]), "v"(tch[1]));
        if (lane == 0) __hip_atomic_store(myflag, tag, RLX_AGENT);
#pragma unroll
        for (int i = 0; i < 6; ++i) park(i);
        asm volatile("" ::: "memory");
        __hip_atomic_store((LAS unsigned*)(sm + L_CTL + 32 + wq * 4), 1u, __ATOMIC_RELAXED, __HIP_MEMORY_SCOPE_WORKGROUP);
        WAIT_LGKM0();
#pragma unroll
        for (int i = 0; i < 12; ++i) asm volatile("" : "+v"(w2r[i][0]), "+v"(w2r[i][1]));
#pragma unroll
        for (int i = 0; i < 3; ++i) pair_w2(w2o, 12 + 2 * i, w2i + i * 4096);
#pragma unroll
        for (int i = 0; i < 3; ++i) pair_w2(w2o, 18 + 2 * i, w2i + L_SLOT + i * 4096);
        {
            GAS u64* fp = (GAS u64*)(p.ws + WS_FH + (m64 * 28 + 7 * wq + (lw < 7 ? lw : 0)) * 128);
#pragma unroll 1
            for (unsigned spins = 0; spins < SPIN_CAP; ++spins) {
                if (__all(__hip_atomic_load(fp, RLX_AGENT) == tag)) break;
                __builtin_amdgcn_s_sleep(4);
            }
            __builtin_amdgcn_fence(__ATOMIC_ACQUIRE, "wavefront");
        }
        const unsigned hb = WS_H + (unsigned)(2 * m64 * 112 + 28 * wq) * 1024u + lw * 16u;
        u32x4 hf[28][2];
        auto load_h = [&](int i, int j) {
            int t = i + rot2; t = t >= 28 ? t - 28 : t;
            hf[i][j] = __builtin_amdgcn_raw_buffer_load_b128(wsr, hb + j * (112u * 1024u), t * 1024, 16);
        };
#pragma unroll
        for (int i = 0; i < HD; ++i) { load_h(i, 0); load_h(i, 1); }
        f32x16 ac2[2][2];
#pragma unroll
        for (int i = 0; i < 4; ++i)
#pragma unroll
            for (int r = 0; r < 16; ++r) ac2[i >> 1][i & 1][r] = 0.f;
        u32x4 a0 = w2r[0][0], a1 = w2r[0][1];
        static_for([&](auto ic) {
            constexpr int i = decltype(ic)::value, n = i + 1;
            SCHED();
            if (i == YSTEP) __hip_atomic_store((LAS unsigned*)(sm + L_CTL + 96 + wq * 4), 1u, __ATOMIC_RELAXED, __HIP_MEMORY_SCOPE_WORKGROUP);
            if (i == 23) wait_lds(sm + L_CTL + 80 + wq * 4);
            u32x4 b0, b1;
            if constexpr (n < 12) { b0 = w2r[n][0]; b1 = w2r[n][1]; }
            else if constexpr (n < 28) {
                constexpr int so = n < 24 ? ((n - 12) / 6) * L_SLOT + ((n - 12) % 6) * 2048 : (n - 24) * 2048;
                const int lo = n < 24 ? 0 : latei - w2i;
                b0 = ldstr2(lp(w2a0 + lo) + so, 1024);
                b1 = ldstr2(lp(w2a1 + lo) + so, 1024);
            }
            SCHED();
            ac2[0][0] = mfma32(a0, hf[i][0], ac2[0][0]);
            SCHED();
            constexpr int l0 = i < HG ? HD + 2 * i : HD + HG + i;
            if constexpr (l0 < 28) load_h(l0, 0);
            SCHED();
            ac2[1][0] = mfma32(a1, hf[i][0], ac2[1][0]);
            SCHED();
            if constexpr (l0 < 28) load_h(l0, 1);
            SCHED();
            ac2[0][1] = mfma32(a0, hf[i][1], ac2[0][1]);
            SCHED();
            if constexpr (i < HG) load_h(l0 + 1, 0);
            SCHED();
            ac2[1][1] = mfma32(a1, hf[i][1], ac2[1][1]);
            SCHED();
            if constexpr (i < HG) load_h(l0 + 1, 1);
            a0 = b0; a1 = b1;
        }, std::make_integer_sequence<int, 28>{});
        SCHED();
        WAIT_LGKM0();
#pragma unroll
        for (int v = 0; v < 16; ++v) {
            const f32x16& t = ac2[v >> 3][(v >> 2) & 1];
            const int r0 = 4 * (v & 3);
            *(LAS f32x4*)(sm + w2i + part(v >> 3, (v >> 2) & 1, v & 3) + lw * 16) = (f32x4){t[r0], t[r0 + 1], t[r0 + 2], t[r0 + 3]};
        }
        WAIT_BAR("lgkmcnt(0)");
    } else {
        u32x4 xvo;
#pragma unroll
        for (int j = 0; j < 4; ++j) xvo[j] = (unsigned)((8 * j + (lane >> 3)) * D + ((((lane & 7) ^ (lane >> 4)) ^ (4 * (j & 1))) * 8)) * 2u - 1024u * j;
        const u16* xb = sptr(p.x, (mt * 128 + wq * 32) * D);
        auto group_x = [&](int si) { glds16x4(sptr(xb, kst(si) * 64), xvo, lb + ring(si) + wq * 4096); };
        group_x(0);
        group_x(1); group_w(1, 0); group_w(1, 1);
        const int cbi = L_CONV + chn * C_CH;
        LAS char* cb = sm + cbi;
        unsigned kvo[2], uso;
#pragma unroll
        for (int rd = 0; rd < 2; ++rd) kvo[rd] = (unsigned)((lane >> 3) * D + (((lane & 7) ^ (((lane >> 4) & 1) | (((2 * wq + rd) & 3) << 1))) * 8)) * 2u;
        uso = WS_U + (unsigned)(kc0 + wq * 16 + (lane >> 3)) * 8192u + (unsigned)(1984 - kr0 + (lane & 7) * 8) * 2u;
        int kro = lb + L_KVO + wq * 2048 + (lane >> 3) * 128 + (((lane & 7) ^ (lane >> 3)) * 16);
        const int cc = 2 * par + ((lane >> 2) & 1);
        int zo = cbi + C_U + par * C_US + (248 + (lane < 36 ? lane : 35)) * 16;
        zo = lane < 8 ? cbi + cc * C_HS + (lane & 3) * 16 : zo;
        u32x4 w2o = w2_off();
        const unsigned fo = lane * 16u;
        unsigned ao = (wq < 2 ? WS_CSUM + (unsigned)(nt * 2 + wq) * 1024u : WS_STATS + mt * 1024u) + fo;
        asm volatile("" : "+v"(zo), "+v"(w2o), "+v"(kvo[0]), "+v"(kvo[1]), "+v"(uso), "+v"(kro), "+v"(ao));
        unsigned eo = (lane & 1) * 4u;
        asm volatile("" : "+v"(eo));
        WAIT_VM(16);
        unsigned tag32, taghi;
        u32x4 bval, ug[2];
        unsigned dbits = *(LAS u16*)(sm + wave * 1024 + 240 + (dch & 7) * 2);
        {
            const u32x4 h0 = lds128(sm + wave * 1024 + 224);
            const bool s1 = !(cc & 1), s2 = !(cc & 2);
            const unsigned a0 = s1 ? 0u : h0[0], a1 = s1 ? h0[0] : h0[1], a2 = s1 ? h0[1] : h0[2], a3 = s1 ? h0[2] : h0[3];
            const unsigned lm = (lane == 3 || lane == 7) ? 0xffffffffu : 0u;
            bval = (u32x4){(s2 ? 0u : a0) & lm, (s2 ? 0u : a1) & lm, (s2 ? a0 : a2) & lm, (s2 ? a1 : a3) & lm};
            asm volatile("" : "+v"(bval), "+v"(dbits));
        }
        auto kv_store = [&](int rd) {
            LAS unsigned* kp = (LAS unsigned*)(lp(kro) + rd * 1024);
            ug[0][1] = kp[0]; asm volatile("" ::: "memory"); ug[0][3] = kp[1]; asm volatile("" ::: "memory");
            ug[1][1] = kp[2]; asm volatile("" ::: "memory"); ug[1][3] = kp[3];
            __builtin_amdgcn_raw_buffer_store_b128(ug[0], wsr, uso, rd * 65536, 16);
            __builtin_amdgcn_raw_buffer_store_b128(ug[1], wsr, uso, rd * 65536 + 4096, 16);
        };
        WAIT_BAR("vmcnt(12) lgkmcnt(0)");
#pragma unroll
        for (int w = 0; w < 5; ++w) {
            if (w == 3) glds4c(p.ws + WS_EPOCH, eo, lb + L_RAW + wq * 1024);
            group_x(w + 2); group_w(w + 2, 0); group_w(w + 2, 1);
            if (w == 0) {
#pragma unroll
                for (int j = 0; j < 2; ++j) {
                    glds16(sptr(p.k, (kr0 + (2 * wq + j) * 8) * D + kc0), kvo[j], lb + L_KV + (2 * wq + j) * 1024);
                    glds16(sptr(p.v, (kr0 + (2 * wq + j) * 8) * D + kc0), kvo[j], lb + L_KV + 8192 + (2 * wq + j) * 1024);
                }
            }
            if (w == 4) {
                const u32x2 ev = *(LAS u32x2*)(sm + L_RAW + wq * 1024);
                tag32 = __builtin_amdgcn_readfirstlane(ev[0]) ^ (unsigned)TAG_SALT;
                taghi = __builtin_amdgcn_readfirstlane(ev[1]) ^ (unsigned)(TAG_SALT >> 32);
                asm volatile("s_nop 1" : "+s"(tag32), "+s"(taghi));
                ug[0][0] = vmov1(tag32); ug[0][2] = vmov1(tag32); ug[1][0] = vmov1(tag32); ug[1][2] = vmov1(tag32);
                if (lane < 34) *(LAS unsigned*)(sm + L_CTL + lane * 4) = lane < 32 ? 0u : lane == 32 ? tag32 : taghi;
            }
            if (w == 4) kv_store(0);
            if (w == 0) WAIT_BAR("vmcnt(16) lgkmcnt(0)"); else WAIT_BAR("vmcnt(12) lgkmcnt(0)");
        }
        glds16c(p.ws, ao, lb + L_RAW + wq * 1024);
#pragma unroll
        for (int i = 0; i < 3; ++i) pair_w2(w2o, 2 * i, w2i + i * 4096);
        kv_store(1);
        WAIT_BAR("vmcnt(12) lgkmcnt(0)");
        asm volatile("" ::: "memory");
        if (lane == 0) __hip_atomic_fetch_add((LAS unsigned*)(sm + L_CTL + 12), 1u, __ATOMIC_RELAXED, __HIP_MEMORY_SCOPE_WORKGROUP);
        if (wq < 3) {
            u32x4 av = lds128(sm + L_RAW + wq * 1024 + lane * 16);
#pragma unroll 1
            for (unsigned spins = 0; spins < SPIN_CAP; ++spins) {
                if (__all(((av[0] ^ tag32) | (av[2] ^ tag32)) == 0u)) break;
                asm volatile("" ::: "memory");
                av = __builtin_amdgcn_raw_buffer_load_b128(wsr, ao, 0, 16);
                asm volatile("" : "+v"(av));
            }
            *(LAS u32x2*)(sm + L_CSUM + wq * 512 + lane * 8) = (u32x2){av[1], av[3]};
            asm volatile("" ::: "memory");
            if (lane == 0) __hip_atomic_fetch_add((LAS unsigned*)(sm + L_CTL + 64), 1u, __ATOMIC_RELAXED, __HIP_MEMORY_SCOPE_WORKGROUP);
        }
#pragma unroll
        for (int i = 0; i < 3; ++i) pair_w2(w2o, 6 + 2 * i, w2i + L_SLOT + i * 4096);
        WAIT_VM(12);
        __hip_atomic_store((LAS unsigned*)(sm + L_CTL + 48 + wq * 4), 1u, __ATOMIC_RELAXED, __HIP_MEMORY_SCOPE_WORKGROUP);
        wait_lds_eq(sm + L_CTL + 12, 8u);
        __builtin_amdgcn_s_setprio(2);
        const unsigned ubase = WS_U + (unsigned)dch * 8192u;
        glds16x2c(p.ws + ubase + par * 2048, fo, lb + cbi + C_U + par * 2048);
        glds16x2c(p.ws + ubase + 4096 + par * 2048, fo, lb + cbi + C_U + C_US + par * 2048);
        glds4c(p.ws + ubase + (1 - par) * 2048, eo, lb + L_UNX + wq * 256);
        {
            const u32x4 fvo = {fo, fo, fo, lane == 63 ? 62u * 16u : fo};
#pragma unroll
            for (int c = 0; c < 2; ++c) glds16x4(sptr(p.hk, dch * S + 4 * par + 2 * c + 2), fvo, lb + cbi + (2 * par + c) * C_HS + 64);
        }
        *(LAS u32x4*)(sm + zo) = bval;
        WAIT_VM(13);
        __hip_atomic_store((LAS unsigned*)(sm + L_CTL + 112 + wq * 4), 1u, __ATOMIC_RELAXED, __HIP_MEMORY_SCOPE_WORKGROUP);
        WAIT_VM(8);
        {
            u32x4 ua[2], ub[2]; u32x2 un[2];
#pragma unroll
            for (int rd = 0; rd < 2; ++rd) {
                const int x8 = 128 * par + 64 * rd + lane;
                ua[rd] = lds128(cb + C_U + x8 * 16); ub[rd] = lds128(cb + C_U + C_US + x8 * 16);
                un[rd] = *(LAS u32x2*)((rd == 1 && lane == 63) ? sm + L_UNX + wq * 256 : cb + C_U + x8 * 16 + 16);
            }
#pragma unroll 1
            for (unsigned spins = 0; spins < SPIN_CAP; ++spins) {
                unsigned df = 0;
#pragma unroll
                for (int rd = 0; rd < 2; ++rd) df |= (ua[rd][0] ^ tag32) | (ua[rd][2] ^ tag32) | (ub[rd][0] ^ tag32) | (ub[rd][2] ^ tag32) | (un[rd][0] ^ tag32);
                if (__all(df == 0u)) break;
                __builtin_amdgcn_s_sleep(4);
                asm volatile("" ::: "memory");
#pragma unroll
                for (int rd = 0; rd < 2; ++rd) {
                    const int x8 = 128 * par + 64 * rd + lane;
                    const unsigned uo = ubase + (unsigned)x8 * 16u;
                    ua[rd] = __builtin_amdgcn_raw_buffer_load_b128(wsr, uo, 0, 16);
                    ub[rd] = __builtin_amdgcn_raw_buffer_load_b128(wsr, uo + 4096u, 0, 16);
                    un[rd] = __builtin_amdgcn_raw_buffer_load_b64(wsr, x8 == 255 ? uo : uo + 16u, 0, 16);
                }
                asm volatile("" : "+v"(ua[0]), "+v"(ub[0]), "+v"(un[0]), "+v"(ua[1]), "+v"(ub[1]), "+v"(un[1]));
            }
#pragma unroll
            for (int rd = 0; rd < 2; ++rd) {
                const int x8 = 128 * par + 64 * rd + lane;
                const unsigned nx = x8 == 255 ? 0u : un[rd][1];
                *(LAS u32x4*)(cb + C_U + x8 * 16) = (u32x4){__builtin_amdgcn_alignbit(ua[rd][3], ua[rd][1], 16), __builtin_amdgcn_alignbit(ub[rd][1], ua[rd][3], 16),
                                                            __builtin_amdgcn_alignbit(ub[rd][3], ub[rd][1], 16), __builtin_amdgcn_alignbit(nx, ub[rd][3], 16)};
                *(LAS u32x4*)(cb + C_U + C_US + x8 * 16) = (u32x4){ua[rd][1], ua[rd][3], ub[rd][1], ub[rd][3]};
            }
        }
        asm volatile("s_waitcnt vmcnt(0) lgkmcnt(0)" ::: "memory");
        __hip_atomic_store((LAS unsigned*)(sm + L_CTL + 16 + wq * 4), 1u, __ATOMIC_RELAXED, __HIP_MEMORY_SCOPE_WORKGROUP);
        u32x4 lw2[8], xv[2];
        {
            const __amdgpu_buffer_rsrc_t wr2 = __builtin_amdgcn_make_buffer_rsrc((void*)p.w2, 0, F * D * 2, 0x00020000);
#pragma unroll
            for (int j = 0; j < 8; ++j) {
                int t = 24 + (j >> 2) * 2 + rot2; t = t >= 28 ? t - 28 : t;
                lw2[j] = __builtin_amdgcn_raw_buffer_load_b128(wr2, w2o[0], ((wq * 448 + t * 16 + (j & 3) * 8) * D + n64 * 64) * 2, 0);
            }
        }
        const int lf = fresh_lane();
        const int srow = 64 * m64 + 16 * wq + 2 * (lf >> 3);
        const unsigned xoff = (unsigned)(srow * D + 64 * n64 + 8 * (lf & 7)) * 2u;
        {
            const __amdgpu_buffer_rsrc_t xr = __builtin_amdgcn_make_buffer_rsrc((void*)p.x, 0, S * D * 2, 0x00020000);
            xv[0] = __builtin_amdgcn_raw_buffer_load_b128(xr, xoff, 0, 0);
            xv[1] = __builtin_amdgcn_raw_buffer_load_b128(xr, xoff, D * 2, 0);
        }
        SCHED();
        wait_lds(sm + L_CTL + 16 + (wq ^ 1) * 4);
        const int n16 = lane & 15, kg = lane >> 4;
        f32x4 ca[4];
#pragma unroll
        for (int j = 0; j < 4; ++j) ca[j] = (f32x4){0.f, 0.f, 0.f, 0.f};
        int apo = lb + cbi + (n16 & 3) * C_HS + ((n16 >> 2) + kg) * 16;
        int bpo = lb + cbi + C_U + (n16 & 1) * C_US + (252 - 4 * (n16 >> 1) + kg) * 16 - 7 * 512;
        u32x4 fa[2][8], fb[2][8];
#pragma unroll
        for (int i = 0; i < 8; ++i) { fa[0][i] = lds128(lp(apo) + i * 512); fb[0][i] = lds128(lp(bpo) + (7 - i) * 512); }
        const bool odd = lf & 1;
        int ysrc = (2 * (lf >> 3) + (lf & 1) + 16 * ((lf >> 1) & 3)) * 4;
        unsigned yso = WS_Y + (unsigned)dch * 8192u + lf * 16u;
        f32x4 kd[4];
        {
            const float dval = bflo(dbits);
            const int sl0 = 32 * ((lf & 15) >> 1) + 8 * (lf >> 4) + 4 * (lf & 1);
#pragma unroll
            for (int j = 0; j < 4; ++j) {
                const int tj = (j == 0 ? 0 : j == 1 ? 3 : j == 2 ? 4 : 7) ^ par;
                const u32x2 kvr = *(LAS u32x2*)(cb + C_U + C_US + (2044 - 256 * tj - sl0) * 2);
                const unsigned ka = pk(bfhi(kvr[1]) * dval, bflo(kvr[1]) * dval), kb = pk(bfhi(kvr[0]) * dval, bflo(kvr[0]) * dval);
                kd[j] = (f32x4){bflo(ka), bfhi(ka), bflo(kb), bfhi(kb)};
                asm volatile("" : "+v"(kd[j]));
            }
            asm volatile("" : "+v"(ysrc), "+v"(yso));
        }
        wait_lds(sm + L_CTL + 32 + wq * 4);
        auto conv_all = [&](auto pc_) {
            constexpr int P = decltype(pc_)::value;
            static_for([&](auto rc) {
                constexpr int r = decltype(rc)::value, c = r & 1, n = c ^ 1;
                static_for([&](auto kc) {
                    constexpr int k = decltype(kc)::value;
                    constexpr int T[4] = {P, 3 - P, 4 + P, 7 - P};
                    constexpr int j = k <= T[0] ? 0 : k <= T[0] + T[1] + 1 ? 1 : k <= T[0] + T[1] + T[2] + 2 ? 2 : 3;
                    constexpr int pp = k - (j == 0 ? 0 : j == 1 ? T[0] + 1 : j == 2 ? T[0] + T[1] + 2 : T[0] + T[1] + T[2] + 3);
                    ca[j] = mfma16(fa[c][pp], fb[c][T[j] - pp], ca[j]);
                    SCHED();
                    if constexpr (r < 7 && k < 2 * (T[3] + 1)) {
                        if constexpr (k & 1) fb[n][k >> 1] = lds128(lp(bpo) + (r + 1) * 64 + (7 - (k >> 1)) * 512);
                        else fa[n][k >> 1] = lds128(lp(apo) + (r + 1) * 64 + (k >> 1) * 512);
                    }
                    SCHED();
                }, std::make_integer_sequence<int, 18>{});
                asm volatile("" : "+v"(apo), "+v"(bpo));
            }, std::make_integer_sequence<int, 8>{});
        };
        if (par) conv_all(std::integral_constant<int, 1>{}); else conv_all(std::integral_constant<int, 0>{});
        SCHED();
        {
            float r0[4], r1[4];
#pragma unroll
            for (int j = 0; j < 4; ++j) { r0[j] = dpp_xor1(odd ? ca[j][0] : ca[j][2]); r1[j] = dpp_xor1(odd ? ca[j][1] : ca[j][3]); }
            unsigned rp[4][2];
#pragma unroll
            for (int j = 0; j < 4; ++j) {
                const unsigned ya = pk(odd ? r0[j] : ca[j][0], odd ? ca[j][2] : r0[j]), yb = pk(odd ? r1[j] : ca[j][1], odd ? ca[j][3] : r1[j]);
                rp[j][0] = __builtin_amdgcn_ds_bpermute(ysrc, pk(add1(bflo1(ya), kd[j][0]), add1(bfhi1(ya), kd[j][1])));
                rp[j][1] = __builtin_amdgcn_ds_bpermute(ysrc, pk(add1(bflo1(yb), kd[j][2]), add1(bfhi1(yb), kd[j][3])));
            }
#pragma unroll
            for (int j = 0; j < 4; ++j)
                __builtin_amdgcn_raw_buffer_store_b128((u32x4){vmov1(tag32 ^ Y_TAG), rp[j][0], vmov1(tag32 ^ Y_TAG), rp[j][1]}, wsr, yso,
                                                       ((j == 0 ? 0 : j == 1 ? 3 : j == 2 ? 4 : 7) ^ par) * 1024, 16);
        }
        WAIT_LGKM0();
        if (lane == 0) __hip_atomic_fetch_add((LAS unsigned*)(sm + L_CTL + 8), 1u, __ATOMIC_RELAXED, __HIP_MEMORY_SCOPE_WORKGROUP);
#pragma unroll 1
        for (unsigned spins = 0; spins < (1u << 22); ++spins) {
            if (__builtin_amdgcn_readfirstlane(__hip_atomic_load((LAS unsigned*)(sm + L_CTL + 8), __ATOMIC_RELAXED, __HIP_MEMORY_SCOPE_WORKGROUP)) == 4u) break;
            __builtin_amdgcn_s_sleep(1);
        }
        asm volatile("" ::: "memory");
#pragma unroll
        for (int j = 0; j < 8; ++j) *(LAS u32x4*)(sm + latei + j * 1024 + lf * 16) = lw2[j];
        asm volatile("" ::: "memory");
        __hip_atomic_store((LAS unsigned*)(sm + L_CTL + 80 + wq * 4), 1u, __ATOMIC_RELAXED, __HIP_MEMORY_SCOPE_WORKGROUP);
        wait_lds(sm + L_CTL + 96 + wq * 4);
        u32x2 yv[8];
        u32x4 stv;
        const int yl = L_CONV + 32768 + wq * 4096;
        const unsigned yg = (unsigned)(((lf >> 2) & 7) * 8 + (lf >> 5)) * 8192u + (unsigned)((lf & 3) ^ (((lf >> 4) & 1) << 1)) * 16u;
        const u32x4 yvo = {yg, yg + (16384u - 1024u), yg + 2 * (16384u - 1024u), yg + 3 * (16384u - 1024u)};
        const char* ybase = p.ws + WS_Y + (unsigned)(64 * n64) * 8192u + (unsigned)(64 * m64 + 16 * wq) * 4u;
        const int yr = lb + yl + (lf & 7) * 64 + (((lf >> 3) * 8) ^ (((lf >> 2) & 1) << 5));
#pragma unroll 1
        for (unsigned spins = 0; spins < SPIN_CAP; ++spins) {
            glds16x4c(ybase, yvo, lb + yl);
            stv = __builtin_amdgcn_raw_buffer_load_b128(wsr, WS_STATS + (unsigned)srow * 8u, 0, 16);
            WAIT_VM(0);
#pragma unroll
            for (int i = 0; i < 8; ++i) yv[i] = *(LAS u32x2*)(lp(yr) + i * 512);
            unsigned df = (stv[0] ^ tag32) | (stv[2] ^ tag32);
#pragma unroll
            for (int i = 0; i < 8; ++i) df |= yv[i][0] ^ (tag32 ^ Y_TAG);
            if (__all(df == 0u)) break;
            __builtin_amdgcn_s_sleep(8);
        }
        __builtin_amdgcn_s_setprio(0);
        f32x2 sf[8], xf[8];
#pragma unroll
        for (int e = 0; e < 2; ++e) {
            const float nmb = -bflo(stv[1 + 2 * e]);
            float ri = __builtin_amdgcn_rcpf(bfhi(stv[1 + 2 * e]));
            asm volatile("s_nop 1" : "+v"(ri));
#pragma unroll
            for (int j = 0; j < 4; ++j) {
                const int i = 4 * e + j;
                const unsigned xd = xv[e][j];
                xf[i] = (f32x2){bflo1(xd), bfhi1(xd)};
                const unsigned tp = pk(add1(xf[i][0], nmb), add1(xf[i][1], nmb));
                const unsigned np = pk(mul1(bflo1(tp), ri), mul1(bfhi1(tp), ri));
                sf[i] = (f32x2){mul1(bflo1(np), e ? bfhi1(yv[2 * j][1]) : bflo1(yv[2 * j][1])), mul1(bfhi1(np), e ? bfhi1(yv[2 * j + 1][1]) : bflo1(yv[2 * j + 1][1]))};
                asm volatile("" : "+v"(sf[i]), "+v"(xf[i]));
            }
        }
        const int rm = lb + L_W2 + part((lf >> 2) & 1, wq >> 1, lf & 3) + (16 * (wq & 1) + 2 * (lf >> 3)) * 16;
        WAIT_BAR("lgkmcnt(0)");
        f32x4 o[4];
#pragma unroll
        for (int j = 0; j < 4; ++j) {
            LAS char* rp = lp(rm) + (j >> 1) * 16 + (j & 1) * 512;
            o[j] = (*(LAS f32x4*)rp + *(LAS f32x4*)(rp + 12288)) + (*(LAS f32x4*)(rp + 24576) + *(LAS f32x4*)(rp + 36864));
        }
        f32x2 so[8], ev[8];
#pragma unroll
        for (int i = 0; i < 8; ++i) {
            const f32x2 ov = {o[i >> 1][(i & 1) * 2], o[i >> 1][(i & 1) * 2 + 1]};
            const f32x2 t = ov * (f32x2){-1.44269504088896341f, -1.44269504088896341f};
            ev[i] = (f32x2){__builtin_amdgcn_exp2f(t[0]), __builtin_amdgcn_exp2f(t[1])};
            so[i] = sf[i] * ov;
        }
        SCHED();
        u32x4 st[2];
#pragma unroll
        for (int i = 0; i < 8; ++i) {
            const f32x2 dn = ev[i] + (f32x2){1.f, 1.f};
            const f32x2 r = __builtin_elementwise_fma(so[i], (f32x2){__builtin_amdgcn_rcpf(dn[0]), __builtin_amdgcn_rcpf(dn[1])}, xf[i]);
            st[i >> 2][i & 3] = pk(r[0], r[1]);
        }
        const __amdgpu_buffer_rsrc_t orr = __builtin_amdgcn_make_buffer_rsrc((void*)p.out, 0, S * D * 2, 0x00020000);
        __builtin_amdgcn_raw_buffer_store_b128(st[0], orr, xoff, 0, 16);
        __builtin_amdgcn_raw_buffer_store_b128(st[1], orr, xoff, D * 2, 16);
    }
}

extern "C" void kernel_launch(void* const* d_in, const int* in_sizes, int n_in,
                              void* d_out, int out_size, void* d_ws, size_t ws_size,
                              hipStream_t stream) {
    Params p;
    p.x = (const u16*)d_in[0]; p.k = (const u16*)d_in[1]; p.v = (const u16*)d_in[2];
    p.sk = (const u16*)d_in[3]; p.hk = (const u16*)d_in[4]; p.dv = (const u16*)d_in[5];
    p.w1 = (const u16*)d_in[7]; p.w3 = (const u16*)d_in[8]; p.w2 = (const u16*)d_in[9];
    p.out = (u16*)d_out; p.ws = (char*)d_ws;
    (void)hipFuncSetAttribute((const void*)fused_kernel, hipFuncAttributeMaxDynamicSharedMemorySize, L_TOTAL);
    fused_kernel<<<256, 512, L_TOTAL, stream>>>(p);
}
```

```cpp
#include <hip/hip_runtime.h>
#include <stdint.h>
#include <utility>

typedef unsigned short u16;
typedef unsigned long long u64;
typedef short s16x4 __attribute__((ext_vector_type(4)));
typedef short s16x8 __attribute__((ext_vector_type(8)));
typedef __bf16 bf16x2 __attribute__((ext_vector_type(2)));
typedef __bf16 bf16x8 __attribute__((ext_vector_type(8)));
typedef float f32x2 __attribute__((ext_vector_type(2)));
typedef float f32x4 __attribute__((ext_vector_type(4)));
typedef float f32x16 __attribute__((ext_vector_type(16)));
typedef unsigned u32x2 __attribute__((ext_vector_type(2)));
typedef unsigned u32x4 __attribute__((ext_vector_type(4)));
typedef unsigned u32x16 __attribute__((ext_vector_type(16)));
#define LAS __attribute__((address_space(3)))
#define GAS __attribute__((address_space(1)))
#define RLX_AGENT __ATOMIC_RELAXED, __HIP_MEMORY_SCOPE_AGENT

constexpr int S = 2048, D = 448, F = 1792;

constexpr unsigned WS_EPOCH = 0;
constexpr unsigned WS_FH = 65536;
constexpr unsigned WS_STATS = 8192;
constexpr unsigned WS_CSUM = 24576;
constexpr unsigned WS_U = 262144;
constexpr unsigned WS_Y = WS_U;
constexpr unsigned WS_H = WS_Y + D * S * 4;
constexpr unsigned WS_END = WS_H + 64 * 112 * 1024;
constexpr unsigned WS_DBG = WS_END;

constexpr int L_KV = 0;
constexpr int L_CTL = 0;
constexpr int L_CSUM = 256;
constexpr int L_STAT = 1280;
constexpr int L_RAW = 2048;
constexpr int L_KVO = 8192;
constexpr int L_UNX = 6144;
constexpr int L_SLOT = 49152;
constexpr int L_CONV = 12288;
constexpr int C_CH = 26112, C_HS = 4160, C_U = 16640, C_US = 4640;
constexpr int L_W2 = 16384 + L_SLOT;
constexpr int L_TOTAL = 163840;
__host__ __device__ constexpr int part(int db, int sb, int a) { return db * L_SLOT + sb * 4352 + a * 1024 + (a & 1) * 16 + (a >> 1) * 128; }
__host__ __device__ constexpr int ring(int stage) { return 16384 + (stage % 3) * L_SLOT; }
constexpr unsigned CODE_PREFETCH = 36864;
#define POLL_SLEEP 4
#define HDEPTH 6
#define ROT2 (8 * ml)
#define MPRIO 1
#define EPARK 0
#define YSLEEP 16
#define TOUCH 1
#define TOUCH0 4
#define HGROW 4
#define ROTM 6
#define KVW 0
#define MWARM 0
#define URETRY 4
#define KVNT " nt"
#define FLAGW 1
#define AUXW2 1
#define NTOUCH 1
#define YSTEP 18
#define ROT1 ((2 * ml + nl) % ROTM)

constexpr u64 TAG_SALT = 0x9E3779B97F4A7C15ull;
constexpr unsigned Y_TAG = 0x80000000u;
constexpr unsigned SPIN_CAP = 1u << 18;

struct Params {
    const u16* x; const u16* k; const u16* v; const u16* sk; const u16* hk; const u16* dv;
    const u16* w1; const u16* w3; const u16* w2; u16* out; char* ws;
};

__device__ __forceinline__ float bflo(unsigned p) { return __uint_as_float(p << 16); }
__device__ __forceinline__ float bfhi(unsigned p) { return __uint_as_float(p & 0xffff0000u); }
__device__ __forceinline__ float bflo1(unsigned p) { float r; asm("v_lshlrev_b32 %0, 16, %1" : "=v"(r) : "v"(p)); return r; }
__device__ __forceinline__ float bfhi1(unsigned p) { float r; asm("v_and_b32 %0, 0xffff0000, %1" : "=v"(r) : "v"(p)); return r; }
__device__ __forceinline__ float mul1(float a, float b) { float r; asm("v_mul_f32 %0, %1, %2" : "=v"(r) : "v"(a), "v"(b)); return r; }
__device__ __forceinline__ void fmac1(float& d, float a, float b) { asm("v_fmac_f32 %0, %1, %2" : "+v"(d) : "v"(a), "v"(b)); }
__device__ __forceinline__ float dpp_xor1(float v) {
    return __int_as_float(__builtin_amdgcn_update_dpp(0, __float_as_int(v), 0xB1, 0xf, 0xf, true));
}
__device__ __forceinline__ float add1(float a, float b) { float r; asm("v_add_f32 %0, %1, %2" : "=v"(r) : "v"(a), "v"(b)); return r; }
__device__ __forceinline__ unsigned vmov1(unsigned s) { unsigned r; asm volatile("v_mov_b32 %0, %1" : "=v"(r) : "s"(s)); return r; }
__device__ __forceinline__ unsigned pk(float a, float b) {
    f32x2 f = {a, b};
    return __builtin_bit_cast(unsigned, __builtin_convertvector(f, bf16x2));
}
__device__ __forceinline__ float rbf(float a) { return bflo(pk(a, a)); }
__device__ __forceinline__ float dot2(unsigned a, unsigned b, float c) {
    return __builtin_amdgcn_fdot2_f32_bf16(__builtin_bit_cast(bf16x2, a), __builtin_bit_cast(bf16x2, b), c, false);
}
__device__ __forceinline__ void glds16(const void* base, unsigned voff, unsigned ldst) {
    u64 t;
    asm volatile("s_mov_b32 m0, %3\n\ts_mov_b64 %0, %2\n\tglobal_load_lds_dwordx4 %1, %0" KVNT : "=&s"(t) : "v"(voff), "s"(base), "s"(ldst));
}
__device__ __forceinline__ void glds16x4(const void* base, u32x4 vo, unsigned ldst) {
    u64 t;
    asm volatile("s_mov_b32 m0, %6\n\ts_mov_b64 %0, %5\n\tglobal_load_lds_dwordx4 %1, %0\n\tglobal_load_lds_dwordx4 %2, %0 offset:1024\n\t"
                 "global_load_lds_dwordx4 %3, %0 offset:2048\n\tglobal_load_lds_dwordx4 %4, %0 offset:3072"
                 : "=&s"(t) : "v"(vo[0]), "v"(vo[1]), "v"(vo[2]), "v"(vo[3]), "s"(base), "s"(ldst));
}
__device__ __forceinline__ void glds16x4c(const void* base, u32x4 vo, unsigned ldst) {
    u64 t;
    asm volatile("s_mov_b32 m0, %6\n\ts_mov_b64 %0, %5\n\tglobal_load_lds_dwordx4 %1, %0 sc1\n\tglobal_load_lds_dwordx4 %2, %0 offset:1024 sc1\n\t"
                 "global_load_lds_dwordx4 %3, %0 offset:2048 sc1\n\tglobal_load_lds_dwordx4 %4, %0 offset:3072 sc1"
                 : "=&s"(t) : "v"(vo[0]), "v"(vo[1]), "v"(vo[2]), "v"(vo[3]), "s"(base), "s"(ldst) : "memory");
}
__device__ __forceinline__ void glds16x2c(const void* base, unsigned voff, unsigned ldst) {
    u64 t;
    asm volatile("s_mov_b32 m0, %3\n\ts_mov_b64 %0, %2\n\tglobal_load_lds_dwordx4 %1, %0 sc1\n\tglobal_load_lds_dwordx4 %1, %0 offset:1024 sc1"
                 : "=&s"(t) : "v"(voff), "s"(base), "s"(ldst));
}
__device__ __forceinline__ void glds16c(const void* base, unsigned voff, unsigned ldst) {
    u64 t;
    asm volatile("s_mov_b32 m0, %3\n\ts_mov_b64 %0, %2\n\tglobal_load_lds_dwordx4 %1, %0 sc1" : "=&s"(t) : "v"(voff), "s"(base), "s"(ldst));
}
__device__ __forceinline__ void glds4c(const void* base, unsigned voff, unsigned ldst) {
    u64 t;
    asm volatile("s_mov_b32 m0, %3\n\ts_mov_b64 %0, %2\n\tglobal_load_lds_dword %1, %0 sc1" : "=&s"(t) : "v"(voff), "s"(base), "s"(ldst));
}
__device__ __forceinline__ const u16* sptr(const u16* base, int off) {
    const u16* r = base + __builtin_amdgcn_readfirstlane(off);
    asm("" : "+s"(r));
    return r;
}
__device__ __forceinline__ u64 vsel(bool c, const void* a, u64 b) {
    u64 va = (u64)a;
    asm("" : "+v"(va), "+v"(b));
    return c ? va : b;
}
__device__ __forceinline__ LAS char* lp(int a) { return (LAS char*)(size_t)(unsigned)a; }
__device__ __forceinline__ u32x4 lds128(LAS char* p) { return *(LAS u32x4*)p; }
__device__ __forceinline__ u32x2 ldstr(LAS char* p) {
    return __builtin_bit_cast(u32x2, __builtin_amdgcn_ds_read_tr16_b64_v4i16((LAS s16x4*)p));
}
__device__ __forceinline__ u32x4 ldstr2(LAS char* p, int off) {
    u32x2 a = ldstr(p), b = ldstr(p + off);
    return (u32x4){a.x, a.y, b.x, b.y};
}
__device__ __forceinline__ f32x16 mfma32(u32x4 a, u32x4 b, f32x16 c) {
    return __builtin_amdgcn_mfma_f32_32x32x16_bf16(__builtin_bit_cast(bf16x8, a), __builtin_bit_cast(bf16x8, b), c, 0, 0, 0);
}
__device__ __forceinline__ f32x4 mfma16(u32x4 a, u32x4 b, f32x4 c) {
    return __builtin_amdgcn_mfma_f32_16x16x32_bf16(__builtin_bit_cast(bf16x8, a), __builtin_bit_cast(bf16x8, b), c, 0, 0, 0);
}
__device__ __forceinline__ float silu_f(float g) {
    return g * __builtin_amdgcn_rcpf(1.f + __builtin_amdgcn_exp2f(g * -1.44269504088896341f));
}
template <class Fn, int... I>
__device__ __forceinline__ void static_for(Fn&& fn, std::integer_sequence<int, I...>) { (fn(std::integral_constant<int, I>{}), ...); }
#define WAIT_VM(n) asm volatile("s_waitcnt vmcnt(" #n ")" ::: "memory")
#define WAIT_LGKM0() asm volatile("s_waitcnt lgkmcnt(0)" ::: "memory")
#define WAIT_BAR(w) asm volatile("s_waitcnt " w "\n\ts_barrier" ::: "memory")
#define SCHED() __builtin_amdgcn_sched_barrier(0)
#define CAS __attribute__((address_space(4)))
#define NSTAMP 18

__device__ __forceinline__ void wait_lds(LAS char* word) {
#pragma unroll 1
    for (unsigned spins = 0; spins < (SPIN_CAP << 4); ++spins) {
        if (__builtin_amdgcn_readfirstlane(__hip_atomic_load((LAS unsigned*)word, __ATOMIC_RELAXED, __HIP_MEMORY_SCOPE_WORKGROUP))) break;
        __builtin_amdgcn_s_sleep(1);
    }
    asm volatile("" ::: "memory");
}
__device__ __forceinline__ void wait_lds_eq(LAS char* word, unsigned want) {
#pragma unroll 1
    for (unsigned spins = 0; spins < (1u << 22); ++spins) {
        if ((unsigned)__builtin_amdgcn_readfirstlane(__hip_atomic_load((LAS unsigned*)word, __ATOMIC_RELAXED, __HIP_MEMORY_SCOPE_WORKGROUP)) == want) break;
        __builtin_amdgcn_s_sleep(1);
    }
    asm volatile("" ::: "memory");
}
#define STAMP(i)
#define STAMPG(i)
#define STAMPA(i)
#define STAMP_DECL()
#define STAMP_OUT(n)

__global__ __launch_bounds__(512, 2) void fused_kernel(Params p) {
    extern __shared__ __attribute__((aligned(16))) char smem_raw[];
    u64 pc;
    asm volatile("s_getpc_b64 %0" : "=s"(pc));
    LAS char* sm = (LAS char*)smem_raw;
    const int tid = threadIdx.x, lane = tid & 63;
    const int wave = __builtin_amdgcn_readfirstlane(tid >> 6);
    const int xcd = blockIdx.x & 7, idx = blockIdx.x >> 3;
    if (idx == 0 && wave < 5) {
        const unsigned o = (unsigned)tid * 128u;
        asm volatile("s_mov_b32 m0, %2\n\ts_nop 0\n\tglobal_load_lds_dword %0, %1" :: "v"(o < CODE_PREFETCH ? o : 0u), "s"(pc & ~(u64)255),
                     "s"((unsigned)(size_t)sm + 32768u));
    }
    SCHED();
    const __amdgpu_buffer_rsrc_t wsr = __builtin_amdgcn_make_buffer_rsrc(p.ws, 0, WS_END, 0x00020000);
    asm volatile("" :: "s"(p.x), "s"(p.k), "s"(p.v), "s"(p.sk), "s"(p.hk), "s"(p.dv), "s"(p.w1), "s"(p.w3), "s"(p.w2), "s"(p.out), "s"(p.ws));
    STAMP_DECL();
    STAMP(0);

    if (idx < 4) {
        const int ca = (xcd >> 1) * 4 + idx, cmat = ca >= 7, cnt = 7 * (xcd & 1) + (ca >= 7 ? ca - 7 : ca);
        const bool job = ca < 14;
        u32x4 xr[7], wr[14];
        const int row = (8 * (xcd >> 1) + 4 * (xcd & 1) + idx) * 64 + wave * 8 + (lane >> 3);
#pragma unroll
        for (int i = 0; i < 7; ++i) xr[i] = *(const u32x4*)(p.x + (size_t)row * D + i * 64 + (lane & 7) * 8);
        const u64 epoch = __hip_atomic_load((GAS u64*)(p.ws + WS_EPOCH), RLX_AGENT);
        const u64 tag = epoch ^ TAG_SALT;
        const unsigned tag32 = (unsigned)tag;
        unsigned stat;
        {
            float s4[4] = {0.f, 0.f, 0.f, 0.f};
#pragma unroll
            for (int i = 0; i < 7; ++i)
#pragma unroll
                for (int j = 0; j < 4; ++j) s4[j] = dot2(xr[i][j], 0x3f803f80u, s4[j]);
            float sum = (s4[0] + s4[1]) + (s4[2] + s4[3]);
            sum += __shfl_xor(sum, 1); sum += __shfl_xor(sum, 2); sum += __shfl_xor(sum, 4);
            const float mean = sum * (1.f / D);
            f32x2 q4[4] = {{0.f, 0.f}, {0.f, 0.f}, {0.f, 0.f}, {0.f, 0.f}};
#pragma unroll
            for (int i = 0; i < 7; ++i)
#pragma unroll
                for (int j = 0; j < 4; ++j) {
                    const f32x2 d = (f32x2){bflo(xr[i][j]), bfhi(xr[i][j])} - (f32x2){mean, mean};
                    q4[j] = __builtin_elementwise_fma(d, d, q4[j]);
                }
            const f32x2 q2 = (q4[0] + q4[1]) + (q4[2] + q4[3]);
            float sq = q2[0] + q2[1];
            sq += __shfl_xor(sq, 1); sq += __shfl_xor(sq, 2); sq += __shfl_xor(sq, 4);
            stat = pk(mean, __builtin_amdgcn_sqrtf(rbf(rbf(sq * (1.f / D)) + 1e-5f)));
            asm volatile("" : "+v"(stat));
        }
        STAMPA(2);
        if (job) {
            SCHED();
            const u16* wp = (cmat ? p.w3 : p.w1) + (size_t)(wave * 4 + (lane >> 4)) * F + cnt * 128 + (lane & 15) * 8;
#pragma unroll
            for (int i = 0; i < 14; ++i) wr[i] = *(const u32x4*)(wp + (size_t)(32 * i) * F);
            SCHED();
        }
        __builtin_amdgcn_raw_buffer_store_b64((u32x2){tag32, stat}, wsr, (lane & 7) == 0 ? WS_STATS + (unsigned)row * 8u : 0xfffffff0u, 0, 16);
        if (job) {
            float c[8];
#pragma unroll
            for (int j = 0; j < 8; ++j) c[j] = 0.f;
#pragma unroll
            for (int i = 0; i < 14; ++i)
#pragma unroll
                for (int j = 0; j < 4; ++j) { c[2 * j] += bflo(wr[i][j]); c[2 * j + 1] += bfhi(wr[i][j]); }
            LAS char* cp = sm + ((wave * 4 + (lane >> 4)) * 136 + (lane & 15) * 8) * 4;
            *(LAS f32x4*)cp = (f32x4){c[0], c[1], c[2], c[3]};
            *(LAS f32x4*)(cp + 16) = (f32x4){c[4], c[5], c[6], c[7]};
        }
        STAMPA(3);
        if (job) {
            WAIT_BAR("lgkmcnt(0)");
            float t = 0.f;
#pragma unroll
            for (int i = 0; i < 8; ++i) t += *(LAS float*)(sm + ((i * 4 + (tid & 3)) * 136 + (tid >> 2)) * 4);
            t += __shfl_xor(t, 1); t += __shfl_xor(t, 2);
            __builtin_amdgcn_raw_buffer_store_b64((u32x2){tag32, __float_as_uint(t)}, wsr,
                (tid & 3) == 0 ? WS_CSUM + (unsigned)((cnt * 2 + cmat) * 128 + (tid >> 2)) * 8u : 0xfffffff0u, 0, 16);
        }
        STAMP(1);
        SCHED();
        if (!job) __builtin_amdgcn_s_sleep(AUXW2 ? 40 : 80);
        const unsigned pf2 = *(const unsigned*)((const char*)p.hk + (size_t)((xcd * 4 + idx) * 448 + wave * 56 + (lane < 56 ? lane : 0)) * 128);
        if (AUXW2) {
            const unsigned fo = lane * 16u;
            constexpr int PER = ((4 - NTOUCH) * 14336 + 4095) / 4096;
#pragma unroll 1
            for (int i = wave; i < 7 * PER; i += 8) {
                const int g = i / PER, j = i - g * PER;
                int o = NTOUCH * 14336 + j * 4096; o = o > 57344 - 4096 ? 57344 - 4096 : o;
                glds16x4((const char*)p.w2 + (size_t)idx * 401408 + g * 57344 + o, (u32x4){fo, fo, fo, fo}, (unsigned)(size_t)sm + 65536u + wave * 4096);
            }
        }
        asm volatile("" :: "v"(pf2));
        STAMP_OUT(4);
        if (blockIdx.x == 0 && wave == 7) {
            GAS u64* gp = (GAS u64*)(p.ws + WS_STATS) + lane * 8;
            GAS u64* fp[4];
#pragma unroll
            for (int m = 0; m < 4; ++m) {
                const int g = m * 56 + (lane < 56 ? lane : 0);
                fp[m] = (GAS u64*)(p.ws + WS_U + (unsigned)((g >> 5) * 64 + 8) * 8192u + (unsigned)(1984 - (g & 31) * 64) * 2u);
            }
#pragma unroll 1
            for (unsigned spins = 0; spins < SPIN_CAP; ++spins) {
                __builtin_amdgcn_s_sleep(32);
                bool ok = true;
#pragma unroll
                for (int m = 0; m < 4; ++m)
                    ok &= ((unsigned)__hip_atomic_load(gp + m * 512, RLX_AGENT) == tag32) &
                          ((((unsigned)__hip_atomic_load(fp[m], RLX_AGENT) ^ tag32) & ~Y_TAG) == 0u);
                if (__all(ok)) break;
            }
            if (lane == 0) __hip_atomic_store((GAS u64*)(p.ws + WS_EPOCH), epoch + 1, RLX_AGENT);
        }
        return;
    }

    const int gi = idx - 4, ml = gi & 3, nl = gi >> 2;
    const int mt = 4 * (xcd >> 1) + ml, nt = 7 * (xcd & 1) + nl;
    const int rot1 = ROT1;
    const int m64 = 4 * xcd + ml, n64 = nl;
    const int gpair = xcd * 28 + gi;
    const int r32 = lane & 31, hh = lane >> 5, blk = (lane >> 4) & 1, q4 = (lane & 15) >> 2, p4 = lane & 3;
    const unsigned lb = (unsigned)(size_t)sm;
    const int wq = wave & 3;
    u32x4 wvo;
    wvo[0] = (unsigned)((lane >> 4) * F + (((lane & 15) ^ ((lane >> 4) << 2)) * 8)) * 2u;
#pragma unroll
    for (int j = 1; j < 4; ++j) wvo[j] = wvo[0] + j * (8 * F - 1024);
    const u16* w1b = sptr(p.w1, wq * 16 * F + nt * 128);
    const u16* w3b = sptr(p.w3, wq * 16 * F + nt * 128);
    auto kst = [&](int si) { if (ROTM == 6 && si == 0) return 0; const int s = si + rot1; return s >= 7 ? s - ROTM : s; };
    auto group_w = [&](int si, int m) { glds16x4(sptr(m ? w3b : w1b, kst(si) * 64 * F), wvo, lb + ring(si) + 16384 + m * 16384 + wq * 4096); };
    const int kr0 = (gpair & 31) * 64, kc0 = (gpair >> 5) * 64;
    const int chn = wq >> 1, par = wq & 1, dch = 2 * gpair + chn;
    if (wave < 4) group_w(0, 0);
    {
        const bool isk = lane >= 8 && lane < 14;
        u64 mb_ = vsel(lane == 15, p.dv, vsel(lane == 14, p.hk, vsel(isk, p.k, (u64)p.sk)));
        unsigned mo = (unsigned)((kc0 + wq * 16) * 8 + (lane & 7) * 16);
        mo = isk ? (unsigned)((kr0 ? kr0 - 3 + ((lane - 8) >> 1) : 0) * D + kc0 + wq * 16 + (lane & 1) * 8) * 2u : mo;
        mo = lane == 14 ? (unsigned)dch * 4096u : mo;
        mo = lane == 15 ? (unsigned)(dch & ~7) * 2u : mo;
        const u64 ma = mb_ + mo;
        asm volatile("s_mov_b32 m0, %1\n\ts_nop 0\n\tglobal_load_lds_dwordx4 %0, off" :: "v"(ma), "s"(lb + wave * 1024));
    }
    if (wave < 4) group_w(0, 1);
    SCHED();
    const int rot2 = ROT2;
    const int w2i = L_W2 + wq * 12288;
    auto fresh_lane = [&]() { int l = lane; asm volatile("" : "+v"(l)); return l; };
    const int latei = L_CONV + wq * 8192;
    auto pair_w2 = [&](u32x4 vo, int i, int dst) {
        int t = i + rot2; t = t >= 28 ? t - 28 : t;
        glds16x4(sptr(p.w2, (wq * 448 + t * 16) * D + n64 * 64), vo, lb + dst);
    };
    auto w2_off = [&]() {
        const int l = fresh_lane();
        const unsigned o = (unsigned)((l >> 3) * D + (((l & 7) ^ (((l >> 4) & 1) << 2)) * 8)) * 2u;
        return (u32x4){o, o + (16 * D - 1024), o + 2 * (16 * D - 1024), o + 3 * (16 * D - 1024)};
    };

    if (wave < 4) {
        __builtin_amdgcn_s_setprio(MPRIO);
        const int wm = wave >> 1, wn = wave & 1;
        int xa[4], wa0, wa1;
#pragma unroll
        for (int i = 0; i < 4; ++i) xa[i] = lb + ring(0) + (((64 * wm + r32) * 128 + ((hh ^ ((r32 >> 1) & 7)) * 16)) ^ (i * 32));
        wa0 = 16384 + (8 * hh + q4) * 256 + (((8 * wn + 2 * blk + (p4 >> 1)) ^ (q4 << 2)) * 16) + 8 * (p4 & 1);
        wa1 = (wa0 ^ 64) + lb + ring(0); wa0 += lb + ring(0);
        f32x16 acc[2][2][2];
#pragma unroll
        for (int i = 0; i < 8; ++i)
#pragma unroll
            for (int r = 0; r < 16; ++r) acc[i >> 2][(i >> 1) & 1][i & 1][r] = 0.f;
        u32x4 fx[2][2], fw[2][2][2];
        auto load_frag = [&](int g, int j) {
            const int pp = g & 3, b = g & 1;
            if (j == 0) {
                if (g >= 4) {
                    const int d = ring(g >> 2) - ring((g >> 2) - 1);
                    if (pp == 0) { wa0 += d; wa1 += d; asm volatile("" : "+v"(wa0), "+v"(wa1)); }
                    xa[pp] += d; asm volatile("" : "+v"(xa[pp]));
                }
                fw[b][0][0] = ldstr2(lp(wa0) + pp * 4096, 1024);
            }
            if (j == 1) { fx[b][0] = lds128(lp(xa[pp])); fx[b][1] = lds128(lp(xa[pp]) + 4096); }
            if (j == 2) fw[b][0][1] = ldstr2(lp(wa0) + pp * 4096 + 16384, 1024);
            if (j == 3) fw[b][1][0] = ldstr2(lp(wa1) + pp * 4096, 1024);
            if (j == 4) fw[b][1][1] = ldstr2(lp(wa1) + pp * 4096 + 16384, 1024);
        };
        int ka0[2], kam[2], kwo;
#pragma unroll
        for (int rd = 0; rd < 2; ++rd) {
            const int ck = 2 * wq + rd;
            const int bq = 2 * (lane >> 4) + (p4 >> 1), bm = bq ? bq - 1 : 0;
            ka0[rd] = lb + L_KV + (8 * bq + q4) * 128 + ((ck ^ (((q4 >> 1) & 1) | ((bq & 3) << 1))) * 16) + 8 * (p4 & 1);
            kam[rd] = lb + L_KV + (8 * bm + 4 + q4) * 128 + ((ck ^ (((q4 >> 1) & 1) | ((bm & 3) << 1))) * 16) + 8 * (p4 & 1);
        }
        kwo = lb + L_KVO + wq * 2048 + (lane & 7) * 128 + (((7 - (lane >> 3)) ^ (lane & 7)) * 16);
        asm volatile("" : "+v"(ka0[0]), "+v"(ka0[1]), "+v"(kam[0]), "+v"(kam[1]), "+v"(kwo));
        WAIT_VM(4);
        u32x2 skv[2], hal[2];
#pragma unroll
        for (int rd = 0; rd < 2; ++rd) {
            LAS char* mp = sm + wave * 1024;
            skv[rd] = *(LAS u32x2*)(mp + (rd * 8 + (lane & 7)) * 8);
            const unsigned hm = kr0 ? 0xffffffffu : 0u;
            const unsigned a = *(LAS u16*)(mp + 128 + rd * 16 + (lane & 7) * 2), b = *(LAS u16*)(mp + 160 + rd * 16 + (lane & 7) * 2),
                           c = *(LAS u16*)(mp + 192 + rd * 16 + (lane & 7) * 2);
            hal[rd] = (u32x2){(a << 16) & hm, (b | (c << 16)) & hm};
            asm volatile("" : "+v"(skv[rd]), "+v"(hal[rd]));
        }
        WAIT_BAR("vmcnt(0) lgkmcnt(0)");
        STAMP(1);
        u32x2 kt[2][3], vt[2][2];
        float cf[4], kk[11], ko[8];
        u32x4 kpk;
        auto fill = [&](auto kc) {
            constexpr int k = decltype(kc)::value;
            if constexpr (k < 4) {
                constexpr int rd = k >> 1;
                if constexpr ((k & 1) == 0) { kt[rd][0] = ldstr(lp(kam[rd])); kt[rd][1] = ldstr(lp(ka0[rd])); kt[rd][2] = ldstr(lp(ka0[rd]) + 512); }
                else { vt[rd][0] = ldstr(lp(ka0[rd]) + 8192); vt[rd][1] = ldstr(lp(ka0[rd]) + 8192 + 512); }
            } else if constexpr (k < 4 + 2 * 16) {
                constexpr int rd = (k - 4) / 16, s = (k - 4) % 16;
                if constexpr (s == 0) { cf[0] = bflo1(skv[rd][0]); cf[1] = bfhi1(skv[rd][0]); cf[2] = bflo1(skv[rd][1]); cf[3] = bfhi1(skv[rd][1]); }
                else if constexpr (s == 1) {
                    const unsigned a = lane < 8 ? hal[rd][0] : kt[rd][0][0], b = lane < 8 ? hal[rd][1] : kt[rd][0][1];
                    kk[0] = bfhi1(a); kk[1] = bflo1(b); kk[2] = bfhi1(b);
                } else if constexpr (s < 4) {
                    const u32x2 t = kt[rd][s - 1];
                    kk[4 * s - 5] = bflo1(t[0]); kk[4 * s - 4] = bfhi1(t[0]); kk[4 * s - 3] = bflo1(t[1]); kk[4 * s - 2] = bfhi1(t[1]);
                } else if constexpr (s < 12) {
                    constexpr int tp = (s - 4) >> 1, o0 = ((s - 4) & 1) * 4;
#pragma unroll
                    for (int i = o0; i < o0 + 4; ++i) {
                        if (tp == 0) ko[i] = mul1(kk[i], cf[3]);
                        else fmac1(ko[i], kk[i + tp], cf[3 - tp]);
                    }
                } else {
                    constexpr int j = s - 12;
                    const unsigned v = vt[rd][j >> 1][j & 1];
                    kpk[3 - j] = pk(mul1(ko[2 * j + 1], bfhi1(v)), mul1(ko[2 * j], bflo1(v)));
                    if constexpr (j == 3) *(LAS u32x4*)(lp(kwo) + rd * 1024) = kpk;
                }
            }
        };
#pragma unroll
        for (int j = 0; j < 5; ++j) load_frag(0, j);
        f32x4 cs[2][2][4];
        unsigned tch[4] = {0u, 0u, 0u, 0u}, tch0 = 0;
        unsigned tc0 = (unsigned)((gpair * 4 + wq) * 14 + (lane < 14 ? lane : 13)) * 128u;
        unsigned tco = (unsigned)(gi * 4 + wq + (lane < 28 ? lane : 27) * 448) * 128u;
        asm volatile("" : "+v"(tco));
        static_for([&](auto gc) {
            constexpr int g = decltype(gc)::value, b = g & 1, pp = g & 3;
            SCHED();
            if (g == 11) STAMP(2);
            if (pp == 3 && g < 27) { STAMPG(2 + 2 * (g >> 2)); WAIT_BAR("lgkmcnt(0)"); STAMPG(3 + 2 * (g >> 2)); }
            if (g == 27) {
                WAIT_LGKM0();
                if (lane == 0) __hip_atomic_fetch_add((LAS unsigned*)(sm + L_CTL + 12), 1u, __ATOMIC_RELAXED, __HIP_MEMORY_SCOPE_WORKGROUP);
            }
            if (g == 11) STAMP(3);
            static_for([&](auto ic) {
                constexpr int i = decltype(ic)::value, ft = i >> 2, m = (i >> 1) & 1, sb = i & 1;
                acc[sb][ft][m] = mfma32(fw[b][ft][m], fx[b][sb], acc[sb][ft][m]);
                SCHED();
                if (g < 27 && i < 5) load_frag(g + 1, i);
                if constexpr (MWARM && i == 6 && g == MWARM - 1) {
                    u64 t;
                    asm volatile("s_mov_b64 %1, %3\n\tglobal_load_dword %0, %2, %1" : "=v"(tch0), "=&s"(t) : "v"(tc0), "s"(p.w2));
                }
                if constexpr (TOUCH && i == 4 && pp == 2 && g >= TOUCH0 && g < TOUCH0 + 4 * NTOUCH) {
                    u64 t;
                    asm volatile("s_mov_b64 %1, %3\n\tglobal_load_dword %0, %2, %1" : "=v"(tch[(g - TOUCH0) >> 2]), "=&s"(t)
                                 : "v"(tco), "s"((const char*)p.w2 + ((g - TOUCH0) >> 2) * (112 * 128)));
                }
                if constexpr (i >= 5 && g >= 7 + 4 * KVW) fill(std::integral_constant<int, (g - 7 - 4 * KVW) * 3 + i - 5>{});
                SCHED();
            }, std::make_integer_sequence<int, 8>{});
        }, std::make_integer_sequence<int, 28>{});
        STAMP(4);
        wait_lds_eq(sm + L_CTL + 64, 3u);
        unsigned sa[2];
#pragma unroll
        for (int i = 0; i < 8; ++i) cs[i >> 2][0][i & 3] = *(LAS f32x4*)(sm + L_CSUM + (64 * wn + 32 * (i >> 2) + 8 * (i & 3) + 4 * hh) * 4);
        sa[0] = *(LAS unsigned*)(sm + L_STAT + (64 * wm + r32) * 4);
        sa[1] = *(LAS unsigned*)(sm + L_STAT + (64 * wm + 32 + r32) * 4);
#pragma unroll
        for (int i = 0; i < 8; ++i) cs[i >> 2][1][i & 3] = *(LAS f32x4*)(sm + L_CSUM + 512 + (64 * wn + 32 * (i >> 2) + 8 * (i & 3) + 4 * hh) * 4);
        const int lw = fresh_lane();
        const int w2a0 = lb + w2i + (4 * (lw >> 5) + ((lw & 15) >> 2)) * 128 + (((2 * ((lw >> 4) & 1) + ((lw & 3) >> 1)) ^ (((lw >> 3) & 1) << 2)) * 16) + 8 * (lw & 1);
        const int w2a1 = w2a0 ^ 64;
        u32x4 w2r[12][2];
        auto park = [&](int i) {
            w2r[i][0] = ldstr2(lp(w2a0) + (i / 6) * L_SLOT + (i % 6) * 2048, 1024);
            w2r[i][1] = ldstr2(lp(w2a1) + (i / 6) * L_SLOT + (i % 6) * 2048, 1024);
        };
#pragma unroll
        for (int sb = 0; sb < 2; ++sb) {
            const float ri = __builtin_amdgcn_rcpf(bfhi(sa[sb])) * -1.44269504088896341f, nm = -bflo(sa[sb]), d2 = bfhi(sa[sb]) * bfhi(sa[sb]);
            const f32x2 ri2 = {ri, ri}, nm2 = {nm, nm}, dd2 = {d2, d2};
#pragma unroll
            for (int ft = 0; ft < 2; ++ft) {
#pragma unroll
                for (int t2 = 0; t2 < 2; ++t2) {
                    const int grp = sb * 4 + ft * 2 + t2;
                    if (EPARK && grp == 2) { SCHED(); wait_lds(sm + L_CTL + 48 + wq * 4); }
                    if (EPARK && grp >= 2) { SCHED(); park(grp - 2); }
                    f32x2 gu[4], ee[4];
#pragma unroll
                    for (int j = 0; j < 4; ++j) {
                        const int r = 8 * t2 + 2 * j;
                        const f32x2 g = __builtin_elementwise_fma(nm2, (f32x2){cs[ft][0][r >> 2][r & 3], cs[ft][0][r >> 2][(r & 3) + 1]}, (f32x2){acc[sb][ft][0][r], acc[sb][ft][0][r + 1]});
                        const f32x2 u = __builtin_elementwise_fma(nm2, (f32x2){cs[ft][1][r >> 2][r & 3], cs[ft][1][r >> 2][(r & 3) + 1]}, (f32x2){acc[sb][ft][1][r], acc[sb][ft][1][r + 1]});
                        const f32x2 t = g * ri2;
                        ee[j] = (f32x2){__builtin_amdgcn_exp2f(t[0]), __builtin_amdgcn_exp2f(t[1])};
                        gu[j] = g * u;
                    }
                    u32x4 st;
#pragma unroll
                    for (int j = 0; j < 4; ++j) {
                        const f32x2 dn = __builtin_elementwise_fma(ee[j], dd2, dd2);
                        const f32x2 hv = gu[j] * (f32x2){__builtin_amdgcn_rcpf(dn[0]), __builtin_amdgcn_rcpf(dn[1])};
                        st[j] = pk(hv[0], hv[1]);
                    }
                    const unsigned fi = (unsigned)((4 * mt + 2 * wm + sb) * 112 + 8 * nt + 4 * wn + 2 * ft + t2);
                    __builtin_amdgcn_raw_buffer_store_b128(st, wsr, WS_H + fi * 1024u + lane * 16u, 0, 16);
                }
            }
        }
        SCHED();
        STAMP(5);
        const u32x4 w2o = w2_off();
        const u64 tag = *(LAS u64*)(sm + L_CTL + 128);
        GAS u64* myflag = (GAS u64*)(p.ws + WS_FH + ((2 * mt + wm) * 28 + 2 * nt + wn) * 128) + (lw & 15);
        if (!__builtin_amdgcn_readfirstlane(__hip_atomic_load((LAS unsigned*)(sm + L_CTL + 112 + wq * 4), __ATOMIC_RELAXED, __HIP_MEMORY_SCOPE_WORKGROUP))) {
            WAIT_VM(0);
            if (lane < FLAGW) __hip_atomic_store(myflag, tag, RLX_AGENT);
            wait_lds(sm + L_CTL + 112 + wq * 4);
        }
        asm volatile("" ::: "memory");
#pragma unroll
        for (int i = 6; i < 12; ++i) park(i);
        WAIT_VM(0);
        if (TOUCH) asm volatile("" :: "v"(tch[0]), "v"(tch[1]), "v"(tch[2]), "v"(tch[3]));
        asm volatile("" :: "v"(tch0));
        if (lane < FLAGW) __hip_atomic_store(myflag, tag, RLX_AGENT);
        STAMP(6);
        if (!EPARK) {
#pragma unroll
            for (int i = 0; i < 6; ++i) park(i);
        }
        asm volatile("" ::: "memory");
        __hip_atomic_store((LAS unsigned*)(sm + L_CTL + 32 + wq * 4), 1u, __ATOMIC_RELAXED, __HIP_MEMORY_SCOPE_WORKGROUP);
        WAIT_LGKM0();
#pragma unroll
        for (int i = 0; i < 12; ++i) asm volatile("" : "+v"(w2r[i][0]), "+v"(w2r[i][1]));
#pragma unroll
        for (int i = 0; i < 3; ++i) pair_w2(w2o, 12 + 2 * i, w2i + i * 4096);
#pragma unroll
        for (int i = 0; i < 3; ++i) pair_w2(w2o, 18 + 2 * i, w2i + L_SLOT + i * 4096);
        STAMP(7);
        {
            GAS u64* fp = (GAS u64*)(p.ws + WS_FH + (m64 * 28 + 7 * wq + (lw < 7 ? lw : 0)) * 128);
#pragma unroll 1
            for (unsigned spins = 0; spins < SPIN_CAP; ++spins) {
                if (__all(__hip_atomic_load(fp, RLX_AGENT) == tag)) break;
                __builtin_amdgcn_s_sleep(POLL_SLEEP);
            }
            __builtin_amdgcn_fence(__ATOMIC_ACQUIRE, "wavefront");
        }
        STAMP(11);
        const unsigned hb = WS_H + (unsigned)(2 * m64 * 112 + 28 * wq) * 1024u + lw * 16u;
        u32x4 hf[28][2];
        auto load_h = [&](int i, int j) {
            int t = i + rot2; t = t >= 28 ? t - 28 : t;
            hf[i][j] = __builtin_amdgcn_raw_buffer_load_b128(wsr, hb + j * (112u * 1024u), t * 1024, 16);
        };
        constexpr int HD = HDEPTH, HG = HGROW;
#pragma unroll
        for (int i = 0; i < HD; ++i) { load_h(i, 0); load_h(i, 1); }
        STAMP(14);
        f32x16 ac2[2][2];
#pragma unroll
        for (int i = 0; i < 4; ++i)
#pragma unroll
            for (int r = 0; r < 16; ++r) ac2[i >> 1][i & 1][r] = 0.f;
        u32x4 a0 = w2r[0][0], a1 = w2r[0][1];
        static_for([&](auto ic) {
            constexpr int i = decltype(ic)::value, n = i + 1;
            SCHED();
            if (YSTEP && i == YSTEP) __hip_atomic_store((LAS unsigned*)(sm + L_CTL + 96 + wq * 4), 1u, __ATOMIC_RELAXED, __HIP_MEMORY_SCOPE_WORKGROUP);
            if (i == 23) wait_lds(sm + L_CTL + 80 + wq * 4);
            u32x4 b0, b1;
            if constexpr (n < 12) { b0 = w2r[n][0]; b1 = w2r[n][1]; }
            else if constexpr (n < 28) {
                constexpr int so = n < 24 ? ((n - 12) / 6) * L_SLOT + ((n - 12) % 6) * 2048 : (n - 24) * 2048;
                const int lo = n < 24 ? 0 : latei - w2i;
                b0 = ldstr2(lp(w2a0 + lo) + so, 1024);
                b1 = ldstr2(lp(w2a1 + lo) + so, 1024);
            }
            SCHED();
            ac2[0][0] = mfma32(a0, hf[i][0], ac2[0][0]);
            SCHED();
            constexpr int l0 = i < HG ? HD + 2 * i : HD + HG + i;
            if constexpr (l0 < 28) load_h(l0, 0);
            SCHED();
            ac2[1][0] = mfma32(a1, hf[i][0], ac2[1][0]);
            SCHED();
            if constexpr (l0 < 28) load_h(l0, 1);
            SCHED();
            ac2[0][1] = mfma32(a0, hf[i][1], ac2[0][1]);
            SCHED();
            if constexpr (i < HG) load_h(l0 + 1, 0);
            SCHED();
            ac2[1][1] = mfma32(a1, hf[i][1], ac2[1][1]);
            SCHED();
            if constexpr (i < HG) load_h(l0 + 1, 1);
            a0 = b0; a1 = b1;
        }, std::make_integer_sequence<int, 28>{});
        SCHED();
        STAMP(15);
        WAIT_LGKM0();
#pragma unroll
        for (int v = 0; v < 16; ++v) {
            const f32x16& t = ac2[v >> 3][(v >> 2) & 1];
            const int r0 = 4 * (v & 3);
            *(LAS f32x4*)(sm + w2i + part(v >> 3, (v >> 2) & 1, v & 3) + lw * 16) = (f32x4){t[r0], t[r0 + 1], t[r0 + 2], t[r0 + 3]};
        }
        WAIT_BAR("lgkmcnt(0)");
        STAMP(16);
        STAMP(17);
        STAMP_OUT(18);
    } else {
        u32x4 xvo;
#pragma unroll
        for (int j = 0; j < 4; ++j) xvo[j] = (unsigned)((8 * j + (lane >> 3)) * D + ((((lane & 7) ^ (lane >> 4)) ^ (4 * (j & 1))) * 8)) * 2u - 1024u * j;
        const u16* xb = sptr(p.x, (mt * 128 + wq * 32) * D);
        auto group_x = [&](int si) { glds16x4(sptr(xb, kst(si) * 64), xvo, lb + ring(si) + wq * 4096); };
        group_x(0);
        group_x(1); group_w(1, 0); group_w(1, 1);
        const int cbi = L_CONV + chn * C_CH;
        LAS char* cb = sm + cbi;
        unsigned kvo[2], uso;
#pragma unroll
        for (int rd = 0; rd < 2; ++rd) kvo[rd] = (unsigned)((lane >> 3) * D + (((lane & 7) ^ (((lane >> 4) & 1) | (((2 * wq + rd) & 3) << 1))) * 8)) * 2u;
        uso = WS_U + (unsigned)(kc0 + wq * 16 + (lane >> 3)) * 8192u + (unsigned)(1984 - kr0 + (lane & 7) * 8) * 2u;
        int kro = lb + L_KVO + wq * 2048 + (lane >> 3) * 128 + (((lane & 7) ^ (lane >> 3)) * 16);
        const int cc = 2 * par + ((lane >> 2) & 1);
        int zo = cbi + C_U + par * C_US + (248 + (lane < 36 ? lane : 35)) * 16;
        zo = lane < 8 ? cbi + cc * C_HS + (lane & 3) * 16 : zo;
        u32x4 w2o = w2_off();
        const unsigned fo = lane * 16u;
        unsigned ao = (wq < 2 ? WS_CSUM + (unsigned)(nt * 2 + wq) * 1024u : WS_STATS + mt * 1024u) + fo;
        asm volatile("" : "+v"(zo), "+v"(w2o), "+v"(kvo[0]), "+v"(kvo[1]), "+v"(uso), "+v"(kro), "+v"(ao));
        unsigned eo = (lane & 1) * 4u;
        asm volatile("" : "+v"(eo));
        WAIT_VM(16);
        unsigned tag32, taghi;
        u32x4 bval, ug[2];
        unsigned dbits = *(LAS u16*)(sm + wave * 1024 + 240 + (dch & 7) * 2);
        {
            const u32x4 h0 = lds128(sm + wave * 1024 + 224);
            const bool s1 = !(cc & 1), s2 = !(cc & 2);
            const unsigned a0 = s1 ? 0u : h0[0], a1 = s1 ? h0[0] : h0[1], a2 = s1 ? h0[1] : h0[2], a3 = s1 ? h0[2] : h0[3];
            const unsigned lm = (lane == 3 || lane == 7) ? 0xffffffffu : 0u;
            bval = (u32x4){(s2 ? 0u : a0) & lm, (s2 ? 0u : a1) & lm, (s2 ? a0 : a2) & lm, (s2 ? a1 : a3) & lm};
            asm volatile("" : "+v"(bval), "+v"(dbits));
        }
        auto kv_store = [&](int rd) {
            LAS unsigned* kp = (LAS unsigned*)(lp(kro) + rd * 1024);
            ug[0][1] = kp[0]; asm volatile("" ::: "memory"); ug[0][3] = kp[1]; asm volatile("" ::: "memory");
            ug[1][1] = kp[2]; asm volatile("" ::: "memory"); ug[1][3] = kp[3];
            __builtin_amdgcn_raw_buffer_store_b128(ug[0], wsr, uso, rd * 65536, 16);
            __builtin_amdgcn_raw_buffer_store_b128(ug[1], wsr, uso, rd * 65536 + 4096, 16);
        };
        WAIT_BAR("vmcnt(12) lgkmcnt(0)");
        STAMP(1);
#pragma unroll
        for (int w = 0; w < 5; ++w) {
            if (w == 3) glds4c(p.ws + WS_EPOCH, eo, lb + L_RAW + wq * 1024);
            group_x(w + 2); group_w(w + 2, 0); group_w(w + 2, 1);
            if (w == KVW) {
#pragma unroll
                for (int j = 0; j < 2; ++j) {
                    glds16(sptr(p.k, (kr0 + (2 * wq + j) * 8) * D + kc0), kvo[j], lb + L_KV + (2 * wq + j) * 1024);
                    glds16(sptr(p.v, (kr0 + (2 * wq + j) * 8) * D + kc0), kvo[j], lb + L_KV + 8192 + (2 * wq + j) * 1024);
                }
            }
            if (w == 4) {
                const u32x2 ev = *(LAS u32x2*)(sm + L_RAW + wq * 1024);
                tag32 = __builtin_amdgcn_readfirstlane(ev[0]) ^ (unsigned)TAG_SALT;
                taghi = __builtin_amdgcn_readfirstlane(ev[1]) ^ (unsigned)(TAG_SALT >> 32);
                asm volatile("s_nop 1" : "+s"(tag32), "+s"(taghi));
                ug[0][0] = vmov1(tag32); ug[0][2] = vmov1(tag32); ug[1][0] = vmov1(tag32); ug[1][2] = vmov1(tag32);
                if (lane < 34) *(LAS unsigned*)(sm + L_CTL + lane * 4) = lane < 32 ? 0u : lane == 32 ? tag32 : taghi;
            }
            if (w == 4 && KVW == 0) kv_store(0);
            if (w == 2) STAMP(2);
            STAMPG(2 + 2 * w);
            if (w == KVW) WAIT_BAR("vmcnt(16) lgkmcnt(0)"); else WAIT_BAR("vmcnt(12) lgkmcnt(0)");
            STAMPG(3 + 2 * w);
            if (w == 2) STAMP(3);
        }
        glds16c(p.ws, ao, lb + L_RAW + wq * 1024);
#pragma unroll
        for (int i = 0; i < 3; ++i) pair_w2(w2o, 2 * i, w2i + i * 4096);
        kv_store(KVW ? 0 : 1);
        STAMPG(12); WAIT_BAR("vmcnt(12) lgkmcnt(0)"); STAMPG(13);
        if (KVW) kv_store(1);
        asm volatile("" ::: "memory");
        if (lane == 0) __hip_atomic_fetch_add((LAS unsigned*)(sm + L_CTL + 12), 1u, __ATOMIC_RELAXED, __HIP_MEMORY_SCOPE_WORKGROUP);
        if (wq < 3) {
            u32x4 av = lds128(sm + L_RAW + wq * 1024 + lane * 16);
#pragma unroll 1
            for (unsigned spins = 0; spins < SPIN_CAP; ++spins) {
                if (__all(((av[0] ^ tag32) | (av[2] ^ tag32)) == 0u)) break;
                asm volatile("" ::: "memory");
                av = __builtin_amdgcn_raw_buffer_load_b128(wsr, ao, 0, 16);
                asm volatile("" : "+v"(av));
            }
            *(LAS u32x2*)(sm + L_CSUM + wq * 512 + lane * 8) = (u32x2){av[1], av[3]};
            asm volatile("" ::: "memory");
            if (lane == 0) __hip_atomic_fetch_add((LAS unsigned*)(sm + L_CTL + 64), 1u, __ATOMIC_RELAXED, __HIP_MEMORY_SCOPE_WORKGROUP);
        }
        STAMP(4);
#pragma unroll
        for (int i = 0; i < 3; ++i) pair_w2(w2o, 6 + 2 * i, w2i + L_SLOT + i * 4096);
        WAIT_VM(12);
        __hip_atomic_store((LAS unsigned*)(sm + L_CTL + 48 + wq * 4), 1u, __ATOMIC_RELAXED, __HIP_MEMORY_SCOPE_WORKGROUP);
        wait_lds_eq(sm + L_CTL + 12, 8u);
        STAMP(5);
        __builtin_amdgcn_s_setprio(2);
        const unsigned ubase = WS_U + (unsigned)dch * 8192u;
        glds16x2c(p.ws + ubase + par * 2048, fo, lb + cbi + C_U + par * 2048);
        glds16x2c(p.ws + ubase + 4096 + par * 2048, fo, lb + cbi + C_U + C_US + par * 2048);
        glds4c(p.ws + ubase + (1 - par) * 2048, eo, lb + L_UNX + wq * 256);
        {
            const u32x4 fvo = {fo, fo, fo, lane == 63 ? 62u * 16u : fo};
#pragma unroll
            for (int c = 0; c < 2; ++c) glds16x4(sptr(p.hk, dch * S + 4 * par + 2 * c + 2), fvo, lb + cbi + (2 * par + c) * C_HS + 64);
        }
        *(LAS u32x4*)(sm + zo) = bval;
        WAIT_VM(13);
        __hip_atomic_store((LAS unsigned*)(sm + L_CTL + 112 + wq * 4), 1u, __ATOMIC_RELAXED, __HIP_MEMORY_SCOPE_WORKGROUP);
        WAIT_VM(8);
        {
            u32x4 ua[2], ub[2]; u32x2 un[2];
#pragma unroll
            for (int rd = 0; rd < 2; ++rd) {
                const int x8 = 128 * par + 64 * rd + lane;
                ua[rd] = lds128(cb + C_U + x8 * 16); ub[rd] = lds128(cb + C_U + C_US + x8 * 16);
                un[rd] = *(LAS u32x2*)((rd == 1 && lane == 63) ? sm + L_UNX + wq * 256 : cb + C_U + x8 * 16 + 16);
            }
#pragma unroll 1
            for (unsigned spins = 0; spins < SPIN_CAP; ++spins) {
                unsigned df = 0;
#pragma unroll
                for (int rd = 0; rd < 2; ++rd) df |= (ua[rd][0] ^ tag32) | (ua[rd][2] ^ tag32) | (ub[rd][0] ^ tag32) | (ub[rd][2] ^ tag32) | (un[rd][0] ^ tag32);
                if (__all(df == 0u)) break;
                __builtin_amdgcn_s_sleep(URETRY);
                asm volatile("" ::: "memory");
#pragma unroll
                for (int rd = 0; rd < 2; ++rd) {
                    const int x8 = 128 * par + 64 * rd + lane;
                    const unsigned uo = ubase + (unsigned)x8 * 16u;
                    ua[rd] = __builtin_amdgcn_raw_buffer_load_b128(wsr, uo, 0, 16);
                    ub[rd] = __builtin_amdgcn_raw_buffer_load_b128(wsr, uo + 4096u, 0, 16);
                    un[rd] = __builtin_amdgcn_raw_buffer_load_b64(wsr, x8 == 255 ? uo : uo + 16u, 0, 16);
                }
                asm volatile("" : "+v"(ua[0]), "+v"(ub[0]), "+v"(un[0]), "+v"(ua[1]), "+v"(ub[1]), "+v"(un[1]));
            }
#pragma unroll
            for (int rd = 0; rd < 2; ++rd) {
                const int x8 = 128 * par + 64 * rd + lane;
                const unsigned nx = x8 == 255 ? 0u : un[rd][1];
                *(LAS u32x4*)(cb + C_U + x8 * 16) = (u32x4){__builtin_amdgcn_alignbit(ua[rd][3], ua[rd][1], 16), __builtin_amdgcn_alignbit(ub[rd][1], ua[rd][3], 16),
                                                            __builtin_amdgcn_alignbit(ub[rd][3], ub[rd][1], 16), __builtin_amdgcn_alignbit(nx, ub[rd][3], 16)};
                *(LAS u32x4*)(cb + C_U + C_US + x8 * 16) = (u32x4){ua[rd][1], ua[rd][3], ub[rd][1], ub[rd][3]};
            }
        }
        asm volatile("s_waitcnt vmcnt(0) lgkmcnt(0)" ::: "memory");
        __hip_atomic_store((LAS unsigned*)(sm + L_CTL + 16 + wq * 4), 1u, __ATOMIC_RELAXED, __HIP_MEMORY_SCOPE_WORKGROUP);
        u32x4 lw2[8], xv[2];
        {
            const __amdgpu_buffer_rsrc_t wr2 = __builtin_amdgcn_make_buffer_rsrc((void*)p.w2, 0, F * D * 2, 0x00020000);
#pragma unroll
            for (int j = 0; j < 8; ++j) {
                int t = 24 + (j >> 2) * 2 + rot2; t = t >= 28 ? t - 28 : t;
                lw2[j] = __builtin_amdgcn_raw_buffer_load_b128(wr2, w2o[0], ((wq * 448 + t * 16 + (j & 3) * 8) * D + n64 * 64) * 2, 0);
            }
        }
        const int lf = fresh_lane();
        const int srow = 64 * m64 + 16 * wq + 2 * (lf >> 3);
        const unsigned xoff = (unsigned)(srow * D + 64 * n64 + 8 * (lf & 7)) * 2u;
        {
            const __amdgpu_buffer_rsrc_t xr = __builtin_amdgcn_make_buffer_rsrc((void*)p.x, 0, S * D * 2, 0x00020000);
            xv[0] = __builtin_amdgcn_raw_buffer_load_b128(xr, xoff, 0, 0);
            xv[1] = __builtin_amdgcn_raw_buffer_load_b128(xr, xoff, D * 2, 0);
        }
        SCHED();
        wait_lds(sm + L_CTL + 16 + (wq ^ 1) * 4);
        STAMP(7);
        STAMP(6);
        const int n16 = lane & 15, kg = lane >> 4;
        f32x4 ca[4];
#pragma unroll
        for (int j = 0; j < 4; ++j) ca[j] = (f32x4){0.f, 0.f, 0.f, 0.f};
        int apo = lb + cbi + (n16 & 3) * C_HS + ((n16 >> 2) + kg) * 16;
        int bpo = lb + cbi + C_U + (n16 & 1) * C_US + (252 - 4 * (n16 >> 1) + kg) * 16 - 7 * 512;
        u32x4 fa[2][8], fb[2][8];
#pragma unroll
        for (int i = 0; i < 8; ++i) { fa[0][i] = lds128(lp(apo) + i * 512); fb[0][i] = lds128(lp(bpo) + (7 - i) * 512); }
        const bool odd = lf & 1;
        int ysrc = (2 * (lf >> 3) + (lf & 1) + 16 * ((lf >> 1) & 3)) * 4;
        unsigned yso = WS_Y + (unsigned)dch * 8192u + lf * 16u;
        f32x4 kd[4];
        {
            const float dval = bflo(dbits);
            const int sl0 = 32 * ((lf & 15) >> 1) + 8 * (lf >> 4) + 4 * (lf & 1);
#pragma unroll
            for (int j = 0; j < 4; ++j) {
                const int tj = (j == 0 ? 0 : j == 1 ? 3 : j == 2 ? 4 : 7) ^ par;
                const u32x2 kvr = *(LAS u32x2*)(cb + C_U + C_US + (2044 - 256 * tj - sl0) * 2);
                const unsigned ka = pk(bfhi(kvr[1]) * dval, bflo(kvr[1]) * dval), kb = pk(bfhi(kvr[0]) * dval, bflo(kvr[0]) * dval);
                kd[j] = (f32x4){bflo(ka), bfhi(ka), bflo(kb), bfhi(kb)};
                asm volatile("" : "+v"(kd[j]));
            }
            asm volatile("" : "+v"(ysrc), "+v"(yso));
        }
        wait_lds(sm + L_CTL + 32 + wq * 4);
        STAMP(8);
        auto conv_all = [&](auto pc_) {
            constexpr int P = decltype(pc_)::value;
            static_for([&](auto rc) {
                constexpr int r = decltype(rc)::value, c = r & 1, n = c ^ 1;
                static_for([&](auto kc) {
                    constexpr int k = decltype(kc)::value;
                    constexpr int T[4] = {P, 3 - P, 4 + P, 7 - P};
                    constexpr int j = k <= T[0] ? 0 : k <= T[0] + T[1] + 1 ? 1 : k <= T[0] + T[1] + T[2] + 2 ? 2 : 3;
                    constexpr int pp = k - (j == 0 ? 0 : j == 1 ? T[0] + 1 : j == 2 ? T[0] + T[1] + 2 : T[0] + T[1] + T[2] + 3);
                    ca[j] = mfma16(fa[c][pp], fb[c][T[j] - pp], ca[j]);
                    SCHED();
                    if constexpr (r < 7 && k < 2 * (T[3] + 1)) {
                        if constexpr (k & 1) fb[n][k >> 1] = lds128(lp(bpo) + (r + 1) * 64 + (7 - (k >> 1)) * 512);
                        else fa[n][k >> 1] = lds128(lp(apo) + (r + 1) * 64 + (k >> 1) * 512);
                    }
                    SCHED();
                }, std::make_integer_sequence<int, 18>{});
                asm volatile("" : "+v"(apo), "+v"(bpo));
            }, std::make_integer_sequence<int, 8>{});
        };
        if (par) conv_all(std::integral_constant<int, 1>{}); else conv_all(std::integral_constant<int, 0>{});
        SCHED();
        STAMP(9);
        {
            float r0[4], r1[4];
#pragma unroll
            for (int j = 0; j < 4; ++j) { r0[j] = dpp_xor1(odd ? ca[j][0] : ca[j][2]); r1[j] = dpp_xor1(odd ? ca[j][1] : ca[j][3]); }
            unsigned rp[4][2];
#pragma unroll
            for (int j = 0; j < 4; ++j) {
                const unsigned ya = pk(odd ? r0[j] : ca[j][0], odd ? ca[j][2] : r0[j]), yb = pk(odd ? r1[j] : ca[j][1], odd ? ca[j][3] : r1[j]);
                rp[j][0] = __builtin_amdgcn_ds_bpermute(ysrc, pk(add1(bflo1(ya), kd[j][0]), add1(bfhi1(ya), kd[j][1])));
                rp[j][1] = __builtin_amdgcn_ds_bpermute(ysrc, pk(add1(bflo1(yb), kd[j][2]), add1(bfhi1(yb), kd[j][3])));
            }
#pragma unroll
            for (int j = 0; j < 4; ++j)
                __builtin_amdgcn_raw_buffer_store_b128((u32x4){vmov1(tag32 ^ Y_TAG), rp[j][0], vmov1(tag32 ^ Y_TAG), rp[j][1]}, wsr, yso,
                                                       ((j == 0 ? 0 : j == 1 ? 3 : j == 2 ? 4 : 7) ^ par) * 1024, 16);
        }
        STAMP(10);
        WAIT_LGKM0();
        if (lane == 0) __hip_atomic_fetch_add((LAS unsigned*)(sm + L_CTL + 8), 1u, __ATOMIC_RELAXED, __HIP_MEMORY_SCOPE_WORKGROUP);
#pragma unroll 1
        for (unsigned spins = 0; spins < (1u << 22); ++spins) {
            if (__builtin_amdgcn_readfirstlane(__hip_atomic_load((LAS unsigned*)(sm + L_CTL + 8), __ATOMIC_RELAXED, __HIP_MEMORY_SCOPE_WORKGROUP)) == 4u) break;
            __builtin_amdgcn_s_sleep(1);
        }
        asm volatile("" ::: "memory");
#pragma unroll
        for (int j = 0; j < 8; ++j) *(LAS u32x4*)(sm + latei + j * 1024 + lf * 16) = lw2[j];
        asm volatile("" ::: "memory");
        __hip_atomic_store((LAS unsigned*)(sm + L_CTL + 80 + wq * 4), 1u, __ATOMIC_RELAXED, __HIP_MEMORY_SCOPE_WORKGROUP);
        STAMP(11);
        if (YSTEP) wait_lds(sm + L_CTL + 96 + wq * 4); else __builtin_amdgcn_s_sleep(YSLEEP);
        u32x2 yv[8];
        u32x4 stv;
        const int yl = L_CONV + 32768 + wq * 4096;
        const unsigned yg = (unsigned)(((lf >> 2) & 7) * 8 + (lf >> 5)) * 8192u + (unsigned)((lf & 3) ^ (((lf >> 4) & 1) << 1)) * 16u;
        const u32x4 yvo = {yg, yg + (16384u - 1024u), yg + 2 * (16384u - 1024u), yg + 3 * (16384u - 1024u)};
        const char* ybase = p.ws + WS_Y + (unsigned)(64 * n64) * 8192u + (unsigned)(64 * m64 + 16 * wq) * 4u;
        const int yr = lb + yl + (lf & 7) * 64 + (((lf >> 3) * 8) ^ (((lf >> 2) & 1) << 5));
#pragma unroll 1
        for (unsigned spins = 0; spins < SPIN_CAP; ++spins) {
            glds16x4c(ybase, yvo, lb + yl);
            stv = __builtin_amdgcn_raw_buffer_load_b128(wsr, WS_STATS + (unsigned)srow * 8u, 0, 16);
            WAIT_VM(0);
#pragma unroll
            for (int i = 0; i < 8; ++i) yv[i] = *(LAS u32x2*)(lp(yr) + i * 512);
            unsigned df = (stv[0] ^ tag32) | (stv[2] ^ tag32);
#pragma unroll
            for (int i = 0; i < 8; ++i) df |= yv[i][0] ^ (tag32 ^ Y_TAG);
            if (__all(df == 0u)) break;
            __builtin_amdgcn_s_sleep(8);
        }
        STAMP(14);
        __builtin_amdgcn_s_setprio(0);
        f32x2 sf[8], xf[8];
#pragma unroll
        for (int e = 0; e < 2; ++e) {
            const float nmb = -bflo(stv[1 + 2 * e]);
            float ri = __builtin_amdgcn_rcpf(bfhi(stv[1 + 2 * e]));
            asm volatile("s_nop 1" : "+v"(ri));
#pragma unroll
            for (int j = 0; j < 4; ++j) {
                const int i = 4 * e + j;
                const unsigned xd = xv[e][j];
                xf[i] = (f32x2){bflo1(xd), bfhi1(xd)};
                const unsigned tp = pk(add1(xf[i][0], nmb), add1(xf[i][1], nmb));
                const unsigned np = pk(mul1(bflo1(tp), ri), mul1(bfhi1(tp), ri));
                sf[i] = (f32x2){mul1(bflo1(np), e ? bfhi1(yv[2 * j][1]) : bflo1(yv[2 * j][1])), mul1(bfhi1(np), e ? bfhi1(yv[2 * j + 1][1]) : bflo1(yv[2 * j + 1][1]))};
                asm volatile("" : "+v"(sf[i]), "+v"(xf[i]));
            }
        }
        const int rm = lb + L_W2 + part((lf >> 2) & 1, wq >> 1, lf & 3) + (16 * (wq & 1) + 2 * (lf >> 3)) * 16;
        STAMP(15);
        WAIT_BAR("lgkmcnt(0)");
        STAMP(16);
        f32x4 o[4];
#pragma unroll
        for (int j = 0; j < 4; ++j) {
            LAS char* rp = lp(rm) + (j >> 1) * 16 + (j & 1) * 512;
            o[j] = (*(LAS f32x4*)rp + *(LAS f32x4*)(rp + 12288)) + (*(LAS f32x4*)(rp + 24576) + *(LAS f32x4*)(rp + 36864));
        }
        f32x2 so[8], ev[8];
#pragma unroll
        for (int i = 0; i < 8; ++i) {
            const f32x2 ov = {o[i >> 1][(i & 1) * 2], o[i >> 1][(i & 1) * 2 + 1]};
            const f32x2 t = ov * (f32x2){-1.44269504088896341f, -1.44269504088896341f};
            ev[i] = (f32x2){__builtin_amdgcn_exp2f(t[0]), __builtin_amdgcn_exp2f(t[1])};
            so[i] = sf[i] * ov;
        }
        SCHED();
        u32x4 st[2];
#pragma unroll
        for (int i = 0; i < 8; ++i) {
            const f32x2 dn = ev[i] + (f32x2){1.f, 1.f};
            const f32x2 r = __builtin_elementwise_fma(so[i], (f32x2){__builtin_amdgcn_rcpf(dn[0]), __builtin_amdgcn_rcpf(dn[1])}, xf[i]);
            st[i >> 2][i & 3] = pk(r[0], r[1]);
        }
        const __amdgpu_buffer_rsrc_t orr = __builtin_amdgcn_make_buffer_rsrc((void*)p.out, 0, S * D * 2, 0x00020000);
        __builtin_amdgcn_raw_buffer_store_b128(st[0], orr, xoff, 0, 16);
        __builtin_amdgcn_raw_buffer_store_b128(st[1], orr, xoff, D * 2, 16);
        STAMP(17);
        STAMP_OUT(18);
    }
}

extern "C" void kernel_launch(void* const* d_in, const int* in_sizes, int n_in,
                              void* d_out, int out_size, void* d_ws, size_t ws_size,
                              hipStream_t stream) {
    Params p;
    p.x = (const u16*)d_in[0]; p.k = (const u16*)d_in[1]; p.v = (const u16*)d_in[2];
    p.sk = (const u16*)d_in[3]; p.hk = (const u16*)d_in[4]; p.dv = (const u16*)d_in[5];
    p.w1 = (const u16*)d_in[7]; p.w3 = (const u16*)d_in[8]; p.w2 = (const u16*)d_in[9];
    p.out = (u16*)d_out; p.ws = (char*)d_ws;
    (void)hipFuncSetAttribute((const void*)fused_kernel, hipFuncAttributeMaxDynamicSharedMemorySize, L_TOTAL);
    fused_kernel<<<256, 512, L_TOTAL, stream>>>(p);
}
```

```cpp
#include <hip/hip_runtime.h>
#include <stdint.h>
#include <utility>

typedef unsigned short u16;
typedef unsigned long long u64;
typedef short s16x4 __attribute__((ext_vector_type(4)));
typedef __bf16 bf16x2 __attribute__((ext_vector_type(2)));
typedef __bf16 bf16x8 __attribute__((ext_vector_type(8)));
typedef float f32x2 __attribute__((ext_vector_type(2)));
typedef float f32x4 __attribute__((ext_vector_type(4)));
typedef float f32x16 __attribute__((ext_vector_type(16)));
typedef unsigned u32x2 __attribute__((ext_vector_type(2)));
typedef unsigned u32x4 __attribute__((ext_vector_type(4)));
#define LAS __attribute__((address_space(3)))
#define GAS __attribute__((address_space(1)))
#define RLX_AGENT __ATOMIC_RELAXED, __HIP_MEMORY_SCOPE_AGENT

constexpr int S = 2048, D = 448, F = 1792;

constexpr unsigned WS_EPOCH = 0;
constexpr unsigned WS_FH = 65536;
constexpr unsigned WS_STATS = 8192;
constexpr unsigned WS_CSUM = 24576;
constexpr unsigned WS_U = 262144;
constexpr unsigned WS_Y = WS_U;
constexpr unsigned WS_H = WS_Y + D * S * 4;
constexpr unsigned WS_END = WS_H + 64 * 112 * 1024;

constexpr int L_KV = 0;
constexpr int L_CTL = 0;
constexpr int L_CSUM = 256;
constexpr int L_STAT = 1280;
constexpr int L_RAW = 2048;
constexpr int L_KVO = 8192;
constexpr int L_UNX = 6144;
constexpr int L_SLOT = 49152;
constexpr int L_CONV = 12288;
constexpr int C_CH = 26112, C_HS = 4160, C_U = 16640, C_US = 4640;
constexpr int L_W2 = 16384 + L_SLOT;
constexpr int L_TOTAL = 163840;
__host__ __device__ constexpr int part(int db, int sb, int a) { return db * L_SLOT + sb * 4352 + a * 1024 + (a & 1) * 16 + (a >> 1) * 128; }
__host__ __device__ constexpr int ring(int stage) { return 16384 + (stage % 3) * L_SLOT; }
constexpr unsigned CODE_PREFETCH = 36864;
constexpr int HD = 6, HG = 4;
constexpr int NTOUCH = 2;
constexpr int YSTEP = 18;

constexpr u64 TAG_SALT = 0x9E3779B97F4A7C15ull;
constexpr unsigned Y_TAG = 0x80000000u;
constexpr unsigned SPIN_CAP = 1u << 18;

struct Params {
    const u16* x; const u16* k; const u16* v; const u16* sk; const u16* hk; const u16* dv;
    const u16* w1; const u16* w3; const u16* w2; u16* out; char* ws;
};

__device__ __forceinline__ float bflo(unsigned p) { return __uint_as_float(p << 16); }
__device__ __forceinline__ float bfhi(unsigned p) { return __uint_as_float(p & 0xffff0000u); }
__device__ __forceinline__ float bflo1(unsigned p) { float r; asm("v_lshlrev_b32 %0, 16, %1" : "=v"(r) : "v"(p)); return r; }
__device__ __forceinline__ float bfhi1(unsigned p) { float r; asm("v_and_b32 %0, 0xffff0000, %1" : "=v"(r) : "v"(p)); return r; }
__device__ __forceinline__ float mul1(float a, float b) { float r; asm("v_mul_f32 %0, %1, %2" : "=v"(r) : "v"(a), "v"(b)); return r; }
__device__ __forceinline__ void fmac1(float& d, float a, float b) { asm("v_fmac_f32 %0, %1, %2" : "+v"(d) : "v"(a), "v"(b)); }
__device__ __forceinline__ float dpp_xor1(float v) {
    return __int_as_float(__builtin_amdgcn_update_dpp(0, __float_as_int(v), 0xB1, 0xf, 0xf, true));
}
__device__ __forceinline__ float add1(float a, float b) { float r; asm("v_add_f32 %0, %1, %2" : "=v"(r) : "v"(a), "v"(b)); return r; }
__device__ __forceinline__ unsigned vmov1(unsigned s) { unsigned r; asm volatile("v_mov_b32 %0, %1" : "=v"(r) : "s"(s)); return r; }
__device__ __forceinline__ unsigned pk(float a, float b) {
    f32x2 f = {a, b};
    return __builtin_bit_cast(unsigned, __builtin_convertvector(f, bf16x2));
}
__device__ __forceinline__ float rbf(float a) { return bflo(pk(a, a)); }
__device__ __forceinline__ float dot2(unsigned a, unsigned b, float c) {
    return __builtin_amdgcn_fdot2_f32_bf16(__builtin_bit_cast(bf16x2, a), __builtin_bit_cast(bf16x2, b), c, false);
}
__device__ __forceinline__ void glds16(const void* base, unsigned voff, unsigned ldst) {
    u64 t;
    asm volatile("s_mov_b32 m0, %3\n\ts_mov_b64 %0, %2\n\tglobal_load_lds_dwordx4 %1, %0 nt" : "=&s"(t) : "v"(voff), "s"(base), "s"(ldst));
}
__device__ __forceinline__ void glds16x4(const void* base, u32x4 vo, unsigned ldst) {
    u64 t;
    asm volatile("s_mov_b32 m0, %6\n\ts_mov_b64 %0, %5\n\tglobal_load_lds_dwordx4 %1, %0\n\tglobal_load_lds_dwordx4 %2, %0 offset:1024\n\t"
                 "global_load_lds_dwordx4 %3, %0 offset:2048\n\tglobal_load_lds_dwordx4 %4, %0 offset:3072"
                 : "=&s"(t) : "v"(vo[0]), "v"(vo[1]), "v"(vo[2]), "v"(vo[3]), "s"(base), "s"(ldst));
}
__device__ __forceinline__ void glds16x4c(const void* base, u32x4 vo, unsigned ldst) {
    u64 t;
    asm volatile("s_mov_b32 m0, %6\n\ts_mov_b64 %0, %5\n\tglobal_load_lds_dwordx4 %1, %0 sc1\n\tglobal_load_lds_dwordx4 %2, %0 offset:1024 sc1\n\t"
                 "global_load_lds_dwordx4 %3, %0 offset:2048 sc1\n\tglobal_load_lds_dwordx4 %4, %0 offset:3072 sc1"
                 : "=&s"(t) : "v"(vo[0]), "v"(vo[1]), "v"(vo[2]), "v"(vo[3]), "s"(base), "s"(ldst) : "memory");
}
__device__ __forceinline__ void glds16x2c(const void* base, unsigned voff, unsigned ldst) {
    u64 t;
    asm volatile("s_mov_b32 m0, %3\n\ts_mov_b64 %0, %2\n\tglobal_load_lds_dwordx4 %1, %0 sc1\n\tglobal_load_lds_dwordx4 %1, %0 offset:1024 sc1"
                 : "=&s"(t) : "v"(voff), "s"(base), "s"(ldst));
}
__device__ __forceinline__ void glds16c(const void* base, unsigned voff, unsigned ldst) {
    u64 t;
    asm volatile("s_mov_b32 m0, %3\n\ts_mov_b64 %0, %2\n\tglobal_load_lds_dwordx4 %1, %0 sc1" : "=&s"(t) : "v"(voff), "s"(base), "s"(ldst));
}
__device__ __forceinline__ void glds4c(const void* base, unsigned voff, unsigned ldst) {
    u64 t;
    asm volatile("s_mov_b32 m0, %3\n\ts_mov_b64 %0, %2\n\tglobal_load_lds_dword %1, %0 sc1" : "=&s"(t) : "v"(voff), "s"(base), "s"(ldst));
}
__device__ __forceinline__ const u16* sptr(const u16* base, int off) {
    const u16* r = base + __builtin_amdgcn_readfirstlane(off);
    asm("" : "+s"(r));
    return r;
}
__device__ __forceinline__ u64 vsel(bool c, const void* a, u64 b) {
    u64 va = (u64)a;
    asm("" : "+v"(va), "+v"(b));
    return c ? va : b;
}
__device__ __forceinline__ LAS char* lp(int a) { return (LAS char*)(size_t)(unsigned)a; }
__device__ __forceinline__ u32x4 lds128(LAS char* p) { return *(LAS u32x4*)p; }
__device__ __forceinline__ u32x2 ldstr(LAS char* p) {
    return __builtin_bit_cast(u32x2, __builtin_amdgcn_ds_read_tr16_b64_v4i16((LAS s16x4*)p));
}
__device__ __forceinline__ u32x4 ldstr2(LAS char* p, int off) {
    u32x2 a = ldstr(p), b = ldstr(p + off);
    return (u32x4){a.x, a.y, b.x, b.y};
}
__device__ __forceinline__ f32x16 mfma32(u32x4 a, u32x4 b, f32x16 c) {
    return __builtin_amdgcn_mfma_f32_32x32x16_bf16(__builtin_bit_cast(bf16x8, a), __builtin_bit_cast(bf16x8, b), c, 0, 0, 0);
}
__device__ __forceinline__ f32x4 mfma16(u32x4 a, u32x4 b, f32x4 c) {
    return __builtin_amdgcn_mfma_f32_16x16x32_bf16(__builtin_bit_cast(bf16x8, a), __builtin_bit_cast(bf16x8, b), c, 0, 0, 0);
}
template <class Fn, int... I>
__device__ __forceinline__ void static_for(Fn&& fn, std::integer_sequence<int, I...>) { (fn(std::integral_constant<int, I>{}), ...); }
#define WAIT_VM(n) asm volatile("s_waitcnt vmcnt(" #n ")" ::: "memory")
#define WAIT_LGKM0() asm volatile("s_waitcnt lgkmcnt(0)" ::: "memory")
#define WAIT_BAR(w) asm volatile("s_waitcnt " w "\n\ts_barrier" ::: "memory")
#define SCHED() __builtin_amdgcn_sched_barrier(0)

__device__ __forceinline__ void wait_lds(LAS char* word) {
#pragma unroll 1
    for (unsigned spins = 0; spins < (SPIN_CAP << 4); ++spins) {
        if (__builtin_amdgcn_readfirstlane(__hip_atomic_load((LAS unsigned*)word, __ATOMIC_RELAXED, __HIP_MEMORY_SCOPE_WORKGROUP))) break;
        __builtin_amdgcn_s_sleep(1);
    }
    asm volatile("" ::: "memory");
}
__device__ __forceinline__ void wait_lds_eq(LAS char* word, unsigned want) {
#pragma unroll 1
    for (unsigned spins = 0; spins < (1u << 22); ++spins) {
        if ((unsigned)__builtin_amdgcn_readfirstlane(__hip_atomic_load((LAS unsigned*)word, __ATOMIC_RELAXED, __HIP_MEMORY_SCOPE_WORKGROUP)) == want) break;
        __builtin_amdgcn_s_sleep(1);
    }
    asm volatile("" ::: "memory");
}
__global__ __launch_bounds__(512, 2) void fused_kernel(Params p) {
    extern __shared__ __attribute__((aligned(16))) char smem_raw[];
    u64 pc;
    asm volatile("s_getpc_b64 %0" : "=s"(pc));
    LAS char* sm = (LAS char*)smem_raw;
    const int tid = threadIdx.x, lane = tid & 63;
    const int wave = __builtin_amdgcn_readfirstlane(tid >> 6);
    const int xcd = blockIdx.x & 7, idx = blockIdx.x >> 3;
    if (idx == 0 && wave < 5) {
        const unsigned o = (unsigned)tid * 128u;
        asm volatile("s_mov_b32 m0, %2\n\ts_nop 0\n\tglobal_load_lds_dword %0, %1" :: "v"(o < CODE_PREFETCH ? o : 0u), "s"(pc & ~(u64)255),
                     "s"((unsigned)(size_t)sm + 32768u));
    }
    SCHED();
    const __amdgpu_buffer_rsrc_t wsr = __builtin_amdgcn_make_buffer_rsrc(p.ws, 0, WS_END, 0x00020000);
    asm volatile("" :: "s"(p.x), "s"(p.k), "s"(p.v), "s"(p.sk), "s"(p.hk), "s"(p.dv), "s"(p.w1), "s"(p.w3), "s"(p.w2), "s"(p.out), "s"(p.ws));

    if (idx < 4) {
        const int ca = (xcd >> 1) * 4 + idx, cmat = ca >= 7, cnt = 7 * (xcd & 1) + (ca >= 7 ? ca - 7 : ca);
        const bool job = ca < 14;
        u32x4 xr[7], wr[14];
        const int row = (8 * (xcd >> 1) + 4 * (xcd & 1) + idx) * 64 + wave * 8 + (lane >> 3);
#pragma unroll
        for (int i = 0; i < 7; ++i) xr[i] = *(const u32x4*)(p.x + (size_t)row * D + i * 64 + (lane & 7) * 8);
        const u64 epoch = __hip_atomic_load((GAS u64*)(p.ws + WS_EPOCH), RLX_AGENT);
        const u64 tag = epoch ^ TAG_SALT;
        const unsigned tag32 = (unsigned)tag;
        unsigned stat;
        {
            float s4[4] = {0.f, 0.f, 0.f, 0.f};
#pragma unroll
            for (int i = 0; i < 7; ++i)
#pragma unroll
                for (int j = 0; j < 4; ++j) s4[j] = dot2(xr[i][j], 0x3f803f80u, s4[j]);
            float sum = (s4[0] + s4[1]) + (s4[2] + s4[3]);
            sum += __shfl_xor(sum, 1); sum += __shfl_xor(sum, 2); sum += __shfl_xor(sum, 4);
            const float mean = sum * (1.f / D);
            f32x2 q4[4] = {{0.f, 0.f}, {0.f, 0.f}, {0.f, 0.f}, {0.f, 0.f}};
#pragma unroll
            for (int i = 0; i < 7; ++i)
#pragma unroll
                for (int j = 0; j < 4; ++j) {
                    const f32x2 d = (f32x2){bflo(xr[i][j]), bfhi(xr[i][j])} - (f32x2){mean, mean};
                    q4[j] = __builtin_elementwise_fma(d, d, q4[j]);
                }
            const f32x2 q2 = (q4[0] + q4[1]) + (q4[2] + q4[3]);
            float sq = q2[0] + q2[1];
            sq += __shfl_xor(sq, 1); sq += __shfl_xor(sq, 2); sq += __shfl_xor(sq, 4);
            stat = pk(mean, __builtin_amdgcn_sqrtf(rbf(rbf(sq * (1.f / D)) + 1e-5f)));
            asm volatile("" : "+v"(stat));
        }
        if (job) {
            SCHED();
            const u16* wp = (cmat ? p.w3 : p.w1) + (size_t)(wave * 4 + (lane >> 4)) * F + cnt * 128 + (lane & 15) * 8;
#pragma unroll
            for (int i = 0; i < 14; ++i) wr[i] = *(const u32x4*)(wp + (size_t)(32 * i) * F);
            SCHED();
        }
        __builtin_amdgcn_raw_buffer_store_b64((u32x2){tag32, stat}, wsr, (lane & 7) == 0 ? WS_STATS + (unsigned)row * 8u : 0xfffffff0u, 0, 16);
        if (job) {
            float c[8];
#pragma unroll
            for (int j = 0; j < 8; ++j) c[j] = 0.f;
#pragma unroll
            for (int i = 0; i < 14; ++i)
#pragma unroll
                for (int j = 0; j < 4; ++j) { c[2 * j] += bflo(wr[i][j]); c[2 * j + 1] += bfhi(wr[i][j]); }
            LAS char* cp = sm + ((wave * 4 + (lane >> 4)) * 136 + (lane & 15) * 8) * 4;
            *(LAS f32x4*)cp = (f32x4){c[0], c[1], c[2], c[3]};
            *(LAS f32x4*)(cp + 16) = (f32x4){c[4], c[5], c[6], c[7]};
        }
        if (job) {
            WAIT_BAR("lgkmcnt(0)");
            float t = 0.f;
#pragma unroll
            for (int i = 0; i < 8; ++i) t += *(LAS float*)(sm + ((i * 4 + (tid & 3)) * 136 + (tid >> 2)) * 4);
            t += __shfl_xor(t, 1); t += __shfl_xor(t, 2);
            __builtin_amdgcn_raw_buffer_store_b64((u32x2){tag32, __float_as_uint(t)}, wsr,
                (tid & 3) == 0 ? WS_CSUM + (unsigned)((cnt * 2 + cmat) * 128 + (tid >> 2)) * 8u : 0xfffffff0u, 0, 16);
        }
        SCHED();
        if (!job) __builtin_amdgcn_s_sleep(40);
        const unsigned pf2 = *(const unsigned*)((const char*)p.hk + (size_t)((xcd * 4 + idx) * 448 + wave * 56 + (lane < 56 ? lane : 0)) * 128);
        {
            const unsigned fo = lane * 16u;
            constexpr int PER = ((4 - NTOUCH) * 14336 + 4095) / 4096;
#pragma unroll 1
            for (int i = wave; i < 7 * PER; i += 8) {
                const int g = i / PER, j = i - g * PER;
                int o = NTOUCH * 14336 + j * 4096; o = o > 57344 - 4096 ? 57344 - 4096 : o;
                glds16x4((const char*)p.w2 + (size_t)idx * 401408 + g * 57344 + o, (u32x4){fo, fo, fo, fo}, (unsigned)(size_t)sm + 65536u + wave * 4096);
            }
        }
        asm volatile("" :: "v"(pf2));
        if (blockIdx.x == 0 && wave == 7) {
            GAS u64* gp = (GAS u64*)(p.ws + WS_STATS) + lane * 8;
            GAS u64* fp[4];
#pragma unroll
            for (int m = 0; m < 4; ++m) {
                const int g = m * 56 + (lane < 56 ? lane : 0);
                fp[m] = (GAS u64*)(p.ws + WS_U + (unsigned)((g >> 5) * 64 + 8) * 8192u + (unsigned)(1984 - (g & 31) * 64) * 2u);
            }
#pragma unroll 1
            for (unsigned spins = 0; spins < SPIN_CAP; ++spins) {
                __builtin_amdgcn_s_sleep(32);
                bool ok = true;
#pragma unroll
                for (int m = 0; m < 4; ++m)
                    ok &= ((unsigned)__hip_atomic_load(gp + m * 512, RLX_AGENT) == tag32) &
                          ((((unsigned)__hip_atomic_load(fp[m], RLX_AGENT) ^ tag32) & ~Y_TAG) == 0u);
                if (__all(ok)) break;
            }
            if (lane == 0) __hip_atomic_store((GAS u64*)(p.ws + WS_EPOCH), epoch + 1, RLX_AGENT);
        }
        return;
    }

    const int gi = idx - 4, ml = gi & 3, nl = gi >> 2;
    const int mt = 4 * (xcd >> 1) + ml, nt = 7 * (xcd & 1) + nl;
    const int rot1 = (2 * ml + nl) % 6;
    const int m64 = 4 * xcd + ml, n64 = nl;
    const int gpair = xcd * 28 + gi;
    const int r32 = lane & 31, hh = lane >> 5, blk = (lane >> 4) & 1, q4 = (lane & 15) >> 2, p4 = lane & 3;
    const unsigned lb = (unsigned)(size_t)sm;
    const int wq = wave & 3;
    u32x4 wvo;
    wvo[0] = (unsigned)((lane >> 4) * F + (((lane & 15) ^ ((lane >> 4) << 2)) * 8)) * 2u;
#pragma unroll
    for (int j = 1; j < 4; ++j) wvo[j] = wvo[0] + j * (8 * F - 1024);
    const u16* w1b = sptr(p.w1, wq * 16 * F + nt * 128);
    const u16* w3b = sptr(p.w3, wq * 16 * F + nt * 128);
    auto kst = [&](int si) { if (si == 0) return 0; const int s = si + rot1; return s >= 7 ? s - 6 : s; };
    auto group_w = [&](int si, int m) { glds16x4(sptr(m ? w3b : w1b, kst(si) * 64 * F), wvo, lb + ring(si) + 16384 + m * 16384 + wq * 4096); };
    const int kr0 = (gpair & 31) * 64, kc0 = (gpair >> 5) * 64;
    const int chn = wq >> 1, par = wq & 1, dch = 2 * gpair + chn;
    if (wave < 4) group_w(0, 0);
    {
        const bool isk = lane >= 8 && lane < 14;
        u64 mb_ = vsel(lane == 15, p.dv, vsel(lane == 14, p.hk, vsel(isk, p.k, (u64)p.sk)));
        unsigned mo = (unsigned)((kc0 + wq * 16) * 8 + (lane & 7) * 16);
        mo = isk ? (unsigned)((kr0 ? kr0 - 3 + ((lane - 8) >> 1) : 0) * D + kc0 + wq * 16 + (lane & 1) * 8) * 2u : mo;
        mo = lane == 14 ? (unsigned)dch * 4096u : mo;
        mo = lane == 15 ? (unsigned)(dch & ~7) * 2u : mo;
        const u64 ma = mb_ + mo;
        asm volatile("s_mov_b32 m0, %1\n\ts_nop 0\n\tglobal_load_lds_dwordx4 %0, off" :: "v"(ma), "s"(lb + wave * 1024));
    }
    if (wave < 4) group_w(0, 1);
    SCHED();
    const int rot2 = 8 * ml;
    const int w2i = L_W2 + wq * 12288;
    auto fresh_lane = [&]() { int l = lane; asm volatile("" : "+v"(l)); return l; };
    const int latei = L_CONV + wq * 8192;
    auto pair_w2 = [&](u32x4 vo, int i, int dst) {
        int t = i + rot2; t = t >= 28 ? t - 28 : t;
        glds16x4(sptr(p.w2, (wq * 448 + t * 16) * D + n64 * 64), vo, lb + dst);
    };
    auto w2_off = [&]() {
        const int l = fresh_lane();
        const unsigned o = (unsigned)((l >> 3) * D + (((l & 7) ^ (((l >> 4) & 1) << 2)) * 8)) * 2u;
        return (u32x4){o, o + (16 * D - 1024), o + 2 * (16 * D - 1024), o + 3 * (16 * D - 1024)};
    };

    if (wave < 4) {
        __builtin_amdgcn_s_setprio(1);
        const int wm = wave >> 1, wn = wave & 1;
        int xa[4], wa0, wa1;
#pragma unroll
        for (int i = 0; i < 4; ++i) xa[i] = lb + ring(0) + (((64 * wm + r32) * 128 + ((hh ^ ((r32 >> 1) & 7)) * 16)) ^ (i * 32));
        wa0 = 16384 + (8 * hh + q4) * 256 + (((8 * wn + 2 * blk + (p4 >> 1)) ^ (q4 << 2)) * 16) + 8 * (p4 & 1);
        wa1 = (wa0 ^ 64) + lb + ring(0); wa0 += lb + ring(0);
        f32x16 acc[2][2][2];
#pragma unroll
        for (int i = 0; i < 8; ++i)
#pragma unroll
            for (int r = 0; r < 16; ++r) acc[i >> 2][(i >> 1) & 1][i & 1][r] = 0.f;
        u32x4 fx[2][2], fw[2][2][2];
        auto load_frag = [&](int g, int j) {
            const int pp = g & 3, b = g & 1;
            if (j == 0) {
                if (g >= 4) {
                    const int d = ring(g >> 2) - ring((g >> 2) - 1);
                    if (pp == 0) { wa0 += d; wa1 += d; asm volatile("" : "+v"(wa0), "+v"(wa1)); }
                    xa[pp] += d; asm volatile("" : "+v"(xa[pp]));
                }
                fw[b][0][0] = ldstr2(lp(wa0) + pp * 4096, 1024);
            }
            if (j == 1) { fx[b][0] = lds128(lp(xa[pp])); fx[b][1] = lds128(lp(xa[pp]) + 4096); }
            if (j == 2) fw[b][0][1] = ldstr2(lp(wa0) + pp * 4096 + 16384, 1024);
            if (j == 3) fw[b][1][0] = ldstr2(lp(wa1) + pp * 4096, 1024);
            if (j == 4) fw[b][1][1] = ldstr2(lp(wa1) + pp * 4096 + 16384, 1024);
        };
        int ka0[2], kam[2], kwo;
#pragma unroll
        for (int rd = 0; rd < 2; ++rd) {
            const int ck = 2 * wq + rd;
            const int bq = 2 * (lane >> 4) + (p4 >> 1), bm = bq ? bq - 1 : 0;
            ka0[rd] = lb + L_KV + (8 * bq + q4) * 128 + ((ck ^ (((q4 >> 1) & 1) | ((bq & 3) << 1))) * 16) + 8 * (p4 & 1);
            kam[rd] = lb + L_KV + (8 * bm + 4 + q4) * 128 + ((ck ^ (((q4 >> 1) & 1) | ((bm & 3) << 1))) * 16) + 8 * (p4 & 1);
        }
        kwo = lb + L_KVO + wq * 2048 + (lane & 7) * 128 + (((7 - (lane >> 3)) ^ (lane & 7)) * 16);
        asm volatile("" : "+v"(ka0[0]), "+v"(ka0[1]), "+v"(kam[0]), "+v"(kam[1]), "+v"(kwo));
        WAIT_VM(4);
        u32x2 skv[2], hal[2];
#pragma unroll
        for (int rd = 0; rd < 2; ++rd) {
            LAS char* mp = sm + wave * 1024;
            skv[rd] = *(LAS u32x2*)(mp + (rd * 8 + (lane & 7)) * 8);
            const unsigned hm = kr0 ? 0xffffffffu : 0u;
            const unsigned a = *(LAS u16*)(mp + 128 + rd * 16 + (lane & 7) * 2), b = *(LAS u16*)(mp + 160 + rd * 16 + (lane & 7) * 2),
                           c = *(LAS u16*)(mp + 192 + rd * 16 + (lane & 7) * 2);
            hal[rd] = (u32x2){(a << 16) & hm, (b | (c << 16)) & hm};
            asm volatile("" : "+v"(skv[rd]), "+v"(hal[rd]));
        }
        WAIT_BAR("vmcnt(0) lgkmcnt(0)");
        u32x2 kt[2][3], vt[2][2];
        float cf[4], kk[11], ko[8];
        u32x4 kpk;
        auto fill = [&](auto kc) {
            constexpr int k = decltype(kc)::value;
            if constexpr (k < 4) {
                constexpr int rd = k >> 1;
                if constexpr ((k & 1) == 0) { kt[rd][0] = ldstr(lp(kam[rd])); kt[rd][1] = ldstr(lp(ka0[rd])); kt[rd][2] = ldstr(lp(ka0[rd]) + 512); }
                else { vt[rd][0] = ldstr(lp(ka0[rd]) + 8192); vt[rd][1] = ldstr(lp(ka0[rd]) + 8192 + 512); }
            } else if constexpr (k < 4 + 2 * 16) {
                constexpr int rd = (k - 4) / 16, s = (k - 4) % 16;
                if constexpr (s == 0) { cf[0] = bflo1(skv[rd][0]); cf[1] = bfhi1(skv[rd][0]); cf[2] = bflo1(skv[rd][1]); cf[3] = bfhi1(skv[rd][1]); }
                else if constexpr (s == 1) {
                    const unsigned a = lane < 8 ? hal[rd][0] : kt[rd][0][0], b = lane < 8 ? hal[rd][1] : kt[rd][0][1];
                    kk[0] = bfhi1(a); kk[1] = bflo1(b); kk[2] = bfhi1(b);
                } else if constexpr (s < 4) {
                    const u32x2 t = kt[rd][s - 1];
                    kk[4 * s - 5] = bflo1(t[0]); kk[4 * s - 4] = bfhi1(t[0]); kk[4 * s - 3] = bflo1(t[1]); kk[4 * s - 2] = bfhi1(t[1]);
                } else if constexpr (s < 12) {
                    constexpr int tp = (s - 4) >> 1, o0 = ((s - 4) & 1) * 4;
#pragma unroll
                    for (int i = o0; i < o0 + 4; ++i) {
                        if (tp == 0) ko[i] = mul1(kk[i], cf[3]);
                        else fmac1(ko[i], kk[i + tp], cf[3 - tp]);
                    }
                } else {
                    constexpr int j = s - 12;
                    const unsigned v = vt[rd][j >> 1][j & 1];
                    kpk[3 - j] = pk(mul1(ko[2 * j + 1], bfhi1(v)), mul1(ko[2 * j], bflo1(v)));
                    if constexpr (j == 3) *(LAS u32x4*)(lp(kwo) + rd * 1024) = kpk;
                }
            }
        };
#pragma unroll
        for (int j = 0; j < 5; ++j) load_frag(0, j);
        f32x4 cs[2][2][4];
        unsigned tch[NTOUCH], sa[2], rdy;
        int cso = lb + L_CSUM + (64 * wn + 4 * hh) * 4, sto = lb + L_STAT + (64 * wm + r32) * 4;
        asm volatile("" : "+v"(cso), "+v"(sto));
        unsigned tco = (unsigned)(gi * 4 + wq + (lane < 28 ? lane : 27) * 448) * 128u;
        asm volatile("" : "+v"(tco));
        static_for([&](auto gc) {
            constexpr int g = decltype(gc)::value, b = g & 1, pp = g & 3;
            SCHED();
            if (pp == 3 && g < 27) { WAIT_BAR("lgkmcnt(0)"); }
            if (g == 27) {
                WAIT_LGKM0();
                if (lane == 0) __hip_atomic_fetch_add((LAS unsigned*)(sm + L_CTL + 12), 1u, __ATOMIC_RELAXED, __HIP_MEMORY_SCOPE_WORKGROUP);
                if (__builtin_amdgcn_readfirstlane(rdy) != 3u) wait_lds_eq(sm + L_CTL + 64, 3u);
                asm volatile("" ::: "memory");
            }
            static_for([&](auto ic) {
                constexpr int i = decltype(ic)::value, ft = i >> 2, m = (i >> 1) & 1, sb = i & 1;
                acc[sb][ft][m] = mfma32(fw[b][ft][m], fx[b][sb], acc[sb][ft][m]);
                SCHED();
                if (g < 27 && i < 5) load_frag(g + 1, i);
                if (g == 26 && i == 5) rdy = __hip_atomic_load((LAS unsigned*)(sm + L_CTL + 64), __ATOMIC_RELAXED, __HIP_MEMORY_SCOPE_WORKGROUP);
                if constexpr (g == 27) {
                    if (i == 0) { sa[0] = *(LAS unsigned*)lp(sto); sa[1] = *(LAS unsigned*)(lp(sto) + 128); }
                    cs[i >> 2][0][i & 3] = *(LAS f32x4*)(lp(cso) + i * 32);
                    cs[i >> 2][1][i & 3] = *(LAS f32x4*)(lp(cso) + 512 + i * 32);
                }
                if constexpr (i == 4 && pp == 2 && g >= 4 && g < 4 + 4 * NTOUCH) {
                    u64 t;
                    asm volatile("s_mov_b64 %1, %3\n\tglobal_load_dword %0, %2, %1" : "=v"(tch[(g - 4) >> 2]), "=&s"(t)
                                 : "v"(tco), "s"((const char*)p.w2 + ((g - 4) >> 2) * (112 * 128)));
                }
                if constexpr (i >= 5 && g >= 7) fill(std::integral_constant<int, (g - 7) * 3 + i - 5>{});
                SCHED();
            }, std::make_integer_sequence<int, 8>{});
        }, std::make_integer_sequence<int, 28>{});
        const int lw = fresh_lane();
        const int w2a0 = lb + w2i + (4 * (lw >> 5) + ((lw & 15) >> 2)) * 128 + (((2 * ((lw >> 4) & 1) + ((lw & 3) >> 1)) ^ (((lw >> 3) & 1) << 2)) * 16) + 8 * (lw & 1);
        const int w2a1 = w2a0 ^ 64;
        u32x4 w2r[12][2];
        auto park = [&](int i) {
            w2r[i][0] = ldstr2(lp(w2a0) + (i / 6) * L_SLOT + (i % 6) * 2048, 1024);
            w2r[i][1] = ldstr2(lp(w2a1) + (i / 6) * L_SLOT + (i % 6) * 2048, 1024);
        };
#pragma unroll
        for (int sb = 0; sb < 2; ++sb) {
            const float ri = __builtin_amdgcn_rcpf(bfhi(sa[sb])) * -1.44269504088896341f, nm = -bflo(sa[sb]), d2 = bfhi(sa[sb]) * bfhi(sa[sb]);
            const f32x2 ri2 = {ri, ri}, nm2 = {nm, nm}, dd2 = {d2, d2};
#pragma unroll
            for (int ft = 0; ft < 2; ++ft) {
#pragma unroll
                for (int t2 = 0; t2 < 2; ++t2) {
                    f32x2 gu[4], ee[4];
#pragma unroll
                    for (int j = 0; j < 4; ++j) {
                        const int r = 8 * t2 + 2 * j;
                        const f32x2 g = __builtin_elementwise_fma(nm2, (f32x2){cs[ft][0][r >> 2][r & 3], cs[ft][0][r >> 2][(r & 3) + 1]}, (f32x2){acc[sb][ft][0][r], acc[sb][ft][0][r + 1]});
                        const f32x2 u = __builtin_elementwise_fma(nm2, (f32x2){cs[ft][1][r >> 2][r & 3], cs[ft][1][r >> 2][(r & 3) + 1]}, (f32x2){acc[sb][ft][1][r], acc[sb][ft][1][r + 1]});
                        const f32x2 t = g * ri2;
                        ee[j] = (f32x2){__builtin_amdgcn_exp2f(t[0]), __builtin_amdgcn_exp2f(t[1])};
                        gu[j] = g * u;
                    }
                    u32x4 st;
#pragma unroll
                    for (int j = 0; j < 4; ++j) {
                        const f32x2 dn = __builtin_elementwise_fma(ee[j], dd2, dd2);
                        const f32x2 hv = gu[j] * (f32x2){__builtin_amdgcn_rcpf(dn[0]), __builtin_amdgcn_rcpf(dn[1])};
                        st[j] = pk(hv[0], hv[1]);
                    }
                    const unsigned fi = (unsigned)((4 * mt + 2 * wm + sb) * 112 + 8 * nt + 4 * wn + 2 * ft + t2);
                    __builtin_amdgcn_raw_buffer_store_b128(st, wsr, WS_H + fi * 1024u + lane * 16u, 0, 16);
                }
            }
        }
        SCHED();
        const u32x4 w2o = w2_off();
        const u64 tag = *(LAS u64*)(sm + L_CTL + 128);
        GAS u64* myflag = (GAS u64*)(p.ws + WS_FH + ((2 * mt + wm) * 28 + 2 * nt + wn) * 128);
        if (!__builtin_amdgcn_readfirstlane(__hip_atomic_load((LAS unsigned*)(sm + L_CTL + 112 + wq * 4), __ATOMIC_RELAXED, __HIP_MEMORY_SCOPE_WORKGROUP))) {
            WAIT_VM(0);
            if (lane == 0) __hip_atomic_store(myflag, tag, RLX_AGENT);
            wait_lds(sm + L_CTL + 112 + wq * 4);
        }
        asm volatile("" ::: "memory");
#pragma unroll
        for (int i = 6; i < 12; ++i) park(i);
        WAIT_VM(0);
        asm volatile("" :: "v"(tch[0]), "v"(tch[1]));
        if (lane == 0) __hip_atomic_store(myflag, tag, RLX_AGENT);
#pragma unroll
        for (int i = 0; i < 6; ++i) park(i);
        asm volatile("" ::: "memory");
        __hip_atomic_store((LAS unsigned*)(sm + L_CTL + 32 + wq * 4), 1u, __ATOMIC_RELAXED, __HIP_MEMORY_SCOPE_WORKGROUP);
        WAIT_LGKM0();
#pragma unroll
        for (int i = 0; i < 12; ++i) asm volatile("" : "+v"(w2r[i][0]), "+v"(w2r[i][1]));
#pragma unroll
        for (int i = 0; i < 3; ++i) pair_w2(w2o, 12 + 2 * i, w2i + i * 4096);
#pragma unroll
        for (int i = 0; i < 3; ++i) pair_w2(w2o, 18 + 2 * i, w2i + L_SLOT + i * 4096);
        {
            GAS u64* fp = (GAS u64*)(p.ws + WS_FH + (m64 * 28 + 7 * wq + (lw < 7 ? lw : 0)) * 128);
#pragma unroll 1
            for (unsigned spins = 0; spins < SPIN_CAP; ++spins) {
                if (__all(__hip_atomic_load(fp, RLX_AGENT) == tag)) break;
                __builtin_amdgcn_s_sleep(4);
            }
            WAIT_VM(0);
            __builtin_amdgcn_fence(__ATOMIC_ACQUIRE, "wavefront");
        }
        const unsigned hb = WS_H + (unsigned)(2 * m64 * 112 + 28 * wq) * 1024u + lw * 16u;
        u32x4 hf[28][2];
        auto load_h = [&](int i, int j) {
            int t = i + rot2; t = t >= 28 ? t - 28 : t;
            hf[i][j] = __builtin_amdgcn_raw_buffer_load_b128(wsr, hb + j * (112u * 1024u), t * 1024, 16);
        };
#pragma unroll
        for (int i = 0; i < HD; ++i) { load_h(i, 0); load_h(i, 1); }
        f32x16 ac2[2][2];
#pragma unroll
        for (int i = 0; i < 4; ++i)
#pragma unroll
            for (int r = 0; r < 16; ++r) ac2[i >> 1][i & 1][r] = 0.f;
        u32x4 a0 = w2r[0][0], a1 = w2r[0][1];
        static_for([&](auto ic) {
            constexpr int i = decltype(ic)::value, n = i + 1;
            SCHED();
            if (i == YSTEP) __hip_atomic_store((LAS unsigned*)(sm + L_CTL + 96 + wq * 4), 1u, __ATOMIC_RELAXED, __HIP_MEMORY_SCOPE_WORKGROUP);
            if (i == 23) wait_lds(sm + L_CTL + 80 + wq * 4);
            u32x4 b0, b1;
            if constexpr (n < 12) { b0 = w2r[n][0]; b1 = w2r[n][1]; }
            else if constexpr (n < 28) {
                constexpr int so = n < 24 ? ((n - 12) / 6) * L_SLOT + ((n - 12) % 6) * 2048 : (n - 24) * 2048;
                const int lo = n < 24 ? 0 : latei - w2i;
                b0 = ldstr2(lp(w2a0 + lo) + so, 1024);
                b1 = ldstr2(lp(w2a1 + lo) + so, 1024);
            }
            SCHED();
            ac2[0][0] = mfma32(a0, hf[i][0], ac2[0][0]);
            SCHED();
            constexpr int l0 = i < HG ? HD + 2 * i : HD + HG + i;
            if constexpr (l0 < 28) load_h(l0, 0);
            SCHED();
            ac2[1][0] = mfma32(a1, hf[i][0], ac2[1][0]);
            SCHED();
            if constexpr (l0 < 28) load_h(l0, 1);
            SCHED();
            ac2[0][1] = mfma32(a0, hf[i][1], ac2[0][1]);
            SCHED();
            if constexpr (i < HG) load_h(l0 + 1, 0);
            SCHED();
            ac2[1][1] = mfma32(a1, hf[i][1], ac2[1][1]);
            SCHED();
            if constexpr (i < HG) load_h(l0 + 1, 1);
            a0 = b0; a1 = b1;
        }, std::make_integer_sequence<int, 28>{});
        SCHED();
        WAIT_LGKM0();
#pragma unroll
        for (int v = 0; v < 16; ++v) {
            const f32x16& t = ac2[v >> 3][(v >> 2) & 1];
            const int r0 = 4 * (v & 3);
            *(LAS f32x4*)(sm + w2i + part(v >> 3, (v >> 2) & 1, v & 3) + lw * 16) = (f32x4){t[r0], t[r0 + 1], t[r0 + 2], t[r0 + 3]};
        }
        WAIT_BAR("lgkmcnt(0)");
    } else {
        u32x4 xvo;
#pragma unroll
        for (int j = 0; j < 4; ++j) xvo[j] = (unsigned)((8 * j + (lane >> 3)) * D + ((((lane & 7) ^ (lane >> 4)) ^ (4 * (j & 1))) * 8)) * 2u - 1024u * j;
        const u16* xb = sptr(p.x, (mt * 128 + wq * 32) * D);
        auto group_x = [&](int si) { glds16x4(sptr(xb, kst(si) * 64), xvo, lb + ring(si) + wq * 4096); };
        group_x(0);
        group_x(1); group_w(1, 0); group_w(1, 1);
        const int cbi = L_CONV + chn * C_CH;
        LAS char* cb = sm + cbi;
        unsigned kvo[2], uso;
#pragma unroll
        for (int rd = 0; rd < 2; ++rd) kvo[rd] = (unsigned)((lane >> 3) * D + (((lane & 7) ^ (((lane >> 4) & 1) | (((2 * wq + rd) & 3) << 1))) * 8)) * 2u;
        uso = WS_U + (unsigned)(kc0 + wq * 16 + (lane >> 3)) * 8192u + (unsigned)(1984 - kr0 + (lane & 7) * 8) * 2u;
        int kro = lb + L_KVO + wq * 2048 + (lane >> 3) * 128 + (((lane & 7) ^ (lane >> 3)) * 16);
        const int cc = 2 * par + ((lane >> 2) & 1);
        int zo = cbi + C_U + par * C_US + (248 + (lane < 36 ? lane : 35)) * 16;
        zo = lane < 8 ? cbi + cc * C_HS + (lane & 3) * 16 : zo;
        u32x4 w2o = w2_off();
        const unsigned fo = lane * 16u;
        unsigned ao = (wq < 2 ? WS_CSUM + (unsigned)(nt * 2 + wq) * 1024u : WS_STATS + mt * 1024u) + fo;
        asm volatile("" : "+v"(zo), "+v"(w2o), "+v"(kvo[0]), "+v"(kvo[1]), "+v"(uso), "+v"(kro), "+v"(ao));
        unsigned eo = (lane & 1) * 4u;
        asm volatile("" : "+v"(eo));
        WAIT_VM(16);
        unsigned tag32, taghi;
        u32x4 bval, ug[2];
        unsigned dbits = *(LAS u16*)(sm + wave * 1024 + 240 + (dch & 7) * 2);
        {
            const u32x4 h0 = lds128(sm + wave * 1024 + 224);
            const bool s1 = !(cc & 1), s2 = !(cc & 2);
            const unsigned a0 = s1 ? 0u : h0[0], a1 = s1 ? h0[0] : h0[1], a2 = s1 ? h0[1] : h0[2], a3 = s1 ? h0[2] : h0[3];
            const unsigned lm = (lane == 3 || lane == 7) ? 0xffffffffu : 0u;
            bval = (u32x4){(s2 ? 0u : a0) & lm, (s2 ? 0u : a1) & lm, (s2 ? a0 : a2) & lm, (s2 ? a1 : a3) & lm};
            asm volatile("" : "+v"(bval), "+v"(dbits));
        }
        auto kv_store = [&](int rd) {
            LAS unsigned* kp = (LAS unsigned*)(lp(kro) + rd * 1024);
            ug[0][1] = kp[0]; asm volatile("" ::: "memory"); ug[0][3] = kp[1]; asm volatile("" ::: "memory");
            ug[1][1] = kp[2]; asm volatile("" ::: "memory"); ug[1][3] = kp[3];
            __builtin_amdgcn_raw_buffer_store_b128(ug[0], wsr, uso, rd * 65536, 16);
            __builtin_amdgcn_raw_buffer_store_b128(ug[1], wsr, uso, rd * 65536 + 4096, 16);
        };
        WAIT_BAR("vmcnt(12) lgkmcnt(0)");
#pragma unroll
        for (int w = 0; w < 5; ++w) {
            if (w == 3) glds4c(p.ws + WS_EPOCH, eo, lb + L_RAW + wq * 1024);
            group_x(w + 2); group_w(w + 2, 0); group_w(w + 2, 1);
            if (w == 0) {
#pragma unroll
                for (int j = 0; j < 2; ++j) {
                    glds16(sptr(p.k, (kr0 + (2 * wq + j) * 8) * D + kc0), kvo[j], lb + L_KV + (2 * wq + j) * 1024);
                    glds16(sptr(p.v, (kr0 + (2 * wq + j) * 8) * D + kc0), kvo[j], lb + L_KV + 8192 + (2 * wq + j) * 1024);
                }
            }
            if (w == 4) {
                const u32x2 ev = *(LAS u32x2*)(sm + L_RAW + wq * 1024);
                tag32 = __builtin_amdgcn_readfirstlane(ev[0]) ^ (unsigned)TAG_SALT;
                taghi = __builtin_amdgcn_readfirstlane(ev[1]) ^ (unsigned)(TAG_SALT >> 32);
                asm volatile("s_nop 1" : "+s"(tag32), "+s"(taghi));
                ug[0][0] = vmov1(tag32); ug[0][2] = vmov1(tag32); ug[1][0] = vmov1(tag32); ug[1][2] = vmov1(tag32);
                if (lane < 34) *(LAS unsigned*)(sm + L_CTL + lane * 4) = lane < 32 ? 0u : lane == 32 ? tag32 : taghi;
            }
            if (w == 4) kv_store(0);
            if (w == 0) WAIT_BAR("vmcnt(16) lgkmcnt(0)"); else WAIT_BAR("vmcnt(12) lgkmcnt(0)");
        }
        glds16c(p.ws, ao, lb + L_RAW + wq * 1024);
#pragma unroll
        for (int i = 0; i < 3; ++i) pair_w2(w2o, 2 * i, w2i + i * 4096);
        kv_store(1);
        WAIT_BAR("vmcnt(12) lgkmcnt(0)");
        asm volatile("" ::: "memory");
        if (lane == 0) __hip_atomic_fetch_add((LAS unsigned*)(sm + L_CTL + 12), 1u, __ATOMIC_RELAXED, __HIP_MEMORY_SCOPE_WORKGROUP);
        if (wq < 3) {
            u32x4 av = lds128(sm + L_RAW + wq * 1024 + lane * 16);
#pragma unroll 1
            for (unsigned spins = 0; spins < SPIN_CAP; ++spins) {
                if (__all(((av[0] ^ tag32) | (av[2] ^ tag32)) == 0u)) break;
                asm volatile("" ::: "memory");
                av = __builtin_amdgcn_raw_buffer_load_b128(wsr, ao, 0, 16);
                asm volatile("" : "+v"(av));
            }
            *(LAS u32x2*)(sm + L_CSUM + wq * 512 + lane * 8) = (u32x2){av[1], av[3]};
            asm volatile("" ::: "memory");
            if (lane == 0) __hip_atomic_fetch_add((LAS unsigned*)(sm + L_CTL + 64), 1u, __ATOMIC_RELAXED, __HIP_MEMORY_SCOPE_WORKGROUP);
        }
#pragma unroll
        for (int i = 0; i < 3; ++i) pair_w2(w2o, 6 + 2 * i, w2i + L_SLOT + i * 4096);
        WAIT_VM(12);
        wait_lds_eq(sm + L_CTL + 12, 8u);
        __builtin_amdgcn_s_setprio(2);
        const unsigned ubase = WS_U + (unsigned)dch * 8192u;
        glds16x2c(p.ws + ubase + par * 2048, fo, lb + cbi + C_U + par * 2048);
        glds16x2c(p.ws + ubase + 4096 + par * 2048, fo, lb + cbi + C_U + C_US + par * 2048);
        glds4c(p.ws + ubase + (1 - par) * 2048, eo, lb + L_UNX + wq * 256);
        {
            const u32x4 fvo = {fo, fo, fo, lane == 63 ? 62u * 16u : fo};
#pragma unroll
            for (int c = 0; c < 2; ++c) glds16x4(sptr(p.hk, dch * S + 4 * par + 2 * c + 2), fvo, lb + cbi + (2 * par + c) * C_HS + 64);
        }
        *(LAS u32x4*)(sm + zo) = bval;
        WAIT_VM(13);
        __hip_atomic_store((LAS unsigned*)(sm + L_CTL + 112 + wq * 4), 1u, __ATOMIC_RELAXED, __HIP_MEMORY_SCOPE_WORKGROUP);
        WAIT_VM(8);
        {
            u32x4 ua[2], ub[2]; u32x2 un[2];
#pragma unroll
            for (int rd = 0; rd < 2; ++rd) {
                const int x8 = 128 * par + 64 * rd + lane;
                ua[rd] = lds128(cb + C_U + x8 * 16); ub[rd] = lds128(cb + C_U + C_US + x8 * 16);
                un[rd] = *(LAS u32x2*)((rd == 1 && lane == 63) ? sm + L_UNX + wq * 256 : cb + C_U + x8 * 16 + 16);
            }
#pragma unroll 1
            for (unsigned spins = 0; spins < SPIN_CAP; ++spins) {
                unsigned df = 0;
#pragma unroll
                for (int rd = 0; rd < 2; ++rd) df |= (ua[rd][0] ^ tag32) | (ua[rd][2] ^ tag32) | (ub[rd][0] ^ tag32) | (ub[rd][2] ^ tag32) | (un[rd][0] ^ tag32);
                if (__all(df == 0u)) break;
                __builtin_amdgcn_s_sleep(4);
                asm volatile("" ::: "memory");
#pragma unroll
                for (int rd = 0; rd < 2; ++rd) {
                    const int x8 = 128 * par + 64 * rd + lane;
                    const unsigned uo = ubase + (unsigned)x8 * 16u;
                    ua[rd] = __builtin_amdgcn_raw_buffer_load_b128(wsr, uo, 0, 16);
                    ub[rd] = __builtin_amdgcn_raw_buffer_load_b128(wsr, uo + 4096u, 0, 16);
                    un[rd] = __builtin_amdgcn_raw_buffer_load_b64(wsr, x8 == 255 ? uo : uo + 16u, 0, 16);
                }
                asm volatile("" : "+v"(ua[0]), "+v"(ub[0]), "+v"(un[0]), "+v"(ua[1]), "+v"(ub[1]), "+v"(un[1]));
            }
#pragma unroll
            for (int rd = 0; rd < 2; ++rd) {
                const int x8 = 128 * par + 64 * rd + lane;
                const unsigned nx = x8 == 255 ? 0u : un[rd][1];
                *(LAS u32x4*)(cb + C_U + x8 * 16) = (u32x4){__builtin_amdgcn_alignbit(ua[rd][3], ua[rd][1], 16), __builtin_amdgcn_alignbit(ub[rd][1], ua[rd][3], 16),
                                                            __builtin_amdgcn_alignbit(ub[rd][3], ub[rd][1], 16), __builtin_amdgcn_alignbit(nx, ub[rd][3], 16)};
                *(LAS u32x4*)(cb + C_U + C_US + x8 * 16) = (u32x4){ua[rd][1], ua[rd][3], ub[rd][1], ub[rd][3]};
            }
        }
        asm volatile("s_waitcnt vmcnt(0) lgkmcnt(0)" ::: "memory");
        __hip_atomic_store((LAS unsigned*)(sm + L_CTL + 16 + wq * 4), 1u, __ATOMIC_RELAXED, __HIP_MEMORY_SCOPE_WORKGROUP);
        u32x4 lw2[8], xv[2];
        {
            const __amdgpu_buffer_rsrc_t wr2 = __builtin_amdgcn_make_buffer_rsrc((void*)p.w2, 0, F * D * 2, 0x00020000);
#pragma unroll
            for (int j = 0; j < 8; ++j) {
                int t = 24 + (j >> 2) * 2 + rot2; t = t >= 28 ? t - 28 : t;
                lw2[j] = __builtin_amdgcn_raw_buffer_load_b128(wr2, w2o[0], ((wq * 448 + t * 16 + (j & 3) * 8) * D + n64 * 64) * 2, 0);
            }
        }
        const int lf = fresh_lane();
        const int srow = 64 * m64 + 16 * wq + 2 * (lf >> 3);
        const unsigned xoff = (unsigned)(srow * D + 64 * n64 + 8 * (lf & 7)) * 2u;
        {
            const __amdgpu_buffer_rsrc_t xr = __builtin_amdgcn_make_buffer_rsrc((void*)p.x, 0, S * D * 2, 0x00020000);
            xv[0] = __builtin_amdgcn_raw_buffer_load_b128(xr, xoff, 0, 0);
            xv[1] = __builtin_amdgcn_raw_buffer_load_b128(xr, xoff, D * 2, 0);
        }
        SCHED();
        wait_lds(sm + L_CTL + 16 + (wq ^ 1) * 4);
        const int n16 = lane & 15, kg = lane >> 4;
        f32x4 ca[4];
#pragma unroll
        for (int j = 0; j < 4; ++j) ca[j] = (f32x4){0.f, 0.f, 0.f, 0.f};
        int apo = lb + cbi + (n16 & 3) * C_HS + ((n16 >> 2) + kg) * 16;
        int bpo = lb + cbi + C_U + (n16 & 1) * C_US + (252 - 4 * (n16 >> 1) + kg) * 16 - 7 * 512;
        u32x4 fa[2][8], fb[2][8];
#pragma unroll
        for (int i = 0; i < 8; ++i) { fa[0][i] = lds128(lp(apo) + i * 512); fb[0][i] = lds128(lp(bpo) + (7 - i) * 512); }
        const bool odd = lf & 1;
        int ysrc = (2 * (lf >> 3) + (lf & 1) + 16 * ((lf >> 1) & 3)) * 4;
        unsigned yso = WS_Y + (unsigned)dch * 8192u + lf * 16u;
        f32x4 kd[4];
        {
            const float dval = bflo(dbits);
            const int sl0 = 32 * ((lf & 15) >> 1) + 8 * (lf >> 4) + 4 * (lf & 1);
#pragma unroll
            for (int j = 0; j < 4; ++j) {
                const int tj = (j == 0 ? 0 : j == 1 ? 3 : j == 2 ? 4 : 7) ^ par;
                const u32x2 kvr = *(LAS u32x2*)(cb + C_U + C_US + (2044 - 256 * tj - sl0) * 2);
                const unsigned ka = pk(bfhi(kvr[1]) * dval, bflo(kvr[1]) * dval), kb = pk(bfhi(kvr[0]) * dval, bflo(kvr[0]) * dval);
                kd[j] = (f32x4){bflo(ka), bfhi(ka), bflo(kb), bfhi(kb)};
                asm volatile("" : "+v"(kd[j]));
            }
            asm volatile("" : "+v"(ysrc), "+v"(yso));
        }
        wait_lds(sm + L_CTL + 32 + wq * 4);
        auto conv_all = [&](auto pc_) {
            constexpr int P = decltype(pc_)::value;
            static_for([&](auto rc) {
                constexpr int r = decltype(rc)::value, c = r & 1, n = c ^ 1;
                static_for([&](auto kc) {
                    constexpr int k = decltype(kc)::value;
                    constexpr int T[4] = {P, 3 - P, 4 + P, 7 - P};
                    constexpr int j = k <= T[0] ? 0 : k <= T[0] + T[1] + 1 ? 1 : k <= T[0] + T[1] + T[2] + 2 ? 2 : 3;
                    constexpr int pp = k - (j == 0 ? 0 : j == 1 ? T[0] + 1 : j == 2 ? T[0] + T[1] + 2 : T[0] + T[1] + T[2] + 3);
                    ca[j] = mfma16(fa[c][pp], fb[c][T[j] - pp], ca[j]);
                    SCHED();
                    if constexpr (r < 7 && k < 2 * (T[3] + 1)) {
                        if constexpr (k & 1) fb[n][k >> 1] = lds128(lp(bpo) + (r + 1) * 64 + (7 - (k >> 1)) * 512);
                        else fa[n][k >> 1] = lds128(lp(apo) + (r + 1) * 64 + (k >> 1) * 512);
                    }
                    SCHED();
                }, std::make_integer_sequence<int, 18>{});
                asm volatile("" : "+v"(apo), "+v"(bpo));
            }, std::make_integer_sequence<int, 8>{});
        };
        if (par) conv_all(std::integral_constant<int, 1>{}); else conv_all(std::integral_constant<int, 0>{});
        SCHED();
        {
            float r0[4], r1[4];
#pragma unroll
            for (int j = 0; j < 4; ++j) { r0[j] = dpp_xor1(odd ? ca[j][0] : ca[j][2]); r1[j] = dpp_xor1(odd ? ca[j][1] : ca[j][3]); }
            unsigned rp[4][2];
#pragma unroll
            for (int j = 0; j < 4; ++j) {
                const unsigned ya = pk(odd ? r0[j] : ca[j][0], odd ? ca[j][2] : r0[j]), yb = pk(odd ? r1[j] : ca[j][1], odd ? ca[j][3] : r1[j]);
                rp[j][0] = __builtin_amdgcn_ds_bpermute(ysrc, pk(add1(bflo1(ya), kd[j][0]), add1(bfhi1(ya), kd[j][1])));
                rp[j][1] = __builtin_amdgcn_ds_bpermute(ysrc, pk(add1(bflo1(yb), kd[j][2]), add1(bfhi1(yb), kd[j][3])));
            }
#pragma unroll
            for (int j = 0; j < 4; ++j)
                __builtin_amdgcn_raw_buffer_store_b128((u32x4){vmov1(tag32 ^ Y_TAG), rp[j][0], vmov1(tag32 ^ Y_TAG), rp[j][1]}, wsr, yso,
                                                       ((j == 0 ? 0 : j == 1 ? 3 : j == 2 ? 4 : 7) ^ par) * 1024, 16);
        }
        WAIT_LGKM0();
        if (lane == 0) __hip_atomic_fetch_add((LAS unsigned*)(sm + L_CTL + 8), 1u, __ATOMIC_RELAXED, __HIP_MEMORY_SCOPE_WORKGROUP);
#pragma unroll 1
        for (unsigned spins = 0; spins < (1u << 22); ++spins) {
            if (__builtin_amdgcn_readfirstlane(__hip_atomic_load((LAS unsigned*)(sm + L_CTL + 8), __ATOMIC_RELAXED, __HIP_MEMORY_SCOPE_WORKGROUP)) == 4u) break;
            __builtin_amdgcn_s_sleep(1);
        }
        asm volatile("" ::: "memory");
#pragma unroll
        for (int j = 0; j < 8; ++j) *(LAS u32x4*)(sm + latei + j * 1024 + lf * 16) = lw2[j];
        asm volatile("" ::: "memory");
        __hip_atomic_store((LAS unsigned*)(sm + L_CTL + 80 + wq * 4), 1u, __ATOMIC_RELAXED, __HIP_MEMORY_SCOPE_WORKGROUP);
        wait_lds(sm + L_CTL + 96 + wq * 4);
        u32x2 yv[8];
        u32x4 stv;
        const int yl = L_CONV + 32768 + wq * 4096;
        const unsigned yg = (unsigned)(((lf >> 2) & 7) * 8 + (lf >> 5)) * 8192u + (unsigned)((lf & 3) ^ (((lf >> 4) & 1) << 1)) * 16u;
        const u32x4 yvo = {yg, yg + (16384u - 1024u), yg + 2 * (16384u - 1024u), yg + 3 * (16384u - 1024u)};
        const char* ybase = p.ws + WS_Y + (unsigned)(64 * n64) * 8192u + (unsigned)(64 * m64 + 16 * wq) * 4u;
        const int yr = lb + yl + (lf & 7) * 64 + (((lf >> 3) * 8) ^ (((lf >> 2) & 1) << 5));
#pragma unroll 1
        for (unsigned spins = 0; spins < SPIN_CAP; ++spins) {
            glds16x4c(ybase, yvo, lb + yl);
            stv = __builtin_amdgcn_raw_buffer_load_b128(wsr, WS_STATS + (unsigned)srow * 8u, 0, 16);
            WAIT_VM(0);
#pragma unroll
            for (int i = 0; i < 8; ++i) yv[i] = *(LAS u32x2*)(lp(yr) + i * 512);
            unsigned df = (stv[0] ^ tag32) | (stv[2] ^ tag32);
#pragma unroll
            for (int i = 0; i < 8; ++i) df |= yv[i][0] ^ (tag32 ^ Y_TAG);
            if (__all(df == 0u)) break;
            __builtin_amdgcn_s_sleep(8);
        }
        __builtin_amdgcn_s_setprio(0);
        f32x2 sf[8], xf[8];
#pragma unroll
        for (int e = 0; e < 2; ++e) {
            const float nmb = -bflo(stv[1 + 2 * e]);
            float ri = __builtin_amdgcn_rcpf(bfhi(stv[1 + 2 * e]));
            asm volatile("s_nop 1" : "+v"(ri));
#pragma unroll
            for (int j = 0; j < 4; ++j) {
                const int i = 4 * e + j;
                const unsigned xd = xv[e][j];
                xf[i] = (f32x2){bflo1(xd), bfhi1(xd)};
                const unsigned tp = pk(add1(xf[i][0], nmb), add1(xf[i][1], nmb));
                const unsigned np = pk(mul1(bflo1(tp), ri), mul1(bfhi1(tp), ri));
                sf[i] = (f32x2){mul1(bflo1(np), e ? bfhi1(yv[2 * j][1]) : bflo1(yv[2 * j][1])), mul1(bfhi1(np), e ? bfhi1(yv[2 * j + 1][1]) : bflo1(yv[2 * j + 1][1]))};
                asm volatile("" : "+v"(sf[i]), "+v"(xf[i]));
            }
        }
        const int rm = lb + L_W2 + part((lf >> 2) & 1, wq >> 1, lf & 3) + (16 * (wq & 1) + 2 * (lf >> 3)) * 16;
        WAIT_BAR("lgkmcnt(0)");
        f32x4 o[4];
#pragma unroll
        for (int j = 0; j < 4; ++j) {
            LAS char* rp = lp(rm) + (j >> 1) * 16 + (j & 1) * 512;
            o[j] = (*(LAS f32x4*)rp + *(LAS f32x4*)(rp + 12288)) + (*(LAS f32x4*)(rp + 24576) + *(LAS f32x4*)(rp + 36864));
        }
        f32x2 so[8], ev[8];
#pragma unroll
        for (int i = 0; i < 8; ++i) {
            const f32x2 ov = {o[i >> 1][(i & 1) * 2], o[i >> 1][(i & 1) * 2 + 1]};
            const f32x2 t = ov * (f32x2){-1.44269504088896341f, -1.44269504088896341f};
            ev[i] = (f32x2){__builtin_amdgcn_exp2f(t[0]), __builtin_amdgcn_exp2f(t[1])};
            so[i] = sf[i] * ov;
        }
        SCHED();
        u32x4 st[2];
#pragma unroll
        for (int i = 0; i < 8; ++i) {
            const f32x2 dn = ev[i] + (f32x2){1.f, 1.f};
            const f32x2 r = __builtin_elementwise_fma(so[i], (f32x2){__builtin_amdgcn_rcpf(dn[0]), __builtin_amdgcn_rcpf(dn[1])}, xf[i]);
            st[i >> 2][i & 3] = pk(r[0], r[1]);
        }
        const __amdgpu_buffer_rsrc_t orr = __builtin_amdgcn_make_buffer_rsrc((void*)p.out, 0, S * D * 2, 0x00020000);
        __builtin_amdgcn_raw_buffer_store_b128(st[0], orr, xoff, 0, 16);
        __builtin_amdgcn_raw_buffer_store_b128(st[1], orr, xoff, D * 2, 16);
    }
}

extern "C" void kernel_launch(void* const* d_in, const int* in_sizes, int n_in,
                              void* d_out, int out_size, void* d_ws, size_t ws_size,
                              hipStream_t stream) {
    Params p;
    p.x = (const u16*)d_in[0]; p.k = (const u16*)d_in[1]; p.v = (const u16*)d_in[2];
    p.sk = (const u16*)d_in[3]; p.hk = (const u16*)d_in[4]; p.dv = (const u16*)d_in[5];
    p.w1 = (const u16*)d_in[7]; p.w3 = (const u16*)d_in[8]; p.w2 = (const u16*)d_in[9];
    p.out = (u16*)d_out; p.ws = (char*)d_ws;
    (void)hipFuncSetAttribute((const void*)fused_kernel, hipFuncAttributeMaxDynamicSharedMemorySize, L_TOTAL);
    fused_kernel<<<256, 512, L_TOTAL, stream>>>(p);
}
```

```cpp
#include <hip/hip_runtime.h>
#include <stdint.h>
#include <utility>

typedef unsigned short u16;
typedef unsigned long long u64;
typedef short s16x4 __attribute__((ext_vector_type(4)));
typedef __bf16 bf16x2 __attribute__((ext_vector_type(2)));
typedef __bf16 bf16x8 __attribute__((ext_vector_type(8)));
typedef float f32x2 __attribute__((ext_vector_type(2)));
typedef float f32x4 __attribute__((ext_vector_type(4)));
typedef float f32x16 __attribute__((ext_vector_type(16)));
typedef unsigned u32x2 __attribute__((ext_vector_type(2)));
typedef unsigned u32x4 __attribute__((ext_vector_type(4)));
#define LAS __attribute__((address_space(3)))
#define GAS __attribute__((address_space(1)))
#define RLX_AGENT __ATOMIC_RELAXED, __HIP_MEMORY_SCOPE_AGENT

constexpr int S = 2048, D = 448, F = 1792;

constexpr unsigned WS_EPOCH = 0;
constexpr unsigned WS_FH = 65536;
constexpr unsigned WS_STATS = 8192;
constexpr unsigned WS_CSUM = 24576;
constexpr unsigned WS_U = 262144;
constexpr unsigned WS_Y = WS_U;
constexpr unsigned WS_H = WS_Y + D * S * 4;
constexpr unsigned WS_END = WS_H + 64 * 112 * 1024;

constexpr int L_KV = 0;
constexpr int L_CTL = 0;
constexpr int L_CSUM = 256;
constexpr int L_STAT = 1280;
constexpr int L_RAW = 2048;
constexpr int L_KVO = 8192;
constexpr int L_UNX = 6144;
constexpr int L_SLOT = 49152;
constexpr int L_CONV = 12288;
constexpr int C_CH = 26112, C_HS = 4160, C_U = 16640, C_US = 4640;
constexpr int L_W2 = 16384 + L_SLOT;
constexpr int L_TOTAL = 163840;
__host__ __device__ constexpr int part(int db, int sb, int a) { return db * L_SLOT + sb * 4352 + a * 1024 + (a & 1) * 16 + (a >> 1) * 128; }
__host__ __device__ constexpr int ring(int stage) { return 16384 + (stage % 3) * L_SLOT; }
constexpr unsigned CODE_PREFETCH = 36864;
constexpr int HD = 6, HG = 4;
constexpr int NTOUCH = 2;
constexpr int YSTEP = 18;

constexpr u64 TAG_SALT = 0x9E3779B97F4A7C15ull;
constexpr unsigned Y_TAG = 0x80000000u;
constexpr unsigned SPIN_CAP = 1u << 18;

struct Params {
    const u16* x; const u16* k; const u16* v; const u16* sk; const u16* hk; const u16* dv;
    const u16* w1; const u16* w3; const u16* w2; u16* out; char* ws;
};

__device__ __forceinline__ float bflo(unsigned p) { return __uint_as_float(p << 16); }
__device__ __forceinline__ float bfhi(unsigned p) { return __uint_as_float(p & 0xffff0000u); }
__device__ __forceinline__ float bflo1(unsigned p) { float r; asm("v_lshlrev_b32 %0, 16, %1" : "=v"(r) : "v"(p)); return r; }
__device__ __forceinline__ float bfhi1(unsigned p) { float r; asm("v_and_b32 %0, 0xffff0000, %1" : "=v"(r) : "v"(p)); return r; }
__device__ __forceinline__ float mul1(float a, float b) { float r; asm("v_mul_f32 %0, %1, %2" : "=v"(r) : "v"(a), "v"(b)); return r; }
__device__ __forceinline__ void fmac1(float& d, float a, float b) { asm("v_fmac_f32 %0, %1, %2" : "+v"(d) : "v"(a), "v"(b)); }
__device__ __forceinline__ float dpp_xor1(float v) {
    return __int_as_float(__builtin_amdgcn_update_dpp(0, __float_as_int(v), 0xB1, 0xf, 0xf, true));
}
__device__ __forceinline__ float add1(float a, float b) { float r; asm("v_add_f32 %0, %1, %2" : "=v"(r) : "v"(a), "v"(b)); return r; }
__device__ __forceinline__ unsigned vmov1(unsigned s) { unsigned r; asm volatile("v_mov_b32 %0, %1" : "=v"(r) : "s"(s)); return r; }
__device__ __forceinline__ unsigned pk(float a, float b) {
    f32x2 f = {a, b};
    return __builtin_bit_cast(unsigned, __builtin_convertvector(f, bf16x2));
}
__device__ __forceinline__ float rbf(float a) { return bflo(pk(a, a)); }
__device__ __forceinline__ float dot2(unsigned a, unsigned b, float c) {
    return __builtin_amdgcn_fdot2_f32_bf16(__builtin_bit_cast(bf16x2, a), __builtin_bit_cast(bf16x2, b), c, false);
}
__device__ __forceinline__ void glds16(const void* base, unsigned voff, unsigned ldst) {
    u64 t;
    asm volatile("s_mov_b32 m0, %3\n\ts_mov_b64 %0, %2\n\tglobal_load_lds_dwordx4 %1, %0 nt" : "=&s"(t) : "v"(voff), "s"(base), "s"(ldst));
}
__device__ __forceinline__ void glds16x4(const void* base, u32x4 vo, unsigned ldst) {
    u64 t;
    asm volatile("s_mov_b32 m0, %6\n\ts_mov_b64 %0, %5\n\tglobal_load_lds_dwordx4 %1, %0\n\tglobal_load_lds_dwordx4 %2, %0 offset:1024\n\t"
                 "global_load_lds_dwordx4 %3, %0 offset:2048\n\tglobal_load_lds_dwordx4 %4, %0 offset:3072"
                 : "=&s"(t) : "v"(vo[0]), "v"(vo[1]), "v"(vo[2]), "v"(vo[3]), "s"(base), "s"(ldst));
}
__device__ __forceinline__ void glds16x4c(const void* base, u32x4 vo, unsigned ldst) {
    u64 t;
    asm volatile("s_mov_b32 m0, %6\n\ts_mov_b64 %0, %5\n\tglobal_load_lds_dwordx4 %1, %0 sc1\n\tglobal_load_lds_dwordx4 %2, %0 offset:1024 sc1\n\t"
                 "global_load_lds_dwordx4 %3, %0 offset:2048 sc1\n\tglobal_load_lds_dwordx4 %4, %0 offset:3072 sc1"
                 : "=&s"(t) : "v"(vo[0]), "v"(vo[1]), "v"(vo[2]), "v"(vo[3]), "s"(base), "s"(ldst) : "memory");
}
__device__ __forceinline__ void glds16x2c(const void* base, unsigned voff, unsigned ldst) {
    u64 t;
    asm volatile("s_mov_b32 m0, %3\n\ts_mov_b64 %0, %2\n\tglobal_load_lds_dwordx4 %1, %0 sc1\n\tglobal_load_lds_dwordx4 %1, %0 offset:1024 sc1"
                 : "=&s"(t) : "v"(voff), "s"(base), "s"(ldst));
}
__device__ __forceinline__ void glds16c(const void* base, unsigned voff, unsigned ldst) {
    u64 t;
    asm volatile("s_mov_b32 m0, %3\n\ts_mov_b64 %0, %2\n\tglobal_load_lds_dwordx4 %1, %0 sc1" : "=&s"(t) : "v"(voff), "s"(base), "s"(ldst));
}
__device__ __forceinline__ void glds4c(const void* base, unsigned voff, unsigned ldst) {
    u64 t;
    asm volatile("s_mov_b32 m0, %3\n\ts_mov_b64 %0, %2\n\tglobal_load_lds_dword %1, %0 sc1" : "=&s"(t) : "v"(voff), "s"(base), "s"(ldst));
}
__device__ __forceinline__ const u16* sptr(const u16* base, int off) {
    const u16* r = base + __builtin_amdgcn_readfirstlane(off);
    asm("" : "+s"(r));
    return r;
}
__device__ __forceinline__ u64 vsel(bool c, const void* a, u64 b) {
    u64 va = (u64)a;
    asm("" : "+v"(va), "+v"(b));
    return c ? va : b;
}
__device__ __forceinline__ LAS char* lp(int a) { return (LAS char*)(size_t)(unsigned)a; }
__device__ __forceinline__ u32x4 lds128(LAS char* p) { return *(LAS u32x4*)p; }
__device__ __forceinline__ u32x2 ldstr(LAS char* p) {
    return __builtin_bit_cast(u32x2, __builtin_amdgcn_ds_read_tr16_b64_v4i16((LAS s16x4*)p));
}
__device__ __forceinline__ u32x4 ldstr2(LAS char* p, int off) {
    u32x2 a = ldstr(p), b = ldstr(p + off);
    return (u32x4){a.x, a.y, b.x, b.y};
}
__device__ __forceinline__ f32x16 mfma32(u32x4 a, u32x4 b, f32x16 c) {
    return __builtin_amdgcn_mfma_f32_32x32x16_bf16(__builtin_bit_cast(bf16x8, a), __builtin_bit_cast(bf16x8, b), c, 0, 0, 0);
}
__device__ __forceinline__ f32x4 mfma16(u32x4 a, u32x4 b, f32x4 c) {
    return __builtin_amdgcn_mfma_f32_16x16x32_bf16(__builtin_bit_cast(bf16x8, a), __builtin_bit_cast(bf16x8, b), c, 0, 0, 0);
}
template <class Fn, int... I>
__device__ __forceinline__ void static_for(Fn&& fn, std::integer_sequence<int, I...>) { (fn(std::integral_constant<int, I>{}), ...); }
#define WAIT_VM(n) asm volatile("s_waitcnt vmcnt(" #n ")" ::: "memory")
#define WAIT_LGKM0() asm volatile("s_waitcnt lgkmcnt(0)" ::: "memory")
#define WAIT_BAR(w) asm volatile("s_waitcnt " w "\n\ts_barrier" ::: "memory")
#define SCHED() __builtin_amdgcn_sched_barrier(0)

__device__ __forceinline__ void wait_lds(LAS char* word) {
#pragma unroll 1
    for (unsigned spins = 0; spins < (SPIN_CAP << 4); ++spins) {
        if (__builtin_amdgcn_readfirstlane(__hip_atomic_load((LAS unsigned*)word, __ATOMIC_RELAXED, __HIP_MEMORY_SCOPE_WORKGROUP))) break;
        __builtin_amdgcn_s_sleep(1);
    }
    asm volatile("" ::: "memory");
}
__device__ __forceinline__ void wait_lds_eq(LAS char* word, unsigned want) {
#pragma unroll 1
    for (unsigned spins = 0; spins < (1u << 22); ++spins) {
        if ((unsigned)__builtin_amdgcn_readfirstlane(__hip_atomic_load((LAS unsigned*)word, __ATOMIC_RELAXED, __HIP_MEMORY_SCOPE_WORKGROUP)) == want) break;
        __builtin_amdgcn_s_sleep(1);
    }
    asm volatile("" ::: "memory");
}
__global__ __launch_bounds__(512, 2) void fused_kernel(Params p) {
    extern __shared__ __attribute__((aligned(16))) char smem_raw[];
    u64 pc;
    asm volatile("s_getpc_b64 %0" : "=s"(pc));
    LAS char* sm = (LAS char*)smem_raw;
    const int tid = threadIdx.x, lane = tid & 63;
    const int wave = __builtin_amdgcn_readfirstlane(tid >> 6);
    const int xcd = blockIdx.x & 7, idx = blockIdx.x >> 3;
    if (idx == 0 && wave < 5) {
        const unsigned o = (unsigned)tid * 128u;
        asm volatile("s_mov_b32 m0, %2\n\ts_nop 0\n\tglobal_load_lds_dword %0, %1" :: "v"(o < CODE_PREFETCH ? o : 0u), "s"(pc & ~(u64)255),
                     "s"((unsigned)(size_t)sm + 32768u));
    }
    SCHED();
    const __amdgpu_buffer_rsrc_t wsr = __builtin_amdgcn_make_buffer_rsrc(p.ws, 0, WS_END, 0x00020000);
    asm volatile("" :: "s"(p.x), "s"(p.k), "s"(p.v), "s"(p.sk), "s"(p.hk), "s"(p.dv), "s"(p.w1), "s"(p.w3), "s"(p.w2), "s"(p.out), "s"(p.ws));

    if (idx < 4) {
        const int ca = (xcd >> 1) * 4 + idx, cmat = ca >= 7, cnt = 7 * (xcd & 1) + (ca >= 7 ? ca - 7 : ca);
        const bool job = ca < 14;
        u32x4 xr[7], wr[14];
        const int row = (8 * (xcd >> 1) + 4 * (xcd & 1) + idx) * 64 + wave * 8 + (lane >> 3);
#pragma unroll
        for (int i = 0; i < 7; ++i) xr[i] = *(const u32x4*)(p.x + (size_t)row * D + i * 64 + (lane & 7) * 8);
        const u64 epoch = __hip_atomic_load((GAS u64*)(p.ws + WS_EPOCH), RLX_AGENT);
        const u64 tag = epoch ^ TAG_SALT;
        const unsigned tag32 = (unsigned)tag;
        unsigned stat;
        {
            float s4[4] = {0.f, 0.f, 0.f, 0.f};
#pragma unroll
            for (int i = 0; i < 7; ++i)
#pragma unroll
                for (int j = 0; j < 4; ++j) s4[j] = dot2(xr[i][j], 0x3f803f80u, s4[j]);
            float sum = (s4[0] + s4[1]) + (s4[2] + s4[3]);
            sum += __shfl_xor(sum, 1); sum += __shfl_xor(sum, 2); sum += __shfl_xor(sum, 4);
            const float mean = sum * (1.f / D);
            f32x2 q4[4] = {{0.f, 0.f}, {0.f, 0.f}, {0.f, 0.f}, {0.f, 0.f}};
#pragma unroll
            for (int i = 0; i < 7; ++i)
#pragma unroll
                for (int j = 0; j < 4; ++j) {
                    const f32x2 d = (f32x2){bflo(xr[i][j]), bfhi(xr[i][j])} - (f32x2){mean, mean};
                    q4[j] = __builtin_elementwise_fma(d, d, q4[j]);
                }
            const f32x2 q2 = (q4[0] + q4[1]) + (q4[2] + q4[3]);
            float sq = q2[0] + q2[1];
            sq += __shfl_xor(sq, 1); sq += __shfl_xor(sq, 2); sq += __shfl_xor(sq, 4);
            stat = pk(mean, __builtin_amdgcn_sqrtf(rbf(rbf(sq * (1.f / D)) + 1e-5f)));
            asm volatile("" : "+v"(stat));
        }
        if (job) {
            SCHED();
            const u16* wp = (cmat ? p.w3 : p.w1) + (size_t)(wave * 4 + (lane >> 4)) * F + cnt * 128 + (lane & 15) * 8;
#pragma unroll
            for (int i = 0; i < 14; ++i) wr[i] = *(const u32x4*)(wp + (size_t)(32 * i) * F);
            SCHED();
        }
        __builtin_amdgcn_raw_buffer_store_b64((u32x2){tag32, stat}, wsr, (lane & 7) == 0 ? WS_STATS + (unsigned)row * 8u : 0xfffffff0u, 0, 16);
        if (job) {
            float c[8];
#pragma unroll
            for (int j = 0; j < 8; ++j) c[j] = 0.f;
#pragma unroll
            for (int i = 0; i < 14; ++i)
#pragma unroll
                for (int j = 0; j < 4; ++j) { c[2 * j] += bflo(wr[i][j]); c[2 * j + 1] += bfhi(wr[i][j]); }
            LAS char* cp = sm + ((wave * 4 + (lane >> 4)) * 136 + (lane & 15) * 8) * 4;
            *(LAS f32x4*)cp = (f32x4){c[0], c[1], c[2], c[3]};
            *(LAS f32x4*)(cp + 16) = (f32x4){c[4], c[5], c[6], c[7]};
        }
        if (job) {
            WAIT_BAR("lgkmcnt(0)");
            float t = 0.f;
#pragma unroll
            for (int i = 0; i < 8; ++i) t += *(LAS float*)(sm + ((i * 4 + (tid & 3)) * 136 + (tid >> 2)) * 4);
            t += __shfl_xor(t, 1); t += __shfl_xor(t, 2);
            __builtin_amdgcn_raw_buffer_store_b64((u32x2){tag32, __float_as_uint(t)}, wsr,
                (tid & 3) == 0 ? WS_CSUM + (unsigned)((cnt * 2 + cmat) * 128 + (tid >> 2)) * 8u : 0xfffffff0u, 0, 16);
        }
        SCHED();
        if (!job) __builtin_amdgcn_s_sleep(40);
        const unsigned pf2 = *(const unsigned*)((const char*)p.hk + (size_t)((xcd * 4 + idx) * 448 + wave * 56 + (lane < 56 ? lane : 0)) * 128);
        {
            const unsigned fo = lane * 16u;
            constexpr int PER = ((4 - NTOUCH) * 14336 + 4095) / 4096;
#pragma unroll 1
            for (int i = wave; i < 7 * PER; i += 8) {
                const int g = i / PER, j = i - g * PER;
                int o = NTOUCH * 14336 + j * 4096; o = o > 57344 - 4096 ? 57344 - 4096 : o;
                glds16x4((const char*)p.w2 + (size_t)idx * 401408 + g * 57344 + o, (u32x4){fo, fo, fo, fo}, (unsigned)(size_t)sm + 65536u + wave * 4096);
            }
        }
        asm volatile("" :: "v"(pf2));
        if (blockIdx.x == 0 && wave == 7) {
            GAS u64* gp = (GAS u64*)(p.ws + WS_STATS) + lane * 8;
            GAS u64* fp[4];
#pragma unroll
            for (int m = 0; m < 4; ++m) {
                const int g = m * 56 + (lane < 56 ? lane : 0);
                fp[m] = (GAS u64*)(p.ws + WS_U + (unsigned)((g >> 5) * 64 + 8) * 8192u + (unsigned)(1984 - (g & 31) * 64) * 2u);
            }
#pragma unroll 1
            for (unsigned spins = 0; spins < SPIN_CAP; ++spins) {
                __builtin_amdgcn_s_sleep(32);
                bool ok = true;
#pragma unroll
                for (int m = 0; m < 4; ++m)
                    ok &= ((unsigned)__hip_atomic_load(gp + m * 512, RLX_AGENT) == tag32) &
                          ((((unsigned)__hip_atomic_load(fp[m], RLX_AGENT) ^ tag32) & ~Y_TAG) == 0u);
                if (__all(ok)) break;
            }
            if (lane == 0) __hip_atomic_store((GAS u64*)(p.ws + WS_EPOCH), epoch + 1, RLX_AGENT);
        }
        return;
    }

    const int gi = idx - 4, ml = gi & 3, nl = gi >> 2;
    const int mt = 4 * (xcd >> 1) + ml, nt = 7 * (xcd & 1) + nl;
    const int rot1 = (2 * ml + nl) % 6;
    const int m64 = 4 * xcd + ml, n64 = nl;
    const int gpair = xcd * 28 + gi;
    const unsigned lb = (unsigned)(size_t)sm;
    const int wq = wave & 3;
    u32x4 wvo;
    wvo[0] = (unsigned)((lane >> 4) * F + (((lane & 15) ^ ((lane >> 4) << 2)) * 8)) * 2u;
#pragma unroll
    for (int j = 1; j < 4; ++j) wvo[j] = wvo[0] + j * (8 * F - 1024);
    const u16* w1b = sptr(p.w1, wq * 16 * F + nt * 128);
    const u16* w3b = sptr(p.w3, wq * 16 * F + nt * 128);
    auto kst = [&](int si) { if (si == 0) return 0; const int s = si + rot1; return s >= 7 ? s - 6 : s; };
    auto group_w = [&](int si, int m) { glds16x4(sptr(m ? w3b : w1b, kst(si) * 64 * F), wvo, lb + ring(si) + 16384 + m * 16384 + wq * 4096); };
    const int kr0 = (gpair & 31) * 64, kc0 = (gpair >> 5) * 64;
    const int chn = wq >> 1, par = wq & 1, dch = 2 * gpair + chn;
    if (wave < 4) group_w(0, 0);
    {
        const bool isk = lane >= 8 && lane < 14;
        u64 mb_ = vsel(lane == 15, p.dv, vsel(lane == 14, p.hk, vsel(isk, p.k, (u64)p.sk)));
        unsigned mo = (unsigned)((kc0 + wq * 16) * 8 + (lane & 7) * 16);
        mo = isk ? (unsigned)((kr0 ? kr0 - 3 + ((lane - 8) >> 1) : 0) * D + kc0 + wq * 16 + (lane & 1) * 8) * 2u : mo;
        mo = lane == 14 ? (unsigned)dch * 4096u : mo;
        mo = lane == 15 ? (unsigned)(dch & ~7) * 2u : mo;
        const u64 ma = mb_ + mo;
        asm volatile("s_mov_b32 m0, %1\n\ts_nop 0\n\tglobal_load_lds_dwordx4 %0, off" :: "v"(ma), "s"(lb + wave * 1024));
    }
    if (wave < 4) group_w(0, 1);
    SCHED();
    const int rot2 = 8 * ml;
    const int w2i = L_W2 + wq * 12288;
    auto fresh_lane = [&]() { int l = lane; asm volatile("" : "+v"(l)); return l; };
    const int latei = L_CONV + wq * 8192;
    auto pair_w2 = [&](u32x4 vo, int i, int dst) {
        int t = i + rot2; t = t >= 28 ? t - 28 : t;
        glds16x4(sptr(p.w2, (wq * 448 + t * 16) * D + n64 * 64), vo, lb + dst);
    };
    auto w2_off = [&]() {
        const int l = fresh_lane();
        const unsigned o = (unsigned)((l >> 3) * D + (((l & 7) ^ (((l >> 4) & 1) << 2)) * 8)) * 2u;
        return (u32x4){o, o + (16 * D - 1024), o + 2 * (16 * D - 1024), o + 3 * (16 * D - 1024)};
    };

    if (wave < 4) {
        __builtin_amdgcn_s_setprio(1);
        const int wm = wave >> 1, wn = wave & 1;
        const int lm = fresh_lane();
        const int r32 = lm & 31, hh = lm >> 5, blk = (lm >> 4) & 1, q4 = (lm & 15) >> 2, p4 = lm & 3;
        int xa[4], wa0, wa1;
#pragma unroll
        for (int i = 0; i < 4; ++i) xa[i] = lb + ring(0) + (((64 * wm + r32) * 128 + ((hh ^ ((r32 >> 1) & 7)) * 16)) ^ (i * 32));
        wa0 = 16384 + (8 * hh + q4) * 256 + (((8 * wn + 2 * blk + (p4 >> 1)) ^ (q4 << 2)) * 16) + 8 * (p4 & 1);
        wa1 = (wa0 ^ 64) + lb + ring(0); wa0 += lb + ring(0);
        f32x16 acc[2][2][2];
#pragma unroll
        for (int i = 0; i < 8; ++i)
#pragma unroll
            for (int r = 0; r < 16; ++r) acc[i >> 2][(i >> 1) & 1][i & 1][r] = 0.f;
        u32x4 fx[2][2], fw[2][2][2];
        auto load_frag = [&](int g, int j) {
            const int pp = g & 3, b = g & 1;
            if (j == 0) {
                if (g >= 4) {
                    const int d = ring(g >> 2) - ring((g >> 2) - 1);
                    if (pp == 0) { wa0 += d; wa1 += d; asm volatile("" : "+v"(wa0), "+v"(wa1)); }
                    xa[pp] += d; asm volatile("" : "+v"(xa[pp]));
                }
                fw[b][0][0] = ldstr2(lp(wa0) + pp * 4096, 1024);
            }
            if (j == 1) { fx[b][0] = lds128(lp(xa[pp])); fx[b][1] = lds128(lp(xa[pp]) + 4096); }
            if (j == 2) fw[b][0][1] = ldstr2(lp(wa0) + pp * 4096 + 16384, 1024);
            if (j == 3) fw[b][1][0] = ldstr2(lp(wa1) + pp * 4096, 1024);
            if (j == 4) fw[b][1][1] = ldstr2(lp(wa1) + pp * 4096 + 16384, 1024);
        };
        int ka0[2], kam[2], kwo;
#pragma unroll
        for (int rd = 0; rd < 2; ++rd) {
            const int ck = 2 * wq + rd;
            const int bq = 2 * (lm >> 4) + (p4 >> 1), bm = bq ? bq - 1 : 0;
            ka0[rd] = lb + L_KV + (8 * bq + q4) * 128 + ((ck ^ (((q4 >> 1) & 1) | ((bq & 3) << 1))) * 16) + 8 * (p4 & 1);
            kam[rd] = lb + L_KV + (8 * bm + 4 + q4) * 128 + ((ck ^ (((q4 >> 1) & 1) | ((bm & 3) << 1))) * 16) + 8 * (p4 & 1);
        }
        kwo = lb + L_KVO + wq * 2048 + (lm & 7) * 128 + (((7 - (lm >> 3)) ^ (lm & 7)) * 16);
        asm volatile("" : "+v"(ka0[0]), "+v"(ka0[1]), "+v"(kam[0]), "+v"(kam[1]), "+v"(kwo));
        WAIT_VM(4);
        u32x2 skv[2], hal[2];
#pragma unroll
        for (int rd = 0; rd < 2; ++rd) {
            LAS char* mp = sm + wave * 1024;
            skv[rd] = *(LAS u32x2*)(mp + (rd * 8 + (lm & 7)) * 8);
            const unsigned hm = kr0 ? 0xffffffffu : 0u;
            const unsigned a = *(LAS u16*)(mp + 128 + rd * 16 + (lm & 7) * 2), b = *(LAS u16*)(mp + 160 + rd * 16 + (lm & 7) * 2),
                           c = *(LAS u16*)(mp + 192 + rd * 16 + (lm & 7) * 2);
            hal[rd] = (u32x2){(a << 16) & hm, (b | (c << 16)) & hm};
            asm volatile("" : "+v"(skv[rd]), "+v"(hal[rd]));
        }
        WAIT_BAR("vmcnt(0) lgkmcnt(0)");
        u32x2 kt[2][3], vt[2][2];
        float cf[4], kk[11], ko[8];
        u32x4 kpk;
        auto fill = [&](auto kc) {
            constexpr int k = decltype(kc)::value;
            if constexpr (k < 4) {
                constexpr int rd = k >> 1;
                if constexpr ((k & 1) == 0) { kt[rd][0] = ldstr(lp(kam[rd])); kt[rd][1] = ldstr(lp(ka0[rd])); kt[rd][2] = ldstr(lp(ka0[rd]) + 512); }
                else { vt[rd][0] = ldstr(lp(ka0[rd]) + 8192); vt[rd][1] = ldstr(lp(ka0[rd]) + 8192 + 512); }
            } else if constexpr (k < 4 + 2 * 16) {
                constexpr int rd = (k - 4) / 16, s = (k - 4) % 16;
                if constexpr (s == 0) { cf[0] = bflo1(skv[rd][0]); cf[1] = bfhi1(skv[rd][0]); cf[2] = bflo1(skv[rd][1]); cf[3] = bfhi1(skv[rd][1]); }
                else if constexpr (s == 1) {
                    const unsigned a = lm < 8 ? hal[rd][0] : kt[rd][0][0], b = lm < 8 ? hal[rd][1] : kt[rd][0][1];
                    kk[0] = bfhi1(a); kk[1] = bflo1(b); kk[2] = bfhi1(b);
                } else if constexpr (s < 4) {
                    const u32x2 t = kt[rd][s - 1];
                    kk[4 * s - 5] = bflo1(t[0]); kk[4 * s - 4] = bfhi1(t[0]); kk[4 * s - 3] = bflo1(t[1]); kk[4 * s - 2] = bfhi1(t[1]);
                } else if constexpr (s < 12) {
                    constexpr int tp = (s - 4) >> 1, o0 = ((s - 4) & 1) * 4;
#pragma unroll
                    for (int i = o0; i < o0 + 4; ++i) {
                        if (tp == 0) ko[i] = mul1(kk[i], cf[3]);
                        else fmac1(ko[i], kk[i + tp], cf[3 - tp]);
                    }
                } else {
                    constexpr int j = s - 12;
                    const unsigned v = vt[rd][j >> 1][j & 1];
                    kpk[3 - j] = pk(mul1(ko[2 * j + 1], bfhi1(v)), mul1(ko[2 * j], bflo1(v)));
                    if constexpr (j == 3) *(LAS u32x4*)(lp(kwo) + rd * 1024) = kpk;
                }
            }
        };
#pragma unroll
        for (int j = 0; j < 5; ++j) load_frag(0, j);
        f32x4 cs[2][2][4];
        unsigned tch[NTOUCH], sa[2], rdy;
        int cso = lb + L_CSUM + (64 * wn + 4 * hh) * 4, sto = lb + L_STAT + (64 * wm + r32) * 4;
        asm volatile("" : "+v"(cso), "+v"(sto));
        unsigned tco = (unsigned)(gi * 4 + wq + (lm < 28 ? lm : 27) * 448) * 128u;
        asm volatile("" : "+v"(tco));
        static_for([&](auto gc) {
            constexpr int g = decltype(gc)::value, b = g & 1, pp = g & 3;
            SCHED();
            if (pp == 3 && g < 27) { WAIT_BAR("lgkmcnt(0)"); }
            if (g == 27) {
                WAIT_LGKM0();
                if (lane == 0) __hip_atomic_fetch_add((LAS unsigned*)(sm + L_CTL + 12), 1u, __ATOMIC_RELAXED, __HIP_MEMORY_SCOPE_WORKGROUP);
                if (__builtin_amdgcn_readfirstlane(rdy) != 3u) wait_lds_eq(sm + L_CTL + 64, 3u);
                asm volatile("" ::: "memory");
            }
            static_for([&](auto ic) {
                constexpr int i = decltype(ic)::value, ft = i >> 2, m = (i >> 1) & 1, sb = i & 1;
                acc[sb][ft][m] = mfma32(fw[b][ft][m], fx[b][sb], acc[sb][ft][m]);
                SCHED();
                if (g < 27 && i < 5) load_frag(g + 1, i);
                if (g == 26 && i == 5) rdy = __hip_atomic_load((LAS unsigned*)(sm + L_CTL + 64), __ATOMIC_RELAXED, __HIP_MEMORY_SCOPE_WORKGROUP);
                if constexpr (g == 27) {
                    if (i == 0) { sa[0] = *(LAS unsigned*)lp(sto); sa[1] = *(LAS unsigned*)(lp(sto) + 128); }
                    cs[i >> 2][0][i & 3] = *(LAS f32x4*)(lp(cso) + i * 32);
                    cs[i >> 2][1][i & 3] = *(LAS f32x4*)(lp(cso) + 512 + i * 32);
                }
                if constexpr (i == 4 && pp == 2 && g >= 4 && g < 4 + 4 * NTOUCH) {
                    u64 t;
                    asm volatile("s_mov_b64 %1, %3\n\tglobal_load_dword %0, %2, %1" : "=v"(tch[(g - 4) >> 2]), "=&s"(t)
                                 : "v"(tco), "s"((const char*)p.w2 + ((g - 4) >> 2) * (112 * 128)));
                }
                if constexpr (i >= 5 && g >= 7) fill(std::integral_constant<int, (g - 7) * 3 + i - 5>{});
                SCHED();
            }, std::make_integer_sequence<int, 8>{});
        }, std::make_integer_sequence<int, 28>{});
        const int lw = fresh_lane();
        const int w2a0 = lb + w2i + (4 * (lw >> 5) + ((lw & 15) >> 2)) * 128 + (((2 * ((lw >> 4) & 1) + ((lw & 3) >> 1)) ^ (((lw >> 3) & 1) << 2)) * 16) + 8 * (lw & 1);
        const int w2a1 = w2a0 ^ 64;
        u32x4 w2r[12][2];
        auto park = [&](int i) {
            w2r[i][0] = ldstr2(lp(w2a0) + (i / 6) * L_SLOT + (i % 6) * 2048, 1024);
            w2r[i][1] = ldstr2(lp(w2a1) + (i / 6) * L_SLOT + (i % 6) * 2048, 1024);
        };
#pragma unroll
        for (int sb = 0; sb < 2; ++sb) {
            const float ri = __builtin_amdgcn_rcpf(bfhi(sa[sb])) * -1.44269504088896341f, nm = -bflo(sa[sb]), d2 = bfhi(sa[sb]) * bfhi(sa[sb]);
            const f32x2 ri2 = {ri, ri}, nm2 = {nm, nm}, dd2 = {d2, d2};
#pragma unroll
            for (int ft = 0; ft < 2; ++ft) {
#pragma unroll
                for (int t2 = 0; t2 < 2; ++t2) {
                    f32x2 gu[4], ee[4];
#pragma unroll
                    for (int j = 0; j < 4; ++j) {
                        const int r = 8 * t2 + 2 * j;
                        const f32x2 g = __builtin_elementwise_fma(nm2, (f32x2){cs[ft][0][r >> 2][r & 3], cs[ft][0][r >> 2][(r & 3) + 1]}, (f32x2){acc[sb][ft][0][r], acc[sb][ft][0][r + 1]});
                        const f32x2 u = __builtin_elementwise_fma(nm2, (f32x2){cs[ft][1][r >> 2][r & 3], cs[ft][1][r >> 2][(r & 3) + 1]}, (f32x2){acc[sb][ft][1][r], acc[sb][ft][1][r + 1]});
                        const f32x2 t = g * ri2;
                        ee[j] = (f32x2){__builtin_amdgcn_exp2f(t[0]), __builtin_amdgcn_exp2f(t[1])};
                        gu[j] = g * u;
                    }
                    u32x4 st;
#pragma unroll
                    for (int j = 0; j < 4; ++j) {
                        const f32x2 dn = __builtin_elementwise_fma(ee[j], dd2, dd2);
                        const f32x2 hv = gu[j] * (f32x2){__builtin_amdgcn_rcpf(dn[0]), __builtin_amdgcn_rcpf(dn[1])};
                        st[j] = pk(hv[0], hv[1]);
                    }
                    const unsigned fi = (unsigned)((4 * mt + 2 * wm + sb) * 112 + 8 * nt + 4 * wn + 2 * ft + t2);
                    __builtin_amdgcn_raw_buffer_store_b128(st, wsr, WS_H + fi * 1024u + lm * 16u, 0, 16);
                }
            }
        }
        SCHED();
        const u32x4 w2o = w2_off();
        const u64 tag = *(LAS u64*)(sm + L_CTL + 128);
        GAS u64* myflag = (GAS u64*)(p.ws + WS_FH + ((2 * mt + wm) * 28 + 2 * nt + wn) * 128);
        if (!__builtin_amdgcn_readfirstlane(__hip_atomic_load((LAS unsigned*)(sm + L_CTL + 112 + wq * 4), __ATOMIC_RELAXED, __HIP_MEMORY_SCOPE_WORKGROUP))) {
            WAIT_VM(0);
            if (lane == 0) __hip_atomic_store(myflag, tag, RLX_AGENT);
            wait_lds(sm + L_CTL + 112 + wq * 4);
        }
        asm volatile("" ::: "memory");
#pragma unroll
        for (int i = 6; i < 12; ++i) park(i);
        WAIT_VM(0);
        asm volatile("" :: "v"(tch[0]), "v"(tch[1]));
        if (lane == 0) __hip_atomic_store(myflag, tag, RLX_AGENT);
#pragma unroll
        for (int i = 0; i < 6; ++i) park(i);
        asm volatile("" ::: "memory");
        __hip_atomic_store((LAS unsigned*)(sm + L_CTL + 32 + wq * 4), 1u, __ATOMIC_RELAXED, __HIP_MEMORY_SCOPE_WORKGROUP);
        WAIT_LGKM0();
#pragma unroll
        for (int i = 0; i < 12; ++i) asm volatile("" : "+v"(w2r[i][0]), "+v"(w2r[i][1]));
#pragma unroll
        for (int i = 0; i < 3; ++i) pair_w2(w2o, 12 + 2 * i, w2i + i * 4096);
#pragma unroll
        for (int i = 0; i < 3; ++i) pair_w2(w2o, 18 + 2 * i, w2i + L_SLOT + i * 4096);
        {
            GAS u64* fp = (GAS u64*)(p.ws + WS_FH + (m64 * 28 + 7 * wq + (lw < 7 ? lw : 0)) * 128);
#pragma unroll 1
            for (unsigned spins = 0; spins < SPIN_CAP; ++spins) {
                if (__all(__hip_atomic_load(fp, RLX_AGENT) == tag)) break;
                __builtin_amdgcn_s_sleep(4);
            }
            WAIT_VM(0);
            __builtin_amdgcn_fence(__ATOMIC_ACQUIRE, "wavefront");
        }
        const unsigned hb = WS_H + (unsigned)(2 * m64 * 112 + 28 * wq) * 1024u + lw * 16u;
        u32x4 hf[28][2];
        auto load_h = [&](int i, int j) {
            int t = i + rot2; t = t >= 28 ? t - 28 : t;
            hf[i][j] = __builtin_amdgcn_raw_buffer_load_b128(wsr, hb + j * (112u * 1024u), t * 1024, 16);
        };
#pragma unroll
        for (int i = 0; i < HD; ++i) { load_h(i, 0); load_h(i, 1); }
        f32x16 ac2[2][2];
#pragma unroll
        for (int i = 0; i < 4; ++i)
#pragma unroll
            for (int r = 0; r < 16; ++r) ac2[i >> 1][i & 1][r] = 0.f;
        u32x4 a0 = w2r[0][0], a1 = w2r[0][1];
        static_for([&](auto ic) {
            constexpr int i = decltype(ic)::value, n = i + 1;
            SCHED();
            if (i == YSTEP) __hip_atomic_store((LAS unsigned*)(sm + L_CTL + 96 + wq * 4), 1u, __ATOMIC_RELAXED, __HIP_MEMORY_SCOPE_WORKGROUP);
            if (i == 23) wait_lds(sm + L_CTL + 80 + wq * 4);
            u32x4 b0, b1;
            if constexpr (n < 12) { b0 = w2r[n][0]; b1 = w2r[n][1]; }
            else if constexpr (n < 28) {
                constexpr int so = n < 24 ? ((n - 12) / 6) * L_SLOT + ((n - 12) % 6) * 2048 : (n - 24) * 2048;
                const int lo = n < 24 ? 0 : latei - w2i;
                b0 = ldstr2(lp(w2a0 + lo) + so, 1024);
                b1 = ldstr2(lp(w2a1 + lo) + so, 1024);
            }
            SCHED();
            ac2[0][0] = mfma32(a0, hf[i][0], ac2[0][0]);
            SCHED();
            constexpr int l0 = i < HG ? HD + 2 * i : HD + HG + i;
            if constexpr (l0 < 28) load_h(l0, 0);
            SCHED();
            ac2[1][0] = mfma32(a1, hf[i][0], ac2[1][0]);
            SCHED();
            if constexpr (l0 < 28) load_h(l0, 1);
            SCHED();
            ac2[0][1] = mfma32(a0, hf[i][1], ac2[0][1]);
            SCHED();
            if constexpr (i < HG) load_h(l0 + 1, 0);
            SCHED();
            ac2[1][1] = mfma32(a1, hf[i][1], ac2[1][1]);
            SCHED();
            if constexpr (i < HG) load_h(l0 + 1, 1);
            a0 = b0; a1 = b1;
        }, std::make_integer_sequence<int, 28>{});
        SCHED();
        WAIT_LGKM0();
#pragma unroll
        for (int v = 0; v < 16; ++v) {
            const f32x16& t = ac2[v >> 3][(v >> 2) & 1];
            const int r0 = 4 * (v & 3);
            *(LAS f32x4*)(sm + w2i + part(v >> 3, (v >> 2) & 1, v & 3) + lw * 16) = (f32x4){t[r0], t[r0 + 1], t[r0 + 2], t[r0 + 3]};
        }
        WAIT_BAR("lgkmcnt(0)");
    } else {
        u32x4 xvo;
#pragma unroll
        for (int j = 0; j < 4; ++j) xvo[j] = (unsigned)((8 * j + (lane >> 3)) * D + ((((lane & 7) ^ (lane >> 4)) ^ (4 * (j & 1))) * 8)) * 2u - 1024u * j;
        const u16* xb = sptr(p.x, (mt * 128 + wq * 32) * D);
        auto group_x = [&](int si) { glds16x4(sptr(xb, kst(si) * 64), xvo, lb + ring(si) + wq * 4096); };
        group_x(0);
        group_x(1); group_w(1, 0); group_w(1, 1);
        const int cbi = L_CONV + chn * C_CH;
        LAS char* cb = sm + cbi;
        unsigned kvo[2], uso;
#pragma unroll
        for (int rd = 0; rd < 2; ++rd) kvo[rd] = (unsigned)((lane >> 3) * D + (((lane & 7) ^ (((lane >> 4) & 1) | (((2 * wq + rd) & 3) << 1))) * 8)) * 2u;
        uso = WS_U + (unsigned)(kc0 + wq * 16 + (lane >> 3)) * 8192u + (unsigned)(1984 - kr0 + (lane & 7) * 8) * 2u;
        int kro = lb + L_KVO + wq * 2048 + (lane >> 3) * 128 + (((lane & 7) ^ (lane >> 3)) * 16);
        const int cc = 2 * par + ((lane >> 2) & 1);
        int zo = cbi + C_U + par * C_US + (248 + (lane < 36 ? lane : 35)) * 16;
        zo = lane < 8 ? cbi + cc * C_HS + (lane & 3) * 16 : zo;
        u32x4 w2o = w2_off();
        const unsigned fo = lane * 16u;
        unsigned ao = (wq < 2 ? WS_CSUM + (unsigned)(nt * 2 + wq) * 1024u : WS_STATS + mt * 1024u) + fo;
        asm volatile("" : "+v"(zo), "+v"(w2o), "+v"(kvo[0]), "+v"(kvo[1]), "+v"(uso), "+v"(kro), "+v"(ao));
        unsigned eo = (lane & 1) * 4u;
        asm volatile("" : "+v"(eo));
        WAIT_VM(16);
        unsigned tag32, taghi;
        u32x4 bval, ug[2];
        unsigned dbits = *(LAS u16*)(sm + wave * 1024 + 240 + (dch & 7) * 2);
        {
            const u32x4 h0 = lds128(sm + wave * 1024 + 224);
            const bool s1 = !(cc & 1), s2 = !(cc & 2);
            const unsigned a0 = s1 ? 0u : h0[0], a1 = s1 ? h0[0] : h0[1], a2 = s1 ? h0[1] : h0[2], a3 = s1 ? h0[2] : h0[3];
            const unsigned lm = (lane == 3 || lane == 7) ? 0xffffffffu : 0u;
            bval = (u32x4){(s2 ? 0u : a0) & lm, (s2 ? 0u : a1) & lm, (s2 ? a0 : a2) & lm, (s2 ? a1 : a3) & lm};
            asm volatile("" : "+v"(bval), "+v"(dbits));
        }
        auto kv_store = [&](int rd) {
            LAS unsigned* kp = (LAS unsigned*)(lp(kro) + rd * 1024);
            ug[0][1] = kp[0]; asm volatile("" ::: "memory"); ug[0][3] = kp[1]; asm volatile("" ::: "memory");
            ug[1][1] = kp[2]; asm volatile("" ::: "memory"); ug[1][3] = kp[3];
            __builtin_amdgcn_raw_buffer_store_b128(ug[0], wsr, uso, rd * 65536, 16);
            __builtin_amdgcn_raw_buffer_store_b128(ug[1], wsr, uso, rd * 65536 + 4096, 16);
        };
        WAIT_BAR("vmcnt(12) lgkmcnt(0)");
#pragma unroll
        for (int w = 0; w < 5; ++w) {
            if (w == 3) glds4c(p.ws + WS_EPOCH, eo, lb + L_RAW + wq * 1024);
            group_x(w + 2); group_w(w + 2, 0); group_w(w + 2, 1);
            if (w == 0) {
#pragma unroll
                for (int j = 0; j < 2; ++j) {
                    glds16(sptr(p.k, (kr0 + (2 * wq + j) * 8) * D + kc0), kvo[j], lb + L_KV + (2 * wq + j) * 1024);
                    glds16(sptr(p.v, (kr0 + (2 * wq + j) * 8) * D + kc0), kvo[j], lb + L_KV + 8192 + (2 * wq + j) * 1024);
                }
            }
            if (w == 4) {
                const u32x2 ev = *(LAS u32x2*)(sm + L_RAW + wq * 1024);
                tag32 = __builtin_amdgcn_readfirstlane(ev[0]) ^ (unsigned)TAG_SALT;
                taghi = __builtin_amdgcn_readfirstlane(ev[1]) ^ (unsigned)(TAG_SALT >> 32);
                asm volatile("s_nop 1" : "+s"(tag32), "+s"(taghi));
                ug[0][0] = vmov1(tag32); ug[0][2] = vmov1(tag32); ug[1][0] = vmov1(tag32); ug[1][2] = vmov1(tag32);
                if (lane < 34) *(LAS unsigned*)(sm + L_CTL + lane * 4) = lane < 32 ? 0u : lane == 32 ? tag32 : taghi;
            }
            if (w == 4) kv_store(0);
            if (w == 0) WAIT_BAR("vmcnt(16) lgkmcnt(0)"); else WAIT_BAR("vmcnt(12) lgkmcnt(0)");
        }
        glds16c(p.ws, ao, lb + L_RAW + wq * 1024);
#pragma unroll
        for (int i = 0; i < 3; ++i) pair_w2(w2o, 2 * i, w2i + i * 4096);
        kv_store(1);
        WAIT_BAR("vmcnt(12) lgkmcnt(0)");
        asm volatile("" ::: "memory");
        if (lane == 0) __hip_atomic_fetch_add((LAS unsigned*)(sm + L_CTL + 12), 1u, __ATOMIC_RELAXED, __HIP_MEMORY_SCOPE_WORKGROUP);
        if (wq < 3) {
            u32x4 av = lds128(sm + L_RAW + wq * 1024 + lane * 16);
#pragma unroll 1
            for (unsigned spins = 0; spins < SPIN_CAP; ++spins) {
                if (__all(((av[0] ^ tag32) | (av[2] ^ tag32)) == 0u)) break;
                asm volatile("" ::: "memory");
                av = __builtin_amdgcn_raw_buffer_load_b128(wsr, ao, 0, 16);
                asm volatile("" : "+v"(av));
            }
            *(LAS u32x2*)(sm + L_CSUM + wq * 512 + lane * 8) = (u32x2){av[1], av[3]};
            asm volatile("" ::: "memory");
            if (lane == 0) __hip_atomic_fetch_add((LAS unsigned*)(sm + L_CTL + 64), 1u, __ATOMIC_RELAXED, __HIP_MEMORY_SCOPE_WORKGROUP);
        }
#pragma unroll
        for (int i = 0; i < 3; ++i) pair_w2(w2o, 6 + 2 * i, w2i + L_SLOT + i * 4096);
        WAIT_VM(12);
        wait_lds_eq(sm + L_CTL + 12, 8u);
        __builtin_amdgcn_s_setprio(2);
        const unsigned ubase = WS_U + (unsigned)dch * 8192u;
        glds16x2c(p.ws + ubase + par * 2048, fo, lb + cbi + C_U + par * 2048);
        glds16x2c(p.ws + ubase + 4096 + par * 2048, fo, lb + cbi + C_U + C_US + par * 2048);
        glds4c(p.ws + ubase + (1 - par) * 2048, eo, lb + L_UNX + wq * 256);
        {
            const u32x4 fvo = {fo, fo, fo, lane == 63 ? 62u * 16u : fo};
#pragma unroll
            for (int c = 0; c < 2; ++c) glds16x4(sptr(p.hk, dch * S + 4 * par + 2 * c + 2), fvo, lb + cbi + (2 * par + c) * C_HS + 64);
        }
        *(LAS u32x4*)(sm + zo) = bval;
        WAIT_VM(13);
        __hip_atomic_store((LAS unsigned*)(sm + L_CTL + 112 + wq * 4), 1u, __ATOMIC_RELAXED, __HIP_MEMORY_SCOPE_WORKGROUP);
        WAIT_VM(8);
        {
            u32x4 ua[2], ub[2]; u32x2 un[2];
#pragma unroll
            for (int rd = 0; rd < 2; ++rd) {
                const int x8 = 128 * par + 64 * rd + lane;
                ua[rd] = lds128(cb + C_U + x8 * 16); ub[rd] = lds128(cb + C_U + C_US + x8 * 16);
                un[rd] = *(LAS u32x2*)((rd == 1 && lane == 63) ? sm + L_UNX + wq * 256 : cb + C_U + x8 * 16 + 16);
            }
#pragma unroll 1
            for (unsigned spins = 0; spins < SPIN_CAP; ++spins) {
                unsigned df = 0;
#pragma unroll
                for (int rd = 0; rd < 2; ++rd) df |= (ua[rd][0] ^ tag32) | (ua[rd][2] ^ tag32) | (ub[rd][0] ^ tag32) | (ub[rd][2] ^ tag32) | (un[rd][0] ^ tag32);
                if (__all(df == 0u)) break;
                __builtin_amdgcn_s_sleep(4);
                asm volatile("" ::: "memory");
#pragma unroll
                for (int rd = 0; rd < 2; ++rd) {
                    const int x8 = 128 * par + 64 * rd + lane;
                    const unsigned uo = ubase + (unsigned)x8 * 16u;
                    ua[rd] = __builtin_amdgcn_raw_buffer_load_b128(wsr, uo, 0, 16);
                    ub[rd] = __builtin_amdgcn_raw_buffer_load_b128(wsr, uo + 4096u, 0, 16);
                    un[rd] = __builtin_amdgcn_raw_buffer_load_b64(wsr, x8 == 255 ? uo : uo + 16u, 0, 16);
                }
                asm volatile("" : "+v"(ua[0]), "+v"(ub[0]), "+v"(un[0]), "+v"(ua[1]), "+v"(ub[1]), "+v"(un[1]));
            }
#pragma unroll
            for (int rd = 0; rd < 2; ++rd) {
                const int x8 = 128 * par + 64 * rd + lane;
                const unsigned nx = x8 == 255 ? 0u : un[rd][1];
                *(LAS u32x4*)(cb + C_U + x8 * 16) = (u32x4){__builtin_amdgcn_alignbit(ua[rd][3], ua[rd][1], 16), __builtin_amdgcn_alignbit(ub[rd][1], ua[rd][3], 16),
                                                            __builtin_amdgcn_alignbit(ub[rd][3], ub[rd][1], 16), __builtin_amdgcn_alignbit(nx, ub[rd][3], 16)};
                *(LAS u32x4*)(cb + C_U + C_US + x8 * 16) = (u32x4){ua[rd][1], ua[rd][3], ub[rd][1], ub[rd][3]};
            }
        }
        asm volatile("s_waitcnt vmcnt(0) lgkmcnt(0)" ::: "memory");
        __hip_atomic_store((LAS unsigned*)(sm + L_CTL + 16 + wq * 4), 1u, __ATOMIC_RELAXED, __HIP_MEMORY_SCOPE_WORKGROUP);
        u32x4 lw2[8], xv[2];
        {
            const __amdgpu_buffer_rsrc_t wr2 = __builtin_amdgcn_make_buffer_rsrc((void*)p.w2, 0, F * D * 2, 0x00020000);
#pragma unroll
            for (int j = 0; j < 8; ++j) {
                int t = 24 + (j >> 2) * 2 + rot2; t = t >= 28 ? t - 28 : t;
                lw2[j] = __builtin_amdgcn_raw_buffer_load_b128(wr2, w2o[0], ((wq * 448 + t * 16 + (j & 3) * 8) * D + n64 * 64) * 2, 0);
            }
        }
        const int lf = fresh_lane();
        const int srow = 64 * m64 + 16 * wq + 2 * (lf >> 3);
        const unsigned xoff = (unsigned)(srow * D + 64 * n64 + 8 * (lf & 7)) * 2u;
        {
            const __amdgpu_buffer_rsrc_t xr = __builtin_amdgcn_make_buffer_rsrc((void*)p.x, 0, S * D * 2, 0x00020000);
            xv[0] = __builtin_amdgcn_raw_buffer_load_b128(xr, xoff, 0, 0);
            xv[1] = __builtin_amdgcn_raw_buffer_load_b128(xr, xoff, D * 2, 0);
        }
        SCHED();
        wait_lds(sm + L_CTL + 16 + (wq ^ 1) * 4);
        const int n16 = lane & 15, kg = lane >> 4;
        f32x4 ca[4];
#pragma unroll
        for (int j = 0; j < 4; ++j) ca[j] = (f32x4){0.f, 0.f, 0.f, 0.f};
        int apo = lb + cbi + (n16 & 3) * C_HS + ((n16 >> 2) + kg) * 16;
        int bpo = lb + cbi + C_U + (n16 & 1) * C_US + (252 - 4 * (n16 >> 1) + kg) * 16 - 7 * 512;
        u32x4 fa[2][8], fb[2][8];
#pragma unroll
        for (int i = 0; i < 8; ++i) { fa[0][i] = lds128(lp(apo) + i * 512); fb[0][i] = lds128(lp(bpo) + (7 - i) * 512); }
        const bool odd = lf & 1;
        int ysrc = (2 * (lf >> 3) + (lf & 1) + 16 * ((lf >> 1) & 3)) * 4;
        unsigned yso = WS_Y + (unsigned)dch * 8192u + lf * 16u;
        f32x4 kd[4];
        {
            const float dval = bflo(dbits);
            const int sl0 = 32 * ((lf & 15) >> 1) + 8 * (lf >> 4) + 4 * (lf & 1);
#pragma unroll
            for (int j = 0; j < 4; ++j) {
                const int tj = (j == 0 ? 0 : j == 1 ? 3 : j == 2 ? 4 : 7) ^ par;
                const u32x2 kvr = *(LAS u32x2*)(cb + C_U + C_US + (2044 - 256 * tj - sl0) * 2);
                const unsigned ka = pk(bfhi(kvr[1]) * dval, bflo(kvr[1]) * dval), kb = pk(bfhi(kvr[0]) * dval, bflo(kvr[0]) * dval);
                kd[j] = (f32x4){bflo(ka), bfhi(ka), bflo(kb), bfhi(kb)};
                asm volatile("" : "+v"(kd[j]));
            }
            asm volatile("" : "+v"(ysrc), "+v"(yso));
        }
        wait_lds(sm + L_CTL + 32 + wq * 4);
        auto conv_all = [&](auto pc_) {
            constexpr int P = decltype(pc_)::value;
            static_for([&](auto rc) {
                constexpr int r = decltype(rc)::value, c = r & 1, n = c ^ 1;
                static_for([&](auto kc) {
                    constexpr int k = decltype(kc)::value;
                    constexpr int T[4] = {P, 3 - P, 4 + P, 7 - P};
                    constexpr int j = k <= T[0] ? 0 : k <= T[0] + T[1] + 1 ? 1 : k <= T[0] + T[1] + T[2] + 2 ? 2 : 3;
                    constexpr int pp = k - (j == 0 ? 0 : j == 1 ? T[0] + 1 : j == 2 ? T[0] + T[1] + 2 : T[0] + T[1] + T[2] + 3);
                    ca[j] = mfma16(fa[c][pp], fb[c][T[j] - pp], ca[j]);
                    SCHED();
                    if constexpr (r < 7 && k < 2 * (T[3] + 1)) {
                        if constexpr (k & 1) fb[n][k >> 1] = lds128(lp(bpo) + (r + 1) * 64 + (7 - (k >> 1)) * 512);
                        else fa[n][k >> 1] = lds128(lp(apo) + (r + 1) * 64 + (k >> 1) * 512);
                    }
                    SCHED();
                }, std::make_integer_sequence<int, 18>{});
                asm volatile("" : "+v"(apo), "+v"(bpo));
            }, std::make_integer_sequence<int, 8>{});
        };
        if (par) conv_all(std::integral_constant<int, 1>{}); else conv_all(std::integral_constant<int, 0>{});
        SCHED();
        {
            float r0[4], r1[4];
#pragma unroll
            for (int j = 0; j < 4; ++j) { r0[j] = dpp_xor1(odd ? ca[j][0] : ca[j][2]); r1[j] = dpp_xor1(odd ? ca[j][1] : ca[j][3]); }
            unsigned rp[4][2];
#pragma unroll
            for (int j = 0; j < 4; ++j) {
                const unsigned ya = pk(odd ? r0[j] : ca[j][0], odd ? ca[j][2] : r0[j]), yb = pk(odd ? r1[j] : ca[j][1], odd ? ca[j][3] : r1[j]);
                rp[j][0] = __builtin_amdgcn_ds_bpermute(ysrc, pk(add1(bflo1(ya), kd[j][0]), add1(bfhi1(ya), kd[j][1])));
                rp[j][1] = __builtin_amdgcn_ds_bpermute(ysrc, pk(add1(bflo1(yb), kd[j][2]), add1(bfhi1(yb), kd[j][3])));
            }
#pragma unroll
            for (int j = 0; j < 4; ++j)
                __builtin_amdgcn_raw_buffer_store_b128((u32x4){vmov1(tag32 ^ Y_TAG), rp[j][0], vmov1(tag32 ^ Y_TAG), rp[j][1]}, wsr, yso,
                                                       ((j == 0 ? 0 : j == 1 ? 3 : j == 2 ? 4 : 7) ^ par) * 1024, 16);
        }
        WAIT_LGKM0();
        if (lane == 0) __hip_atomic_fetch_add((LAS unsigned*)(sm + L_CTL + 8), 1u, __ATOMIC_RELAXED, __HIP_MEMORY_SCOPE_WORKGROUP);
#pragma unroll 1
        for (unsigned spins = 0; spins < (1u << 22); ++spins) {
            if (__builtin_amdgcn_readfirstlane(__hip_atomic_load((LAS unsigned*)(sm + L_CTL + 8), __ATOMIC_RELAXED, __HIP_MEMORY_SCOPE_WORKGROUP)) == 4u) break;
            __builtin_amdgcn_s_sleep(1);
        }
        asm volatile("" ::: "memory");
#pragma unroll
        for (int j = 0; j < 8; ++j) *(LAS u32x4*)(sm + latei + j * 1024 + lf * 16) = lw2[j];
        asm volatile("" ::: "memory");
        __hip_atomic_store((LAS unsigned*)(sm + L_CTL + 80 + wq * 4), 1u, __ATOMIC_RELAXED, __HIP_MEMORY_SCOPE_WORKGROUP);
        wait_lds(sm + L_CTL + 96 + wq * 4);
        u32x2 yv[8];
        u32x4 stv;
        const int yl = L_CONV + 32768 + wq * 4096;
        const unsigned yg = (unsigned)(((lf >> 2) & 7) * 8 + (lf >> 5)) * 8192u + (unsigned)((lf & 3) ^ (((lf >> 4) & 1) << 1)) * 16u;
        const u32x4 yvo = {yg, yg + (16384u - 1024u), yg + 2 * (16384u - 1024u), yg + 3 * (16384u - 1024u)};
        const char* ybase = p.ws + WS_Y + (unsigned)(64 * n64) * 8192u + (unsigned)(64 * m64 + 16 * wq) * 4u;
        const int yr = lb + yl + (lf & 7) * 64 + (((lf >> 3) * 8) ^ (((lf >> 2) & 1) << 5));
#pragma unroll 1
        for (unsigned spins = 0; spins < SPIN_CAP; ++spins) {
            glds16x4c(ybase, yvo, lb + yl);
            stv = __builtin_amdgcn_raw_buffer_load_b128(wsr, WS_STATS + (unsigned)srow * 8u, 0, 16);
            WAIT_VM(0);
#pragma unroll
            for (int i = 0; i < 8; ++i) yv[i] = *(LAS u32x2*)(lp(yr) + i * 512);
            unsigned df = (stv[0] ^ tag32) | (stv[2] ^ tag32);
#pragma unroll
            for (int i = 0; i < 8; ++i) df |= yv[i][0] ^ (tag32 ^ Y_TAG);
            if (__all(df == 0u)) break;
            __builtin_amdgcn_s_sleep(8);
        }
        __builtin_amdgcn_s_setprio(0);
        f32x2 sf[8], xf[8];
#pragma unroll
        for (int e = 0; e < 2; ++e) {
            const float nmb = -bflo(stv[1 + 2 * e]);
            float ri = __builtin_amdgcn_rcpf(bfhi(stv[1 + 2 * e]));
            asm volatile("s_nop 1" : "+v"(ri));
#pragma unroll
            for (int j = 0; j < 4; ++j) {
                const int i = 4 * e + j;
                const unsigned xd = xv[e][j];
                xf[i] = (f32x2){bflo1(xd), bfhi1(xd)};
                const unsigned tp = pk(add1(xf[i][0], nmb), add1(xf[i][1], nmb));
                const unsigned np = pk(mul1(bflo1(tp), ri), mul1(bfhi1(tp), ri));
                sf[i] = (f32x2){mul1(bflo1(np), e ? bfhi1(yv[2 * j][1]) : bflo1(yv[2 * j][1])), mul1(bfhi1(np), e ? bfhi1(yv[2 * j + 1][1]) : bflo1(yv[2 * j + 1][1]))};
                asm volatile("" : "+v"(sf[i]), "+v"(xf[i]));
            }
        }
        const int rm = lb + L_W2 + part((lf >> 2) & 1, wq >> 1, lf & 3) + (16 * (wq & 1) + 2 * (lf >> 3)) * 16;
        WAIT_BAR("lgkmcnt(0)");
        f32x4 o[4];
#pragma unroll
        for (int j = 0; j < 4; ++j) {
            LAS char* rp = lp(rm) + (j >> 1) * 16 + (j & 1) * 512;
            o[j] = (*(LAS f32x4*)rp + *(LAS f32x4*)(rp + 12288)) + (*(LAS f32x4*)(rp + 24576) + *(LAS f32x4*)(rp + 36864));
        }
        f32x2 so[8], ev[8];
#pragma unroll
        for (int i = 0; i < 8; ++i) {
            const f32x2 ov = {o[i >> 1][(i & 1) * 2], o[i >> 1][(i & 1) * 2 + 1]};
            const f32x2 t = ov * (f32x2){-1.44269504088896341f, -1.44269504088896341f};
            ev[i] = (f32x2){__builtin_amdgcn_exp2f(t[0]), __builtin_amdgcn_exp2f(t[1])};
            so[i] = sf[i] * ov;
        }
        SCHED();
        u32x4 st[2];
#pragma unroll
        for (int i = 0; i < 8; ++i) {
            const f32x2 dn = ev[i] + (f32x2){1.f, 1.f};
            const f32x2 r = __builtin_elementwise_fma(so[i], (f32x2){__builtin_amdgcn_rcpf(dn[0]), __builtin_amdgcn_rcpf(dn[1])}, xf[i]);
            st[i >> 2][i & 3] = pk(r[0], r[1]);
        }
        const __amdgpu_buffer_rsrc_t orr = __builtin_amdgcn_make_buffer_rsrc((void*)p.out, 0, S * D * 2, 0x00020000);
        __builtin_amdgcn_raw_buffer_store_b128(st[0], orr, xoff, 0, 16);
        __builtin_amdgcn_raw_buffer_store_b128(st[1], orr, xoff, D * 2, 16);
    }
}

extern "C" void kernel_launch(void* const* d_in, const int* in_sizes, int n_in,
                              void* d_out, int out_size, void* d_ws, size_t ws_size,
                              hipStream_t stream) {
    Params p;
    p.x = (const u16*)d_in[0]; p.k = (const u16*)d_in[1]; p.v = (const u16*)d_in[2];
    p.sk = (const u16*)d_in[3]; p.hk = (const u16*)d_in[4]; p.dv = (const u16*)d_in[5];
    p.w1 = (const u16*)d_in[7]; p.w3 = (const u16*)d_in[8]; p.w2 = (const u16*)d_in[9];
    p.out = (u16*)d_out; p.ws = (char*)d_ws;
    (void)hipFuncSetAttribute((const void*)fused_kernel, hipFuncAttributeMaxDynamicSharedMemorySize, L_TOTAL);
    fused_kernel<<<256, 512, L_TOTAL, stream>>>(p);
}
```
